```python
import torch

def _softmax(x, axis=-1):
    return torch.softmax(x, dim=axis)
def _gelu(x):
    return torch.nn.functional.gelu(x, approximate="tanh")
def _silu(x):
    return torch.nn.functional.silu(x)
def _sigmoid(x):
    return torch.sigmoid(x)
def _layernorm(x, axis=-1, eps=1e-5):
    m = x.mean(dim=axis, keepdim=True)
    v = x.var(dim=axis, keepdim=True, unbiased=False)
    return (x - m) / torch.sqrt(v + eps)
def _scatter_add(tgt, idx, src, axis=0):
    return tgt.clone().index_add_(axis, idx, src)
def _segment_sum(src, seg, n):
    out = torch.zeros((n,) + src.shape[1:], dtype=src.dtype)
    return out.index_add_(0, seg, src)
def _pad(x, pw):
    flat = tuple(v for pair in reversed(pw) for v in pair)
    return torch.nn.functional.pad(x, flat)

kernel_name = 'sequential_preLN__spectral_fnet__dense_activation_squared_relu__prompt'

def setup_inputs():

    _w = (lambda a: (lambda t: t.to(torch.bfloat16) if t.is_floating_point() else t)(torch.as_tensor(a)))
    def _A_setup():
        B, S, D = 1, 2048, 1024
        ns = torch.arange(S)[:, None] * torch.arange(S)[None, :]
        nd = torch.arange(D)[:, None] * torch.arange(D)[None, :]
        Ws_re = torch.cos(-2.0 * torch.pi * ns / S).to(float)
        Ws_im = torch.sin(-2.0 * torch.pi * ns / S).to(float)
        Wd_re = torch.cos(-2.0 * torch.pi * nd / D).to(float)
        Wd_im = torch.sin(-2.0 * torch.pi * nd / D).to(float)
        return {
            "x": _w((lambda *s: torch.randn(*s, dtype=torch.bfloat16) * 0.05)(B, S, D)),
            "dft_seq_re": _w(Ws_re),
            "dft_seq_im": _w(Ws_im),
            "dft_hid_re": _w(Wd_re),
            "dft_hid_im": _w(Wd_im),
        }
    _A_in = _A_setup()

    def _B_setup():
        B, S, D, F = 1, 2048, 1024, 3584
        return {
            "x": _w((lambda *s: torch.randn(*s, dtype=torch.bfloat16) * 0.05)(B, S, D)),
            "w1": _w((lambda *s: torch.randn(*s, dtype=torch.bfloat16) * 0.2)(D, F)),
            "b1": _w((lambda *s: torch.randn(*s, dtype=torch.bfloat16) * 0.2)(F)),
            "w2": _w((lambda *s: torch.randn(*s, dtype=torch.bfloat16) * 0.2)(F, D)),
            "b2": _w((lambda *s: torch.randn(*s, dtype=torch.bfloat16) * 0.2)(D)),
        }
    _B_in = _B_setup()

    return {
        "A_x": _A_in["x"],
        "A_dft_seq_re": _A_in["dft_seq_re"],
        "A_dft_seq_im": _A_in["dft_seq_im"],
        "A_dft_hid_re": _A_in["dft_hid_re"],
        "A_dft_hid_im": _A_in["dft_hid_im"],
        "B_x": _B_in["x"],
        "B_w1": _B_in["w1"],
        "B_b1": _B_in["b1"],
        "B_w2": _B_in["w2"],
        "B_b2": _B_in["b2"],
    }

def reference(A_x, A_dft_seq_re, A_dft_seq_im, A_dft_hid_re, A_dft_hid_im, B_x, B_w1, B_b1, B_w2, B_b2):
    def _A_ref(x, dft_seq_re, dft_seq_im, dft_hid_re, dft_hid_im):
        a = torch.einsum("bsd,ed->bse", x, dft_hid_re)
        b = torch.einsum("bsd,ed->bse", x, dft_hid_im)
        y = torch.einsum("ts,bse->bte", dft_seq_re, a) - torch.einsum("ts,bse->bte", dft_seq_im, b)
        return y

    def _B_ref(x, w1, b1, w2, b2):
        h = torch.einsum("bsd,df->bsf", x, w1) + b1
        zero = torch.zeros_like(h)
        r = torch.where(h > zero, h, zero)
        h = r * r
        out = torch.einsum("bsf,fd->bsd", h, w2) + b2
        return out

    x = A_x
    h = x + _A_ref(_layernorm(x, axis=-1), A_dft_seq_re, A_dft_seq_im, A_dft_hid_re, A_dft_hid_im)
    h2 = h + _B_ref(_layernorm(h, axis=-1), B_w1, B_b1, B_w2, B_b2)
    return h2
```

```cpp
#include <hip/hip_runtime.h>
#include <hip/hip_bf16.h>
typedef unsigned short u16;
__device__ __forceinline__ float b2f(u16 v){ return __uint_as_float(((unsigned)v)<<16); }
__device__ __forceinline__ u16 f2b(float f){ unsigned u=__float_as_uint(f); return (u16)((u + 0x7FFF + ((u>>16)&1))>>16); }
constexpr int S=2048, D=1024, F=3584;

__global__ void ln_kernel(const u16* x, u16* y){
  int row=blockIdx.x; int t=threadIdx.x; __shared__ float red[256];
  float v[4]; float s=0;
  for(int i=0;i<4;i++){ v[i]=b2f(x[row*D+t*4+i]); s+=v[i]; }
  red[t]=s; __syncthreads();
  for(int o=128;o>0;o>>=1){ if(t<o) red[t]+=red[t+o]; __syncthreads(); }
  float m=red[0]/D; __syncthreads();
  float q=0; for(int i=0;i<4;i++){ q+=(v[i]-m)*(v[i]-m); }
  red[t]=q; __syncthreads();
  for(int o=128;o>0;o>>=1){ if(t<o) red[t]+=red[t+o]; __syncthreads(); }
  float r=rsqrtf(red[0]/D+1e-5f);
  for(int i=0;i<4;i++) y[row*D+t*4+i]=f2b((v[i]-m)*r);
}
template<int MODE>
__global__ void gemm(const u16* A,int lda,const u16* B,int sbk,int sbn,int K,
                     const u16* A2,const u16* B2,
                     u16* C,int ldc,const u16* bias,const u16* res){
  __shared__ float As[32][33], Bs[32][33];
  int tx=threadIdx.x&31, ty=threadIdx.x>>5; int m0=blockIdx.y*32, n0=blockIdx.x*32;
  float acc[4]={0,0,0,0};
  for(int pass=0;pass<(MODE==1?2:1);pass++){
    const u16* a=pass?A2:A; const u16* b=pass?B2:B; float sg=pass?-1.f:1.f;
    for(int k0=0;k0<K;k0+=32){
      for(int i=0;i<4;i++){ int r=ty+i*8; As[r][tx]=sg*b2f(a[(size_t)(m0+r)*lda+k0+tx]);
        if(sbk==1) Bs[tx][r]=b2f(b[(size_t)(n0+r)*sbn+k0+tx]); else Bs[r][tx]=b2f(b[(size_t)(k0+r)*sbk+n0+tx]); }
      __syncthreads();
      for(int k=0;k<32;k++){ float bv=Bs[k][tx]; for(int i=0;i<4;i++) acc[i]+=As[ty+i*8][k]*bv; }
      __syncthreads();
    }
  }
  for(int i=0;i<4;i++){ int m=m0+ty+i*8, n=n0+tx; float v=acc[i];
    if(MODE==1) v=v+b2f(res[(size_t)m*ldc+n]);
    if(MODE==2){ v+=b2f(bias[n]); v=v>0?v*v:0; }
    if(MODE==3){ v+=b2f(bias[n]); v+=b2f(res[(size_t)m*ldc+n]); }
    C[(size_t)m*ldc+n]=f2b(v);
  }
}
extern "C" void kernel_launch(void* const* d_in, const int* in_sizes, int n_in,
                              void* d_out, int out_size, void* d_ws, size_t ws_size,
                              hipStream_t stream) {
  const u16* x=(const u16*)d_in[0]; const u16* Cr=(const u16*)d_in[1]; const u16* Ci=(const u16*)d_in[2];
  const u16* Dr=(const u16*)d_in[3]; const u16* Di=(const u16*)d_in[4];
  const u16* w1=(const u16*)d_in[6]; const u16* b1=(const u16*)d_in[7]; const u16* w2=(const u16*)d_in[8]; const u16* b2=(const u16*)d_in[9];
  u16* ws=(u16*)d_ws; u16* xn=ws; u16* a=xn+S*D; u16* b=a+S*D; u16* h=b+S*D; u16* hn=h+S*D; u16* r=hn+S*D;
  ln_kernel<<<S,256,0,stream>>>(x,xn);
  gemm<0><<<dim3(D/32,S/32),256,0,stream>>>(xn,D,Dr,1,D,D,nullptr,nullptr,a,D,nullptr,nullptr);
  gemm<0><<<dim3(D/32,S/32),256,0,stream>>>(xn,D,Di,1,D,D,nullptr,nullptr,b,D,nullptr,nullptr);
  gemm<1><<<dim3(D/32,S/32),256,0,stream>>>(Cr,S,a,D,1,S,Ci,b,h,D,nullptr,x);
  ln_kernel<<<S,256,0,stream>>>(h,hn);
  gemm<2><<<dim3(F/32,S/32),256,0,stream>>>(hn,D,w1,F,1,D,nullptr,nullptr,r,F,b1,nullptr);
  gemm<3><<<dim3(D/32,S/32),256,0,stream>>>(r,F,w2,D,1,F,nullptr,nullptr,(u16*)d_out,D,b2,h);
}
```

```cpp
#include <hip/hip_runtime.h>
#include <hip/hip_bf16.h>

typedef unsigned short u16;
typedef unsigned int u32;
typedef unsigned long long u64;
typedef __bf16 bf16x8 __attribute__((ext_vector_type(8)));
typedef __bf16 bf16x2 __attribute__((ext_vector_type(2)));
typedef short s16x4 __attribute__((ext_vector_type(4)));
typedef short s16x8 __attribute__((ext_vector_type(8)));
typedef float f32x2 __attribute__((ext_vector_type(2)));
typedef float f32x4 __attribute__((ext_vector_type(4)));
typedef u32 u32x2 __attribute__((ext_vector_type(2)));
typedef u32 u32x4 __attribute__((ext_vector_type(4)));
typedef __amdgpu_buffer_rsrc_t rsrc_t;
#define LAS __attribute__((address_space(3)))
#define DI __device__ __forceinline__

namespace {

constexpr int S_ = 2048, D_ = 1024, F_ = 3584;
constexpr float LN_EPS = 1e-5f;

constexpr size_t WS_AB = 0;
constexpr size_t WS_H = 8u << 20;
constexpr size_t WS_R = 12u << 20;
constexpr size_t WS_X2 = 27u << 20;
constexpr size_t WS_X4 = 31u << 20;
constexpr size_t WS_ST = 35u << 20;
constexpr size_t WS_CS = 36u << 20;
constexpr u32 SENTINEL = 0xFFFFFFFFu;

#define DIAG_ARG
#define DIAG_BEGIN
#define DIAG_L0
#define DIAG_L1
#define DIAG_END

DI u32 rfl(u32 x) { return __builtin_amdgcn_readfirstlane(x); }
DI u32 cvtpk(float lo, float hi) { f32x2 v = {lo, hi}; return __builtin_bit_cast(u32, __builtin_convertvector(v, bf16x2)); }
DI float blo(u32 v) { return __uint_as_float(v << 16); }
DI float bhi(u32 v) { return __uint_as_float(v & 0xFFFF0000u); }
DI rsrc_t mk_rsrc(const void* p, u32 bytes) { return __builtin_amdgcn_make_buffer_rsrc((void*)p, 0, bytes, 0x00020000); }
DI void bar() { asm volatile("s_barrier" ::: "memory"); }
DI void wait_lgkm0() { __builtin_amdgcn_s_waitcnt(0xC07F); }
template <int N> DI void wait_vm() { asm volatile("s_waitcnt vmcnt(%0)" ::"i"(N) : "memory"); }

template <int N> DI void dma(u32 m0, const u32* v, rsrc_t r, u32 so) {
  if constexpr (N == 4)
    asm volatile("s_mov_b32 m0, %0\n\ts_nop 0\n\t"
                 "buffer_load_dwordx4 %1, %5, %6 offen lds\n\t"
                 "buffer_load_dwordx4 %2, %5, %6 offen offset:1024 lds\n\t"
                 "buffer_load_dwordx4 %3, %5, %6 offen offset:2048 lds\n\t"
                 "buffer_load_dwordx4 %4, %5, %6 offen offset:3072 lds"
                 ::"s"(m0), "v"(v[0]), "v"(v[1]), "v"(v[2]), "v"(v[3]), "s"(r), "s"(so) : "memory");
  else if constexpr (N == 3)
    asm volatile("s_mov_b32 m0, %0\n\ts_nop 0\n\t"
                 "buffer_load_dwordx4 %1, %4, %5 offen lds\n\t"
                 "buffer_load_dwordx4 %2, %4, %5 offen offset:1024 lds\n\t"
                 "buffer_load_dwordx4 %3, %4, %5 offen offset:2048 lds"
                 ::"s"(m0), "v"(v[0]), "v"(v[1]), "v"(v[2]), "s"(r), "s"(so) : "memory");
}

template <int NT> DI int kstep(int t, int phi) {
  int k = t;
  if ((t & 3) == phi) { k = t + 4; if (k >= NT) k -= NT; }
  return k;
}

template <int NST, int NT, int PER, class Issue, class Side>
DI void producer_loop(Issue issue, Side side) {
#pragma unroll
  for (int s = 0; s < NST - 1; s++) issue(s, s);
  wait_vm<(NST - 2) * PER>();
  bar();
  int slot = NST - 1, cur = 0;
  for (int t = 0; t < NT - NST + 1; t++) {
    issue(t + NST - 1, slot);
    side(cur);
    wait_vm<(NST - 2) * PER>();
    bar();
    slot = (slot + 1 == NST) ? 0 : slot + 1;
    cur = (cur + 1 == NST) ? 0 : cur + 1;
  }
  if constexpr (NST == 4) {
    side(cur); wait_vm<PER>(); bar();
    cur = (cur + 1 == NST) ? 0 : cur + 1;
  }
  side(cur); wait_vm<0>(); bar();
  cur = (cur + 1 == NST) ? 0 : cur + 1;
  side(cur);
}

enum { M_ROW = 0, M_TILED = 1, M_TR448 = 2, M_TR256 = 3 };

template <int PT, int PMODE, int QMODE, int PBYTES, int NST, int NT>
struct Consumer {
  static constexpr int QT = 4;
  static constexpr int NPB = PMODE == M_ROW ? 2 : PMODE == M_TILED ? 1 : PMODE == M_TR448 ? 3 : 4;
  static constexpr int QOFF = NST * PBYTES;
  const LAS char* pb[NPB];
  const LAS char* ph[3];
  const LAS char* qb[2];
  bf16x8 fp[2][PT], fq[2][QT];

  DI void init(const LAS char* sm, int wP, int wQ, int lane) {
    const int r = lane & 15, g = lane >> 4;
    if constexpr (PMODE == M_ROW) {
      const int a = (64 * wP + r) * 128 + ((g ^ (r >> 1)) * 16);
      pb[0] = sm + a; pb[1] = sm + (a ^ 64);
    } else if constexpr (PMODE == M_TILED) {
      pb[0] = sm + (4 * wP * 1024 + lane * 16);
    } else if constexpr (PMODE == M_TR448) {
      const int b = g & 1, a = (4 * g + (r >> 2)) * 448 + (r & 3) * 8;
      pb[0] = sm + (a + (8 * wP + b) * 32);
      pb[1] = sm + (a + (8 * wP - b) * 32);
      pb[2] = sm + (a + ((6 + wP) ^ b) * 32);
#pragma unroll
      for (int i = 0; i < 3; i++) { ph[i] = pb[i] + 2 * PBYTES; asm volatile("" : "+v"(ph[i])); }
    } else {
      const int k = 4 * g + (r >> 2), a = k * 256 + (r & 3) * 8;
#pragma unroll
      for (int i = 0; i < 4; i++) pb[i] = sm + (a + (((4 * wP + i) ^ (k & 7)) * 32));
    }
    if constexpr (QMODE == M_ROW) {
      const int a = QOFF + (64 * wQ + r) * 128 + ((g ^ (r >> 1)) * 16);
      qb[0] = sm + a; qb[1] = sm + (a ^ 64);
    } else {
      qb[0] = sm + (QOFF + 4 * wQ * 1024 + lane * 16); qb[1] = qb[0];
    }
    asm volatile("" : "+v"(qb[0]));
    if constexpr (QMODE == M_ROW) asm volatile("" : "+v"(qb[1]));
  }
  static DI bf16x8 tr2(const LAS char* p, int second) {
    s16x4 a = __builtin_amdgcn_ds_read_tr16_b64_v4i16((LAS s16x4*)p);
    s16x4 b = __builtin_amdgcn_ds_read_tr16_b64_v4i16((LAS s16x4*)(p + second));
    return __builtin_bit_cast(bf16x8, __builtin_shufflevector(a, b, 0, 1, 2, 3, 4, 5, 6, 7));
  }
  template <int SLOT, int KS> DI bf16x8 ldp(int i) {
    constexpr int so = SLOT * PBYTES;
    if constexpr (PMODE == M_ROW) return *(const LAS bf16x8*)(pb[KS] + (so + i * 2048));
    else if constexpr (PMODE == M_TILED) return *(const LAS bf16x8*)(pb[0] + (so + KS * 8192 + i * 1024));
    else if constexpr (PMODE == M_TR448) {
      if constexpr (SLOT < 2) return tr2((i == 6 ? pb[2] : pb[i & 1] + i * 32) + (so + KS * 32 * 448), 16 * 448);
      else return tr2((i == 6 ? ph[2] : ph[i & 1] + i * 32) + KS * 32 * 448, 16 * 448);
    }
    else return tr2(pb[i] + (so + KS * 32 * 256), 16 * 256);
  }
  template <int SLOT, int KS> DI bf16x8 ldq(int j) {
    constexpr int so = SLOT * 16384;
    if constexpr (QMODE == M_ROW) return *(const LAS bf16x8*)(qb[KS] + (so + j * 2048));
    else return *(const LAS bf16x8*)(qb[0] + (so + KS * 8192 + j * 1024));
  }
  template <int SLOT, int KS> DI void load() {
    fp[KS][0] = ldp<SLOT, KS>(0);
#pragma unroll
    for (int j = 0; j < QT; j++) fq[KS][j] = ldq<SLOT, KS>(j);
#pragma unroll
    for (int i = 1; i < PT; i++) fp[KS][i] = ldp<SLOT, KS>(i);
  }
  template <int KS> DI void mma(f32x4 (&acc)[PT][QT]) {
#pragma unroll
    for (int i = 0; i < PT; i++)
#pragma unroll
      for (int j = 0; j < QT; j++) acc[i][j] = __builtin_amdgcn_mfma_f32_16x16x32_bf16(fp[KS][i], fq[KS][j], acc[i][j], 0, 0, 0);
  }
  static DI void interleave() {
    constexpr int NR = PT * (PMODE >= M_TR448 ? 2 : 1) + QT, NM = PT * QT;
#pragma unroll
    for (int i = 0; i < NR; i++) { __builtin_amdgcn_sched_group_barrier(0x8, 1, 0); __builtin_amdgcn_sched_group_barrier(0x100, 1, 0); }
    __builtin_amdgcn_sched_group_barrier(0x8, NM - NR, 0);
    __builtin_amdgcn_sched_barrier(0);
  }
  template <int SLOT> DI void step(f32x4 (&acc)[PT][QT]) {
    load<SLOT, 1>(); mma<0>(acc); interleave();
    wait_lgkm0(); bar();
    __builtin_amdgcn_sched_barrier(0);
    load<(SLOT + 1) % NST, 0>(); mma<1>(acc); interleave();
  }
  static DI void zero(f32x4 (&acc)[PT][QT]) {
#pragma unroll
    for (int i = 0; i < PT; i++)
#pragma unroll
      for (int j = 0; j < QT; j++) { acc[i][j] = (f32x4){0.f, 0.f, 0.f, 0.f}; asm volatile("" : "+v"(acc[i][j])); }
  }
  DI void first() { load<0, 0>(); }
  DI void loop(f32x4 (&acc)[PT][QT]) {
    for (int it = 0; it < (NT - 1) / NST; it++) {
      step<0>(acc); step<1>(acc); step<2>(acc);
      if constexpr (NST == 4) step<3>(acc);
    }
    constexpr int REM = (NT - 1) % NST;
    if constexpr (REM > 0) step<0>(acc);
    if constexpr (REM > 1) step<1>(acc);
    if constexpr (REM > 2) step<2>(acc);
  }
  DI void last(f32x4 (&acc)[PT][QT]) {
    load<(NT - 1) % NST, 1>(); mma<0>(acc); interleave();
    mma<1>(acc);
  }
};

DI u32 row_src(int imgrow, int srcrow, int lane, int ldbytes, int i) {
  return srcrow * ldbytes + (((lane & 7) ^ ((imgrow >> 1) & 7)) * 16) - i * 1024;
}

DI void clear_slots(char* x, int p, int lane) {
  u32x4 s = {SENTINEL, SENTINEL, SENTINEL, SENTINEL};
#pragma unroll
  for (int i = 0; i < 4; i++) *(u32x4*)(x + ((size_t)blockIdx.x * 16 + p * 4 + i) * 1024 + lane * 16) = s;
}

DI void xch_send(char* x, const f32x4 (&acc)[4][4], int wid, int lane) {
  rsrc_t r = mk_rsrc(x + ((size_t)blockIdx.x * 4 + wid) * 4096, 4096);
#pragma unroll
  for (int jj = 0; jj < 2; jj++)
#pragma unroll
    for (int u = 0; u < 2; u++) {
      const f32x4 a = acc[2 * u][2 + jj], b = acc[2 * u + 1][2 + jj];
      u32x4 v = {cvtpk(a[0], a[1]), cvtpk(a[2], a[3]), cvtpk(b[0], b[1]), cvtpk(b[2], b[3])};
#pragma unroll
      for (int e = 0; e < 4; e++) v[e] = min(v[e], SENTINEL - 1);
      __builtin_amdgcn_raw_buffer_store_b128(v, r, lane * 16 + (jj * 2 + u) * 1024, 0, 16);
    }
}
DI void xch_recv(char* x, u32x4 (&v)[4], int wid, int lane) {
  rsrc_t r = mk_rsrc(x + ((size_t)(blockIdx.x ^ 4) * 4 + wid) * 4096, 4096);
  const u32 vo = lane * 16;
  for (int spin = 0; spin < (1 << 20); spin++) {
    asm volatile("buffer_load_dwordx4 %0, %4, %5, 0 offen sc1\n\t"
                 "buffer_load_dwordx4 %1, %4, %5, 0 offen offset:1024 sc1\n\t"
                 "buffer_load_dwordx4 %2, %4, %5, 0 offen offset:2048 sc1\n\t"
                 "buffer_load_dwordx4 %3, %4, %5, 0 offen offset:3072 sc1\n\t"
                 "s_waitcnt vmcnt(0)"
                 : "=&v"(v[0]), "=&v"(v[1]), "=&v"(v[2]), "=&v"(v[3]) : "v"(vo), "s"(r) : "memory");
    u32 m = 0;
#pragma unroll
    for (int q = 0; q < 4; q++) m = max(max(m, max(v[q][0], v[q][1])), max(v[q][2], v[q][3]));
    if (__builtin_amdgcn_ballot_w64(m == SENTINEL) == 0) break;
    __builtin_amdgcn_s_sleep(2);
  }
}

constexpr int G1_LDS = 4 * 32768 + 2048;
__global__ __launch_bounds__(512, 2) void g1_kernel(const u16* __restrict__ x, const u16* __restrict__ dre, const u16* __restrict__ dim,
                                                    char* __restrict__ ws DIAG_ARG) {
  extern __shared__ __attribute__((aligned(16))) char smem[];
  DIAG_BEGIN
  constexpr int NST = 4, NT = 16, STB = 32768;
  const int lane = threadIdx.x & 63, wid = rfl(threadIdx.x >> 6);
  const int xcd = blockIdx.x & 7, loc = blockIdx.x >> 3, li = loc >> 2, lj = loc & 3;
  const int half = xcd >> 2, et = ((xcd >> 1) & 1) * 4 + lj, stile = (xcd & 1) * 8 + li;
  LAS char* sm = (LAS char*)smem;
  LAS float* tabA = (LAS float*)(sm + NST * STB);
  LAS float* tabB = tabA + 128;
  LAS float* tabD = tabA + 256;
  if (wid >= 4) {
    const int p = wid - 4, phi = (li + lj) & 3;
    const u32 ldsb = (u32)(size_t)smem + p * 4096;
    rsrc_t rp = mk_rsrc(x + (size_t)stile * 128 * D_, 128 * D_ * 2);
    rsrc_t rq = mk_rsrc((half ? dim : dre) + (size_t)et * 128 * D_, 128 * D_ * 2);
    u32 vp[4], vq[4];
#pragma unroll
    for (int i = 0; i < 4; i++) {
      const int row = 32 * p + 8 * i + (lane >> 3), u = (row >> 4) & 1, ii = row & 15;
      vp[i] = row_src(row, 32 * p + 8 * (ii >> 2) + 4 * u + (ii & 3), lane, D_ * 2, i);
      vq[i] = row_src(row, row, lane, D_ * 2, i);
    }
    float sx[4] = {0, 0, 0, 0}, sxx[4] = {0, 0, 0, 0}, sd[4] = {0, 0, 0, 0};
    const bf16x2 ones = {(__bf16)1.0f, (__bf16)1.0f};
    const LAS char* rd = sm + (p * 4096 + lane * 16);
    producer_loop<NST, NT, 8>(
        [&](int t, int slot) {
          const u32 so = kstep<NT>(t, phi) * 128;
          dma<4>(ldsb + slot * 16384, vp, rp, so);
          dma<4>(ldsb + slot * 16384 + 65536, vq, rq, so);
        },
        [&](int slot) {
          const LAS char* b = rd + slot * 16384;
          const LAS char* b2 = b + 65536;
#pragma unroll
          for (int i = 0; i < 4; i++) {
            const u32x4 a = *(const LAS u32x4*)(b + i * 1024), d = *(const LAS u32x4*)(b2 + i * 1024);
#pragma unroll
            for (int e = 0; e < 4; e++) {
              const u32 ae = a[e], de = d[e];
              const bf16x2 av = __builtin_bit_cast(bf16x2, ae), dv = __builtin_bit_cast(bf16x2, de);
              sx[i] = __builtin_amdgcn_fdot2_f32_bf16(av, ones, sx[i], false);
              sxx[i] = __builtin_amdgcn_fdot2_f32_bf16(av, av, sxx[i], false);
              sd[i] = __builtin_amdgcn_fdot2_f32_bf16(dv, ones, sd[i], false);
            }
          }
          wait_lgkm0();
        });
#pragma unroll
    for (int i = 0; i < 4; i++) {
#pragma unroll
      for (int o = 1; o < 8; o <<= 1) { sx[i] += __shfl_xor(sx[i], o); sxx[i] += __shfl_xor(sxx[i], o); sd[i] += __shfl_xor(sd[i], o); }
      const float mean = sx[i] * (1.0f / D_), var = fmaxf(sxx[i] * (1.0f / D_) - mean * mean, 0.f);
      const float rstd = 1.0f / sqrtf(var + LN_EPS);
      const int row = 32 * p + 8 * i + (lane >> 3);
      if ((lane & 7) == 0) { tabA[row] = rstd; tabB[row] = -mean * rstd; tabD[row] = sd[i]; }
    }
    wait_lgkm0(); bar();
    clear_slots(ws + WS_X2, p, lane);
  } else {
    const int wP = wid >> 1, wQ = wid & 1, c = lane & 15, g = lane >> 4;
    Consumer<4, M_ROW, M_ROW, 16384, NST, NT> C;
    C.init(sm, wP, wQ, lane);
    f32x4 acc[4][4];
    C.zero(acc);
    bar();
    DIAG_L0
    C.first(); C.loop(acc); C.last(acc);
    DIAG_L1
    bar();
    char* dst = ws + WS_AB + ((size_t)((half * 8 + et) * 32 + 2 * stile + wP) * 16 + 4 * wQ) * 1024 + lane * 16;
    f32x4 ra[4], rb[4];
    float sd[4];
#pragma unroll
    for (int i = 0; i < 4; i++) {
      ra[i] = *(const LAS f32x4*)(tabA + 64 * wP + 16 * i + 4 * g);
      rb[i] = *(const LAS f32x4*)(tabB + 64 * wP + 16 * i + 4 * g);
      sd[i] = tabD[64 * wQ + 16 * i + c];
    }
#pragma unroll
    for (int u = 0; u < 2; u++)
#pragma unroll
      for (int j = 0; j < 4; j++) {
        const f32x4 a = ra[2 * u] * acc[2 * u][j] + rb[2 * u] * sd[j], b = ra[2 * u + 1] * acc[2 * u + 1][j] + rb[2 * u + 1] * sd[j];
        *(u32x4*)(dst + (u * 8 + j) * 1024) = (u32x4){cvtpk(a[0], a[1]), cvtpk(a[2], a[3]), cvtpk(b[0], b[1]), cvtpk(b[2], b[3])};
      }
    DIAG_END
  }
}

constexpr int G2_LDS = 4 * 32768;
__global__ __launch_bounds__(512, 2) void g2_kernel(const u16* __restrict__ x, const u16* __restrict__ cre, const u16* __restrict__ cim,
                                                    const u16* __restrict__ w1, char* __restrict__ ws DIAG_ARG) {
  extern __shared__ __attribute__((aligned(16))) char smem[];
  DIAG_BEGIN
  constexpr int NST = 4, NT = 32, STB = 32768;
  const int lane = threadIdx.x & 63, wid = rfl(threadIdx.x >> 6);
  const int xcd = blockIdx.x & 7, loc = blockIdx.x >> 3, li = loc >> 2, lj = loc & 3;
  const int half = xcd >> 2, et = ((xcd >> 1) & 1) * 4 + lj, tt = (xcd & 1) * 8 + li;
  LAS char* sm = (LAS char*)smem;
  if (wid >= 4) {
    const int p = wid - 4, phi = (li + lj) & 3;
    const u32 ldsb = (u32)(size_t)smem + p * 4096;
    rsrc_t rp = mk_rsrc(ws + WS_AB + (size_t)(half * 8 + et) * 32 * 16384, 32 * 16384);
    u32 vp[4], vq[4];
#pragma unroll
    for (int i = 0; i < 4; i++) {
      const int row = 32 * p + 8 * i + (lane >> 3);
      vp[i] = p * 4096 + lane * 16;
      vq[i] = row_src(row, row ^ (half * 32), lane, S_ * 2, 0) + 4096 - i * 1024;
    }
    rsrc_t rq = mk_rsrc((const char*)(half ? cim : cre) + (size_t)tt * 128 * S_ * 2 - 4096, 128 * S_ * 2 + 4096);
    producer_loop<NST, NT, 8>(
        [&](int t, int slot) {
          const int k = kstep<NT>(t, phi);
          dma<4>(ldsb + slot * 16384, vp, rp, k * 16384);
          dma<4>(ldsb + slot * 16384 + 65536, vq, rq, k * 128);
        },
        [&](int) {});
    const int job = blockIdx.x * 4 + p;
    if (job < 896) {
      const int rb = job / 56, cb = job % 56;
      const u16* src = w1 + (size_t)(rb * 64 + (lane >> 3)) * F_ + cb * 64 + (lane & 7) * 8;
      u32x4 v[8];
#pragma unroll
      for (int i = 0; i < 8; i++) v[i] = *(const u32x4*)(src + (size_t)i * 8 * F_);
      float s[8] = {0, 0, 0, 0, 0, 0, 0, 0};
#pragma unroll
      for (int i = 0; i < 8; i++)
#pragma unroll
        for (int e = 0; e < 4; e++) { s[2 * e] += blo(v[i][e]); s[2 * e + 1] += bhi(v[i][e]); }
#pragma unroll
      for (int e = 0; e < 8; e++)
#pragma unroll
        for (int o = 8; o < 64; o <<= 1) s[e] += __shfl_xor(s[e], o);
      if (lane < 8) {
        float* d = (float*)(ws + WS_CS) + rb * F_ + cb * 64 + lane * 8;
        *(f32x4*)d = (f32x4){s[0], s[1], s[2], s[3]};
        *(f32x4*)(d + 4) = (f32x4){s[4], s[5], s[6], s[7]};
      }
    }
  } else {
    const int wP = wid >> 1, wQ = wid & 1, c = lane & 15, g = lane >> 4;
    Consumer<4, M_TILED, M_ROW, 16384, NST, NT> C;
    C.init(sm, wP, wQ, lane);
    f32x4 acc[4][4];
    C.zero(acc);
    bar();
    DIAG_L0
    C.first(); C.loop(acc);
    const int m0 = 4 * wQ + 2 * half;
    u32x2 xr[2][4];
#pragma unroll
    for (int jj = 0; jj < 2; jj++)
#pragma unroll
      for (int i = 0; i < 4; i++) xr[jj][i] = *(const u32x2*)(x + (size_t)(tt * 128 + 16 * (m0 + jj) + c) * D_ + et * 128 + 64 * wP + 16 * i + 4 * g);
    __builtin_amdgcn_sched_barrier(0);
    C.last(acc);
    DIAG_L1
    xch_send(ws + WS_X2, acc, wid, lane);
    u32x4 rv[4];
    xch_recv(ws + WS_X2, rv, wid, lane);
    const float sg = half ? -1.f : 1.f;
#pragma unroll
    for (int jj = 0; jj < 2; jj++) {
      float s = 0.f, q = 0.f;
#pragma unroll
      for (int u = 0; u < 2; u++) {
        const u32x4 o = rv[jj * 2 + u];
        const f32x4 a = acc[2 * u][jj], b = acc[2 * u + 1][jj];
        const u32x2 xa = xr[jj][2 * u], xb = xr[jj][2 * u + 1];
        u32x4 h;
        h[0] = cvtpk(sg * (a[0] - blo(o[0])) + blo(xa[0]), sg * (a[1] - bhi(o[0])) + bhi(xa[0]));
        h[1] = cvtpk(sg * (a[2] - blo(o[1])) + blo(xa[1]), sg * (a[3] - bhi(o[1])) + bhi(xa[1]));
        h[2] = cvtpk(sg * (b[0] - blo(o[2])) + blo(xb[0]), sg * (b[1] - bhi(o[2])) + bhi(xb[0]));
        h[3] = cvtpk(sg * (b[2] - blo(o[3])) + blo(xb[1]), sg * (b[3] - bhi(o[3])) + bhi(xb[1]));
        *(u32x4*)(ws + WS_H + ((size_t)(tt * 32 + 4 * et + 2 * wP + u) * 8 + m0 + jj) * 1024 + lane * 16) = h;
#pragma unroll
        for (int e = 0; e < 4; e++) { const float lo = blo(h[e]), hi = bhi(h[e]); s += lo + hi; q += lo * lo + hi * hi; }
      }
      s += __shfl_xor(s, 16); q += __shfl_xor(q, 16);
      s += __shfl_xor(s, 32); q += __shfl_xor(q, 32);
      if (g == 0) ((f32x2*)(ws + WS_ST))[(tt * 16 + 2 * et + wP) * 128 + 16 * (m0 + jj) + c] = (f32x2){s, q};
    }
    DIAG_END
  }
}

constexpr int G3_STB = 28672 + 16384;
constexpr int G3_LDS = 3 * G3_STB + 4096;
__global__ __launch_bounds__(512, 2) void g3_kernel(const u16* __restrict__ w1, const u16* __restrict__ b1, char* __restrict__ ws DIAG_ARG) {
  extern __shared__ __attribute__((aligned(16))) char smem[];
  DIAG_BEGIN
  constexpr int NST = 3, NT = 16, STB = G3_STB;
  const int lane = threadIdx.x & 63, wid = rfl(threadIdx.x >> 6);
  const int xcd = blockIdx.x & 7, loc = blockIdx.x >> 3, li = loc >> 2, lj = loc & 3;
  const int ft = (xcd >> 1) * 4 + lj, tt = (xcd & 1) * 8 + li;
  LAS char* sm = (LAS char*)smem;
  LAS float* tabA = (LAS float*)(sm + NST * STB);
  LAS float* tabB = tabA + 128;
  LAS float* tabC = tabA + 256;
  LAS float* tabBias = tabA + 512;
  if (wid >= 4) {
    const int p = wid - 4, phi = (li + lj) & 3;
    const u32 ldsb = (u32)(size_t)smem;
    rsrc_t rp = mk_rsrc((const char*)w1 + ft * 448 - 4096, D_ * F_ * 2 - ft * 448 + 4096);
    rsrc_t rq = mk_rsrc(ws + WS_H + (size_t)tt * 32 * 8192, 32 * 8192);
    u32 vp[7], vq[4];
#pragma unroll
    for (int i = 0; i < 7; i++) {
      const int o = (p * 7 + i) * 64 + lane, k = o / 28, ch = o % 28;
      vp[i] = k * (F_ * 2) + ((ch ^ (((k >> 2) & 1) << 1)) * 16) + 4096 - (i & 3) * 1024;
    }
#pragma unroll
    for (int i = 0; i < 4; i++) vq[i] = p * 4096 + lane * 16;
    producer_loop<NST, NT, 11>(
        [&](int t, int slot) {
          const int k = kstep<NT>(t, phi);
          dma<4>(ldsb + 3 * 28672 + slot * 16384 + p * 4096, vq, rq, k * 16384);
          dma<4>(ldsb + slot * 28672 + p * 7168, vp, rp, k * (64 * F_ * 2));
          dma<3>(ldsb + slot * 28672 + p * 7168 + 4096, vp + 4, rp, k * (64 * F_ * 2));
        },
        [&](int) {});
    clear_slots(ws + WS_X4, p, lane);
    const int pt = threadIdx.x - 256;
    {
      const int row = pt >> 1, hf = pt & 1;
      const f32x2* sp = (const f32x2*)(ws + WS_ST) + (tt * 16 + hf * 8) * 128 + row;
      float s = 0.f, q = 0.f;
#pragma unroll
      for (int i = 0; i < 8; i++) { const f32x2 v = sp[i * 128]; s += v[0]; q += v[1]; }
      s += __shfl_xor(s, 1); q += __shfl_xor(q, 1);
      const float mean = s * (1.0f / D_), var = fmaxf(q * (1.0f / D_) - mean * mean, 0.f);
      const float rstd = 1.0f / sqrtf(var + LN_EPS);
      if (hf == 0) { tabA[row] = rstd; tabB[row] = -mean * rstd; }
    }
    if (pt < 224) {
      const float* cs = (const float*)(ws + WS_CS) + ft * 224 + pt;
      float s = 0.f;
#pragma unroll
      for (int i = 0; i < 16; i++) s += cs[i * F_];
      tabC[pt] = s;
      tabBias[pt] = __uint_as_float((u32)b1[ft * 224 + pt] << 16);
    }
    wait_lgkm0(); bar();
  } else {
    const int wP = wid >> 1, wQ = wid & 1, c = lane & 15, g = lane >> 4;
    Consumer<7, M_TR448, M_TILED, 28672, NST, NT> C;
    C.init(sm, wP, wQ, lane);
    f32x4 acc[7][4];
    C.zero(acc);
    bar();
    DIAG_L0
    C.first(); C.loop(acc); C.last(acc);
    DIAG_L1
    bar();
    float ra[4], rb[4];
#pragma unroll
    for (int j = 0; j < 4; j++) { ra[j] = tabA[64 * wQ + 16 * j + c]; rb[j] = tabB[64 * wQ + 16 * j + c]; }
    char* dst = ws + WS_R + ((size_t)(tt * 112 + 7 * ft) * 8 + 4 * wQ) * 1024 + lane * 16;
    auto act = [&](int i, int col, int j, u32& lo, u32& hi) {
      const f32x4 cs = *(const LAS f32x4*)(tabC + col), bi = *(const LAS f32x4*)(tabBias + col);
      f32x4 v = ra[j] * acc[i][j] + (rb[j] * cs + bi);
#pragma unroll
      for (int e = 0; e < 4; e++) { v[e] = v[e] > 0.f ? v[e] : 0.f; v[e] *= v[e]; }
      lo = cvtpk(v[0], v[1]); hi = cvtpk(v[2], v[3]);
    };
#pragma unroll
    for (int u = 0; u < 3; u++)
#pragma unroll
      for (int j = 0; j < 4; j++) {
        u32x4 o;
        u32 a, b;
        act(2 * u, 128 * wP + 32 * u + 4 * g, j, a, b); o[0] = a; o[1] = b;
        act(2 * u + 1, 128 * wP + 32 * u + 16 + 4 * g, j, a, b); o[2] = a; o[3] = b;
        *(u32x4*)(dst + ((4 * wP + u) * 8 + j) * 1024) = o;
      }
#pragma unroll
    for (int j = 0; j < 4; j++) {
      u32 a, b;
      act(6, 96 + 16 * wP + 4 * g, j, a, b);
      *(u32x2*)(dst + (3 * 8 + j) * 1024 + 8 * wP) = (u32x2){a, b};
    }
    DIAG_END
  }
}

constexpr int G4_LDS = 4 * 32768;
__global__ __launch_bounds__(512, 2) void g4_kernel(const u16* __restrict__ w2, const u16* __restrict__ b2, char* __restrict__ ws,
                                                    u16* __restrict__ out DIAG_ARG) {
  extern __shared__ __attribute__((aligned(16))) char smem[];
  DIAG_BEGIN
  constexpr int NST = 4, NT = 28, STB = 32768;
  const int lane = threadIdx.x & 63, wid = rfl(threadIdx.x >> 6);
  const int xcd = blockIdx.x & 7, loc = blockIdx.x >> 3, li = loc >> 2, lj = loc & 3;
  const int half = xcd >> 2, et = ((xcd >> 1) & 1) * 4 + lj, tt = (xcd & 1) * 8 + li;
  LAS char* sm = (LAS char*)smem;
  if (wid >= 4) {
    const int p = wid - 4, phi = (li + lj) & 3;
    const u32 ldsb = (u32)(size_t)smem + p * 4096;
    rsrc_t rp = mk_rsrc(w2 + (size_t)half * 1792 * D_ + et * 128, 1792 * D_ * 2 - et * 256);
    rsrc_t rq = mk_rsrc(ws + WS_R + ((size_t)tt * 112 + half * 56) * 8192 - 4096, 56 * 8192 + 4096);
    u32 vp[4], vq[4];
#pragma unroll
    for (int i = 0; i < 4; i++) {
      const int k = 16 * p + 4 * i + (lane >> 4);
      vp[i] = k * (D_ * 2) + (((lane & 15) ^ ((k & 7) << 1)) * 16) - i * 1024;
      vq[i] = ((4 * p + i) ^ (2 * half)) * 1024 + lane * 16 + 4096 - i * 1024;
    }
    producer_loop<NST, NT, 8>(
        [&](int t, int slot) {
          const int k = kstep<NT>(t, phi);
          dma<4>(ldsb + slot * 16384, vp, rp, k * (64 * D_ * 2));
          dma<4>(ldsb + slot * 16384 + 65536, vq, rq, k * 16384);
        },
        [&](int) {});
  } else {
    const int wP = wid >> 1, wQ = wid & 1, c = lane & 15, g = lane >> 4;
    Consumer<4, M_TR256, M_TILED, 16384, NST, NT> C;
    C.init(sm, wP, wQ, lane);
    f32x4 acc[4][4];
    C.zero(acc);
    bar();
    DIAG_L0
    C.first(); C.loop(acc);
    const int m0 = 4 * wQ + 2 * half;
    u32x4 hr[2][2];
    u32x2 br[4];
#pragma unroll
    for (int jj = 0; jj < 2; jj++)
#pragma unroll
      for (int u = 0; u < 2; u++)
        hr[jj][u] = *(const u32x4*)(ws + WS_H + ((size_t)(tt * 32 + 4 * et + 2 * wP + u) * 8 + m0 + jj) * 1024 + lane * 16);
#pragma unroll
    for (int i = 0; i < 4; i++) br[i] = *(const u32x2*)(b2 + et * 128 + 64 * wP + 16 * i + 4 * g);
    __builtin_amdgcn_sched_barrier(0);
    C.last(acc);
    DIAG_L1
    xch_send(ws + WS_X4, acc, wid, lane);
    u32x4 rv[4];
    xch_recv(ws + WS_X4, rv, wid, lane);
#pragma unroll
    for (int jj = 0; jj < 2; jj++)
#pragma unroll
      for (int u = 0; u < 2; u++) {
        const u32x4 o = rv[jj * 2 + u], h = hr[jj][u];
        const f32x4 a = acc[2 * u][jj], b = acc[2 * u + 1][jj];
        const u32x2 ba = br[2 * u], bb = br[2 * u + 1];
        const u32 a0 = cvtpk(a[0] + blo(o[0]) + blo(ba[0]) + blo(h[0]), a[1] + bhi(o[0]) + bhi(ba[0]) + bhi(h[0]));
        const u32 a1 = cvtpk(a[2] + blo(o[1]) + blo(ba[1]) + blo(h[1]), a[3] + bhi(o[1]) + bhi(ba[1]) + bhi(h[1]));
        const u32 b0 = cvtpk(b[0] + blo(o[2]) + blo(bb[0]) + blo(h[2]), b[1] + bhi(o[2]) + bhi(bb[0]) + bhi(h[2]));
        const u32 b1 = cvtpk(b[2] + blo(o[3]) + blo(bb[1]) + blo(h[3]), b[3] + bhi(o[3]) + bhi(bb[1]) + bhi(h[3]));
        const auto s0 = __builtin_amdgcn_permlane16_swap(a0, b0, false, false);
        const auto s1 = __builtin_amdgcn_permlane16_swap(a1, b1, false, false);
        *(u32x4*)(out + (size_t)(tt * 128 + 16 * (m0 + jj) + c) * D_ + et * 128 + 64 * wP + 32 * u + 16 * (g & 1) + 8 * (g >> 1)) =
            (u32x4){s0[0], s1[0], s0[1], s1[1]};
      }
    DIAG_END
  }
}

}

extern "C" void kernel_launch(void* const* d_in, const int* in_sizes, int n_in, void* d_out, int out_size, void* d_ws, size_t ws_size,
                              hipStream_t stream) {
  const u16* x = (const u16*)d_in[0];
  const u16* cre = (const u16*)d_in[1];
  const u16* cim = (const u16*)d_in[2];
  const u16* dre = (const u16*)d_in[3];
  const u16* dim = (const u16*)d_in[4];
  const u16* w1 = (const u16*)d_in[6];
  const u16* b1 = (const u16*)d_in[7];
  const u16* w2 = (const u16*)d_in[8];
  const u16* b2 = (const u16*)d_in[9];
  char* ws = (char*)d_ws;
  (void)hipFuncSetAttribute((const void*)g1_kernel, hipFuncAttributeMaxDynamicSharedMemorySize, G1_LDS);
  (void)hipFuncSetAttribute((const void*)g2_kernel, hipFuncAttributeMaxDynamicSharedMemorySize, G2_LDS);
  (void)hipFuncSetAttribute((const void*)g3_kernel, hipFuncAttributeMaxDynamicSharedMemorySize, G3_LDS);
  (void)hipFuncSetAttribute((const void*)g4_kernel, hipFuncAttributeMaxDynamicSharedMemorySize, G4_LDS);
  g1_kernel<<<256, 512, G1_LDS, stream>>>(x, dre, dim, ws);
  g2_kernel<<<256, 512, G2_LDS, stream>>>(x, cre, cim, w1, ws);
  g3_kernel<<<256, 512, G3_LDS, stream>>>(w1, b1, ws);
  g4_kernel<<<256, 512, G4_LDS, stream>>>(w2, b2, ws, (u16*)d_out);
}
```

```cpp
#include <hip/hip_runtime.h>
#include <hip/hip_bf16.h>

typedef unsigned short u16;
typedef unsigned int u32;
typedef unsigned long long u64;
typedef __bf16 bf16x8 __attribute__((ext_vector_type(8)));
typedef __bf16 bf16x2 __attribute__((ext_vector_type(2)));
typedef short s16x4 __attribute__((ext_vector_type(4)));
typedef short s16x8 __attribute__((ext_vector_type(8)));
typedef float f32x2 __attribute__((ext_vector_type(2)));
typedef float f32x4 __attribute__((ext_vector_type(4)));
typedef u32 u32x2 __attribute__((ext_vector_type(2)));
typedef u32 u32x4 __attribute__((ext_vector_type(4)));
typedef __amdgpu_buffer_rsrc_t rsrc_t;
#define LAS __attribute__((address_space(3)))
#define DI __device__ __forceinline__

namespace {

constexpr int S_ = 2048, D_ = 1024, F_ = 3584;
constexpr float LN_EPS = 1e-5f;

constexpr size_t WS_AB = 0;
constexpr size_t WS_H = 8u << 20;
constexpr size_t WS_R = 12u << 20;
constexpr size_t WS_X2 = 27u << 20;
constexpr size_t WS_X4 = 31u << 20;
constexpr size_t WS_ST = 35u << 20;
constexpr size_t WS_CS = 36u << 20;
constexpr size_t WS_FL = 37u << 20;
constexpr u32 SENTINEL = 0xFFFFFFFFu;

#define DIAG_ARG
#define DIAG_BEGIN
#define STAMP(i)
#define DIAG_END(i)

DI u32 rfl(u32 x) { return __builtin_amdgcn_readfirstlane(x); }
DI u32 cvtpk(float lo, float hi) { f32x2 v = {lo, hi}; return __builtin_bit_cast(u32, __builtin_convertvector(v, bf16x2)); }
DI float blo(u32 v) { return __uint_as_float(v << 16); }
DI float bhi(u32 v) { return __uint_as_float(v & 0xFFFF0000u); }
DI rsrc_t mk_rsrc(const void* p, u32 bytes) { return __builtin_amdgcn_make_buffer_rsrc((void*)p, 0, bytes, 0x00020000); }
DI void bar() { asm volatile("s_barrier" ::: "memory"); }
DI void wait_lgkm0() { __builtin_amdgcn_s_waitcnt(0xC07F); }
template <int N> DI void wait_vm() { asm volatile("s_waitcnt vmcnt(%0)" ::"i"(N) : "memory"); }

template <int N> DI void dma(u32 m0, const u32* v, rsrc_t r, u32 so) {
  if constexpr (N == 4)
    asm volatile("s_mov_b32 m0, %0\n\ts_nop 0\n\t"
                 "buffer_load_dwordx4 %1, %5, %6 offen lds\n\t"
                 "buffer_load_dwordx4 %2, %5, %6 offen offset:1024 lds\n\t"
                 "buffer_load_dwordx4 %3, %5, %6 offen offset:2048 lds\n\t"
                 "buffer_load_dwordx4 %4, %5, %6 offen offset:3072 lds"
                 ::"s"(m0), "v"(v[0]), "v"(v[1]), "v"(v[2]), "v"(v[3]), "s"(r), "s"(so) : "memory");
  else if constexpr (N == 3)
    asm volatile("s_mov_b32 m0, %0\n\ts_nop 0\n\t"
                 "buffer_load_dwordx4 %1, %4, %5 offen lds\n\t"
                 "buffer_load_dwordx4 %2, %4, %5 offen offset:1024 lds\n\t"
                 "buffer_load_dwordx4 %3, %4, %5 offen offset:2048 lds"
                 ::"s"(m0), "v"(v[0]), "v"(v[1]), "v"(v[2]), "s"(r), "s"(so) : "memory");
  else
    asm volatile("s_mov_b32 m0, %0\n\ts_nop 0\n\t"
                 "buffer_load_dwordx4 %1, %3, %4 offen lds\n\t"
                 "buffer_load_dwordx4 %2, %3, %4 offen offset:1024 lds"
                 ::"s"(m0), "v"(v[0]), "v"(v[1]), "s"(r), "s"(so) : "memory");
}
template <int N> DI void dma_sc1(u32 m0, u32 v, rsrc_t r, u32 so) {
  if constexpr (N == 4)
    asm volatile("s_mov_b32 m0, %0\n\ts_nop 0\n\t"
                 "buffer_load_dwordx4 %1, %2, %3 offen sc1 lds\n\t"
                 "buffer_load_dwordx4 %1, %2, %3 offen offset:1024 sc1 lds\n\t"
                 "buffer_load_dwordx4 %1, %2, %3 offen offset:2048 sc1 lds\n\t"
                 "buffer_load_dwordx4 %1, %2, %3 offen offset:3072 sc1 lds"
                 ::"s"(m0), "v"(v), "s"(r), "s"(so) : "memory");
  else
    asm volatile("s_mov_b32 m0, %0\n\ts_nop 0\n\t"
                 "buffer_load_dwordx4 %1, %2, %3 offen sc1 lds\n\t"
                 "buffer_load_dwordx4 %1, %2, %3 offen offset:1024 sc1 lds"
                 ::"s"(m0), "v"(v), "s"(r), "s"(so) : "memory");
}

DI int kstep(int t, int n, int phi) {
  int k = t;
  if ((t & 3) == phi) { k = t + 4; if (k >= n) k -= n; }
  return k;
}

constexpr int NST = 4;
constexpr int SLOT = 16384;
constexpr int QOFF = NST * SLOT;
constexpr int TOFF = 2 * NST * SLOT;
constexpr int LDS_BYTES = TOFF + 4096;

enum { M_ROW = 0, M_TILED = 1, M_TR192 = 2, M_TR256 = 3 };

template <int PT, int PMODE, int QMODE>
struct Consumer {
  const LAS char* pb[4];
  const LAS char* qb[4];
  bf16x8 fp[2][PT], fq[2][4];

  DI void init(const LAS char* sm, int wP, int wQ, int lane, int prot = 0, int qrot = 0) {
    const int r = lane & 15, g = lane >> 4;
    if constexpr (PMODE == M_ROW) {
      const int a = (64 * wP + r) * 128 + ((g ^ (r >> 1)) * 16);
      pb[0] = sm + (a + prot * 4096); pb[1] = sm + ((a ^ 64) + prot * 4096);
      pb[2] = sm + (a + 4096 - prot * 4096); pb[3] = sm + ((a ^ 64) + 4096 - prot * 4096);
    } else if constexpr (PMODE == M_TILED) {
      pb[0] = sm + (4 * wP * 1024 + lane * 16);
    } else if constexpr (PMODE == M_TR192) {
      const int b = g & 1, a = (4 * g + (r >> 2)) * 192 + (r & 3) * 8;
      pb[0] = sm + (a + (4 * wP + b) * 32);
      pb[1] = sm + (a + (4 * wP + 1 - b) * 32);
      pb[2] = sm + (a + ((2 + wP) ^ b) * 32);
    } else {
      const int k = 4 * g + (r >> 2), a = k * 256 + (r & 3) * 8;
#pragma unroll
      for (int i = 0; i < 4; i++) pb[i] = sm + (a + (((4 * wP + i) ^ (k & 7)) * 32));
    }
    if constexpr (QMODE == M_ROW) {
      const int a = QOFF + (64 * wQ + r) * 128 + ((g ^ (r >> 1)) * 16);
      qb[0] = sm + (a + qrot * 4096); qb[1] = sm + ((a ^ 64) + qrot * 4096);
      qb[2] = sm + (a + 4096 - qrot * 4096); qb[3] = sm + ((a ^ 64) + 4096 - qrot * 4096);
      asm volatile("" : "+v"(qb[0]), "+v"(qb[1]), "+v"(qb[2]), "+v"(qb[3]));
    } else {
      qb[0] = sm + (QOFF + 4 * wQ * 1024 + lane * 16);
      asm volatile("" : "+v"(qb[0]));
    }
  }
  static DI bf16x8 tr2(const LAS char* p, int second) {
    s16x4 a = __builtin_amdgcn_ds_read_tr16_b64_v4i16((LAS s16x4*)p);
    s16x4 b = __builtin_amdgcn_ds_read_tr16_b64_v4i16((LAS s16x4*)(p + second));
    return __builtin_bit_cast(bf16x8, __builtin_shufflevector(a, b, 0, 1, 2, 3, 4, 5, 6, 7));
  }
  template <int S, int KS> DI bf16x8 ldp(int i) {
    if constexpr (PMODE == M_ROW) return *(const LAS bf16x8*)(pb[KS + (i & 2)] + (S * SLOT + (i & 1) * 2048));
    else if constexpr (PMODE == M_TILED) return *(const LAS bf16x8*)(pb[0] + (S * SLOT + KS * 8192 + i * 1024));
    else if constexpr (PMODE == M_TR192) return tr2(pb[i] + (S * SLOT + KS * 32 * 192), 16 * 192);
    else return tr2(pb[i] + (S * SLOT + KS * 32 * 256), 16 * 256);
  }
  template <int S, int KS> DI bf16x8 ldq(int j) {
    if constexpr (QMODE == M_ROW) return *(const LAS bf16x8*)(qb[KS + (j & 2)] + (S * SLOT + (j & 1) * 2048));
    else return *(const LAS bf16x8*)(qb[0] + (S * SLOT + KS * 8192 + j * 1024));
  }
  template <int S, int KS, int QN> DI void load() {
    fp[KS][0] = ldp<S, KS>(0);
#pragma unroll
    for (int j = 0; j < QN; j++) fq[KS][j] = ldq<S, KS>(j);
#pragma unroll
    for (int i = 1; i < PT; i++) fp[KS][i] = ldp<S, KS>(i);
  }
  template <int KS, int QN> DI void mma(f32x4 (&acc)[PT][4]) {
#pragma unroll
    for (int i = 0; i < PT; i++)
#pragma unroll
      for (int j = 0; j < QN; j++) acc[i][j] = __builtin_amdgcn_mfma_f32_16x16x32_bf16(fp[KS][i], fq[KS][j], acc[i][j], 0, 0, 0);
  }
  template <int KS> DI void stats(f32x4 (&sx)[2], f32x4 (&sxx)[2], f32x4 (&sd)[2], bf16x8 ones) {
#pragma unroll
    for (int i = 0; i < 2; i++) {
      sx[i] = __builtin_amdgcn_mfma_f32_16x16x32_bf16(fp[KS][i], ones, sx[i], 0, 0, 0);
      sxx[i] = __builtin_amdgcn_mfma_f32_16x16x32_bf16(fp[KS][i], fp[KS][i], sxx[i], 0, 0, 0);
      sd[i] = __builtin_amdgcn_mfma_f32_16x16x32_bf16(ones, fq[KS][i], sd[i], 0, 0, 0);
    }
  }
  template <int NM, int QN> static DI void interleave() {
    constexpr int NR = PT * (PMODE >= M_TR192 ? 2 : 1) + QN;
    if constexpr (NR <= NM) {
#pragma unroll
      for (int i = 0; i < NR; i++) { __builtin_amdgcn_sched_group_barrier(0x8, 1, 0); __builtin_amdgcn_sched_group_barrier(0x100, 1, 0); }
      __builtin_amdgcn_sched_group_barrier(0x8, NM - NR, 0);
    } else {
#pragma unroll
      for (int i = 0; i < NR / 2; i++) { __builtin_amdgcn_sched_group_barrier(0x8, 1, 0); __builtin_amdgcn_sched_group_barrier(0x100, 2, 0); }
      __builtin_amdgcn_sched_group_barrier(0x8, NM - NR / 2, 0);
    }
    __builtin_amdgcn_sched_barrier(0);
  }
  template <int S, int QC, int QN> DI void step(f32x4 (&acc)[PT][4]) {
    load<S, 1, QC>(); mma<0, QC>(acc); interleave<PT * QC, QC>();
    wait_lgkm0(); bar();
    __builtin_amdgcn_sched_barrier(0);
    load<(S + 1) % NST, 0, QN>(); mma<1, QC>(acc); interleave<PT * QC, QN>();
  }
  template <int QC, int QN> DI void step4(f32x4 (&acc)[PT][4]) {
    step<0, QC, QC>(acc); step<1, QC, QC>(acc); step<2, QC, QC>(acc); step<3, QC, QN>(acc);
  }
  template <int QC> DI void last4(f32x4 (&acc)[PT][4]) {
    step<0, QC, QC>(acc); step<1, QC, QC>(acc); step<2, QC, QC>(acc);
    load<3, 1, QC>(); mma<0, QC>(acc); interleave<PT * QC, QC>();
    mma<1, QC>(acc);
  }
  static DI void zero(f32x4 (&acc)[PT][4]) {
#pragma unroll
    for (int i = 0; i < PT; i++)
#pragma unroll
      for (int j = 0; j < 4; j++) { acc[i][j] = (f32x4){0.f, 0.f, 0.f, 0.f}; asm volatile("" : "+v"(acc[i][j])); }
  }
  DI void first() { load<0, 0, 4>(); }
};

DI u32 row_src(int imgrow, int srcrow, int lane, int ldbytes) {
  return srcrow * ldbytes + (((lane & 7) ^ ((imgrow >> 1) & 7)) * 16);
}
DI void store_sc1(rsrc_t r, u32 off, u32x4 v) { __builtin_amdgcn_raw_buffer_store_b128(v, r, off, 0, 16); }

DI void clear_slots(char* x, int wg, int w, int lane) {
  rsrc_t r = mk_rsrc(x + ((size_t)wg * 4 + w) * 4096, 4096);
#pragma unroll
  for (int i = 0; i < 4; i++) store_sc1(r, i * 1024 + lane * 16, (u32x4){SENTINEL, SENTINEL, SENTINEL, SENTINEL});
}

DI void xch_send(char* x, const f32x4 (&acc)[4][4], int wid, int lane) {
  rsrc_t r = mk_rsrc(x + ((size_t)blockIdx.x * 4 + wid) * 4096, 4096);
#pragma unroll
  for (int jj = 0; jj < 2; jj++)
#pragma unroll
    for (int u = 0; u < 2; u++) {
      const f32x4 a = acc[2 * u][2 + jj], b = acc[2 * u + 1][2 + jj];
      u32x4 v = {cvtpk(a[0], a[1]), cvtpk(a[2], a[3]), cvtpk(b[0], b[1]), cvtpk(b[2], b[3])};
#pragma unroll
      for (int e = 0; e < 4; e++) v[e] = min(v[e], SENTINEL - 1);
      store_sc1(r, lane * 16 + (jj * 2 + u) * 1024, v);
    }
}
DI void xch_request(rsrc_t r, u32x4 (&v)[4], int lane) {
  asm volatile("buffer_load_dwordx4 %0, %4, %5, 0 offen sc1\n\t"
               "buffer_load_dwordx4 %1, %4, %5, 0 offen offset:1024 sc1\n\t"
               "buffer_load_dwordx4 %2, %4, %5, 0 offen offset:2048 sc1\n\t"
               "buffer_load_dwordx4 %3, %4, %5, 0 offen offset:3072 sc1"
               : "=&v"(v[0]), "=&v"(v[1]), "=&v"(v[2]), "=&v"(v[3]) : "v"(lane * 16), "s"(r) : "memory");
}
DI bool xch_missing(const u32x4 (&v)[4]) {
  u32 m = 0;
#pragma unroll
  for (int q = 0; q < 4; q++) m = max(max(m, max(v[q][0], v[q][1])), max(v[q][2], v[q][3]));
  return __builtin_amdgcn_ballot_w64(m == SENTINEL) != 0;
}
DI void xch_finish(rsrc_t r, u32x4 (&v)[4], int lane) {
  asm volatile("s_waitcnt vmcnt(0)" : "+v"(v[0]), "+v"(v[1]), "+v"(v[2]), "+v"(v[3])::"memory");
#pragma unroll 1
  for (int spin = 0; spin < (1 << 20) && xch_missing(v); spin++)
    asm volatile("buffer_load_dwordx4 %0, %4, %5, 0 offen sc1\n\t"
                 "buffer_load_dwordx4 %1, %4, %5, 0 offen offset:1024 sc1\n\t"
                 "buffer_load_dwordx4 %2, %4, %5, 0 offen offset:2048 sc1\n\t"
                 "buffer_load_dwordx4 %3, %4, %5, 0 offen offset:3072 sc1\n\t"
                 "s_waitcnt vmcnt(0)"
                 : "=&v"(v[0]), "=&v"(v[1]), "=&v"(v[2]), "=&v"(v[3]) : "v"(lane * 16), "s"(r) : "memory");
}

DI void flag_set(char* ws, int which, int tt, int cell) {
  wait_vm<0>();
  if ((threadIdx.x & 63) == 0)
    __hip_atomic_store((u32*)(ws + WS_FL + ((size_t)(which * 16 + tt) * 64 + cell) * 64), 1u, __ATOMIC_RELAXED, __HIP_MEMORY_SCOPE_AGENT);
}
DI void flag_request(rsrc_t r, u32 off, u32& v) {
  asm volatile("buffer_load_dword %0, %1, %2, 0 offen sc1" : "=&v"(v) : "v"(off), "s"(r) : "memory");
}
DI void flag_wait(rsrc_t r, u32 off, u32& v) {
  asm volatile("" : "+v"(v)::"memory");
#pragma unroll 1
  for (int spin = 0; spin < (1 << 20); spin++) {
    if (__builtin_amdgcn_ballot_w64(v != 1u) == 0) break;
    __builtin_amdgcn_s_sleep(1);
    asm volatile("buffer_load_dword %0, %1, %2, 0 offen sc1\n\ts_waitcnt vmcnt(0)" : "=&v"(v) : "v"(off), "s"(r) : "memory");
  }
}

__global__ __launch_bounds__(512, 2) void ka_kernel(const u16* __restrict__ x, const u16* __restrict__ dre, const u16* __restrict__ dim,
                                                    const u16* __restrict__ w1, char* __restrict__ ws DIAG_ARG) {
  extern __shared__ __attribute__((aligned(16))) char smem[];
  DIAG_BEGIN
  const int lane = threadIdx.x & 63, wid = rfl(threadIdx.x >> 6);
  const int xcd = blockIdx.x & 7, loc = blockIdx.x >> 3, li = loc >> 2, lj = loc & 3;
  const int half = xcd >> 2, et = ((xcd >> 1) & 1) * 4 + lj, stile = (xcd & 1) * 8 + li;
  LAS char* sm = (LAS char*)smem;
  if (wid >= 4) {
    const int p = wid - 4, phi = (li + lj) & 3;
    const u32 ldsb = (u32)(size_t)smem + p * 4096;
    rsrc_t rp = mk_rsrc(x + (size_t)stile * 128 * D_, 128 * D_ * 2);
    rsrc_t rq = mk_rsrc((half ? dim : dre) + (size_t)et * 128 * D_, 128 * D_ * 2);
    u32 vp[4], vq[4];
#pragma unroll
    for (int i = 0; i < 4; i++) {
      const int row = 32 * p + 8 * i + (lane >> 3), u = (row >> 4) & 1, ii = row & 15;
      vp[i] = row_src(row, 32 * p + 8 * (ii >> 2) + 4 * u + (ii & 3), lane, D_ * 2) - i * 1024;
      vq[i] = row_src(row, row, lane, D_ * 2) - i * 1024;
    }
    auto issue = [&](int g) {
      const u32 so = kstep(g, 16, phi) * 128, m = ldsb + (g & 3) * SLOT;
      dma<4>(m, vp, rp, so);
      dma<4>(m + QOFF, vq, rq, so);
    };
    issue(0); issue(1); issue(2);
    wait_vm<16>(); bar();
    for (int g = 3; g < 16; g++) { issue(g); wait_vm<16>(); bar(); }
    const int job = min((int)blockIdx.x * 4 + p, 895), rb = job / 56, cb = job % 56;
    rsrc_t rw = mk_rsrc(w1 + (size_t)rb * 64 * F_ + cb * 64, 64 * F_ * 2 - cb * 128);
    u32x4 v[8];
    const u32 vo = (lane >> 3) * (F_ * 2) + (lane & 7) * 16;
#pragma unroll
    for (int i = 0; i < 8; i++)
      asm volatile("buffer_load_dwordx4 %0, %1, %2, %3 offen" : "=&v"(v[i]) : "v"(vo), "s"(rw), "s"(i * 8 * F_ * 2) : "memory");
    wait_vm<16>(); bar();
    wait_vm<8>(); bar();
    bar();
    asm volatile("s_waitcnt vmcnt(0)" : "+v"(v[0]), "+v"(v[1]), "+v"(v[2]), "+v"(v[3]), "+v"(v[4]), "+v"(v[5]), "+v"(v[6]), "+v"(v[7])::"memory");
    float s[8] = {0, 0, 0, 0, 0, 0, 0, 0};
#pragma unroll
    for (int i = 0; i < 8; i++)
#pragma unroll
      for (int e = 0; e < 4; e++) { s[2 * e] += blo(v[i][e]); s[2 * e + 1] += bhi(v[i][e]); }
#pragma unroll
    for (int e = 0; e < 8; e++)
#pragma unroll
      for (int o = 8; o < 64; o <<= 1) s[e] += __shfl_xor(s[e], o);
    if (lane < 8) {
      float* d = (float*)(ws + WS_CS) + rb * F_ + cb * 64 + lane * 8;
      *(f32x4*)d = (f32x4){s[0], s[1], s[2], s[3]};
      *(f32x4*)(d + 4) = (f32x4){s[4], s[5], s[6], s[7]};
    }
  } else {
    const int wP = wid >> 1, wQ = wid & 1, c = lane & 15, g = lane >> 4;
    LAS float* tabA = (LAS float*)(sm + TOFF);
    LAS float* tabB = tabA + 128;
    LAS float* tabD = tabA + 256;
    clear_slots(ws + WS_X2, blockIdx.x, wid, lane);
    if (lane < 3) *(u32*)(ws + WS_FL + ((size_t)lane * 1024 + blockIdx.x * 4 + wid) * 64) = 0;
    Consumer<4, M_ROW, M_ROW> C;
    C.init(sm, wP, wQ, lane, wQ, wP);
    f32x4 acc[4][4], sx[2], sxx[2], sd[2];
    C.zero(acc);
#pragma unroll
    for (int i = 0; i < 2; i++) sx[i] = sxx[i] = sd[i] = (f32x4){0.f, 0.f, 0.f, 0.f};
    const u32x4 ones4 = {0x3F803F80u, 0x3F803F80u, 0x3F803F80u, 0x3F803F80u};
    bf16x8 ones = __builtin_bit_cast(bf16x8, ones4);
    asm volatile("" : "+v"(ones));
    bar();
    STAMP(1)
    C.first();
#define KA_HALF0(S) C.load<S, 1, 4>(); C.mma<0, 4>(acc); C.stats<0>(sx, sxx, sd, ones); C.interleave<22, 4>();
#define KA_STEP(S) KA_HALF0(S) wait_lgkm0(); bar(); __builtin_amdgcn_sched_barrier(0); \
    C.load<(S + 1) % NST, 0, 4>(); C.mma<1, 4>(acc); C.stats<1>(sx, sxx, sd, ones); C.interleave<22, 4>();
    for (int it = 0; it < 3; it++) { KA_STEP(0) KA_STEP(1) KA_STEP(2) KA_STEP(3) }
    KA_STEP(0) KA_STEP(1) KA_STEP(2) KA_HALF0(3)
    C.stats<1>(sx, sxx, sd, ones);
    C.mma<1, 4>(acc);
    STAMP(2)
    const float sgn = half ? -1.f : 1.f;
#pragma unroll
    for (int i = 0; i < 2; i++) {
      const int e = c & 3;
      const float q = e == 0 ? sxx[i][0] : e == 1 ? sxx[i][1] : e == 2 ? sxx[i][2] : sxx[i][3];
      const float s = e == 0 ? sx[i][0] : e == 1 ? sx[i][1] : e == 2 ? sx[i][2] : sx[i][3];
      const float mean = s * (1.0f / D_), var = fmaxf(q * (1.0f / D_) - mean * mean, 0.f);
      const float rstd = sgn / sqrtf(var + LN_EPS);
      if ((c >> 2) == g) { tabA[64 * wP + 32 * wQ + 16 * i + c] = rstd; tabB[64 * wP + 32 * wQ + 16 * i + c] = -mean * rstd; }
      if (g == 0) tabD[64 * wQ + 32 * wP + 16 * i + c] = sd[i][0];
    }
    wait_lgkm0(); bar();
    rsrc_t ro = mk_rsrc(ws + WS_AB + ((size_t)((half * 8 + et) * 32 + 2 * stile + wP) * 16) * 1024, 16384);
    f32x4 ra[4], rb[4];
    float rs[4];
#pragma unroll
    for (int i = 0; i < 4; i++) {
      ra[i] = *(const LAS f32x4*)(tabA + 64 * wP + 16 * (i ^ (2 * wQ)) + 4 * g);
      rb[i] = *(const LAS f32x4*)(tabB + 64 * wP + 16 * (i ^ (2 * wQ)) + 4 * g);
      rs[i] = tabD[64 * wQ + 16 * (i ^ (2 * wP)) + c];
    }
#pragma unroll
    for (int u = 0; u < 2; u++)
#pragma unroll
      for (int j = 0; j < 4; j++) {
        const f32x4 a = ra[2 * u] * acc[2 * u][j] + rb[2 * u] * rs[j], b = ra[2 * u + 1] * acc[2 * u + 1][j] + rb[2 * u + 1] * rs[j];
        store_sc1(ro, ((u ^ wQ) * 8 + 4 * wQ + (j ^ (2 * wP))) * 1024 + lane * 16,
                  (u32x4){cvtpk(a[0], a[1]), cvtpk(a[2], a[3]), cvtpk(b[0], b[1]), cvtpk(b[2], b[3])});
      }
    DIAG_END(3)
  }
}

__global__ __launch_bounds__(512, 2) void kb_kernel(const u16* __restrict__ x, const u16* __restrict__ cre, const u16* __restrict__ cim,
                                                    const u16* __restrict__ w1, const u16* __restrict__ b1, const u16* __restrict__ w2,
                                                    const u16* __restrict__ b2, char* __restrict__ ws, u16* __restrict__ out DIAG_ARG) {
  extern __shared__ __attribute__((aligned(16))) char smem[];
  DIAG_BEGIN
  const int lane = threadIdx.x & 63, wid = rfl(threadIdx.x >> 6);
  const int xcd = blockIdx.x & 7, loc = blockIdx.x >> 3, li = loc >> 2, lj = loc & 3;
  const int half = xcd >> 2, et = ((xcd >> 1) & 1) * 4 + lj, tt = (xcd & 1) * 8 + li, ft = (xcd >> 1) * 4 + lj;
  LAS char* sm = (LAS char*)smem;
  LAS float* tabA = (LAS float*)(sm + TOFF);
  LAS float* tabB = tabA + 128;
  LAS float* tabC = tabA + 256;
  LAS float* tabBias = tabA + 512;
  if (wid >= 4) {
    const int p = wid - 4, phi = (li + lj) & 3;
    const u32 lds0 = (u32)(size_t)smem, ldsb = lds0 + p * 4096, vlin = p * 4096 + lane * 16;
    {
      rsrc_t rp = mk_rsrc(ws + WS_AB + (size_t)et * 32 * 16384, 9 * 32 * 16384);
      rsrc_t rqo = mk_rsrc((const char*)(half ? cim : cre) + (size_t)tt * 128 * S_ * 2 - 4096, 128 * S_ * 2 + 4096);
      rsrc_t rq0 = mk_rsrc((const char*)cre + (size_t)tt * 128 * S_ * 2 - 4096, 128 * S_ * 2 + 4096);
      rsrc_t rq1 = mk_rsrc((const char*)cim + (size_t)tt * 128 * S_ * 2 - 4096, 128 * S_ * 2 + 4096);
      u32 vp[4] = {vlin, vlin, vlin, vlin}, vq[4], vh[2];
#pragma unroll
      for (int i = 0; i < 4; i++) {
        const int row = 32 * p + 8 * i + (lane >> 3);
        vq[i] = row_src(row, row ^ (half * 32), lane, S_ * 2) + 4096 - i * 1024;
      }
#pragma unroll
      for (int i = 0; i < 2; i++) {
        const int row = 64 * (p >> 1) + 16 * (p & 1) + 8 * i + (lane >> 3);
        vh[i] = row_src(row, row ^ (half * 32), lane, S_ * 2) + 4096 - i * 1024;
      }
      const u32 ldsh = lds0 + QOFF + (p >> 1) * 8192 + (p & 1) * 2048;
      auto full = [&](int g) {
        const int k = kstep(g, 28, phi);
        const u32 m = ldsb + (g & 3) * SLOT;
        dma<4>(m, vp, rp, (half * 256 + k) * 16384);
        dma<4>(m + QOFF, vq, rqo, k * 128);
      };
      auto halfs = [&](int g) {
        const int k = kstep(g, 8, phi), term = k >> 2, ks = 28 + (k & 3);
        dma<4>(ldsb + (g & 3) * SLOT, vp, rp, (term * 256 + ks) * 16384);
        if (term) dma<2>(ldsh + (g & 3) * SLOT, vh, rq1, ks * 128);
        else dma<2>(ldsh + (g & 3) * SLOT, vh, rq0, ks * 128);
      };
      full(0); full(1); full(2);
      wait_vm<16>(); bar();
      for (int g = 3; g < 28; g++) { full(g); wait_vm<16>(); bar(); }
      halfs(0); wait_vm<14>(); bar();
      for (int g = 1; g < 8; g++) { halfs(g); wait_vm<12>(); bar(); }
    }
    rsrc_t rfl_ = mk_rsrc(ws + WS_FL + (size_t)tt * 4096, 3 * 16 * 4096);
    u32 fv;
    {
      rsrc_t rp = mk_rsrc((const char*)w1 + ft * 448, D_ * F_ * 2 - ft * 448);
      rsrc_t rq = mk_rsrc(ws + WS_H + (size_t)tt * 32 * 8192, 32 * 8192);
      u32 va[4], vb[3];
#pragma unroll
      for (int i = 0; i < 4; i++) {
        const int k = 16 * p + 4 * i + (lane >> 4);
        va[i] = k * (F_ * 2) + (((lane & 15) ^ ((k & 7) << 1)) * 16) - i * 1024;
      }
#pragma unroll
      for (int i = 0; i < 3; i++) {
        const int o = (p * 3 + i) * 64 + lane, k = o / 12, ch = o % 12;
        vb[i] = k * (F_ * 2) + 256 + ((ch ^ (((k >> 2) & 1) << 1)) * 16) - i * 1024;
      }
      auto pa = [&](int g) { dma<4>(ldsb + (g & 3) * SLOT, va, rp, kstep(g, 16, phi) * (64 * F_ * 2)); };
      auto pbb = [&](int g) { dma<3>(lds0 + p * 3072 + (g & 3) * SLOT, vb, rp, kstep(g, 16, phi) * (64 * F_ * 2)); };
      auto q = [&](int g) { dma_sc1<4>(ldsb + QOFF + (g & 3) * SLOT, vlin, rq, kstep(g, 16, phi) * 16384); };
      pa(0); wait_vm<10>(); bar();
      pa(1); wait_vm<8>(); bar();
      pa(2);
      const int pt = threadIdx.x - 256;
      if (pt < 224) {
        const float* cs = (const float*)(ws + WS_CS) + ft * 224 + pt;
        float s = 0.f;
#pragma unroll
        for (int i = 0; i < 16; i++) s += cs[i * F_];
        tabC[pt] = s;
        tabBias[pt] = __uint_as_float((u32)b1[ft * 224 + pt] << 16);
      }
      flag_request(rfl_, lane * 64, fv);
      wait_vm<0>();
      flag_wait(rfl_, lane * 64, fv);
      q(0); q(1); q(2);
      rsrc_t rs = mk_rsrc(ws + WS_ST + (size_t)tt * 16384, 16384);
      u32x2 sv[8];
      const u32 so = (pt & 1) * 8192 + (pt >> 1) * 8;
#pragma unroll
      for (int i = 0; i < 8; i++)
        asm volatile("buffer_load_dwordx2 %0, %1, %2, %3 offen sc1" : "=&v"(sv[i]) : "v"(so), "s"(rs), "s"(i * 1024) : "memory");
      wait_vm<16>(); bar();
      pa(3); q(3); wait_vm<20>(); bar();
      pa(4); q(4); wait_vm<24>(); bar();
      pa(5); q(5); wait_vm<16>();
      asm volatile("" : "+v"(sv[0]), "+v"(sv[1]), "+v"(sv[2]), "+v"(sv[3]), "+v"(sv[4]), "+v"(sv[5]), "+v"(sv[6]), "+v"(sv[7])::"memory");
      bar();
      {
        float s = 0.f, qq = 0.f;
#pragma unroll
        for (int i = 0; i < 8; i++) { s += __uint_as_float(sv[i][0]); qq += __uint_as_float(sv[i][1]); }
        s += __shfl_xor(s, 1); qq += __shfl_xor(qq, 1);
        const float mean = s * (1.0f / D_), var = fmaxf(qq * (1.0f / D_) - mean * mean, 0.f);
        const float rstd = 1.0f / sqrtf(var + LN_EPS);
        if ((pt & 1) == 0) { tabA[pt >> 1] = rstd; tabB[pt >> 1] = -mean * rstd; }
        wait_lgkm0();
      }
      for (int g = 6; g < 16; g++) { pa(g); q(g); wait_vm<16>(); bar(); }
      pbb(0); q(0); wait_vm<15>(); bar();
      for (int g = 1; g < 14; g++) { pbb(g); q(g); wait_vm<14>(); bar(); }
      flag_request(rfl_, 65536 + lane * 64, fv);
      pbb(14); q(14); wait_vm<14>(); bar();
      pbb(15); q(15); wait_vm<14>();
      flag_wait(rfl_, 65536 + lane * 64, fv);
      bar();
    }
    {
      rsrc_t rp = mk_rsrc(w2 + et * 128, F_ * D_ * 2 - et * 256);
      rsrc_t rq = mk_rsrc(ws + WS_R + (size_t)tt * 112 * 8192, 112 * 8192);
      u32 vp[4];
#pragma unroll
      for (int i = 0; i < 4; i++) {
        const int k = 16 * (p & 1) + 4 * i + (lane >> 4);
        vp[i] = k * (D_ * 2) + (((lane & 15) ^ ((k & 7) << 1)) * 16) - i * 1024;
      }
      const u32 vq0 = (4 * (p & 1) + 2 * half) * 1024 + lane * 16, vq1 = (4 * (p & 1) + 2 - 2 * half) * 1024 + lane * 16;
      const u32 ldsh = lds0 + QOFF + p * 4096;
      auto bblock = [&](int hf, int n) { return 7 * (8 * hf + n / 3) + 4 + n % 3; };
      auto full = [&](int g) {
        const int k = kstep(g, 24, phi), n = 2 * k + (p >> 1);
        const int blk = k < 16 ? 7 * (8 * half + (n >> 2)) + (n & 3) : bblock(half, n - 32);
        const u32 m = ldsb + (g & 3) * SLOT;
        dma<4>(m, vp, rp, blk * (32 * D_ * 2));
        dma_sc1<2>(m + QOFF, vq0, rq, blk * 8192);
        dma_sc1<2>(m + QOFF + 2048, vq1, rq, blk * 8192);
      };
      auto halfs = [&](int g) {
        const int k = kstep(g, 8, phi), blk = bblock(k >> 2, 16 + 2 * (k & 3) + (p >> 1));
        dma<4>(ldsb + (g & 3) * SLOT, vp, rp, blk * (32 * D_ * 2));
        dma_sc1<2>(ldsh + (g & 3) * SLOT, vq0, rq, blk * 8192);
      };
      full(0); wait_vm<15>(); bar();
      for (int g = 1; g < 10; g++) { full(g); wait_vm<16>(); bar(); }
      flag_request(rfl_, 131072 + lane * 64, fv);
      full(10); wait_vm<16>(); bar();
      full(11); wait_vm<16>();
      flag_wait(rfl_, 131072 + lane * 64, fv);
      bar();
      for (int g = 12; g < 24; g++) { full(g); wait_vm<16>(); bar(); }
      halfs(0); wait_vm<14>(); bar();
      for (int g = 1; g < 8; g++) { halfs(g); wait_vm<12>(); bar(); }
      wait_vm<6>(); bar();
      wait_vm<0>(); bar();
    }
  } else {
    const int wP = wid >> 1, wQ = wid & 1, c = lane & 15, g = lane >> 4;
    const int m0 = 4 * wQ + 2 * half, cell = ft * 4 + wid;
    u32x4 hk[2][2];
    clear_slots(ws + WS_X4, blockIdx.x ^ 4, wid, lane);
    {
      Consumer<4, M_TILED, M_ROW> C;
      C.init(sm, wP, wQ, lane);
      f32x4 acc[4][4];
      C.zero(acc);
      bar();
      STAMP(1)
      C.first();
      for (int it = 0; it < 6; it++) C.step4<4, 4>(acc);
      C.step4<4, 2>(acc);
      STAMP(2)
      xch_send(ws + WS_X2, acc, wid, lane);
      u32x2 xr[2][4];
#pragma unroll
      for (int jj = 0; jj < 2; jj++)
#pragma unroll
        for (int i = 0; i < 4; i++)
          xr[jj][i] = *(const u32x2*)(x + (size_t)(tt * 128 + 16 * (m0 + jj) + c) * D_ + et * 128 + 64 * wP + 16 * i + 4 * g);
      __builtin_amdgcn_sched_barrier(0);
      C.step4<2, 2>(acc);
      C.step<0, 2, 2>(acc);
      rsrc_t rx = mk_rsrc(ws + WS_X2 + ((size_t)(blockIdx.x ^ 4) * 4 + wid) * 4096, 4096);
      u32x4 rv[4];
      C.step<1, 2, 2>(acc);
      xch_request(rx, rv, lane);
      __builtin_amdgcn_sched_barrier(0);
      C.step<2, 2, 2>(acc);
      C.load<3, 1, 2>(); C.mma<0, 2>(acc); C.interleave<8, 2>();
      C.mma<1, 2>(acc);
      STAMP(3)
      xch_finish(rx, rv, lane);
      STAMP(4)
      rsrc_t rh = mk_rsrc(ws + WS_H + ((size_t)(tt * 32 + 4 * et + 2 * wP) * 8 + m0) * 1024, 16384);
      rsrc_t rs = mk_rsrc(ws + WS_ST + ((size_t)(tt * 16 + 2 * et + wP) * 128 + 16 * m0) * 8, 256);
#pragma unroll
      for (int jj = 0; jj < 2; jj++) {
        float s = 0.f, q = 0.f;
#pragma unroll
        for (int u = 0; u < 2; u++) {
          const u32x4 o = rv[jj * 2 + u];
          const f32x4 a = acc[2 * u][jj], b = acc[2 * u + 1][jj];
          const u32x2 xa = xr[jj][2 * u], xb = xr[jj][2 * u + 1];
          u32x4 h;
          h[0] = cvtpk(a[0] + blo(o[0]) + blo(xa[0]), a[1] + bhi(o[0]) + bhi(xa[0]));
          h[1] = cvtpk(a[2] + blo(o[1]) + blo(xa[1]), a[3] + bhi(o[1]) + bhi(xa[1]));
          h[2] = cvtpk(b[0] + blo(o[2]) + blo(xb[0]), b[1] + bhi(o[2]) + bhi(xb[0]));
          h[3] = cvtpk(b[2] + blo(o[3]) + blo(xb[1]), b[3] + bhi(o[3]) + bhi(xb[1]));
          store_sc1(rh, (u * 8 + jj) * 1024 + lane * 16, h);
          hk[jj][u] = h;
#pragma unroll
          for (int e = 0; e < 4; e++) { const float lo = blo(h[e]), hi = bhi(h[e]); s += lo + hi; q += lo * lo + hi * hi; }
        }
        s += __shfl_xor(s, 16); q += __shfl_xor(q, 16);
        s += __shfl_xor(s, 32); q += __shfl_xor(q, 32);
        if (g == 0) __builtin_amdgcn_raw_buffer_store_b64((u32x2){__float_as_uint(s), __float_as_uint(q)}, rs, (16 * jj + c) * 8, 0, 16);
      }
      flag_set(ws, 0, tt, cell);
      STAMP(5)
    }
    rsrc_t rr = mk_rsrc(ws + WS_R + ((size_t)(tt * 112 + 7 * ft) * 8 + 4 * wQ) * 1024, 7 * 8192);
    float ra[4], rb[4];
    auto act = [&](const f32x4& a, int col, int j, u32& lo, u32& hi) {
      const f32x4 cs = *(const LAS f32x4*)(tabC + col), bi = *(const LAS f32x4*)(tabBias + col);
      f32x4 v = ra[j] * a + (rb[j] * cs + bi);
#pragma unroll
      for (int e = 0; e < 4; e++) { v[e] = v[e] > 0.f ? v[e] : 0.f; v[e] *= v[e]; }
      lo = cvtpk(v[0], v[1]); hi = cvtpk(v[2], v[3]);
    };
    {
      Consumer<4, M_TR256, M_TILED> C;
      C.init(sm, wP, wQ, lane);
      f32x4 acc[4][4];
      C.zero(acc);
      bar();
      STAMP(6)
      C.first();
      for (int it = 0; it < 3; it++) C.step4<4, 4>(acc);
      C.last4<4>(acc);
      STAMP(7)
#pragma unroll
      for (int j = 0; j < 4; j++) { ra[j] = tabA[64 * wQ + 16 * j + c]; rb[j] = tabB[64 * wQ + 16 * j + c]; }
#pragma unroll
      for (int u = 0; u < 2; u++)
#pragma unroll
        for (int j = 0; j < 4; j++) {
          u32 a0, a1, b0, b1_;
          act(acc[2 * u][j], 64 * wP + 32 * u + 4 * g, j, a0, a1);
          act(acc[2 * u + 1][j], 64 * wP + 32 * u + 16 + 4 * g, j, b0, b1_);
          store_sc1(rr, ((2 * wP + u) * 8 + j) * 1024 + lane * 16, (u32x4){a0, a1, b0, b1_});
        }
    }
    {
      Consumer<3, M_TR192, M_TILED> C;
      C.init(sm, wP, wQ, lane);
      f32x4 acc[3][4];
      C.zero(acc);
      bar();
      STAMP(8)
      C.first();
      C.step4<4, 4>(acc);
      flag_set(ws, 1, tt, cell);
      for (int it = 0; it < 2; it++) C.step4<4, 4>(acc);
      C.last4<4>(acc);
      STAMP(9)
#pragma unroll
      for (int j = 0; j < 4; j++) {
        u32 a0, a1, b0, b1_;
        act(acc[0][j], 128 + 64 * wP + 4 * g, j, a0, a1);
        act(acc[1][j], 128 + 64 * wP + 16 + 4 * g, j, b0, b1_);
        store_sc1(rr, ((4 + 2 * wP) * 8 + j) * 1024 + lane * 16, (u32x4){a0, a1, b0, b1_});
        act(acc[2][j], 160 + 16 * wP + 4 * g, j, a0, a1);
        __builtin_amdgcn_raw_buffer_store_b64((u32x2){a0, a1}, rr, (5 * 8 + j) * 1024 + lane * 16 + 8 * wP, 0, 16);
      }
    }
    {
      Consumer<4, M_TR256, M_TILED> C;
      C.init(sm, wP, wQ, lane);
      f32x4 acc[4][4];
      C.zero(acc);
      bar();
      STAMP(10)
      C.first();
      C.step4<4, 4>(acc);
      flag_set(ws, 2, tt, cell);
      for (int it = 0; it < 4; it++) C.step4<4, 4>(acc);
      C.step4<4, 2>(acc);
      STAMP(11)
      xch_send(ws + WS_X4, acc, wid, lane);
      u32x2 br[4];
#pragma unroll
      for (int i = 0; i < 4; i++) br[i] = *(const u32x2*)(b2 + et * 128 + 64 * wP + 16 * i + 4 * g);
      __builtin_amdgcn_sched_barrier(0);
      C.step4<2, 2>(acc);
      C.step<0, 2, 2>(acc);
      rsrc_t rx = mk_rsrc(ws + WS_X4 + ((size_t)(blockIdx.x ^ 4) * 4 + wid) * 4096, 4096);
      u32x4 rv[4];
      C.step<1, 2, 2>(acc);
      xch_request(rx, rv, lane);
      __builtin_amdgcn_sched_barrier(0);
      C.step<2, 2, 2>(acc);
      C.load<3, 1, 2>(); C.mma<0, 2>(acc); C.interleave<8, 2>();
      C.mma<1, 2>(acc);
      STAMP(12)
      xch_finish(rx, rv, lane);
      STAMP(13)
#pragma unroll
      for (int jj = 0; jj < 2; jj++)
#pragma unroll
        for (int u = 0; u < 2; u++) {
          const u32x4 o = rv[jj * 2 + u], h = hk[jj][u];
          const f32x4 a = acc[2 * u][jj], b = acc[2 * u + 1][jj];
          const u32x2 ba = br[2 * u], bb = br[2 * u + 1];
          const u32 a0 = cvtpk(a[0] + blo(o[0]) + blo(ba[0]) + blo(h[0]), a[1] + bhi(o[0]) + bhi(ba[0]) + bhi(h[0]));
          const u32 a1 = cvtpk(a[2] + blo(o[1]) + blo(ba[1]) + blo(h[1]), a[3] + bhi(o[1]) + bhi(ba[1]) + bhi(h[1]));
          const u32 b0 = cvtpk(b[0] + blo(o[2]) + blo(bb[0]) + blo(h[2]), b[1] + bhi(o[2]) + bhi(bb[0]) + bhi(h[2]));
          const u32 b1_ = cvtpk(b[2] + blo(o[3]) + blo(bb[1]) + blo(h[3]), b[3] + bhi(o[3]) + bhi(bb[1]) + bhi(h[3]));
          const auto s0 = __builtin_amdgcn_permlane16_swap(a0, b0, false, false);
          const auto s1 = __builtin_amdgcn_permlane16_swap(a1, b1_, false, false);
          *(u32x4*)(out + (size_t)(tt * 128 + 16 * (m0 + jj) + c) * D_ + et * 128 + 64 * wP + 32 * u + 16 * (g & 1) + 8 * (g >> 1)) =
              (u32x4){s0[0], s1[0], s0[1], s1[1]};
        }
      DIAG_END(14)
    }
  }
}

}

extern "C" void kernel_launch(void* const* d_in, const int* in_sizes, int n_in, void* d_out, int out_size, void* d_ws, size_t ws_size,
                              hipStream_t stream) {
  const u16* const* I = (const u16* const*)d_in;
  char* ws = (char*)d_ws;
  (void)hipFuncSetAttribute((const void*)ka_kernel, hipFuncAttributeMaxDynamicSharedMemorySize, LDS_BYTES);
  (void)hipFuncSetAttribute((const void*)kb_kernel, hipFuncAttributeMaxDynamicSharedMemorySize, LDS_BYTES);
  ka_kernel<<<256, 512, LDS_BYTES, stream>>>(I[0], I[3], I[4], I[6], ws);
  kb_kernel<<<256, 512, LDS_BYTES, stream>>>(I[0], I[1], I[2], I[6], I[7], I[8], I[9], ws, (u16*)d_out);
}
```

```cpp
#include <hip/hip_runtime.h>
#include <hip/hip_bf16.h>

typedef unsigned short u16;
typedef unsigned int u32;
typedef unsigned long long u64;
typedef __bf16 bf16x8 __attribute__((ext_vector_type(8)));
typedef __bf16 bf16x2 __attribute__((ext_vector_type(2)));
typedef short s16x4 __attribute__((ext_vector_type(4)));
typedef short s16x8 __attribute__((ext_vector_type(8)));
typedef float f32x2 __attribute__((ext_vector_type(2)));
typedef float f32x4 __attribute__((ext_vector_type(4)));
typedef u32 u32x2 __attribute__((ext_vector_type(2)));
typedef u32 u32x4 __attribute__((ext_vector_type(4)));
typedef __amdgpu_buffer_rsrc_t rsrc_t;
#define LAS __attribute__((address_space(3)))
#define DI __device__ __forceinline__

namespace {

constexpr int S_ = 2048, D_ = 1024, F_ = 3584;
constexpr float LN_EPS = 1e-5f;

constexpr size_t WS_AB = 0;
constexpr size_t WS_H = 8u << 20;
constexpr size_t WS_R = 12u << 20;
constexpr size_t WS_X2 = 27u << 20;
constexpr size_t WS_X4 = 31u << 20;
constexpr size_t WS_ST = 35u << 20;
constexpr size_t WS_CS = 36u << 20;
constexpr size_t WS_FL = 37u << 20;
constexpr u32 SENTINEL = 0xFFFFFFFFu;

#define DIAG_ARG
#define DIAG_BEGIN
#define STAMP(i)
#define DIAG_END(i)

DI u32 rfl(u32 x) { return __builtin_amdgcn_readfirstlane(x); }
DI u32 cvtpk(float lo, float hi) { f32x2 v = {lo, hi}; return __builtin_bit_cast(u32, __builtin_convertvector(v, bf16x2)); }
DI float blo(u32 v) { return __uint_as_float(v << 16); }
DI float bhi(u32 v) { return __uint_as_float(v & 0xFFFF0000u); }
DI rsrc_t mk_rsrc(const void* p, u32 bytes) { return __builtin_amdgcn_make_buffer_rsrc((void*)p, 0, bytes, 0x00020000); }
DI void bar() { asm volatile("s_barrier" ::: "memory"); }
DI void wait_lgkm0() { __builtin_amdgcn_s_waitcnt(0xC07F); }
template <int N> DI void wait_vm() { asm volatile("s_waitcnt vmcnt(%0)" ::"i"(N) : "memory"); }

template <int N> DI void dma(u32 m0, const u32* v, rsrc_t r, u32 so) {
  if constexpr (N == 4)
    asm volatile("s_mov_b32 m0, %0\n\ts_nop 0\n\t"
                 "buffer_load_dwordx4 %1, %5, %6 offen lds\n\t"
                 "buffer_load_dwordx4 %2, %5, %6 offen offset:1024 lds\n\t"
                 "buffer_load_dwordx4 %3, %5, %6 offen offset:2048 lds\n\t"
                 "buffer_load_dwordx4 %4, %5, %6 offen offset:3072 lds"
                 ::"s"(m0), "v"(v[0]), "v"(v[1]), "v"(v[2]), "v"(v[3]), "s"(r), "s"(so) : "memory");
  else if constexpr (N == 3)
    asm volatile("s_mov_b32 m0, %0\n\ts_nop 0\n\t"
                 "buffer_load_dwordx4 %1, %4, %5 offen lds\n\t"
                 "buffer_load_dwordx4 %2, %4, %5 offen offset:1024 lds\n\t"
                 "buffer_load_dwordx4 %3, %4, %5 offen offset:2048 lds"
                 ::"s"(m0), "v"(v[0]), "v"(v[1]), "v"(v[2]), "s"(r), "s"(so) : "memory");
  else
    asm volatile("s_mov_b32 m0, %0\n\ts_nop 0\n\t"
                 "buffer_load_dwordx4 %1, %3, %4 offen lds\n\t"
                 "buffer_load_dwordx4 %2, %3, %4 offen offset:1024 lds"
                 ::"s"(m0), "v"(v[0]), "v"(v[1]), "s"(r), "s"(so) : "memory");
}
template <int N> DI void dma_sc1(u32 m0, u32 v, rsrc_t r, u32 so) {
  if constexpr (N == 4)
    asm volatile("s_mov_b32 m0, %0\n\ts_nop 0\n\t"
                 "buffer_load_dwordx4 %1, %2, %3 offen sc1 lds\n\t"
                 "buffer_load_dwordx4 %1, %2, %3 offen offset:1024 sc1 lds\n\t"
                 "buffer_load_dwordx4 %1, %2, %3 offen offset:2048 sc1 lds\n\t"
                 "buffer_load_dwordx4 %1, %2, %3 offen offset:3072 sc1 lds"
                 ::"s"(m0), "v"(v), "s"(r), "s"(so) : "memory");
  else
    asm volatile("s_mov_b32 m0, %0\n\ts_nop 0\n\t"
                 "buffer_load_dwordx4 %1, %2, %3 offen sc1 lds\n\t"
                 "buffer_load_dwordx4 %1, %2, %3 offen offset:1024 sc1 lds"
                 ::"s"(m0), "v"(v), "s"(r), "s"(so) : "memory");
}

DI float xsum16(float v) { const auto r = __builtin_amdgcn_permlane16_swap(__float_as_uint(v), __float_as_uint(v), false, false); return __uint_as_float(r[0]) + __uint_as_float(r[1]); }
DI float xsum32(float v) { const auto r = __builtin_amdgcn_permlane32_swap(__float_as_uint(v), __float_as_uint(v), false, false); return __uint_as_float(r[0]) + __uint_as_float(r[1]); }

DI int kstep(int t, int n, int phi) {
  int k = t;
  if ((t & 3) == phi) { k = t + 4; if (k >= n) k -= n; }
  return k;
}

DI int kstep2(int t, int n, int phi, int side) {
  const int k = kstep(t, n, phi), k2 = k ^ (side << 2);
  return k2 < n ? k2 : k;
}

constexpr int NST = 4;
constexpr int SLOT = 16384;
constexpr int QOFF = NST * SLOT;
constexpr int TOFF = 2 * NST * SLOT;
constexpr int LDS_BYTES = TOFF + 4096;

enum { M_ROW = 0, M_TILED = 1, M_TR192 = 2, M_TR256 = 3 };

template <int PT, int PMODE, int QMODE>
struct Consumer {
  const LAS char* pb[4];
  const LAS char* qb[4];
  bf16x8 fp[2][PT], fq[2][4];

  DI void init(const LAS char* sm, int wP, int wQ, int lane, int prot = 0, int qrot = 0) {
    const int r = lane & 15, g = lane >> 4;
    if constexpr (PMODE == M_ROW) {
      const int a = (64 * wP + r) * 128 + ((g ^ (r >> 1)) * 16);
      pb[0] = sm + (a + prot * 4096); pb[1] = sm + ((a ^ 64) + prot * 4096);
      pb[2] = sm + (a + 4096 - prot * 4096); pb[3] = sm + ((a ^ 64) + 4096 - prot * 4096);
    } else if constexpr (PMODE == M_TILED) {
      pb[0] = sm + (4 * wP * 1024 + lane * 16);
    } else if constexpr (PMODE == M_TR192) {
      const int b = g & 1, a = (4 * g + (r >> 2)) * 192 + (r & 3) * 8;
      pb[0] = sm + (a + (4 * wP + b) * 32);
      pb[1] = sm + (a + (4 * wP + 1 - b) * 32);
      pb[2] = sm + (a + ((2 + wP) ^ b) * 32);
    } else {
      const int k = 4 * g + (r >> 2), a = k * 256 + (r & 3) * 8;
#pragma unroll
      for (int i = 0; i < 4; i++) pb[i] = sm + (a + (((4 * wP + i) ^ (k & 7)) * 32));
    }
    if constexpr (QMODE == M_ROW) {
      const int a = QOFF + (64 * wQ + r) * 128 + ((g ^ (r >> 1)) * 16);
      qb[0] = sm + (a + qrot * 4096); qb[1] = sm + ((a ^ 64) + qrot * 4096);
      qb[2] = sm + (a + 4096 - qrot * 4096); qb[3] = sm + ((a ^ 64) + 4096 - qrot * 4096);
      asm volatile("" : "+v"(qb[0]), "+v"(qb[1]), "+v"(qb[2]), "+v"(qb[3]));
    } else {
      qb[0] = sm + (QOFF + 4 * wQ * 1024 + lane * 16);
      asm volatile("" : "+v"(qb[0]));
    }
  }
  static DI bf16x8 tr2(const LAS char* p, int second) {
    s16x4 a = __builtin_amdgcn_ds_read_tr16_b64_v4i16((LAS s16x4*)p);
    s16x4 b = __builtin_amdgcn_ds_read_tr16_b64_v4i16((LAS s16x4*)(p + second));
    return __builtin_bit_cast(bf16x8, __builtin_shufflevector(a, b, 0, 1, 2, 3, 4, 5, 6, 7));
  }
  template <int S, int KS> DI bf16x8 ldp(int i) {
    if constexpr (PMODE == M_ROW) return *(const LAS bf16x8*)(pb[KS + (i & 2)] + (S * SLOT + (i & 1) * 2048));
    else if constexpr (PMODE == M_TILED) return *(const LAS bf16x8*)(pb[0] + (S * SLOT + KS * 8192 + i * 1024));
    else if constexpr (PMODE == M_TR192) return tr2(pb[i] + (S * SLOT + KS * 32 * 192), 16 * 192);
    else return tr2(pb[i] + (S * SLOT + KS * 32 * 256), 16 * 256);
  }
  template <int S, int KS> DI bf16x8 ldq(int j) {
    if constexpr (QMODE == M_ROW) return *(const LAS bf16x8*)(qb[KS + (j & 2)] + (S * SLOT + (j & 1) * 2048));
    else return *(const LAS bf16x8*)(qb[0] + (S * SLOT + KS * 8192 + j * 1024));
  }
  template <int S, int KS, int QN> DI void load() {
    fp[KS][0] = ldp<S, KS>(0);
#pragma unroll
    for (int j = 0; j < QN; j++) fq[KS][j] = ldq<S, KS>(j);
#pragma unroll
    for (int i = 1; i < PT; i++) fp[KS][i] = ldp<S, KS>(i);
  }
  template <int KS, int QN> DI void mma(f32x4 (&acc)[PT][4]) {
#pragma unroll
    for (int i = 0; i < PT; i++)
#pragma unroll
      for (int j = 0; j < QN; j++) acc[i][j] = __builtin_amdgcn_mfma_f32_16x16x32_bf16(fp[KS][i], fq[KS][j], acc[i][j], 0, 0, 0);
  }
  template <int KS> DI void stats(f32x4 (&sx)[2], f32x4 (&sxx)[2], f32x4 (&sd)[2], bf16x8 ones) {
#pragma unroll
    for (int i = 0; i < 2; i++) {
      sx[i] = __builtin_amdgcn_mfma_f32_16x16x32_bf16(fp[KS][i], ones, sx[i], 0, 0, 0);
      sxx[i] = __builtin_amdgcn_mfma_f32_16x16x32_bf16(fp[KS][i], fp[KS][i], sxx[i], 0, 0, 0);
      sd[i] = __builtin_amdgcn_mfma_f32_16x16x32_bf16(ones, fq[KS][i], sd[i], 0, 0, 0);
    }
  }
  template <int NM, int QN> static DI void interleave() {
    constexpr int NR = PT * (PMODE >= M_TR192 ? 2 : 1) + QN;
    if constexpr (NR <= NM) {
#pragma unroll
      for (int i = 0; i < NR; i++) { __builtin_amdgcn_sched_group_barrier(0x8, 1, 0); __builtin_amdgcn_sched_group_barrier(0x100, 1, 0); }
      __builtin_amdgcn_sched_group_barrier(0x8, NM - NR, 0);
    } else {
#pragma unroll
      for (int i = 0; i < NR / 2; i++) { __builtin_amdgcn_sched_group_barrier(0x8, 1, 0); __builtin_amdgcn_sched_group_barrier(0x100, 2, 0); }
      __builtin_amdgcn_sched_group_barrier(0x8, NM - NR / 2, 0);
    }
    __builtin_amdgcn_sched_barrier(0);
  }
  template <int S, int QC, int QN> DI void step(f32x4 (&acc)[PT][4]) {
    load<S, 1, QC>(); mma<0, QC>(acc); interleave<PT * QC, QC>();
    wait_lgkm0(); bar();
    __builtin_amdgcn_sched_barrier(0);
    load<(S + 1) % NST, 0, QN>(); mma<1, QC>(acc); interleave<PT * QC, QN>();
  }
  template <int QC, int QN> DI void step4(f32x4 (&acc)[PT][4]) {
    step<0, QC, QC>(acc); step<1, QC, QC>(acc); step<2, QC, QC>(acc); step<3, QC, QN>(acc);
  }
  template <int QC> DI void last4(f32x4 (&acc)[PT][4]) {
    step<0, QC, QC>(acc); step<1, QC, QC>(acc); step<2, QC, QC>(acc);
    load<3, 1, QC>(); mma<0, QC>(acc); interleave<PT * QC, QC>();
    mma<1, QC>(acc);
  }
  static DI void zero(f32x4 (&acc)[PT][4]) {
#pragma unroll
    for (int i = 0; i < PT; i++)
#pragma unroll
      for (int j = 0; j < 4; j++) { acc[i][j] = (f32x4){0.f, 0.f, 0.f, 0.f}; asm volatile("" : "+v"(acc[i][j])); }
  }
  DI void first() { load<0, 0, 4>(); }
};

DI u32 row_src(int imgrow, int srcrow, int lane, int ldbytes) {
  return srcrow * ldbytes + (((lane & 7) ^ ((imgrow >> 1) & 7)) * 16);
}
DI void store_sc1(rsrc_t r, u32 off, u32x4 v) { __builtin_amdgcn_raw_buffer_store_b128(v, r, off, 0, 16); }

DI void clear_slots(char* x, int wg, int w, int lane) {
  rsrc_t r = mk_rsrc(x + ((size_t)wg * 4 + w) * 4096, 4096);
#pragma unroll
  for (int i = 0; i < 4; i++) store_sc1(r, i * 1024 + lane * 16, (u32x4){SENTINEL, SENTINEL, SENTINEL, SENTINEL});
}

DI void xch_send(char* x, const f32x4 (&acc)[4][4], int wid, int lane) {
  rsrc_t r = mk_rsrc(x + ((size_t)blockIdx.x * 4 + wid) * 4096, 4096);
#pragma unroll
  for (int jj = 0; jj < 2; jj++)
#pragma unroll
    for (int u = 0; u < 2; u++) {
      const f32x4 a = acc[2 * u][2 + jj], b = acc[2 * u + 1][2 + jj];
      u32x4 v = {cvtpk(a[0], a[1]), cvtpk(a[2], a[3]), cvtpk(b[0], b[1]), cvtpk(b[2], b[3])};
#pragma unroll
      for (int e = 0; e < 4; e++) v[e] = min(v[e], SENTINEL - 1);
      store_sc1(r, lane * 16 + (jj * 2 + u) * 1024, v);
    }
}
DI void xch_request(rsrc_t r, u32x4 (&v)[4], int lane) {
  asm volatile("buffer_load_dwordx4 %0, %4, %5, 0 offen sc1\n\t"
               "buffer_load_dwordx4 %1, %4, %5, 0 offen offset:1024 sc1\n\t"
               "buffer_load_dwordx4 %2, %4, %5, 0 offen offset:2048 sc1\n\t"
               "buffer_load_dwordx4 %3, %4, %5, 0 offen offset:3072 sc1"
               : "=&v"(v[0]), "=&v"(v[1]), "=&v"(v[2]), "=&v"(v[3]) : "v"(lane * 16), "s"(r) : "memory");
}
DI bool xch_missing(const u32x4 (&v)[4]) {
  u32 m = 0;
#pragma unroll
  for (int q = 0; q < 4; q++) m = max(max(m, max(v[q][0], v[q][1])), max(v[q][2], v[q][3]));
  return __builtin_amdgcn_ballot_w64(m == SENTINEL) != 0;
}
DI void xch_finish(rsrc_t r, u32x4 (&v)[4], int lane) {
  asm volatile("s_waitcnt vmcnt(0)" : "+v"(v[0]), "+v"(v[1]), "+v"(v[2]), "+v"(v[3])::"memory");
#pragma unroll 1
  for (int spin = 0; spin < (1 << 20) && xch_missing(v); spin++)
    asm volatile("buffer_load_dwordx4 %0, %4, %5, 0 offen sc1\n\t"
                 "buffer_load_dwordx4 %1, %4, %5, 0 offen offset:1024 sc1\n\t"
                 "buffer_load_dwordx4 %2, %4, %5, 0 offen offset:2048 sc1\n\t"
                 "buffer_load_dwordx4 %3, %4, %5, 0 offen offset:3072 sc1\n\t"
                 "s_waitcnt vmcnt(0)"
                 : "=&v"(v[0]), "=&v"(v[1]), "=&v"(v[2]), "=&v"(v[3]) : "v"(lane * 16), "s"(r) : "memory");
}

DI void flag_set(char* ws, int which, int tt, int cell) {
  wait_vm<0>();
  if ((threadIdx.x & 63) == 0)
    __hip_atomic_store((u32*)(ws + WS_FL + ((size_t)(which * 16 + tt) * 64 + cell) * 64), 1u, __ATOMIC_RELAXED, __HIP_MEMORY_SCOPE_AGENT);
}
DI void flag_request(rsrc_t r, u32 off, u32& v) {
  asm volatile("buffer_load_dword %0, %1, %2, 0 offen sc1" : "=&v"(v) : "v"(off), "s"(r) : "memory");
}
DI void flag_wait(rsrc_t r, u32 off, u32& v) {
  asm volatile("" : "+v"(v)::"memory");
#pragma unroll 1
  for (int spin = 0; spin < (1 << 20); spin++) {
    if (__builtin_amdgcn_ballot_w64(v != 1u) == 0) break;
    __builtin_amdgcn_s_sleep(1);
    asm volatile("buffer_load_dword %0, %1, %2, 0 offen sc1\n\ts_waitcnt vmcnt(0)" : "=&v"(v) : "v"(off), "s"(r) : "memory");
  }
}

__global__ __launch_bounds__(512, 2) void ka_kernel(const u16* __restrict__ x, const u16* __restrict__ dre, const u16* __restrict__ dim,
                                                    const u16* __restrict__ w1, char* __restrict__ ws DIAG_ARG) {
  extern __shared__ __attribute__((aligned(16))) char smem[];
  DIAG_BEGIN
  const int lane = threadIdx.x & 63, wid = rfl(threadIdx.x >> 6);
  const int xcd = blockIdx.x & 7, loc = blockIdx.x >> 3, li = loc >> 2, lj = loc & 3;
  const int half = xcd >> 2, et = ((xcd >> 1) & 1) * 4 + lj, stile = (xcd & 1) * 8 + li;
  LAS char* sm = (LAS char*)smem;
  if (wid >= 4) {
    const int p = wid - 4, phi = (li + lj) & 3;
    const u32 ldsb = (u32)(size_t)smem + p * 4096;
    rsrc_t rp = mk_rsrc(x + (size_t)stile * 128 * D_, 128 * D_ * 2);
    rsrc_t rq = mk_rsrc((half ? dim : dre) + (size_t)et * 128 * D_, 128 * D_ * 2);
    u32 vp[4], vq[4];
#pragma unroll
    for (int i = 0; i < 4; i++) {
      const int row = 32 * p + 8 * i + (lane >> 3), u = (row >> 4) & 1, ii = row & 15;
      vp[i] = row_src(row, 32 * p + 8 * (ii >> 2) + 4 * u + (ii & 3), lane, D_ * 2) - i * 1024;
      vq[i] = row_src(row, row, lane, D_ * 2) - i * 1024;
    }
    auto issue = [&](int g) {
      const u32 so = kstep2(g, 16, phi, (xcd >> 1) ^ (xcd & 1)) * 128, m = ldsb + (g & 3) * SLOT;
      dma<4>(m, vp, rp, so);
      dma<4>(m + QOFF, vq, rq, so);
    };
    issue(0); issue(1); issue(2);
    wait_vm<16>(); bar();
    for (int g = 3; g < 16; g++) { issue(g); wait_vm<16>(); bar(); }
    const int job = min((int)blockIdx.x * 4 + p, 895), rb = job / 56, cb = job % 56;
    rsrc_t rw = mk_rsrc(w1 + (size_t)rb * 64 * F_ + cb * 64, 64 * F_ * 2 - cb * 128);
    u32x4 v[8];
    const u32 vo = (lane >> 3) * (F_ * 2) + (lane & 7) * 16;
#pragma unroll
    for (int i = 0; i < 8; i++)
      asm volatile("buffer_load_dwordx4 %0, %1, %2, %3 offen" : "=&v"(v[i]) : "v"(vo), "s"(rw), "s"(i * 8 * F_ * 2) : "memory");
    wait_vm<16>(); bar();
    wait_vm<8>(); bar();
    bar();
    asm volatile("s_waitcnt vmcnt(0)" : "+v"(v[0]), "+v"(v[1]), "+v"(v[2]), "+v"(v[3]), "+v"(v[4]), "+v"(v[5]), "+v"(v[6]), "+v"(v[7])::"memory");
    float s[8] = {0, 0, 0, 0, 0, 0, 0, 0};
#pragma unroll
    for (int i = 0; i < 8; i++)
#pragma unroll
      for (int e = 0; e < 4; e++) { s[2 * e] += blo(v[i][e]); s[2 * e + 1] += bhi(v[i][e]); }
#pragma unroll
    for (int e = 0; e < 8; e++)
#pragma unroll
      for (int o = 8; o < 64; o <<= 1) s[e] += __shfl_xor(s[e], o);
    if (lane < 8) {
      float* d = (float*)(ws + WS_CS) + rb * F_ + cb * 64 + lane * 8;
      *(f32x4*)d = (f32x4){s[0], s[1], s[2], s[3]};
      *(f32x4*)(d + 4) = (f32x4){s[4], s[5], s[6], s[7]};
    }
  } else {
    const int wP = wid >> 1, wQ = wid & 1, c = lane & 15, g = lane >> 4;
    LAS float* tabA = (LAS float*)(sm + TOFF);
    LAS float* tabB = tabA + 128;
    LAS float* tabD = tabA + 256;
    Consumer<4, M_ROW, M_ROW> C;
    C.init(sm, wP, wQ, lane, wQ, wP);
    f32x4 acc[4][4], sx[2], sxx[2], sd[2];
    C.zero(acc);
#pragma unroll
    for (int i = 0; i < 2; i++) sx[i] = sxx[i] = sd[i] = (f32x4){0.f, 0.f, 0.f, 0.f};
    const u32x4 ones4 = {0x3F803F80u, 0x3F803F80u, 0x3F803F80u, 0x3F803F80u};
    bf16x8 ones = __builtin_bit_cast(bf16x8, ones4);
    asm volatile("" : "+v"(ones));
    bar();
    STAMP(1)
    C.first();
#define KA_HALF0(S) C.load<S, 1, 4>(); C.mma<0, 4>(acc); C.stats<0>(sx, sxx, sd, ones); C.interleave<22, 4>();
#define KA_STEP(S) KA_HALF0(S) wait_lgkm0(); bar(); __builtin_amdgcn_sched_barrier(0); \
    C.load<(S + 1) % NST, 0, 4>(); C.mma<1, 4>(acc); C.stats<1>(sx, sxx, sd, ones); C.interleave<22, 4>();
    for (int it = 0; it < 3; it++) {
      KA_STEP(0) KA_STEP(1) KA_STEP(2) KA_STEP(3)
      if (it == 0) {
        clear_slots(ws + WS_X2, blockIdx.x, wid, lane);
        if (lane < 3) *(u32*)(ws + WS_FL + ((size_t)lane * 1024 + blockIdx.x * 4 + wid) * 64) = 0;
      }
    }
    KA_STEP(0) KA_STEP(1) KA_STEP(2) KA_HALF0(3)
    C.stats<1>(sx, sxx, sd, ones);
    C.mma<1, 4>(acc);
    STAMP(2)
    const float sgn = half ? -1.f : 1.f;
#pragma unroll
    for (int i = 0; i < 2; i++) {
      const int e = c & 3;
      const float q = e == 0 ? sxx[i][0] : e == 1 ? sxx[i][1] : e == 2 ? sxx[i][2] : sxx[i][3];
      const float s = e == 0 ? sx[i][0] : e == 1 ? sx[i][1] : e == 2 ? sx[i][2] : sx[i][3];
      const float mean = s * (1.0f / D_), var = fmaxf(q * (1.0f / D_) - mean * mean, 0.f);
      const float rstd = sgn / sqrtf(var + LN_EPS);
      if ((c >> 2) == g) { tabA[64 * wP + 32 * wQ + 16 * i + c] = rstd; tabB[64 * wP + 32 * wQ + 16 * i + c] = -mean * rstd; }
      if (g == 0) tabD[64 * wQ + 32 * wP + 16 * i + c] = sd[i][0];
    }
    wait_lgkm0(); bar();
    rsrc_t ro = mk_rsrc(ws + WS_AB + ((size_t)((half * 8 + et) * 32 + 2 * stile + wP) * 16) * 1024, 16384);
    f32x4 ra[4], rb[4];
    float rs[4];
#pragma unroll
    for (int i = 0; i < 4; i++) {
      ra[i] = *(const LAS f32x4*)(tabA + 64 * wP + 16 * (i ^ (2 * wQ)) + 4 * g);
      rb[i] = *(const LAS f32x4*)(tabB + 64 * wP + 16 * (i ^ (2 * wQ)) + 4 * g);
      rs[i] = tabD[64 * wQ + 16 * (i ^ (2 * wP)) + c];
    }
#pragma unroll
    for (int u = 0; u < 2; u++)
#pragma unroll
      for (int j = 0; j < 4; j++) {
        const f32x4 a = ra[2 * u] * acc[2 * u][j] + rb[2 * u] * rs[j], b = ra[2 * u + 1] * acc[2 * u + 1][j] + rb[2 * u + 1] * rs[j];
        store_sc1(ro, ((u ^ wQ) * 8 + 4 * wQ + (j ^ (2 * wP))) * 1024 + lane * 16,
                  (u32x4){cvtpk(a[0], a[1]), cvtpk(a[2], a[3]), cvtpk(b[0], b[1]), cvtpk(b[2], b[3])});
      }
    DIAG_END(3)
  }
}

__global__ __launch_bounds__(512, 2) void kb_kernel(const u16* __restrict__ x, const u16* __restrict__ cre, const u16* __restrict__ cim,
                                                    const u16* __restrict__ w1, const u16* __restrict__ b1, const u16* __restrict__ w2,
                                                    const u16* __restrict__ b2, char* __restrict__ ws, u16* __restrict__ out DIAG_ARG) {
  extern __shared__ __attribute__((aligned(16))) char smem[];
  DIAG_BEGIN
  const int lane = threadIdx.x & 63, wid = rfl(threadIdx.x >> 6);
  const int xcd = blockIdx.x & 7, loc = blockIdx.x >> 3, li = loc >> 2, lj = loc & 3;
  const int half = xcd >> 2, et = ((xcd >> 1) & 1) * 4 + lj, tt = (xcd & 1) * 8 + li, ft = (xcd >> 1) * 4 + lj;
  LAS char* sm = (LAS char*)smem;
  LAS float* tabA = (LAS float*)(sm + TOFF);
  LAS float* tabB = tabA + 128;
  LAS float* tabC = tabA + 256;
  LAS float* tabBias = tabA + 512;
  if (wid >= 4) {
    const int p = wid - 4, phi = (li + lj) & 3;
    const u32 lds0 = (u32)(size_t)smem, ldsb = lds0 + p * 4096, vlin = p * 4096 + lane * 16;
    {
      rsrc_t rp = mk_rsrc(ws + WS_AB + (size_t)et * 32 * 16384, 9 * 32 * 16384);
      rsrc_t rqo = mk_rsrc((const char*)(half ? cim : cre) + (size_t)tt * 128 * S_ * 2 - 4096, 128 * S_ * 2 + 4096);
      rsrc_t rq0 = mk_rsrc((const char*)cre + (size_t)tt * 128 * S_ * 2 - 4096, 128 * S_ * 2 + 4096);
      rsrc_t rq1 = mk_rsrc((const char*)cim + (size_t)tt * 128 * S_ * 2 - 4096, 128 * S_ * 2 + 4096);
      u32 vp[4] = {vlin, vlin, vlin, vlin}, vq[4], vh[2];
#pragma unroll
      for (int i = 0; i < 4; i++) {
        const int row = 32 * p + 8 * i + (lane >> 3);
        vq[i] = row_src(row, row ^ (half * 32), lane, S_ * 2) + 4096 - i * 1024;
      }
#pragma unroll
      for (int i = 0; i < 2; i++) {
        const int row = 64 * (p >> 1) + 16 * (p & 1) + 8 * i + (lane >> 3);
        vh[i] = row_src(row, row ^ (half * 32), lane, S_ * 2) + 4096 - i * 1024;
      }
      const u32 ldsh = lds0 + QOFF + (p >> 1) * 8192 + (p & 1) * 2048;
      auto full = [&](int g) {
        const int k = kstep2(g, 28, phi, (xcd >> 1) & 1);
        const u32 m = ldsb + (g & 3) * SLOT;
        dma<4>(m, vp, rp, (half * 256 + k) * 16384);
        dma<4>(m + QOFF, vq, rqo, k * 128);
      };
      auto halfs = [&](int g) {
        const int k = kstep2(g, 8, phi, (xcd >> 1) & 1), term = k >> 2, ks = 28 + (k & 3);
        dma<4>(ldsb + (g & 3) * SLOT, vp, rp, (term * 256 + ks) * 16384);
        if (term) dma<2>(ldsh + (g & 3) * SLOT, vh, rq1, ks * 128);
        else dma<2>(ldsh + (g & 3) * SLOT, vh, rq0, ks * 128);
      };
      full(0); full(1); full(2);
      wait_vm<16>(); bar();
      for (int g = 3; g < 28; g++) { full(g); wait_vm<16>(); bar(); }
      halfs(0); wait_vm<14>(); bar();
      for (int g = 1; g < 8; g++) { halfs(g); wait_vm<12>(); bar(); }
    }
    rsrc_t rfl_ = mk_rsrc(ws + WS_FL + (size_t)tt * 4096, 3 * 16 * 4096);
    u32 fv;
    {
      rsrc_t rp = mk_rsrc((const char*)w1 + ft * 448, D_ * F_ * 2 - ft * 448);
      rsrc_t rq = mk_rsrc(ws + WS_H + (size_t)tt * 32 * 8192, 32 * 8192);
      u32 va[4], vb[3];
#pragma unroll
      for (int i = 0; i < 4; i++) {
        const int k = 16 * p + 4 * i + (lane >> 4);
        va[i] = k * (F_ * 2) + (((lane & 15) ^ ((k & 7) << 1)) * 16) - i * 1024;
      }
#pragma unroll
      for (int i = 0; i < 3; i++) {
        const int o = (p * 3 + i) * 64 + lane, k = o / 12, ch = o % 12;
        vb[i] = k * (F_ * 2) + 256 + ((ch ^ (((k >> 2) & 1) << 1)) * 16) - i * 1024;
      }
      auto pa = [&](int g) { dma<4>(ldsb + (g & 3) * SLOT, va, rp, kstep(g, 16, phi) * (64 * F_ * 2)); };
      auto pbb = [&](int g) { dma<3>(lds0 + p * 3072 + (g & 3) * SLOT, vb, rp, kstep(g, 16, phi) * (64 * F_ * 2)); };
      auto q = [&](int g) { dma_sc1<4>(ldsb + QOFF + (g & 3) * SLOT, vlin, rq, kstep(g, 16, phi) * 16384); };
      pa(0); wait_vm<10>(); bar();
      pa(1); wait_vm<8>(); bar();
      pa(2);
      const int pt = threadIdx.x - 256;
      if (pt < 224) {
        const float* cs = (const float*)(ws + WS_CS) + ft * 224 + pt;
        float s = 0.f;
#pragma unroll
        for (int i = 0; i < 16; i++) s += cs[i * F_];
        tabC[pt] = s;
        tabBias[pt] = __uint_as_float((u32)b1[ft * 224 + pt] << 16);
      }
      flag_request(rfl_, lane * 64, fv);
      wait_vm<0>();
      flag_wait(rfl_, lane * 64, fv);
      q(0); q(1); q(2);
      rsrc_t rs = mk_rsrc(ws + WS_ST + (size_t)tt * 16384, 16384);
      u32x2 sv[8];
      const u32 so = (pt & 1) * 8192 + (pt >> 1) * 8;
#pragma unroll
      for (int i = 0; i < 8; i++)
        asm volatile("buffer_load_dwordx2 %0, %1, %2, %3 offen sc1" : "=&v"(sv[i]) : "v"(so), "s"(rs), "s"(i * 1024) : "memory");
      wait_vm<16>(); bar();
      pa(3); q(3); wait_vm<20>(); bar();
      pa(4); q(4); wait_vm<24>(); bar();
      pa(5); q(5); wait_vm<16>();
      asm volatile("" : "+v"(sv[0]), "+v"(sv[1]), "+v"(sv[2]), "+v"(sv[3]), "+v"(sv[4]), "+v"(sv[5]), "+v"(sv[6]), "+v"(sv[7])::"memory");
      bar();
      {
        float s = 0.f, qq = 0.f;
#pragma unroll
        for (int i = 0; i < 8; i++) { s += __uint_as_float(sv[i][0]); qq += __uint_as_float(sv[i][1]); }
        s += __shfl_xor(s, 1); qq += __shfl_xor(qq, 1);
        const float mean = s * (1.0f / D_), var = fmaxf(qq * (1.0f / D_) - mean * mean, 0.f);
        const float rstd = 1.0f / sqrtf(var + LN_EPS);
        if ((pt & 1) == 0) { tabA[pt >> 1] = rstd; tabB[pt >> 1] = -mean * rstd; }
        wait_lgkm0();
      }
      for (int g = 6; g < 16; g++) { pa(g); q(g); wait_vm<16>(); bar(); }
      pbb(0); q(0); wait_vm<15>(); bar();
      for (int g = 1; g < 14; g++) { pbb(g); q(g); wait_vm<14>(); bar(); }
      flag_request(rfl_, 65536 + lane * 64, fv);
      pbb(14); q(14); wait_vm<14>(); bar();
      pbb(15); q(15); wait_vm<14>();
      flag_wait(rfl_, 65536 + lane * 64, fv);
      bar();
    }
    {
      rsrc_t rp = mk_rsrc(w2 + et * 128, F_ * D_ * 2 - et * 256);
      rsrc_t rq = mk_rsrc(ws + WS_R + (size_t)tt * 112 * 8192, 112 * 8192);
      u32 vp[4];
#pragma unroll
      for (int i = 0; i < 4; i++) {
        const int k = 16 * (p & 1) + 4 * i + (lane >> 4);
        vp[i] = k * (D_ * 2) + (((lane & 15) ^ ((k & 7) << 1)) * 16) - i * 1024;
      }
      const u32 vq0 = (4 * (p & 1) + 2 * half) * 1024 + lane * 16, vq1 = (4 * (p & 1) + 2 - 2 * half) * 1024 + lane * 16;
      const u32 ldsh = lds0 + QOFF + p * 4096;
      auto bblock = [&](int hf, int n) { return 7 * (8 * hf + n / 3) + 4 + n % 3; };
      auto full = [&](int g) {
        const int k = g < 16 ? kstep2(g, 16, phi, xcd & 1) : 16 + kstep2(g - 16, 8, phi, xcd & 1), n = 2 * k + (p >> 1);
        const int blk = k < 16 ? 7 * (8 * half + (n >> 2)) + (n & 3) : bblock(half, n - 32);
        const u32 m = ldsb + (g & 3) * SLOT;
        dma<4>(m, vp, rp, blk * (32 * D_ * 2));
        dma_sc1<2>(m + QOFF, vq0, rq, blk * 8192);
        dma_sc1<2>(m + QOFF + 2048, vq1, rq, blk * 8192);
      };
      auto halfs = [&](int g) {
        const int k = kstep2(g, 8, phi, xcd & 1), blk = bblock(k >> 2, 16 + 2 * (k & 3) + (p >> 1));
        dma<4>(ldsb + (g & 3) * SLOT, vp, rp, blk * (32 * D_ * 2));
        dma_sc1<2>(ldsh + (g & 3) * SLOT, vq0, rq, blk * 8192);
      };
      full(0); wait_vm<15>(); bar();
      for (int g = 1; g < 14; g++) { full(g); wait_vm<16>(); bar(); }
      flag_request(rfl_, 131072 + lane * 64, fv);
      full(14); wait_vm<16>(); bar();
      full(15); wait_vm<16>();
      flag_wait(rfl_, 131072 + lane * 64, fv);
      bar();
      for (int g = 16; g < 24; g++) { full(g); wait_vm<16>(); bar(); }
      halfs(0); wait_vm<14>(); bar();
      for (int g = 1; g < 8; g++) { halfs(g); wait_vm<12>(); bar(); }
      wait_vm<6>(); bar();
      wait_vm<0>(); bar();
    }
  } else {
    const int wP = wid >> 1, wQ = wid & 1, c = lane & 15, g = lane >> 4;
    const int m0 = 4 * wQ + 2 * half, cell = ft * 4 + wid;
    u32x4 hk[2][2];
    {
      Consumer<4, M_TILED, M_ROW> C;
      C.init(sm, wP, wQ, lane);
      f32x4 acc[4][4];
      C.zero(acc);
      bar();
      STAMP(1)
      C.first();
      for (int it = 0; it < 6; it++) {
        C.step4<4, 4>(acc);
        if (it == 0) clear_slots(ws + WS_X4, blockIdx.x ^ 4, wid, lane);
      }
      C.step4<4, 2>(acc);
      STAMP(2)
      xch_send(ws + WS_X2, acc, wid, lane);
      u32x2 xr[2][4];
#pragma unroll
      for (int jj = 0; jj < 2; jj++)
#pragma unroll
        for (int i = 0; i < 4; i++)
          xr[jj][i] = *(const u32x2*)(x + (size_t)(tt * 128 + 16 * (m0 + jj) + c) * D_ + et * 128 + 64 * wP + 16 * i + 4 * g);
      __builtin_amdgcn_sched_barrier(0);
      C.step4<2, 2>(acc);
      C.step<0, 2, 2>(acc);
      rsrc_t rx = mk_rsrc(ws + WS_X2 + ((size_t)(blockIdx.x ^ 4) * 4 + wid) * 4096, 4096);
      u32x4 rv[4];
      C.step<1, 2, 2>(acc);
      xch_request(rx, rv, lane);
      __builtin_amdgcn_sched_barrier(0);
      C.step<2, 2, 2>(acc);
      C.load<3, 1, 2>(); C.mma<0, 2>(acc); C.interleave<8, 2>();
      C.mma<1, 2>(acc);
      STAMP(3)
      xch_finish(rx, rv, lane);
      STAMP(4)
      rsrc_t rh = mk_rsrc(ws + WS_H + ((size_t)(tt * 32 + 4 * et + 2 * wP) * 8 + m0) * 1024, 16384);
      rsrc_t rs = mk_rsrc(ws + WS_ST + ((size_t)(tt * 16 + 2 * et + wP) * 128 + 16 * m0) * 8, 256);
#pragma unroll
      for (int jj = 0; jj < 2; jj++) {
        float s = 0.f, q = 0.f;
#pragma unroll
        for (int u = 0; u < 2; u++) {
          const u32x4 o = rv[jj * 2 + u];
          const f32x4 a = acc[2 * u][jj], b = acc[2 * u + 1][jj];
          const u32x2 xa = xr[jj][2 * u], xb = xr[jj][2 * u + 1];
          u32x4 h;
          h[0] = cvtpk(a[0] + blo(o[0]) + blo(xa[0]), a[1] + bhi(o[0]) + bhi(xa[0]));
          h[1] = cvtpk(a[2] + blo(o[1]) + blo(xa[1]), a[3] + bhi(o[1]) + bhi(xa[1]));
          h[2] = cvtpk(b[0] + blo(o[2]) + blo(xb[0]), b[1] + bhi(o[2]) + bhi(xb[0]));
          h[3] = cvtpk(b[2] + blo(o[3]) + blo(xb[1]), b[3] + bhi(o[3]) + bhi(xb[1]));
          store_sc1(rh, (u * 8 + jj) * 1024 + lane * 16, h);
          hk[jj][u] = h;
#pragma unroll
          for (int e = 0; e < 4; e++) { const float lo = blo(h[e]), hi = bhi(h[e]); s += lo + hi; q += lo * lo + hi * hi; }
        }
        s = xsum32(xsum16(s)); q = xsum32(xsum16(q));
        if (g == 0) __builtin_amdgcn_raw_buffer_store_b64((u32x2){__float_as_uint(s), __float_as_uint(q)}, rs, (16 * jj + c) * 8, 0, 16);
      }
      flag_set(ws, 0, tt, cell);
      STAMP(5)
    }
    rsrc_t rr = mk_rsrc(ws + WS_R + ((size_t)(tt * 112 + 7 * ft) * 8 + 4 * wQ) * 1024, 7 * 8192);
    float ra[4], rb[4];
    auto act = [&](const f32x4& a, int col, int j, u32& lo, u32& hi) {
      const f32x4 cs = *(const LAS f32x4*)(tabC + col), bi = *(const LAS f32x4*)(tabBias + col);
      f32x4 v = ra[j] * a + (rb[j] * cs + bi);
#pragma unroll
      for (int e = 0; e < 4; e++) { v[e] = v[e] > 0.f ? v[e] : 0.f; v[e] *= v[e]; }
      lo = cvtpk(v[0], v[1]); hi = cvtpk(v[2], v[3]);
    };
    {
      Consumer<4, M_TR256, M_TILED> C;
      C.init(sm, wP, wQ, lane);
      f32x4 acc[4][4];
      C.zero(acc);
      bar();
      STAMP(6)
      C.first();
      for (int it = 0; it < 3; it++) C.step4<4, 4>(acc);
      C.last4<4>(acc);
      STAMP(7)
#pragma unroll
      for (int j = 0; j < 4; j++) { ra[j] = tabA[64 * wQ + 16 * j + c]; rb[j] = tabB[64 * wQ + 16 * j + c]; }
#pragma unroll
      for (int u = 0; u < 2; u++)
#pragma unroll
        for (int j = 0; j < 4; j++) {
          u32 a0, a1, b0, b1_;
          act(acc[2 * u][j], 64 * wP + 32 * u + 4 * g, j, a0, a1);
          act(acc[2 * u + 1][j], 64 * wP + 32 * u + 16 + 4 * g, j, b0, b1_);
          store_sc1(rr, ((2 * wP + u) * 8 + j) * 1024 + lane * 16, (u32x4){a0, a1, b0, b1_});
        }
    }
    {
      Consumer<3, M_TR192, M_TILED> C;
      C.init(sm, wP, wQ, lane);
      f32x4 acc[3][4];
      C.zero(acc);
      bar();
      STAMP(8)
      C.first();
      for (int it = 0; it < 3; it++) {
        C.step4<4, 4>(acc);
        if (it == 1) flag_set(ws, 1, tt, cell);
      }
      C.last4<4>(acc);
      STAMP(9)
#pragma unroll
      for (int j = 0; j < 4; j++) {
        u32 a0, a1, b0, b1_;
        act(acc[0][j], 128 + 64 * wP + 4 * g, j, a0, a1);
        act(acc[1][j], 128 + 64 * wP + 16 + 4 * g, j, b0, b1_);
        store_sc1(rr, ((4 + 2 * wP) * 8 + j) * 1024 + lane * 16, (u32x4){a0, a1, b0, b1_});
        act(acc[2][j], 160 + 16 * wP + 4 * g, j, a0, a1);
        __builtin_amdgcn_raw_buffer_store_b64((u32x2){a0, a1}, rr, (5 * 8 + j) * 1024 + lane * 16 + 8 * wP, 0, 16);
      }
    }
    {
      Consumer<4, M_TR256, M_TILED> C;
      C.init(sm, wP, wQ, lane);
      f32x4 acc[4][4];
      C.zero(acc);
      bar();
      STAMP(10)
      C.first();
      for (int it = 0; it < 5; it++) {
        C.step4<4, 4>(acc);
        if (it == 1) flag_set(ws, 2, tt, cell);
      }
      C.step4<4, 2>(acc);
      STAMP(11)
      xch_send(ws + WS_X4, acc, wid, lane);
      u32x2 br[4];
#pragma unroll
      for (int i = 0; i < 4; i++) br[i] = *(const u32x2*)(b2 + et * 128 + 64 * wP + 16 * i + 4 * g);
      __builtin_amdgcn_sched_barrier(0);
      C.step4<2, 2>(acc);
      C.step<0, 2, 2>(acc);
      rsrc_t rx = mk_rsrc(ws + WS_X4 + ((size_t)(blockIdx.x ^ 4) * 4 + wid) * 4096, 4096);
      u32x4 rv[4];
      C.step<1, 2, 2>(acc);
      xch_request(rx, rv, lane);
      __builtin_amdgcn_sched_barrier(0);
      C.step<2, 2, 2>(acc);
      C.load<3, 1, 2>(); C.mma<0, 2>(acc); C.interleave<8, 2>();
      C.mma<1, 2>(acc);
      STAMP(12)
      xch_finish(rx, rv, lane);
      STAMP(13)
#pragma unroll
      for (int jj = 0; jj < 2; jj++)
#pragma unroll
        for (int u = 0; u < 2; u++) {
          const u32x4 o = rv[jj * 2 + u], h = hk[jj][u];
          const f32x4 a = acc[2 * u][jj], b = acc[2 * u + 1][jj];
          const u32x2 ba = br[2 * u], bb = br[2 * u + 1];
          const u32 a0 = cvtpk(a[0] + blo(o[0]) + blo(ba[0]) + blo(h[0]), a[1] + bhi(o[0]) + bhi(ba[0]) + bhi(h[0]));
          const u32 a1 = cvtpk(a[2] + blo(o[1]) + blo(ba[1]) + blo(h[1]), a[3] + bhi(o[1]) + bhi(ba[1]) + bhi(h[1]));
          const u32 b0 = cvtpk(b[0] + blo(o[2]) + blo(bb[0]) + blo(h[2]), b[1] + bhi(o[2]) + bhi(bb[0]) + bhi(h[2]));
          const u32 b1_ = cvtpk(b[2] + blo(o[3]) + blo(bb[1]) + blo(h[3]), b[3] + bhi(o[3]) + bhi(bb[1]) + bhi(h[3]));
          const auto s0 = __builtin_amdgcn_permlane16_swap(a0, b0, false, false);
          const auto s1 = __builtin_amdgcn_permlane16_swap(a1, b1_, false, false);
          *(u32x4*)(out + (size_t)(tt * 128 + 16 * (m0 + jj) + c) * D_ + et * 128 + 64 * wP + 32 * u + 16 * (g & 1) + 8 * (g >> 1)) =
              (u32x4){s0[0], s1[0], s0[1], s1[1]};
        }
      DIAG_END(14)
    }
  }
}

}

extern "C" void kernel_launch(void* const* d_in, const int* in_sizes, int n_in, void* d_out, int out_size, void* d_ws, size_t ws_size,
                              hipStream_t stream) {
  const u16* const* I = (const u16* const*)d_in;
  char* ws = (char*)d_ws;
  (void)hipFuncSetAttribute((const void*)ka_kernel, hipFuncAttributeMaxDynamicSharedMemorySize, LDS_BYTES);
  (void)hipFuncSetAttribute((const void*)kb_kernel, hipFuncAttributeMaxDynamicSharedMemorySize, LDS_BYTES);
  ka_kernel<<<256, 512, LDS_BYTES, stream>>>(I[0], I[3], I[4], I[6], ws);
  kb_kernel<<<256, 512, LDS_BYTES, stream>>>(I[0], I[1], I[2], I[6], I[7], I[8], I[9], ws, (u16*)d_out);
}
```

```cpp
#include <hip/hip_runtime.h>
#include <hip/hip_bf16.h>

typedef unsigned short u16;
typedef unsigned int u32;
typedef unsigned long long u64;
typedef __bf16 bf16x8 __attribute__((ext_vector_type(8)));
typedef __bf16 bf16x2 __attribute__((ext_vector_type(2)));
typedef short s16x4 __attribute__((ext_vector_type(4)));
typedef short s16x8 __attribute__((ext_vector_type(8)));
typedef float f32x2 __attribute__((ext_vector_type(2)));
typedef float f32x4 __attribute__((ext_vector_type(4)));
typedef u32 u32x2 __attribute__((ext_vector_type(2)));
typedef u32 u32x4 __attribute__((ext_vector_type(4)));
typedef __amdgpu_buffer_rsrc_t rsrc_t;
#define LAS __attribute__((address_space(3)))
#define DI __device__ __forceinline__

#define K_FLAGIT 0
#define K_SIDE 1
#define K_CS 0
#define K_XF 4
#define K_SLEEP 1
#define K_OUTSC1 1
namespace {

constexpr int S_ = 2048, D_ = 1024, F_ = 3584;
constexpr float LN_EPS = 1e-5f;

constexpr size_t WS_AB = 0;
constexpr size_t WS_H = 8u << 20;
constexpr size_t WS_R = 12u << 20;
constexpr size_t WS_X2 = 27u << 20;
constexpr size_t WS_X4 = 31u << 20;
constexpr size_t WS_ST = 35u << 20;
constexpr size_t WS_CS = 36u << 20;
constexpr size_t WS_FL = 37u << 20;

#define DIAG_ARG
#define DIAG_BEGIN
#define STAMP(i)
#define PSTAMP(i)
#define PDIAG_END
#define DIAG_END(i)

DI u32 rfl(u32 x) { return __builtin_amdgcn_readfirstlane(x); }
DI u32 cvtpk(float lo, float hi) { f32x2 v = {lo, hi}; return __builtin_bit_cast(u32, __builtin_convertvector(v, bf16x2)); }
DI float blo(u32 v) { return __uint_as_float(v << 16); }
DI float bhi(u32 v) { return __uint_as_float(v & 0xFFFF0000u); }
DI rsrc_t mk_rsrc(const void* p, u32 bytes) { return __builtin_amdgcn_make_buffer_rsrc((void*)p, 0, bytes, 0x00020000); }
DI void bar() { asm volatile("s_barrier" ::: "memory"); }
DI void wait_lgkm0() { __builtin_amdgcn_s_waitcnt(0xC07F); }
template <int N> DI void wait_vm() { asm volatile("s_waitcnt vmcnt(%0)" ::"i"(N) : "memory"); }

template <int N> DI void dma(u32 m0, const u32* v, rsrc_t r, u32 so) {
  if constexpr (N == 4)
    asm volatile("s_mov_b32 m0, %0\n\ts_nop 0\n\t"
                 "buffer_load_dwordx4 %1, %5, %6 offen lds\n\t"
                 "buffer_load_dwordx4 %2, %5, %6 offen offset:1024 lds\n\t"
                 "buffer_load_dwordx4 %3, %5, %6 offen offset:2048 lds\n\t"
                 "buffer_load_dwordx4 %4, %5, %6 offen offset:3072 lds"
                 ::"s"(m0), "v"(v[0]), "v"(v[1]), "v"(v[2]), "v"(v[3]), "s"(r), "s"(so) : "memory");
  else if constexpr (N == 3)
    asm volatile("s_mov_b32 m0, %0\n\ts_nop 0\n\t"
                 "buffer_load_dwordx4 %1, %4, %5 offen lds\n\t"
                 "buffer_load_dwordx4 %2, %4, %5 offen offset:1024 lds\n\t"
                 "buffer_load_dwordx4 %3, %4, %5 offen offset:2048 lds"
                 ::"s"(m0), "v"(v[0]), "v"(v[1]), "v"(v[2]), "s"(r), "s"(so) : "memory");
  else
    asm volatile("s_mov_b32 m0, %0\n\ts_nop 0\n\t"
                 "buffer_load_dwordx4 %1, %3, %4 offen lds\n\t"
                 "buffer_load_dwordx4 %2, %3, %4 offen offset:1024 lds"
                 ::"s"(m0), "v"(v[0]), "v"(v[1]), "s"(r), "s"(so) : "memory");
}
template <int N> DI void dma_sc1(u32 m0, u32 v, rsrc_t r, u32 so) {
  if constexpr (N == 4)
    asm volatile("s_mov_b32 m0, %0\n\ts_nop 0\n\t"
                 "buffer_load_dwordx4 %1, %2, %3 offen sc1 lds\n\t"
                 "buffer_load_dwordx4 %1, %2, %3 offen offset:1024 sc1 lds\n\t"
                 "buffer_load_dwordx4 %1, %2, %3 offen offset:2048 sc1 lds\n\t"
                 "buffer_load_dwordx4 %1, %2, %3 offen offset:3072 sc1 lds"
                 ::"s"(m0), "v"(v), "s"(r), "s"(so) : "memory");
  else
    asm volatile("s_mov_b32 m0, %0\n\ts_nop 0\n\t"
                 "buffer_load_dwordx4 %1, %2, %3 offen sc1 lds\n\t"
                 "buffer_load_dwordx4 %1, %2, %3 offen offset:1024 sc1 lds"
                 ::"s"(m0), "v"(v), "s"(r), "s"(so) : "memory");
}

DI float xsum16(float v) { const auto r = __builtin_amdgcn_permlane16_swap(__float_as_uint(v), __float_as_uint(v), false, false); return __uint_as_float(r[0]) + __uint_as_float(r[1]); }
DI float xsum32(float v) { const auto r = __builtin_amdgcn_permlane32_swap(__float_as_uint(v), __float_as_uint(v), false, false); return __uint_as_float(r[0]) + __uint_as_float(r[1]); }

DI int kstep(int t, int n, int phi) {
  int k = t;
  if ((t & 3) == phi) { k = t + 4; if (k >= n) k -= n; }
  return k;
}

DI int kstep2(int t, int n, int phi, int side) {
  const int k = kstep(t, n, phi), k2 = k ^ ((K_SIDE ? side : 0) << 2);
  return k2 < n ? k2 : k;
}

constexpr int NST = 4;
constexpr int SLOT = 16384;
constexpr int QOFF = NST * SLOT;
constexpr int TOFF = 2 * NST * SLOT;
constexpr int COFF = TOFF + 4096;
constexpr int XOFF = COFF + 4096;
constexpr int LDS_BYTES = XOFF + 16384;

enum { M_ROW = 0, M_TILED = 1, M_TR192 = 2, M_TR256 = 3 };

template <int PT, int PMODE, int QMODE>
struct Consumer {
  const LAS char* pb[4];
  const LAS char* qb[4];
  bf16x8 fp[2][PT], fq[2][4];

  DI void init(const LAS char* sm, int wP, int wQ, int lane, int prot = 0, int qrot = 0) {
    const int r = lane & 15, g = lane >> 4;
    if constexpr (PMODE == M_ROW) {
      const int a = (64 * wP + r) * 128 + ((g ^ (r >> 1)) * 16);
      pb[0] = sm + (a + prot * 4096); pb[1] = sm + ((a ^ 64) + prot * 4096);
      pb[2] = sm + (a + 4096 - prot * 4096); pb[3] = sm + ((a ^ 64) + 4096 - prot * 4096);
    } else if constexpr (PMODE == M_TILED) {
      pb[0] = sm + (4 * wP * 1024 + lane * 16);
    } else if constexpr (PMODE == M_TR192) {
      const int b = g & 1, a = (4 * g + (r >> 2)) * 192 + (r & 3) * 8;
      pb[0] = sm + (a + (4 * wP + b) * 32);
      pb[1] = sm + (a + (4 * wP + 1 - b) * 32);
      pb[2] = sm + (a + ((2 + wP) ^ b) * 32);
    } else {
      const int k = 4 * g + (r >> 2), a = k * 256 + (r & 3) * 8;
#pragma unroll
      for (int i = 0; i < 4; i++) pb[i] = sm + (a + (((4 * wP + i) ^ (k & 7)) * 32));
    }
    if constexpr (QMODE == M_ROW) {
      const int a = QOFF + (64 * wQ + r) * 128 + ((g ^ (r >> 1)) * 16);
      qb[0] = sm + (a + qrot * 4096); qb[1] = sm + ((a ^ 64) + qrot * 4096);
      qb[2] = sm + (a + 4096 - qrot * 4096); qb[3] = sm + ((a ^ 64) + 4096 - qrot * 4096);
      asm volatile("" : "+v"(qb[0]), "+v"(qb[1]), "+v"(qb[2]), "+v"(qb[3]));
    } else {
      qb[0] = sm + (QOFF + 4 * wQ * 1024 + lane * 16);
      asm volatile("" : "+v"(qb[0]));
    }
  }
  static DI bf16x8 tr2(const LAS char* p, int second) {
    s16x4 a = __builtin_amdgcn_ds_read_tr16_b64_v4i16((LAS s16x4*)p);
    s16x4 b = __builtin_amdgcn_ds_read_tr16_b64_v4i16((LAS s16x4*)(p + second));
    return __builtin_bit_cast(bf16x8, __builtin_shufflevector(a, b, 0, 1, 2, 3, 4, 5, 6, 7));
  }
  template <int S, int KS> DI bf16x8 ldp(int i) {
    if constexpr (PMODE == M_ROW) return *(const LAS bf16x8*)(pb[KS + (i & 2)] + (S * SLOT + (i & 1) * 2048));
    else if constexpr (PMODE == M_TILED) return *(const LAS bf16x8*)(pb[0] + (S * SLOT + KS * 8192 + i * 1024));
    else if constexpr (PMODE == M_TR192) return tr2(pb[i] + (S * SLOT + KS * 32 * 192), 16 * 192);
    else return tr2(pb[i] + (S * SLOT + KS * 32 * 256), 16 * 256);
  }
  template <int S, int KS> DI bf16x8 ldq(int j) {
    if constexpr (QMODE == M_ROW) return *(const LAS bf16x8*)(qb[KS + (j & 2)] + (S * SLOT + (j & 1) * 2048));
    else return *(const LAS bf16x8*)(qb[0] + (S * SLOT + KS * 8192 + j * 1024));
  }
  template <int S, int KS, int QN> DI void load() {
    fp[KS][0] = ldp<S, KS>(0);
#pragma unroll
    for (int j = 0; j < QN; j++) fq[KS][j] = ldq<S, KS>(j);
#pragma unroll
    for (int i = 1; i < PT; i++) fp[KS][i] = ldp<S, KS>(i);
  }
  template <int KS, int QN> DI void mma(f32x4 (&acc)[PT][4]) {
#pragma unroll
    for (int i = 0; i < PT; i++)
#pragma unroll
      for (int j = 0; j < QN; j++) acc[i][j] = __builtin_amdgcn_mfma_f32_16x16x32_bf16(fp[KS][i], fq[KS][j], acc[i][j], 0, 0, 0);
  }
  template <int KS> DI void stats(f32x4 (&sx)[2], f32x4 (&sxx)[2], f32x4 (&sd)[2], bf16x8 ones) {
#pragma unroll
    for (int i = 0; i < 2; i++) {
      sx[i] = __builtin_amdgcn_mfma_f32_16x16x32_bf16(fp[KS][i], ones, sx[i], 0, 0, 0);
      sxx[i] = __builtin_amdgcn_mfma_f32_16x16x32_bf16(fp[KS][i], fp[KS][i], sxx[i], 0, 0, 0);
      sd[i] = __builtin_amdgcn_mfma_f32_16x16x32_bf16(ones, fq[KS][i], sd[i], 0, 0, 0);
    }
  }
  template <int NM, int QN> static DI void interleave() {
    constexpr int NR = PT * (PMODE >= M_TR192 ? 2 : 1) + QN;
    if constexpr (NR <= NM) {
#pragma unroll
      for (int i = 0; i < NR; i++) { __builtin_amdgcn_sched_group_barrier(0x8, 1, 0); __builtin_amdgcn_sched_group_barrier(0x100, 1, 0); }
      __builtin_amdgcn_sched_group_barrier(0x8, NM - NR, 0);
    } else {
#pragma unroll
      for (int i = 0; i < NR / 2; i++) { __builtin_amdgcn_sched_group_barrier(0x8, 1, 0); __builtin_amdgcn_sched_group_barrier(0x100, 2, 0); }
      __builtin_amdgcn_sched_group_barrier(0x8, NM - NR / 2, 0);
    }
    __builtin_amdgcn_sched_barrier(0);
  }
  template <int S, int QC, int QN> DI void step(f32x4 (&acc)[PT][4]) {
    load<S, 1, QC>(); mma<0, QC>(acc); interleave<PT * QC, QC>();
    wait_lgkm0(); bar();
    __builtin_amdgcn_sched_barrier(0);
    load<(S + 1) % NST, 0, QN>(); mma<1, QC>(acc); interleave<PT * QC, QN>();
  }
  template <int QC, int QN> DI void step4(f32x4 (&acc)[PT][4]) {
    step<0, QC, QC>(acc); step<1, QC, QC>(acc); step<2, QC, QC>(acc); step<3, QC, QN>(acc);
  }
  template <int QC> DI void last4(f32x4 (&acc)[PT][4]) {
    step<0, QC, QC>(acc); step<1, QC, QC>(acc); step<2, QC, QC>(acc);
    load<3, 1, QC>(); mma<0, QC>(acc); interleave<PT * QC, QC>();
    mma<1, QC>(acc);
  }
  static DI void zero(f32x4 (&acc)[PT][4]) {
#pragma unroll
    for (int i = 0; i < PT; i++)
#pragma unroll
      for (int j = 0; j < 4; j++) { acc[i][j] = (f32x4){0.f, 0.f, 0.f, 0.f}; asm volatile("" : "+v"(acc[i][j])); }
  }
  DI void first() { load<0, 0, 4>(); }
};

DI u32 row_src(int imgrow, int srcrow, int lane, int ldbytes) {
  return srcrow * ldbytes + (((lane & 7) ^ ((imgrow >> 1) & 7)) * 16);
}
DI void store_sc1(rsrc_t r, u32 off, u32x4 v) { __builtin_amdgcn_raw_buffer_store_b128(v, r, off, 0, 16); }

DI void xch_send(char* x, const f32x4 (&acc)[4][4], int wid, int lane) {
  rsrc_t r = mk_rsrc(x + ((size_t)blockIdx.x * 4 + wid) * 4096, 4096);
#pragma unroll
  for (int jj = 0; jj < 2; jj++)
#pragma unroll
    for (int u = 0; u < 2; u++) {
      const f32x4 a = acc[2 * u][2 + jj], b = acc[2 * u + 1][2 + jj];
      u32x4 v = {cvtpk(a[0], a[1]), cvtpk(a[2], a[3]), cvtpk(b[0], b[1]), cvtpk(b[2], b[3])};
      store_sc1(r, lane * 16 + (jj * 2 + u) * 1024, v);
    }
}
DI void xch_request(rsrc_t r, u32x4 (&v)[4], int lane) {
  asm volatile("buffer_load_dwordx4 %0, %4, %5, 0 offen sc1\n\t"
               "buffer_load_dwordx4 %1, %4, %5, 0 offen offset:1024 sc1\n\t"
               "buffer_load_dwordx4 %2, %4, %5, 0 offen offset:2048 sc1\n\t"
               "buffer_load_dwordx4 %3, %4, %5, 0 offen offset:3072 sc1"
               : "=&v"(v[0]), "=&v"(v[1]), "=&v"(v[2]), "=&v"(v[3]) : "v"(lane * 16), "s"(r) : "memory");
}
DI void xch_finish(u32x4 (&v)[4]) { asm volatile("s_waitcnt vmcnt(0)" : "+v"(v[0]), "+v"(v[1]), "+v"(v[2]), "+v"(v[3])::"memory"); }

enum { FL_H = 0, FL_RA = 1, FL_RB = 2, FL_X2 = 3, FL_X4 = 4, FL_ACK = 5, FL_TICK = 6 };
DI void flag_set(char* ws, int which, int tt, int cell) {
  wait_vm<0>();
  if ((threadIdx.x & 63) == 0)
    __hip_atomic_store((u32*)(ws + WS_FL + ((size_t)(which * 16 + tt) * 64 + cell) * 64), 1u, __ATOMIC_RELAXED, __HIP_MEMORY_SCOPE_AGENT);
}
DI void flag_request(rsrc_t r, u32 off, u32& v) {
  asm volatile("buffer_load_dword %0, %1, %2, 0 offen sc1" : "=&v"(v) : "v"(off), "s"(r) : "memory");
}
DI void flag_wait(rsrc_t r, u32 off, u32& v) {
  asm volatile("" : "+v"(v)::"memory");
#pragma unroll 1
  for (int spin = 0; spin < (1 << 20); spin++) {
    if (__builtin_amdgcn_ballot_w64(v != 1u) == 0) break;
    __builtin_amdgcn_s_sleep(K_SLEEP);
    asm volatile("buffer_load_dword %0, %1, %2, 0 offen sc1\n\ts_waitcnt vmcnt(0)" : "=&v"(v) : "v"(off), "s"(r) : "memory");
  }
}

DI void word_store(rsrc_t r, u32 off, u32 v) { __builtin_amdgcn_raw_buffer_store_b32(v, r, off, 0, 16); }
template <class Done>
DI void watch(rsrc_t r, u32 off, u32& v, Done done, u32 ownoff, u32 ownval, u32& bump, bool& ticked, u64 ownmask) {
  asm volatile("" : "+v"(v)::"memory");
#pragma unroll 1
  for (int spin = 0; spin < (1 << 20); spin++) {
    const u64 miss = __builtin_amdgcn_ballot_w64(!done(v));
    if (miss == 0) break;
    ticked = ticked || (miss & ownmask) == 0;
    if (ticked && (spin & 63) == 63) {
      bump++;
      if ((ownmask >> (threadIdx.x & 63)) & 1) word_store(r, ownoff, ownval + bump);
    }
    __builtin_amdgcn_s_sleep(K_SLEEP);
    asm volatile("buffer_load_dword %0, %1, %2, 0 offen sc1\n\ts_waitcnt vmcnt(0)" : "=&v"(v) : "v"(off), "s"(r) : "memory");
  }
}

__global__ __launch_bounds__(512, 2) void fused_kernel(const u16* __restrict__ x, const u16* __restrict__ cre, const u16* __restrict__ cim,
                                                    const u16* __restrict__ dre, const u16* __restrict__ dim,
                                                    const u16* __restrict__ w1, const u16* __restrict__ b1, const u16* __restrict__ w2,
                                                    const u16* __restrict__ b2, char* __restrict__ ws, u16* __restrict__ out DIAG_ARG) {
  extern __shared__ __attribute__((aligned(16))) char smem[];
  DIAG_BEGIN
  const int lane = threadIdx.x & 63, wid = rfl(threadIdx.x >> 6);
  const int xcd = blockIdx.x & 7, loc = blockIdx.x >> 3, li = loc >> 2, lj = loc & 3;
  const int half = xcd >> 2, et = ((xcd >> 1) & 1) * 4 + lj, tt = (xcd & 1) * 8 + li, ft = (xcd >> 1) * 4 + lj;
  LAS char* sm = (LAS char*)smem;
  LAS float* tabA = (LAS float*)(sm + TOFF);
  LAS float* tabB = tabA + 128;
  LAS float* tabC = tabA + 256;
  LAS float* tabBias = tabA + 512;
  if (wid >= 4) {
    const int p = wid - 4, phi = (li + lj) & 3;
    const u32 lds0 = (u32)(size_t)smem, ldsb = lds0 + p * 4096, vlin = p * 4096 + lane * 16;
    rsrc_t rall = mk_rsrc(ws + WS_FL, 7 * 65536);
    const u32 colcell = ((lane >> 2) * 64 + ft * 4 + (lane & 3)) * 64, rowcell = (tt * 64 + (lane & 15) * 4) * 64;
    const u64 ownmask = 15ull << (4 * tt);
    u32 col0, row0, fv, bump = 1;
    bool ticked = false;
    {
      rsrc_t rp = mk_rsrc((const char*)x + (size_t)tt * 128 * D_ * 2 - 4096, 128 * D_ * 2 + 4096);
      rsrc_t rq = mk_rsrc((half ? dim : dre) + (size_t)et * 128 * D_, 128 * D_ * 2);
      u32 vp[4], vq[4], vx[2];
#pragma unroll
      for (int i = 0; i < 4; i++) {
        const int row = 32 * p + 8 * i + (lane >> 3), u = (row >> 4) & 1, ii = row & 15;
        vp[i] = row_src(row, 32 * p + 8 * (ii >> 2) + 4 * u + (ii & 3), lane, D_ * 2) + 4096 - i * 1024;
        vq[i] = row_src(row, row, lane, D_ * 2) - i * 1024;
      }
#pragma unroll
      for (int i = 0; i < 2; i++) {
        const int row = 16 * p + 8 * i + (lane >> 3);
        vx[i] = row_src(row, 64 * (row >> 5) + 32 * half + (row & 31), lane, D_ * 2) + 4096 - i * 1024;
      }
      auto issue = [&](int g) {
        const int k = kstep2(g, 16, phi, (xcd >> 1) ^ (xcd & 1));
        const u32 m = ldsb + (g & 3) * SLOT;
        dma<4>(m, vp, rp, k * 128);
        dma<4>(m + QOFF, vq, rq, k * 128);
        if ((k >> 1) == et) dma<2>(lds0 + XOFF + (k & 1) * 8192 + p * 2048, vx, rp, k * 128);
      };
      issue(0); issue(1); issue(2);
      wait_vm<16>(); bar();
#pragma unroll 1
      for (int g = 3; g < 6; g++) { issue(g); wait_vm<16>(); bar(); }
      flag_request(rall, FL_TICK * 65536 + colcell, col0);
      flag_request(rall, FL_TICK * 65536 + rowcell, row0);
#pragma unroll 1
      for (int g = 6; g < 16; g++) { issue(g); wait_vm<16>(); bar(); }
      asm volatile("" : "+v"(col0), "+v"(row0)::"memory");
    }
    {
      rsrc_t rp = mk_rsrc(ws + WS_AB + (size_t)et * 32 * 16384, 9 * 32 * 16384);
      rsrc_t rqo = mk_rsrc((const char*)(half ? cim : cre) + (size_t)tt * 128 * S_ * 2 - 4096, 128 * S_ * 2 + 4096);
      rsrc_t rq0 = mk_rsrc((const char*)cre + (size_t)tt * 128 * S_ * 2 - 4096, 128 * S_ * 2 + 4096);
      rsrc_t rq1 = mk_rsrc((const char*)cim + (size_t)tt * 128 * S_ * 2 - 4096, 128 * S_ * 2 + 4096);
      u32 vq[4], vh[2];
#pragma unroll
      for (int i = 0; i < 4; i++) {
        const int row = 32 * p + 8 * i + (lane >> 3);
        vq[i] = row_src(row, row ^ (half * 32), lane, S_ * 2) + 4096 - i * 1024;
      }
#pragma unroll
      for (int i = 0; i < 2; i++) {
        const int row = 64 * (p >> 1) + 16 * (p & 1) + 8 * i + (lane >> 3);
        vh[i] = row_src(row, row ^ (half * 32), lane, S_ * 2) + 4096 - i * 1024;
      }
      const u32 ldsh = lds0 + QOFF + (p >> 1) * 8192 + (p & 1) * 2048;
      auto fp = [&](int g) { dma_sc1<4>(ldsb + (g & 3) * SLOT, vlin, rp, (half * 256 + kstep2(g, 28, phi, (xcd >> 1) & 1)) * 16384); };
      auto fq = [&](int g) { dma<4>(ldsb + QOFF + (g & 3) * SLOT, vq, rqo, kstep2(g, 28, phi, (xcd >> 1) & 1) * 128); };
      auto halfs = [&](int g) {
        const int k = kstep2(g, 8, phi, (xcd >> 1) & 1), term = k >> 2, ks = 28 + (k & 3);
        dma_sc1<4>(ldsb + (g & 3) * SLOT, vlin, rp, (term * 256 + ks) * 16384);
        if (term) dma<2>(ldsh + (g & 3) * SLOT, vh, rq1, ks * 128);
        else dma<2>(ldsh + (g & 3) * SLOT, vh, rq0, ks * 128);
      };
      fq(0); wait_vm<12>(); bar();
      fq(1); wait_vm<8>(); bar();
      fq(2);
      bar();
      bar();
      PSTAMP(1)
      rsrc_t rw = mk_rsrc((const char*)w1 + (size_t)(tt * 64 + p * 16) * F_ * 2 + ft * 448, 15 * F_ * 2 + 448);
      u32x4 v[8];
      const int l28 = lane < 28 ? lane : lane < 56 ? lane - 28 : 27;
      const u32 vo = (lane >= 28 ? F_ * 2 : 0) + l28 * 16;
#pragma unroll
      for (int i = 0; i < 8; i++)
        asm volatile("buffer_load_dwordx4 %0, %1, %2, %3 offen" : "=&v"(v[i]) : "v"(vo), "s"(rw), "s"(i * 2 * F_ * 2) : "memory");
      bar();
      if (p == 0) {
        fv = col0;
        watch(rall, FL_TICK * 65536 + colcell, fv, [&](u32 w) { return w != col0; }, FL_TICK * 65536 + colcell, col0, bump, ticked, ownmask);
        ticked = true;
      }
      bar();
      PSTAMP(2)
      fp(0); fp(1); fp(2);
      asm volatile("s_waitcnt vmcnt(12)" : "+v"(v[0]), "+v"(v[1]), "+v"(v[2]), "+v"(v[3]), "+v"(v[4]), "+v"(v[5]), "+v"(v[6]), "+v"(v[7])::"memory");
      {
        float s[8] = {0, 0, 0, 0, 0, 0, 0, 0};
#pragma unroll
        for (int i = 0; i < 8; i++)
#pragma unroll
          for (int e = 0; e < 4; e++) { s[2 * e] += blo(v[i][e]); s[2 * e + 1] += bhi(v[i][e]); }
#pragma unroll
        for (int e = 0; e < 8; e++) s[e] += __shfl(s[e], lane + 28);
        if (lane < 28) {
          LAS float* d = (LAS float*)(sm + COFF) + p * 224 + lane * 8;
          *(LAS f32x4*)d = (f32x4){s[0], s[1], s[2], s[3]};
          *(LAS f32x4*)(d + 4) = (f32x4){s[4], s[5], s[6], s[7]};
        }
        wait_lgkm0();
      }
      wait_vm<8>();
      PSTAMP(9)
      bar();
      fp(3); fq(3);
      if (p == 0 && lane < 56) {
        const LAS f32x4* d = (const LAS f32x4*)(sm + COFF) + lane;
        const f32x4 t = (d[0] + d[56]) + (d[112] + d[168]);
        store_sc1(mk_rsrc(ws + WS_CS + (size_t)tt * F_ * 4 + ft * 896, 896), lane * 16, __builtin_bit_cast(u32x4, t));
      }
      wait_vm<12>(); bar();
      auto run = [&](int g0, int g1) {
#pragma unroll 1
        for (int g = g0; g < g1; g++) { fp(g); fq(g); wait_vm<16>(); bar(); }
      };
      run(4, 8);
      flag_request(rall, FL_TICK * 65536 + rowcell, fv);
      fp(8); fq(8); wait_vm<16>(); bar();
      fp(9); fq(9); wait_vm<16>();
      asm volatile("" : "+v"(fv)::"memory");
      if (p == 0) {
        watch(rall, FL_TICK * 65536 + rowcell, fv, [&](u32 w) { return w != row0; }, FL_TICK * 65536 + colcell, col0, bump, ticked, ownmask);
        if (lane == 0) word_store(rall, FL_ACK * 65536 + (tt * 64 + ft * 4) * 64, 1u);
      }
      bar();
      run(10, 20);
      const u32 ackcell = FL_ACK * 65536 + ((lane & 16) ? rowcell : ((lane & 15) * 64 + ft * 4) * 64);
      flag_request(rall, ackcell, fv);
      fp(20); fq(20); wait_vm<16>(); bar();
      fp(21); fq(21); wait_vm<16>();
      asm volatile("" : "+v"(fv)::"memory");
      if (p == 0) watch(rall, ackcell, fv, [&](u32 w) { return w == 1u; }, FL_TICK * 65536 + colcell, col0, bump, ticked, ownmask);
      bar();
      run(22, 28);
      halfs(0); wait_vm<14>(); bar();
#pragma unroll 1
      for (int g = 1; g < 8; g++) { halfs(g); wait_vm<12>(); bar(); }
    }
    rsrc_t rfl_ = mk_rsrc(ws + WS_FL + (size_t)tt * 4096, 3 * 16 * 4096);
    {
      rsrc_t rp = mk_rsrc((const char*)w1 + ft * 448, D_ * F_ * 2 - ft * 448);
      rsrc_t rq = mk_rsrc(ws + WS_H + (size_t)tt * 32 * 8192, 32 * 8192);
      u32 va[4], vb[3];
#pragma unroll
      for (int i = 0; i < 4; i++) {
        const int k = 16 * p + 4 * i + (lane >> 4);
        va[i] = k * (F_ * 2) + (((lane & 15) ^ ((k & 7) << 1)) * 16) - i * 1024;
      }
#pragma unroll
      for (int i = 0; i < 3; i++) {
        const int o = (p * 3 + i) * 64 + lane, k = o / 12, ch = o % 12;
        vb[i] = k * (F_ * 2) + 256 + ((ch ^ (((k >> 2) & 1) << 1)) * 16) - i * 1024;
      }
      auto pa = [&](int g) { dma<4>(ldsb + (g & 3) * SLOT, va, rp, kstep(g, 16, phi) * (64 * F_ * 2)); };
      auto pbb = [&](int g) { dma<3>(lds0 + p * 3072 + (g & 3) * SLOT, vb, rp, kstep(g, 16, phi) * (64 * F_ * 2)); };
      auto q = [&](int g) { dma_sc1<4>(ldsb + QOFF + (g & 3) * SLOT, vlin, rq, kstep(g, 16, phi) * 16384); };
      wait_vm<6>(); bar();
      wait_vm<0>(); bar();
      bar();
      pa(0); pa(1); pa(2);
      const int pt = threadIdx.x - 256;
      rsrc_t rs = mk_rsrc(ws + WS_ST + (size_t)tt * 16384, 16384);
      rsrc_t rc = mk_rsrc(ws + WS_CS + ft * 896, 15 * F_ * 4 + 896);
      u32x2 sv[8];
      u32 cv[16], bv;
#pragma unroll
      for (int i = 0; i < 16; i++)
        asm volatile("buffer_load_dword %0, %1, %2, %3 offen sc1" : "=&v"(cv[i]) : "v"(min(pt, 223) * 4), "s"(rc), "s"(i * F_ * 4) : "memory");
      asm volatile("buffer_load_ushort %0, %1, %2, 0 offen" : "=&v"(bv) : "v"(min(pt, 223) * 2), "s"(mk_rsrc(b1 + ft * 224, 448)) : "memory");
      if (p == 0) { fv = 0; flag_wait(rfl_, lane * 64, fv); }
      bar();
      PSTAMP(3)
      q(0); q(1); q(2);
      const u32 so = (pt & 1) * 8192 + (pt >> 1) * 8;
#pragma unroll
      for (int i = 0; i < 8; i++)
        asm volatile("buffer_load_dwordx2 %0, %1, %2, %3 offen sc1" : "=&v"(sv[i]) : "v"(so), "s"(rs), "s"(i * 1024) : "memory");
      wait_vm<16>();
      PSTAMP(4)
      bar();
      pa(3); q(3); wait_vm<20>(); bar();
      pa(4); q(4); wait_vm<24>(); bar();
      pa(5); q(5); wait_vm<16>();
      asm volatile("" : "+v"(sv[0]), "+v"(sv[1]), "+v"(sv[2]), "+v"(sv[3]), "+v"(sv[4]), "+v"(sv[5]), "+v"(sv[6]), "+v"(sv[7]), "+v"(bv)::"memory");
      asm volatile("" : "+v"(cv[0]), "+v"(cv[1]), "+v"(cv[2]), "+v"(cv[3]), "+v"(cv[4]), "+v"(cv[5]), "+v"(cv[6]), "+v"(cv[7]), "+v"(cv[8]),
                   "+v"(cv[9]), "+v"(cv[10]), "+v"(cv[11]), "+v"(cv[12]), "+v"(cv[13]), "+v"(cv[14]), "+v"(cv[15])::"memory");
      bar();
      {
        float s = 0.f;
#pragma unroll
        for (int i = 0; i < 16; i++) s += __uint_as_float(cv[i]);
        if (pt < 224) { tabC[pt] = s; tabBias[pt] = __uint_as_float(bv << 16); }
      }
      {
        float s = 0.f, qq = 0.f;
#pragma unroll
        for (int i = 0; i < 8; i++) { s += __uint_as_float(sv[i][0]); qq += __uint_as_float(sv[i][1]); }
        s += __shfl_xor(s, 1); qq += __shfl_xor(qq, 1);
        const float mean = s * (1.0f / D_), var = fmaxf(qq * (1.0f / D_) - mean * mean, 0.f);
        const float rstd = 1.0f / sqrtf(var + LN_EPS);
        if ((pt & 1) == 0) { tabA[pt >> 1] = rstd; tabB[pt >> 1] = -mean * rstd; }
        wait_lgkm0();
      }
#pragma unroll 1
      for (int g = 6; g < 16; g++) { pa(g); q(g); wait_vm<16>(); bar(); }
      pbb(0); q(0); wait_vm<15>(); bar();
#pragma unroll 1
      for (int g = 1; g < 14; g++) { pbb(g); q(g); wait_vm<14>(); bar(); }
      flag_request(rfl_, 65536 + lane * 64, fv);
      pbb(14); q(14); wait_vm<14>(); bar();
      pbb(15); q(15); wait_vm<14>();
      PSTAMP(5)
      flag_wait(rfl_, 65536 + lane * 64, fv);
      PSTAMP(6)
      bar();
    }
    {
      rsrc_t rp = mk_rsrc(w2 + et * 128, F_ * D_ * 2 - et * 256);
      rsrc_t rq = mk_rsrc(ws + WS_R + (size_t)tt * 112 * 8192, 112 * 8192);
      u32 vp[4];
#pragma unroll
      for (int i = 0; i < 4; i++) {
        const int k = 16 * (p & 1) + 4 * i + (lane >> 4);
        vp[i] = k * (D_ * 2) + (((lane & 15) ^ ((k & 7) << 1)) * 16) - i * 1024;
      }
      const u32 vq0 = (4 * (p & 1) + 2 * half) * 1024 + lane * 16, vq1 = (4 * (p & 1) + 2 - 2 * half) * 1024 + lane * 16;
      const u32 ldsh = lds0 + QOFF + p * 4096;
      auto bblock = [&](int hf, int n) { return 7 * (8 * hf + n / 3) + 4 + n % 3; };
      auto full = [&](int g) {
        const int k = g < 16 ? kstep2(g, 16, phi, xcd & 1) : 16 + kstep2(g - 16, 8, phi, xcd & 1), n = 2 * k + (p >> 1);
        const int blk = k < 16 ? 7 * (8 * half + (n >> 2)) + (n & 3) : bblock(half, n - 32);
        const u32 m = ldsb + (g & 3) * SLOT;
        dma<4>(m, vp, rp, blk * (32 * D_ * 2));
        dma_sc1<2>(m + QOFF, vq0, rq, blk * 8192);
        dma_sc1<2>(m + QOFF + 2048, vq1, rq, blk * 8192);
      };
      auto halfs = [&](int g) {
        const int k = kstep2(g, 8, phi, xcd & 1), blk = bblock(k >> 2, 16 + 2 * (k & 3) + (p >> 1));
        dma<4>(ldsb + (g & 3) * SLOT, vp, rp, blk * (32 * D_ * 2));
        dma_sc1<2>(ldsh + (g & 3) * SLOT, vq0, rq, blk * 8192);
      };
      full(0); wait_vm<15>(); bar();
#pragma unroll 1
      for (int g = 1; g < 14; g++) { full(g); wait_vm<16>(); bar(); }
      flag_request(rfl_, 131072 + lane * 64, fv);
      full(14); wait_vm<16>(); bar();
      full(15); wait_vm<16>();
      PSTAMP(7)
      flag_wait(rfl_, 131072 + lane * 64, fv);
      PSTAMP(8)
      bar();
#pragma unroll 1
      for (int g = 16; g < 24; g++) { full(g); wait_vm<16>(); bar(); }
      halfs(0); wait_vm<14>(); bar();
#pragma unroll 1
      for (int g = 1; g < 8; g++) { halfs(g); wait_vm<12>(); bar(); }
      wait_vm<6>(); bar();
      wait_vm<0>(); bar();
      PDIAG_END
    }
  } else {
    const int wP = wid >> 1, wQ = wid & 1, c = lane & 15, g = lane >> 4;
    const int m0 = 4 * wQ + 2 * half, cell = ft * 4 + wid;
    rsrc_t rall = mk_rsrc(ws + WS_FL, 7 * 65536);
    const u32 mycell = (tt * 64 + cell) * 64;
    u32 tick0;
    {
      LAS float* tabD = tabA + 256;
      Consumer<4, M_ROW, M_ROW> C;
      C.init(sm, wP, wQ, lane, wQ, wP);
      f32x4 acc[4][4], sx[2], sxx[2], sd[2];
      C.zero(acc);
#pragma unroll
      for (int i = 0; i < 2; i++) sx[i] = sxx[i] = sd[i] = (f32x4){0.f, 0.f, 0.f, 0.f};
      const u32x4 ones4 = {0x3F803F80u, 0x3F803F80u, 0x3F803F80u, 0x3F803F80u};
      bf16x8 ones = __builtin_bit_cast(bf16x8, ones4);
      asm volatile("" : "+v"(ones));
      bar();
      STAMP(1)
      C.first();
#define KA_HALF0(S) C.load<S, 1, 4>(); C.mma<0, 4>(acc); C.stats<0>(sx, sxx, sd, ones); C.interleave<22, 4>();
#define KA_STEP(S) KA_HALF0(S) wait_lgkm0(); bar(); __builtin_amdgcn_sched_barrier(0); \
      C.load<(S + 1) % NST, 0, 4>(); C.mma<1, 4>(acc); C.stats<1>(sx, sxx, sd, ones); C.interleave<22, 4>();
      for (int it = 0; it < 3; it++) {
        KA_STEP(0) KA_STEP(1) KA_STEP(2) KA_STEP(3)
      }
      if (wid == 0 && lane < 24) word_store(rall, (lane >> 2) * 65536 + mycell + (lane & 3) * 64, 0u);
      flag_request(rall, FL_TICK * 65536 + mycell, tick0);
      __builtin_amdgcn_sched_barrier(0);
      KA_STEP(0) KA_STEP(1) KA_STEP(2) KA_HALF0(3)
      C.stats<1>(sx, sxx, sd, ones);
      C.mma<1, 4>(acc);
      STAMP(2)
      const float sgn = half ? -1.f : 1.f;
#pragma unroll
      for (int i = 0; i < 2; i++) {
        const int e = c & 3;
        const float q = e == 0 ? sxx[i][0] : e == 1 ? sxx[i][1] : e == 2 ? sxx[i][2] : sxx[i][3];
        const float s = e == 0 ? sx[i][0] : e == 1 ? sx[i][1] : e == 2 ? sx[i][2] : sx[i][3];
        const float mean = s * (1.0f / D_), var = fmaxf(q * (1.0f / D_) - mean * mean, 0.f);
        const float rstd = sgn / sqrtf(var + LN_EPS);
        if ((c >> 2) == g) { tabA[64 * wP + 32 * wQ + 16 * i + c] = rstd; tabB[64 * wP + 32 * wQ + 16 * i + c] = -mean * rstd; }
        if (g == 0) tabD[64 * wQ + 32 * wP + 16 * i + c] = sd[i][0];
      }
      wait_lgkm0(); bar();
      rsrc_t ro = mk_rsrc(ws + WS_AB + ((size_t)((half * 8 + et) * 32 + 2 * tt + wP) * 16) * 1024, 16384);
      f32x4 ra[4], rb[4];
      float rs[4];
#pragma unroll
      for (int i = 0; i < 4; i++) {
        ra[i] = *(const LAS f32x4*)(tabA + 64 * wP + 16 * (i ^ (2 * wQ)) + 4 * g);
        rb[i] = *(const LAS f32x4*)(tabB + 64 * wP + 16 * (i ^ (2 * wQ)) + 4 * g);
        rs[i] = tabD[64 * wQ + 16 * (i ^ (2 * wP)) + c];
      }
#pragma unroll
      for (int u = 0; u < 2; u++)
#pragma unroll
        for (int j = 0; j < 4; j++) {
          const f32x4 a = ra[2 * u] * acc[2 * u][j] + rb[2 * u] * rs[j], b = ra[2 * u + 1] * acc[2 * u + 1][j] + rb[2 * u + 1] * rs[j];
          store_sc1(ro, ((u ^ wQ) * 8 + 4 * wQ + (j ^ (2 * wP))) * 1024 + lane * 16,
                    (u32x4){cvtpk(a[0], a[1]), cvtpk(a[2], a[3]), cvtpk(b[0], b[1]), cvtpk(b[2], b[3])});
        }
  bar();
      asm volatile("s_waitcnt vmcnt(0)" : "+v"(tick0)::"memory");
      if (lane == 0) word_store(rall, FL_TICK * 65536 + mycell, tick0 + 1);
      STAMP(3)
      bar();
      bar();
    }
    u32x4 hk[2][2];
    {
      Consumer<4, M_TILED, M_ROW> C;
      C.init(sm, wP, wQ, lane);
      f32x4 acc[4][4];
      C.zero(acc);
      bar();
      STAMP(4)
      C.first();
      for (int it = 0; it < 6; it++) {
        C.step4<4, 4>(acc);
      }
      C.step4<4, 2>(acc);
      STAMP(5)
      xch_send(ws + WS_X2, acc, wid, lane);
      __builtin_amdgcn_sched_barrier(0);
      rsrc_t rx = mk_rsrc(ws + WS_X2 + ((size_t)(blockIdx.x ^ 4) * 4 + wid) * 4096, 4096);
      rsrc_t rf = mk_rsrc(ws + WS_FL + ((size_t)(3 * 16 + tt) * 64 + (cell ^ 32)) * 64, 64);
      u32x4 rv[4];
      u32 fv;
      C.step<0, 2, 2>(acc); C.step<1, 2, 2>(acc); C.step<2, 2, 2>(acc);
      if (K_XF == 3) flag_set(ws, FL_X2, tt, cell);
      C.step<3, 2, 2>(acc);
      if (K_XF == 4) flag_set(ws, FL_X2, tt, cell);
      C.step<0, 2, 2>(acc); C.step<1, 2, 2>(acc);
      flag_request(rf, 0, fv);
      __builtin_amdgcn_sched_barrier(0);
      C.step<2, 2, 2>(acc);
      wait_vm<0>();
      flag_wait(rf, 0, fv);
      xch_request(rx, rv, lane);
      __builtin_amdgcn_sched_barrier(0);
      C.load<3, 1, 2>(); C.mma<0, 2>(acc); C.interleave<8, 2>();
      C.mma<1, 2>(acc);
      STAMP(6)
      xch_finish(rv);
            rsrc_t rh = mk_rsrc(ws + WS_H + ((size_t)(tt * 32 + 4 * et + 2 * wP) * 8 + m0) * 1024, 16384);
      rsrc_t rs = mk_rsrc(ws + WS_ST + ((size_t)(tt * 16 + 2 * et + wP) * 128 + 16 * m0) * 8, 256);
#pragma unroll
      for (int jj = 0; jj < 2; jj++) {
        float s = 0.f, q = 0.f;
#pragma unroll
        for (int u = 0; u < 2; u++) {
          const u32x4 o = rv[jj * 2 + u];
          const f32x4 a = acc[2 * u][jj], b = acc[2 * u + 1][jj];
          const int xrow = 32 * wQ + 16 * jj + c;
          const LAS char* xp = sm + (XOFF + wP * 8192 + xrow * 128 + (g & 1) * 8);
          const u32x2 xa = *(const LAS u32x2*)(xp + (((4 * u + (g >> 1)) ^ ((xrow >> 1) & 7)) * 16));
          const u32x2 xb = *(const LAS u32x2*)(xp + (((4 * u + 2 + (g >> 1)) ^ ((xrow >> 1) & 7)) * 16));
          u32x4 h;
          h[0] = cvtpk(a[0] + blo(o[0]) + blo(xa[0]), a[1] + bhi(o[0]) + bhi(xa[0]));
          h[1] = cvtpk(a[2] + blo(o[1]) + blo(xa[1]), a[3] + bhi(o[1]) + bhi(xa[1]));
          h[2] = cvtpk(b[0] + blo(o[2]) + blo(xb[0]), b[1] + bhi(o[2]) + bhi(xb[0]));
          h[3] = cvtpk(b[2] + blo(o[3]) + blo(xb[1]), b[3] + bhi(o[3]) + bhi(xb[1]));
          store_sc1(rh, (u * 8 + jj) * 1024 + lane * 16, h);
          hk[jj][u] = h;
#pragma unroll
          for (int e = 0; e < 4; e++) { const float lo = blo(h[e]), hi = bhi(h[e]); s += lo + hi; q += lo * lo + hi * hi; }
        }
        s = xsum32(xsum16(s)); q = xsum32(xsum16(q));
        if (g == 0) __builtin_amdgcn_raw_buffer_store_b64((u32x2){__float_as_uint(s), __float_as_uint(q)}, rs, (16 * jj + c) * 8, 0, 16);
      }
      flag_set(ws, FL_H, tt, cell);
      STAMP(8)
      bar();
      bar();
    }
    rsrc_t rr = mk_rsrc(ws + WS_R + ((size_t)(tt * 112 + 7 * ft) * 8 + 4 * wQ) * 1024, 7 * 8192);
    float ra[4], rb[4];
    auto act = [&](const f32x4& a, int col, int j, u32& lo, u32& hi) {
      const f32x4 cs = *(const LAS f32x4*)(tabC + col), bi = *(const LAS f32x4*)(tabBias + col);
      f32x4 v = ra[j] * a + (rb[j] * cs + bi);
#pragma unroll
      for (int e = 0; e < 4; e++) { v[e] = v[e] > 0.f ? v[e] : 0.f; v[e] *= v[e]; }
      lo = cvtpk(v[0], v[1]); hi = cvtpk(v[2], v[3]);
    };
    {
      Consumer<4, M_TR256, M_TILED> C;
      C.init(sm, wP, wQ, lane);
      f32x4 acc[4][4];
      C.zero(acc);
      bar();
      STAMP(9)
      C.first();
      for (int it = 0; it < 3; it++) {
        C.step4<4, 4>(acc);
      }
      C.last4<4>(acc);
      STAMP(10)
#pragma unroll
      for (int j = 0; j < 4; j++) { ra[j] = tabA[64 * wQ + 16 * j + c]; rb[j] = tabB[64 * wQ + 16 * j + c]; }
#pragma unroll
      for (int u = 0; u < 2; u++)
#pragma unroll
        for (int j = 0; j < 4; j++) {
          u32 a0, a1, b0, b1_;
          act(acc[2 * u][j], 64 * wP + 32 * u + 4 * g, j, a0, a1);
          act(acc[2 * u + 1][j], 64 * wP + 32 * u + 16 + 4 * g, j, b0, b1_);
          store_sc1(rr, ((2 * wP + u) * 8 + j) * 1024 + lane * 16, (u32x4){a0, a1, b0, b1_});
        }
    }
    {
      Consumer<3, M_TR192, M_TILED> C;
      C.init(sm, wP, wQ, lane);
      f32x4 acc[3][4];
      C.zero(acc);
      bar();
      STAMP(11)
      C.first();
      for (int it = 0; it < 3; it++) {
        C.step4<4, 4>(acc);
        if (it == K_FLAGIT) flag_set(ws, FL_RA, tt, cell);
      }
      C.last4<4>(acc);
      STAMP(12)
#pragma unroll
      for (int j = 0; j < 4; j++) {
        u32 a0, a1, b0, b1_;
        act(acc[0][j], 128 + 64 * wP + 4 * g, j, a0, a1);
        act(acc[1][j], 128 + 64 * wP + 16 + 4 * g, j, b0, b1_);
        store_sc1(rr, ((4 + 2 * wP) * 8 + j) * 1024 + lane * 16, (u32x4){a0, a1, b0, b1_});
        act(acc[2][j], 160 + 16 * wP + 4 * g, j, a0, a1);
        __builtin_amdgcn_raw_buffer_store_b64((u32x2){a0, a1}, rr, (5 * 8 + j) * 1024 + lane * 16 + 8 * wP, 0, 16);
      }
    }
    {
      Consumer<4, M_TR256, M_TILED> C;
      C.init(sm, wP, wQ, lane);
      f32x4 acc[4][4];
      C.zero(acc);
      bar();
      STAMP(13)
      C.first();
      for (int it = 0; it < 5; it++) {
        C.step4<4, 4>(acc);
        if (it == K_FLAGIT) flag_set(ws, FL_RB, tt, cell);
      }
      C.step4<4, 2>(acc);
      STAMP(14)
      xch_send(ws + WS_X4, acc, wid, lane);
      u32x2 br[4];
#pragma unroll
      for (int i = 0; i < 4; i++) br[i] = *(const u32x2*)(b2 + et * 128 + 64 * wP + 16 * i + 4 * g);
      __builtin_amdgcn_sched_barrier(0);
      rsrc_t rx = mk_rsrc(ws + WS_X4 + ((size_t)(blockIdx.x ^ 4) * 4 + wid) * 4096, 4096);
      rsrc_t rf = mk_rsrc(ws + WS_FL + ((size_t)(4 * 16 + tt) * 64 + (cell ^ 32)) * 64, 64);
      u32x4 rv[4];
      u32 fv;
      C.step<0, 2, 2>(acc); C.step<1, 2, 2>(acc); C.step<2, 2, 2>(acc);
      if (K_XF == 3) flag_set(ws, FL_X4, tt, cell);
      C.step<3, 2, 2>(acc);
      if (K_XF == 4) flag_set(ws, FL_X4, tt, cell);
      C.step<0, 2, 2>(acc); C.step<1, 2, 2>(acc);
      flag_request(rf, 0, fv);
      __builtin_amdgcn_sched_barrier(0);
      C.step<2, 2, 2>(acc);
      wait_vm<0>();
      flag_wait(rf, 0, fv);
      xch_request(rx, rv, lane);
      __builtin_amdgcn_sched_barrier(0);
      C.load<3, 1, 2>(); C.mma<0, 2>(acc); C.interleave<8, 2>();
      C.mma<1, 2>(acc);
      xch_finish(rv);

      rsrc_t ro = mk_rsrc(out + (size_t)(tt * 128 + 16 * m0) * D_ + et * 128 + 64 * wP, 31 * D_ * 2 + 128);
#pragma unroll
      for (int jj = 0; jj < 2; jj++)
#pragma unroll
        for (int u = 0; u < 2; u++) {
          const u32x4 o = rv[jj * 2 + u], h = hk[jj][u];
          const f32x4 a = acc[2 * u][jj], b = acc[2 * u + 1][jj];
          const u32x2 ba = br[2 * u], bb = br[2 * u + 1];
          const u32 a0 = cvtpk(a[0] + blo(o[0]) + blo(ba[0]) + blo(h[0]), a[1] + bhi(o[0]) + bhi(ba[0]) + bhi(h[0]));
          const u32 a1 = cvtpk(a[2] + blo(o[1]) + blo(ba[1]) + blo(h[1]), a[3] + bhi(o[1]) + bhi(ba[1]) + bhi(h[1]));
          const u32 b0 = cvtpk(b[0] + blo(o[2]) + blo(bb[0]) + blo(h[2]), b[1] + bhi(o[2]) + bhi(bb[0]) + bhi(h[2]));
          const u32 b1_ = cvtpk(b[2] + blo(o[3]) + blo(bb[1]) + blo(h[3]), b[3] + bhi(o[3]) + bhi(bb[1]) + bhi(h[3]));
          const auto s0 = __builtin_amdgcn_permlane16_swap(a0, b0, false, false);
          const auto s1 = __builtin_amdgcn_permlane16_swap(a1, b1_, false, false);
          __builtin_amdgcn_raw_buffer_store_b128((u32x4){s0[0], s1[0], s0[1], s1[1]}, ro,
                                                 (16 * jj + c) * (D_ * 2) + (32 * u + 16 * (g & 1) + 8 * (g >> 1)) * 2, 0, K_OUTSC1 ? 16 : 0);
        }
      DIAG_END(15)
    }
  }
}

}

extern "C" void kernel_launch(void* const* d_in, const int* in_sizes, int n_in, void* d_out, int out_size, void* d_ws, size_t ws_size,
                              hipStream_t stream) {
  const u16* const* I = (const u16* const*)d_in;
  (void)hipFuncSetAttribute((const void*)fused_kernel, hipFuncAttributeMaxDynamicSharedMemorySize, LDS_BYTES);
  fused_kernel<<<256, 512, LDS_BYTES, stream>>>(I[0], I[1], I[2], I[3], I[4], I[6], I[7], I[8], I[9], (char*)d_ws, (u16*)d_out);
}
```

```cpp
#include <hip/hip_runtime.h>
#include <hip/hip_bf16.h>

typedef unsigned short u16;
typedef unsigned int u32;
typedef unsigned long long u64;
typedef __bf16 bf16x8 __attribute__((ext_vector_type(8)));
typedef __bf16 bf16x2 __attribute__((ext_vector_type(2)));
typedef short s16x4 __attribute__((ext_vector_type(4)));
typedef short s16x8 __attribute__((ext_vector_type(8)));
typedef float f32x2 __attribute__((ext_vector_type(2)));
typedef float f32x4 __attribute__((ext_vector_type(4)));
typedef u32 u32x2 __attribute__((ext_vector_type(2)));
typedef u32 u32x4 __attribute__((ext_vector_type(4)));
typedef __amdgpu_buffer_rsrc_t rsrc_t;
#define LAS __attribute__((address_space(3)))
#define DI __device__ __forceinline__

#define K_FLAGIT 0
#define K_SIDE 1
#define K_PFC0 99
#define K_PFC1 99
#define K_ORD 3
#define K_PRIO 0
#define K_NOCS 0
#define K_CSN 12
#define K_WN 12
#define K_FQ 2
#define K_PFW 8
#define K_PFPHI 1
#define K_XF 4
#define K_SLEEP 1
#define K_OUTSC1 1
namespace {

constexpr int S_ = 2048, D_ = 1024, F_ = 3584;
constexpr float LN_EPS = 1e-5f;

constexpr size_t WS_AB = 0;
constexpr size_t WS_H = 8u << 20;
constexpr size_t WS_R = 12u << 20;
constexpr size_t WS_X2 = 27u << 20;
constexpr size_t WS_X4 = 31u << 20;
constexpr size_t WS_ST = 35u << 20;
constexpr size_t WS_CS = 36u << 20;
constexpr size_t WS_FL = 37u << 20;

#define DIAG_ARG
#define DIAG_BEGIN u64 rtA = 0, rtB = 0; const u64 rt0 = __builtin_amdgcn_s_memrealtime(); if (0 == 0) rtA = rt0; if (0 == 0) rtB = rt0;
#define STAMP(i) { if (i == 0) { __builtin_amdgcn_sched_barrier(0); rtA = __builtin_amdgcn_s_memrealtime(); __builtin_amdgcn_s_waitcnt(0xC07F); __builtin_amdgcn_sched_barrier(0); } if (i == 0) { __builtin_amdgcn_sched_barrier(0); rtB = __builtin_amdgcn_s_memrealtime(); __builtin_amdgcn_s_waitcnt(0xC07F); __builtin_amdgcn_sched_barrier(0); } }
#define PSTAMP(i)
#define PDIAG_END
#define DIAG_END(i) if (blockIdx.x == 37 && wid == 0) { asm volatile("s_waitcnt vmcnt(0)" ::: "memory"); if (i == 0) rtB = __builtin_amdgcn_s_memrealtime(); const u64 lim = rt0 + 20000 + (0 == 0 ? 0 : 20 * (rtB - rtA)); while (__builtin_amdgcn_s_memrealtime() < lim) __builtin_amdgcn_s_sleep(8); }

DI u32 rfl(u32 x) { return __builtin_amdgcn_readfirstlane(x); }
DI u32 cvtpk(float lo, float hi) { f32x2 v = {lo, hi}; return __builtin_bit_cast(u32, __builtin_convertvector(v, bf16x2)); }
DI float blo(u32 v) { return __uint_as_float(v << 16); }
DI float bhi(u32 v) { return __uint_as_float(v & 0xFFFF0000u); }
DI rsrc_t mk_rsrc(const void* p, u32 bytes) { return __builtin_amdgcn_make_buffer_rsrc((void*)p, 0, bytes, 0x00020000); }
DI void bar() { asm volatile("s_barrier" ::: "memory"); }
DI void wait_lgkm0() { __builtin_amdgcn_s_waitcnt(0xC07F); }
template <int N> DI void wait_vm() { asm volatile("s_waitcnt vmcnt(%0)" ::"i"(N) : "memory"); }

template <int N> DI void dma(u32 m0, const u32* v, rsrc_t r, u32 so) {
  if constexpr (N == 4)
    asm volatile("s_mov_b32 m0, %0\n\ts_nop 0\n\t"
                 "buffer_load_dwordx4 %1, %5, %6 offen lds\n\t"
                 "buffer_load_dwordx4 %2, %5, %6 offen offset:1024 lds\n\t"
                 "buffer_load_dwordx4 %3, %5, %6 offen offset:2048 lds\n\t"
                 "buffer_load_dwordx4 %4, %5, %6 offen offset:3072 lds"
                 ::"s"(m0), "v"(v[0]), "v"(v[1]), "v"(v[2]), "v"(v[3]), "s"(r), "s"(so) : "memory");
  else if constexpr (N == 3)
    asm volatile("s_mov_b32 m0, %0\n\ts_nop 0\n\t"
                 "buffer_load_dwordx4 %1, %4, %5 offen lds\n\t"
                 "buffer_load_dwordx4 %2, %4, %5 offen offset:1024 lds\n\t"
                 "buffer_load_dwordx4 %3, %4, %5 offen offset:2048 lds"
                 ::"s"(m0), "v"(v[0]), "v"(v[1]), "v"(v[2]), "s"(r), "s"(so) : "memory");
  else
    asm volatile("s_mov_b32 m0, %0\n\ts_nop 0\n\t"
                 "buffer_load_dwordx4 %1, %3, %4 offen lds\n\t"
                 "buffer_load_dwordx4 %2, %3, %4 offen offset:1024 lds"
                 ::"s"(m0), "v"(v[0]), "v"(v[1]), "s"(r), "s"(so) : "memory");
}
template <int N> DI void dma_sc1(u32 m0, u32 v, rsrc_t r, u32 so) {
  if constexpr (N == 4)
    asm volatile("s_mov_b32 m0, %0\n\ts_nop 0\n\t"
                 "buffer_load_dwordx4 %1, %2, %3 offen sc1 lds\n\t"
                 "buffer_load_dwordx4 %1, %2, %3 offen offset:1024 sc1 lds\n\t"
                 "buffer_load_dwordx4 %1, %2, %3 offen offset:2048 sc1 lds\n\t"
                 "buffer_load_dwordx4 %1, %2, %3 offen offset:3072 sc1 lds"
                 ::"s"(m0), "v"(v), "s"(r), "s"(so) : "memory");
  else
    asm volatile("s_mov_b32 m0, %0\n\ts_nop 0\n\t"
                 "buffer_load_dwordx4 %1, %2, %3 offen sc1 lds\n\t"
                 "buffer_load_dwordx4 %1, %2, %3 offen offset:1024 sc1 lds"
                 ::"s"(m0), "v"(v), "s"(r), "s"(so) : "memory");
}

DI float xsum16(float v) { const auto r = __builtin_amdgcn_permlane16_swap(__float_as_uint(v), __float_as_uint(v), false, false); return __uint_as_float(r[0]) + __uint_as_float(r[1]); }
DI float xsum32(float v) { const auto r = __builtin_amdgcn_permlane32_swap(__float_as_uint(v), __float_as_uint(v), false, false); return __uint_as_float(r[0]) + __uint_as_float(r[1]); }

DI void pull_lines(u32 m0, u32 v, rsrc_t r, u32 so) {
  asm volatile("s_mov_b32 m0, %0\n\ts_nop 0\n\tbuffer_load_dword %1, %2, %3 offen lds" ::"s"(m0), "v"(v), "s"(r), "s"(so) : "memory");
}

DI int kstep(int t, int n, int phi) {
  int k = t;
  if ((t & 3) == phi) { k = t + 4; if (k >= n) k -= n; }
  return k;
}

DI int kstep2(int t, int n, int phi, int side) {
  const int k = kstep(t, n, phi), k2 = k ^ ((K_SIDE ? side : 0) << 2);
  return k2 < n ? k2 : k;
}

constexpr int NST = 4;
constexpr int SLOT = 16384;
constexpr int QOFF = NST * SLOT;
constexpr int TOFF = 2 * NST * SLOT;
constexpr int COFF = TOFF + 4096;
constexpr int XOFF = COFF + 4096;
constexpr int LDS_BYTES = XOFF + 16384;

enum { M_ROW = 0, M_TILED = 1, M_TR192 = 2, M_TR256 = 3 };

template <int PT, int PMODE, int QMODE>
struct Consumer {
  const LAS char* pb[4];
  const LAS char* qb[4];
  bf16x8 fp[2][PT], fq[2][4];

  DI void init(const LAS char* sm, int wP, int wQ, int lane, int prot = 0, int qrot = 0) {
    const int r = lane & 15, g = lane >> 4;
    if constexpr (PMODE == M_ROW) {
      const int a = (64 * wP + r) * 128 + ((g ^ (r >> 1)) * 16);
      pb[0] = sm + (a + prot * 4096); pb[1] = sm + ((a ^ 64) + prot * 4096);
      pb[2] = sm + (a + 4096 - prot * 4096); pb[3] = sm + ((a ^ 64) + 4096 - prot * 4096);
    } else if constexpr (PMODE == M_TILED) {
      pb[0] = sm + (4 * wP * 1024 + lane * 16);
    } else if constexpr (PMODE == M_TR192) {
      const int b = g & 1, a = (4 * g + (r >> 2)) * 192 + (r & 3) * 8;
      pb[0] = sm + (a + (4 * wP + b) * 32);
      pb[1] = sm + (a + (4 * wP + 1 - b) * 32);
      pb[2] = sm + (a + ((2 + wP) ^ b) * 32);
    } else {
      const int k = 4 * g + (r >> 2), a = k * 256 + (r & 3) * 8;
#pragma unroll
      for (int i = 0; i < 4; i++) pb[i] = sm + (a + (((4 * wP + i) ^ (k & 7)) * 32));
    }
    if constexpr (QMODE == M_ROW) {
      const int a = QOFF + (64 * wQ + r) * 128 + ((g ^ (r >> 1)) * 16);
      qb[0] = sm + (a + qrot * 4096); qb[1] = sm + ((a ^ 64) + qrot * 4096);
      qb[2] = sm + (a + 4096 - qrot * 4096); qb[3] = sm + ((a ^ 64) + 4096 - qrot * 4096);
      asm volatile("" : "+v"(qb[0]), "+v"(qb[1]), "+v"(qb[2]), "+v"(qb[3]));
    } else {
      qb[0] = sm + (QOFF + 4 * wQ * 1024 + lane * 16);
      asm volatile("" : "+v"(qb[0]));
    }
  }
  static DI bf16x8 tr2(const LAS char* p, int second) {
    s16x4 a = __builtin_amdgcn_ds_read_tr16_b64_v4i16((LAS s16x4*)p);
    s16x4 b = __builtin_amdgcn_ds_read_tr16_b64_v4i16((LAS s16x4*)(p + second));
    return __builtin_bit_cast(bf16x8, __builtin_shufflevector(a, b, 0, 1, 2, 3, 4, 5, 6, 7));
  }
  template <int S, int KS> DI bf16x8 ldp(int i) {
    if constexpr (PMODE == M_ROW) return *(const LAS bf16x8*)(pb[KS + (i & 2)] + (S * SLOT + (i & 1) * 2048));
    else if constexpr (PMODE == M_TILED) return *(const LAS bf16x8*)(pb[0] + (S * SLOT + KS * 8192 + i * 1024));
    else if constexpr (PMODE == M_TR192) return tr2(pb[i] + (S * SLOT + KS * 32 * 192), 16 * 192);
    else return tr2(pb[i] + (S * SLOT + KS * 32 * 256), 16 * 256);
  }
  template <int S, int KS> DI bf16x8 ldq(int j) {
    if constexpr (QMODE == M_ROW) return *(const LAS bf16x8*)(qb[KS + (j & 2)] + (S * SLOT + (j & 1) * 2048));
    else return *(const LAS bf16x8*)(qb[0] + (S * SLOT + KS * 8192 + j * 1024));
  }
  template <int S, int KS, int QN> DI void load() {
    fp[KS][0] = ldp<S, KS>(0);
#pragma unroll
    for (int j = 0; j < QN; j++) fq[KS][j] = ldq<S, KS>(j);
#pragma unroll
    for (int i = 1; i < PT; i++) fp[KS][i] = ldp<S, KS>(i);
  }
  template <int KS, int QN> DI void mma(f32x4 (&acc)[PT][4]) {
#pragma unroll
    for (int n = 0; n < PT * QN; n++) {
      int i, j;
      if (K_ORD == 0) { i = n / QN; j = n % QN; }
      else if (K_ORD == 1) { i = n / QN; j = (i & 1) ? QN - 1 - n % QN : n % QN; }
      else if (K_ORD == 2) { j = n / PT; i = n % PT; }
      else { j = n / PT; i = (j & 1) ? PT - 1 - n % PT : n % PT; }
      acc[i][j] = __builtin_amdgcn_mfma_f32_16x16x32_bf16(fp[KS][i], fq[KS][j], acc[i][j], 0, 0, 0);
    }
  }
  template <int KS> DI void stats(f32x4 (&sx)[2], f32x4 (&sxx)[2], f32x4 (&sd)[2], bf16x8 ones) {
#pragma unroll
    for (int i = 0; i < 2; i++) {
      sx[i] = __builtin_amdgcn_mfma_f32_16x16x32_bf16(fp[KS][i], ones, sx[i], 0, 0, 0);
      sxx[i] = __builtin_amdgcn_mfma_f32_16x16x32_bf16(fp[KS][i], fp[KS][i], sxx[i], 0, 0, 0);
      sd[i] = __builtin_amdgcn_mfma_f32_16x16x32_bf16(ones, fq[KS][i], sd[i], 0, 0, 0);
    }
  }
  template <int NM, int QN> static DI void interleave() {
    constexpr int NR = PT * (PMODE >= M_TR192 ? 2 : 1) + QN;
    if constexpr (NR <= NM) {
#pragma unroll
      for (int i = 0; i < NR; i++) { __builtin_amdgcn_sched_group_barrier(0x8, 1, 0); __builtin_amdgcn_sched_group_barrier(0x100, 1, 0); }
      __builtin_amdgcn_sched_group_barrier(0x8, NM - NR, 0);
    } else {
#pragma unroll
      for (int i = 0; i < NR / 2; i++) { __builtin_amdgcn_sched_group_barrier(0x8, 1, 0); __builtin_amdgcn_sched_group_barrier(0x100, 2, 0); }
      __builtin_amdgcn_sched_group_barrier(0x8, NM - NR / 2, 0);
    }
    __builtin_amdgcn_sched_barrier(0);
  }
  template <int S, int QC, int QN> DI void step(f32x4 (&acc)[PT][4]) {
    load<S, 1, QC>(); mma<0, QC>(acc); interleave<PT * QC, QC>();
    wait_lgkm0(); bar();
    __builtin_amdgcn_sched_barrier(0);
    load<(S + 1) % NST, 0, QN>(); mma<1, QC>(acc); interleave<PT * QC, QN>();
  }
  template <int QC, int QN> DI void step4(f32x4 (&acc)[PT][4]) {
    step<0, QC, QC>(acc); step<1, QC, QC>(acc); step<2, QC, QC>(acc); step<3, QC, QN>(acc);
  }
  template <int QC> DI void last4(f32x4 (&acc)[PT][4]) {
    step<0, QC, QC>(acc); step<1, QC, QC>(acc); step<2, QC, QC>(acc);
    load<3, 1, QC>(); mma<0, QC>(acc); interleave<PT * QC, QC>();
    mma<1, QC>(acc);
  }
  static DI void zero(f32x4 (&acc)[PT][4]) {
#pragma unroll
    for (int i = 0; i < PT; i++)
#pragma unroll
      for (int j = 0; j < 4; j++) { acc[i][j] = (f32x4){0.f, 0.f, 0.f, 0.f}; asm volatile("" : "+v"(acc[i][j])); }
  }
  DI void first() { load<0, 0, 4>(); }
};

DI u32 row_src(int imgrow, int srcrow, int lane, int ldbytes) {
  return srcrow * ldbytes + (((lane & 7) ^ ((imgrow >> 1) & 7)) * 16);
}
DI void store_sc1(rsrc_t r, u32 off, u32x4 v) { __builtin_amdgcn_raw_buffer_store_b128(v, r, off, 0, 16); }

DI void xch_send(char* x, const f32x4 (&acc)[4][4], int wid, int lane) {
  rsrc_t r = mk_rsrc(x + ((size_t)blockIdx.x * 4 + wid) * 4096, 4096);
#pragma unroll
  for (int jj = 0; jj < 2; jj++)
#pragma unroll
    for (int u = 0; u < 2; u++) {
      const f32x4 a = acc[2 * u][2 + jj], b = acc[2 * u + 1][2 + jj];
      u32x4 v = {cvtpk(a[0], a[1]), cvtpk(a[2], a[3]), cvtpk(b[0], b[1]), cvtpk(b[2], b[3])};
      store_sc1(r, lane * 16 + (jj * 2 + u) * 1024, v);
    }
}
DI void xch_request(rsrc_t r, u32x4 (&v)[4], int lane) {
  asm volatile("buffer_load_dwordx4 %0, %4, %5, 0 offen sc1\n\t"
               "buffer_load_dwordx4 %1, %4, %5, 0 offen offset:1024 sc1\n\t"
               "buffer_load_dwordx4 %2, %4, %5, 0 offen offset:2048 sc1\n\t"
               "buffer_load_dwordx4 %3, %4, %5, 0 offen offset:3072 sc1"
               : "=&v"(v[0]), "=&v"(v[1]), "=&v"(v[2]), "=&v"(v[3]) : "v"(lane * 16), "s"(r) : "memory");
}
DI void xch_finish(u32x4 (&v)[4]) { asm volatile("s_waitcnt vmcnt(0)" : "+v"(v[0]), "+v"(v[1]), "+v"(v[2]), "+v"(v[3])::"memory"); }

enum { FL_H = 0, FL_RA = 1, FL_RB = 2, FL_X2 = 3, FL_X4 = 4, FL_ACK = 5, FL_TICK = 6 };
DI void flag_set(char* ws, int which, int tt, int cell) {
  wait_vm<0>();
  if ((threadIdx.x & 63) == 0)
    __hip_atomic_store((u32*)(ws + WS_FL + ((size_t)(which * 16 + tt) * 64 + cell) * 64), 1u, __ATOMIC_RELAXED, __HIP_MEMORY_SCOPE_AGENT);
}
DI void flag_request(rsrc_t r, u32 off, u32& v) {
  asm volatile("buffer_load_dword %0, %1, %2, 0 offen sc1" : "=&v"(v) : "v"(off), "s"(r) : "memory");
}
DI void flag_wait(rsrc_t r, u32 off, u32& v) {
  asm volatile("" : "+v"(v)::"memory");
#pragma unroll 1
  for (int spin = 0; spin < (1 << 20); spin++) {
    if (__builtin_amdgcn_ballot_w64(v != 1u) == 0) break;
    __builtin_amdgcn_s_sleep(K_SLEEP);
    asm volatile("buffer_load_dword %0, %1, %2, 0 offen sc1\n\ts_waitcnt vmcnt(0)" : "=&v"(v) : "v"(off), "s"(r) : "memory");
  }
}

DI void word_store(rsrc_t r, u32 off, u32 v) { __builtin_amdgcn_raw_buffer_store_b32(v, r, off, 0, 16); }
template <class Done>
DI void watch(rsrc_t r, u32 off, u32& v, Done done, u32 ownoff, u32 ownval, u32& bump, bool& ticked, u64 ownmask) {
  asm volatile("" : "+v"(v)::"memory");
#pragma unroll 1
  for (int spin = 0; spin < (1 << 20); spin++) {
    const u64 miss = __builtin_amdgcn_ballot_w64(!done(v));
    if (miss == 0) break;
    ticked = ticked || (miss & ownmask) == 0;
    if (ticked && (spin & 63) == 63) {
      bump++;
      if ((ownmask >> (threadIdx.x & 63)) & 1) word_store(r, ownoff, ownval + bump);
    }
    __builtin_amdgcn_s_sleep(K_SLEEP);
    asm volatile("buffer_load_dword %0, %1, %2, 0 offen sc1\n\ts_waitcnt vmcnt(0)" : "=&v"(v) : "v"(off), "s"(r) : "memory");
  }
}

__global__ __launch_bounds__(512, 2) void fused_kernel(const u16* __restrict__ x, const u16* __restrict__ cre, const u16* __restrict__ cim,
                                                    const u16* __restrict__ dre, const u16* __restrict__ dim,
                                                    const u16* __restrict__ w1, const u16* __restrict__ b1, const u16* __restrict__ w2,
                                                    const u16* __restrict__ b2, char* __restrict__ ws, u16* __restrict__ out DIAG_ARG) {
  extern __shared__ __attribute__((aligned(16))) char smem[];
  DIAG_BEGIN
  asm volatile("" ::"s"(x), "s"(cre), "s"(cim), "s"(dre), "s"(dim), "s"(w1), "s"(b1), "s"(w2), "s"(b2), "s"(ws), "s"(out));
  const int lane = threadIdx.x & 63, wid = rfl(threadIdx.x >> 6);
  const int xcd = blockIdx.x & 7, loc = blockIdx.x >> 3, li = loc >> 2, lj = loc & 3;
  const int half = xcd >> 2, et = ((xcd >> 1) & 1) * 4 + lj, tt = (xcd & 1) * 8 + li, ft = (xcd >> 1) * 4 + lj;
  LAS char* sm = (LAS char*)smem;
  LAS float* tabA = (LAS float*)(sm + TOFF);
  LAS float* tabB = tabA + 128;
  LAS float* tabC = tabA + 256;
  LAS float* tabBias = tabA + 512;
  if (__builtin_expect(wid >= 4, 1)) {
    const int p = wid - 4, phi = (li + lj) & 3;
    const u32 lds0 = (u32)(size_t)smem, ldsb = lds0 + p * 4096, vlin = p * 4096 + lane * 16;
    rsrc_t rall = mk_rsrc(ws + WS_FL, 7 * 65536);
    const u32 colcell = ((lane >> 2) * 64 + ft * 4 + (lane & 3)) * 64, rowcell = (tt * 64 + (lane & 15) * 4) * 64;
    const u64 ownmask = 15ull << (4 * tt);
    u32 col0, row0, fv, bump = 1;
    bool ticked = false;
    {
      rsrc_t rp = mk_rsrc((const char*)x + (size_t)tt * 128 * D_ * 2 - 4096, 128 * D_ * 2 + 4096);
      rsrc_t rq = mk_rsrc((half ? dim : dre) + (size_t)et * 128 * D_, 128 * D_ * 2);
      u32 vp[4], vq[4], vx[2];
#pragma unroll
      for (int i = 0; i < 4; i++) {
        const int row = 32 * p + 8 * i + (lane >> 3), u = (row >> 4) & 1, ii = row & 15;
        vp[i] = row_src(row, 32 * p + 8 * (ii >> 2) + 4 * u + (ii & 3), lane, D_ * 2) + 4096 - i * 1024;
        vq[i] = row_src(row, row, lane, D_ * 2) - i * 1024;
      }
#pragma unroll
      for (int i = 0; i < 2; i++) {
        const int row = 16 * p + 8 * i + (lane >> 3);
        vx[i] = row_src(row, 64 * (row >> 5) + 32 * half + (row & 31), lane, D_ * 2) + 4096 - i * 1024;
      }
      auto issue = [&](int g) {
        const int k = kstep2(g, 16, phi, (xcd >> 1) ^ (xcd & 1));
        const u32 m = ldsb + (g & 3) * SLOT;
        dma<4>(m, vp, rp, k * 128);
        dma<4>(m + QOFF, vq, rq, k * 128);
        if ((k >> 1) == et) dma<2>(lds0 + XOFF + (k & 1) * 8192 + p * 2048, vx, rp, k * 128);
      };
      issue(0); issue(1); issue(2);
      wait_vm<16>(); bar();
#pragma unroll 1
      for (int g = 3; g < 6; g++) { issue(g); wait_vm<16>(); bar(); }
      flag_request(rall, FL_TICK * 65536 + colcell, col0);
      flag_request(rall, FL_TICK * 65536 + rowcell, row0);
#pragma unroll 1
      for (int g = 6; g < 16; g++) { issue(g); wait_vm<16>(); bar(); }
      asm volatile("" : "+v"(col0), "+v"(row0)::"memory");
    }
    {
      rsrc_t rp = mk_rsrc(ws + WS_AB + (size_t)et * 32 * 16384, 9 * 32 * 16384);
      rsrc_t rqo = mk_rsrc((const char*)(half ? cim : cre) + (size_t)tt * 128 * S_ * 2 - 4096, 128 * S_ * 2 + 4096);
      rsrc_t rq0 = mk_rsrc((const char*)cre + (size_t)tt * 128 * S_ * 2 - 4096, 128 * S_ * 2 + 4096);
      rsrc_t rq1 = mk_rsrc((const char*)cim + (size_t)tt * 128 * S_ * 2 - 4096, 128 * S_ * 2 + 4096);
      u32 vq[4], vh[2];
#pragma unroll
      for (int i = 0; i < 4; i++) {
        const int row = 32 * p + 8 * i + (lane >> 3);
        vq[i] = row_src(row, row ^ (half * 32), lane, S_ * 2) + 4096 - i * 1024;
      }
#pragma unroll
      for (int i = 0; i < 2; i++) {
        const int row = 64 * (p >> 1) + 16 * (p & 1) + 8 * i + (lane >> 3);
        vh[i] = row_src(row, row ^ (half * 32), lane, S_ * 2) + 4096 - i * 1024;
      }
      const u32 ldsh = lds0 + QOFF + (p >> 1) * 8192 + (p & 1) * 2048;
      auto fp = [&](int g) { dma_sc1<4>(ldsb + (g & 3) * SLOT, vlin, rp, (half * 256 + kstep2(g, 28, phi, (xcd >> 1) & 1)) * 16384); };
      auto fq = [&](int g) { dma<4>(ldsb + QOFF + (g & 3) * SLOT, vq, rqo, kstep2(g, 28, phi, (xcd >> 1) & 1) * 128); };
      auto halfs = [&](int g) {
        const int k = kstep2(g, 8, phi, (xcd >> 1) & 1), term = k >> 2, ks = 28 + (k & 3);
        dma_sc1<4>(ldsb + (g & 3) * SLOT, vlin, rp, (term * 256 + ks) * 16384);
        if (term) dma<2>(ldsh + (g & 3) * SLOT, vh, rq1, ks * 128);
        else dma<2>(ldsh + (g & 3) * SLOT, vh, rq0, ks * 128);
      };
      auto full = [&](int g) { fp(g); fq(g); };
      auto run = [&](int g0, int g1) {
#pragma unroll 1
        for (int g = g0; g < g1; g++) { full(g); wait_vm<16>(); bar(); }
      };
      if (K_FQ >= 1) fq(0);
      wait_vm<K_FQ >= 1 ? 12 : 8>(); bar();
      if (K_FQ >= 2) fq(1);
      wait_vm<K_FQ >= 2 ? 8 : K_FQ * 4>(); bar();
      bar();
      bar();
      PSTAMP(1)
      if (K_FQ < 1) fq(0);
      if (K_FQ < 2) fq(1);
      fq(2);
      bar();
      if (p == 0) {
        fv = col0;
        watch(rall, FL_TICK * 65536 + colcell, fv, [&](u32 w) { return w != col0; }, FL_TICK * 65536 + colcell, col0, bump, ticked, ownmask);
        ticked = true;
      }
      bar();
      PSTAMP(2)
      fp(0); fp(1); fp(2);
      wait_vm<8>();
      PSTAMP(9)
      bar();
      full(3); wait_vm<12>(); bar();
      const int g0 = 4 + loc % K_CSN;
      run(4, g0);
      {
        rsrc_t rw = mk_rsrc((const char*)w1 + (size_t)(tt * 64 + p * 16) * F_ * 2 + ft * 448, 15 * F_ * 2 + 448);
        u32x4 v[8];
        const int l28 = lane < 28 ? lane : lane < 56 ? lane - 28 : 27;
        const u32 vo = (lane >= 28 ? F_ * 2 : 0) + l28 * 16;
        full(g0);
#pragma unroll
        for (int i = 0; i < 8; i++)
          asm volatile("buffer_load_dwordx4 %0, %1, %2, %3 offen" : "=&v"(v[i]) : "v"(vo), "s"(rw), "s"(i * 2 * F_ * 2) : "memory");
        wait_vm<24>(); bar();
        full(g0 + 1); wait_vm<24>(); bar();
        full(g0 + 2); wait_vm<24>(); bar();
        full(g0 + 3);
        asm volatile("s_waitcnt vmcnt(16)" : "+v"(v[0]), "+v"(v[1]), "+v"(v[2]), "+v"(v[3]), "+v"(v[4]), "+v"(v[5]), "+v"(v[6]), "+v"(v[7])::"memory");
        bar();
        full(g0 + 4);
        float s[8] = {0, 0, 0, 0, 0, 0, 0, 0};
#pragma unroll
        for (int i = 0; i < 8; i++)
#pragma unroll
          for (int e = 0; e < 4; e++) { s[2 * e] += blo(v[i][e]); s[2 * e + 1] += bhi(v[i][e]); }
#pragma unroll
        for (int e = 0; e < 8; e++) s[e] += __shfl(s[e], lane + 28);
        if (lane < 28) {
          LAS float* d = (LAS float*)(sm + COFF) + p * 224 + lane * 8;
          *(LAS f32x4*)d = (f32x4){s[0], s[1], s[2], s[3]};
          *(LAS f32x4*)(d + 4) = (f32x4){s[4], s[5], s[6], s[7]};
        }
        wait_lgkm0();
        wait_vm<16>(); bar();
        if (p == 0 && lane < 56) {
          const LAS f32x4* d = (const LAS f32x4*)(sm + COFF) + lane;
          const f32x4 t = (d[0] + d[56]) + (d[112] + d[168]);
          store_sc1(mk_rsrc(ws + WS_CS + (size_t)tt * F_ * 4 + ft * 896, 896), lane * 16, __builtin_bit_cast(u32x4, t));
        }
      }
      run(g0 + 5, 20);
      flag_request(rall, FL_TICK * 65536 + rowcell, fv);
      full(20); wait_vm<16>(); bar();
      full(21); wait_vm<16>();
      asm volatile("" : "+v"(fv)::"memory");
      if (p == 0) {
        watch(rall, FL_TICK * 65536 + rowcell, fv, [&](u32 w) { return w != row0; }, FL_TICK * 65536 + colcell, col0, bump, ticked, ownmask);
        if (lane == 0) word_store(rall, FL_ACK * 65536 + (tt * 64 + ft * 4) * 64, 1u);
      }
      bar();
      run(22, 26);
      const u32 ackcell = FL_ACK * 65536 + ((lane & 16) ? rowcell : ((lane & 15) * 64 + ft * 4) * 64);
      flag_request(rall, ackcell, fv);
      full(26); wait_vm<16>(); bar();
      full(27); wait_vm<16>();
      PSTAMP(10)
      asm volatile("" : "+v"(fv)::"memory");
      if (p == 0) watch(rall, ackcell, fv, [&](u32 w) { return w == 1u; }, FL_TICK * 65536 + colcell, col0, bump, ticked, ownmask);
      PSTAMP(11)
      bar();
      halfs(0); wait_vm<14>(); bar();
#pragma unroll 1
      for (int g = 1; g < 8; g++) { halfs(g); wait_vm<12>(); bar(); }
    }
    rsrc_t rfl_ = mk_rsrc(ws + WS_FL + (size_t)tt * 4096, 3 * 16 * 4096);
    {
      rsrc_t rp = mk_rsrc((const char*)w1 + ft * 448, D_ * F_ * 2 - ft * 448);
      rsrc_t rq = mk_rsrc(ws + WS_H + (size_t)tt * 32 * 8192, 32 * 8192);
      u32 va[4], vb[3];
#pragma unroll
      for (int i = 0; i < 4; i++) {
        const int k = 16 * p + 4 * i + (lane >> 4);
        va[i] = k * (F_ * 2) + (((lane & 15) ^ ((k & 7) << 1)) * 16) - i * 1024;
      }
#pragma unroll
      for (int i = 0; i < 3; i++) {
        const int o = (p * 3 + i) * 64 + lane, k = o / 12, ch = o % 12;
        vb[i] = k * (F_ * 2) + 256 + ((ch ^ (((k >> 2) & 1) << 1)) * 16) - i * 1024;
      }
      rsrc_t rw = mk_rsrc(w2 + (size_t)(half * 1792 + tt * 112 + 28 * p) * D_ + et * 128, 27 * D_ * 2 + 256);
      const u32 vw = (lane >> 1) * (D_ * 2) + (lane & 1) * 128;
      auto pa = [&](int g) {
        if (g == 4 + loc % K_WN) pull_lines(lds0 + COFF + 3584, vw, rw, 0);
        dma<4>(ldsb + (g & 3) * SLOT, va, rp, kstep(g, 16, phi) * (64 * F_ * 2));
      };
      auto pbb = [&](int g) { dma<3>(lds0 + p * 3072 + (g & 3) * SLOT, vb, rp, kstep(g, 16, phi) * (64 * F_ * 2)); };
      auto q = [&](int g) { dma_sc1<4>(ldsb + QOFF + (g & 3) * SLOT, vlin, rq, kstep(g, 16, phi) * 16384); };
      wait_vm<6>(); bar();
      wait_vm<0>(); bar();
      bar();
      pa(0); pa(1); pa(2);
      const int pt = threadIdx.x - 256;
      rsrc_t rs = mk_rsrc(ws + WS_ST + (size_t)tt * 16384, 16384);
      rsrc_t rc = mk_rsrc(ws + WS_CS + ft * 896, 15 * F_ * 4 + 896);
      u32x2 sv[8];
      u32 cv[16], bv;
#pragma unroll
      for (int i = 0; i < 16; i++)
        asm volatile("buffer_load_dword %0, %1, %2, %3 offen sc1" : "=&v"(cv[i]) : "v"(min(pt, 223) * 4), "s"(rc), "s"(i * F_ * 4) : "memory");
      asm volatile("buffer_load_ushort %0, %1, %2, 0 offen" : "=&v"(bv) : "v"(min(pt, 223) * 2), "s"(mk_rsrc(b1 + ft * 224, 448)) : "memory");
      if (p == 0) { fv = 0; flag_wait(rfl_, lane * 64, fv); }
      bar();
      PSTAMP(3)
      q(0); q(1); q(2);
      const u32 so = (pt & 1) * 8192 + (pt >> 1) * 8;
#pragma unroll
      for (int i = 0; i < 8; i++)
        asm volatile("buffer_load_dwordx2 %0, %1, %2, %3 offen sc1" : "=&v"(sv[i]) : "v"(so), "s"(rs), "s"(i * 1024) : "memory");
      wait_vm<16>();
      PSTAMP(4)
      bar();
      pa(3); q(3); wait_vm<20>(); bar();
      pa(4); q(4); wait_vm<24>(); bar();
      pa(5); q(5); wait_vm<16>();
      asm volatile("" : "+v"(sv[0]), "+v"(sv[1]), "+v"(sv[2]), "+v"(sv[3]), "+v"(sv[4]), "+v"(sv[5]), "+v"(sv[6]), "+v"(sv[7]), "+v"(bv)::"memory");
      asm volatile("" : "+v"(cv[0]), "+v"(cv[1]), "+v"(cv[2]), "+v"(cv[3]), "+v"(cv[4]), "+v"(cv[5]), "+v"(cv[6]), "+v"(cv[7]), "+v"(cv[8]),
                   "+v"(cv[9]), "+v"(cv[10]), "+v"(cv[11]), "+v"(cv[12]), "+v"(cv[13]), "+v"(cv[14]), "+v"(cv[15])::"memory");
      bar();
      {
        float s = 0.f;
#pragma unroll
        for (int i = 0; i < 16; i++) s += __uint_as_float(cv[i]);
        if (pt < 224) { tabC[pt] = s; tabBias[pt] = __uint_as_float(bv << 16); }
      }
      {
        float s = 0.f, qq = 0.f;
#pragma unroll
        for (int i = 0; i < 8; i++) { s += __uint_as_float(sv[i][0]); qq += __uint_as_float(sv[i][1]); }
        s += __shfl_xor(s, 1); qq += __shfl_xor(qq, 1);
        const float mean = s * (1.0f / D_), var = fmaxf(qq * (1.0f / D_) - mean * mean, 0.f);
        const float rstd = 1.0f / sqrtf(var + LN_EPS);
        if ((pt & 1) == 0) { tabA[pt >> 1] = rstd; tabB[pt >> 1] = -mean * rstd; }
        wait_lgkm0();
      }
#pragma unroll 1
      for (int g = 6; g < 16; g++) { pa(g); q(g); wait_vm<16>(); bar(); }
      pbb(0); q(0); wait_vm<15>(); bar();
#pragma unroll 1
      for (int g = 1; g < 14; g++) { pbb(g); q(g); wait_vm<14>(); bar(); }
      flag_request(rfl_, 65536 + lane * 64, fv);
      pbb(14); q(14); wait_vm<14>(); bar();
      pbb(15); q(15); wait_vm<14>();
      PSTAMP(5)
      flag_wait(rfl_, 65536 + lane * 64, fv);
      PSTAMP(6)
      bar();
    }
    {
      rsrc_t rp = mk_rsrc(w2 + et * 128, F_ * D_ * 2 - et * 256);
      rsrc_t rq = mk_rsrc(ws + WS_R + (size_t)tt * 112 * 8192, 112 * 8192);
      u32 vp[4];
#pragma unroll
      for (int i = 0; i < 4; i++) {
        const int k = 16 * (p & 1) + 4 * i + (lane >> 4);
        vp[i] = k * (D_ * 2) + (((lane & 15) ^ ((k & 7) << 1)) * 16) - i * 1024;
      }
      const u32 vq0 = (4 * (p & 1) + 2 * half) * 1024 + lane * 16, vq1 = (4 * (p & 1) + 2 - 2 * half) * 1024 + lane * 16;
      const u32 ldsh = lds0 + QOFF + p * 4096;
      auto bblock = [&](int hf, int n) { return 7 * (8 * hf + n / 3) + 4 + n % 3; };
      auto full = [&](int g) {
        const int k = g < 16 ? kstep2(g, 16, phi, xcd & 1) : 16 + kstep2(g - 16, 8, phi, xcd & 1), n = 2 * k + (p >> 1);
        const int blk = k < 16 ? 7 * (8 * half + (n >> 2)) + (n & 3) : bblock(half, n - 32);
        const u32 m = ldsb + (g & 3) * SLOT;
        dma<4>(m, vp, rp, blk * (32 * D_ * 2));
        dma_sc1<2>(m + QOFF, vq0, rq, blk * 8192);
        dma_sc1<2>(m + QOFF + 2048, vq1, rq, blk * 8192);
      };
      auto halfs = [&](int g) {
        const int k = kstep2(g, 8, phi, xcd & 1), blk = bblock(k >> 2, 16 + 2 * (k & 3) + (p >> 1));
        dma<4>(ldsb + (g & 3) * SLOT, vp, rp, blk * (32 * D_ * 2));
        dma_sc1<2>(ldsh + (g & 3) * SLOT, vq0, rq, blk * 8192);
      };
      full(0); wait_vm<15>(); bar();
#pragma unroll 1
      for (int g = 1; g < 14; g++) { full(g); wait_vm<16>(); bar(); }
      flag_request(rfl_, 131072 + lane * 64, fv);
      full(14); wait_vm<16>(); bar();
      full(15); wait_vm<16>();
      PSTAMP(7)
      flag_wait(rfl_, 131072 + lane * 64, fv);
      PSTAMP(8)
      bar();
#pragma unroll 1
      for (int g = 16; g < 24; g++) { full(g); wait_vm<16>(); bar(); }
      halfs(0); wait_vm<14>(); bar();
#pragma unroll 1
      for (int g = 1; g < 8; g++) { halfs(g); wait_vm<12>(); bar(); }
      wait_vm<6>(); bar();
      wait_vm<0>(); bar();
      PDIAG_END
    }
  } else {
    if (K_PRIO) __builtin_amdgcn_s_setprio(K_PRIO);
    const int wP = wid >> 1, wQ = wid & 1, c = lane & 15, g = lane >> 4;
    const int m0 = 4 * wQ + 2 * half, cell = ft * 4 + wid;
    rsrc_t rall = mk_rsrc(ws + WS_FL, 7 * 65536);
    const u32 mycell = (tt * 64 + cell) * 64;
    u32 tick0;
    {
      LAS float* tabD = tabA + 256;
      Consumer<4, M_ROW, M_ROW> C;
      C.init(sm, wP, wQ, lane, wQ, wP);
      f32x4 acc[4][4], sx[2], sxx[2], sd[2];
      C.zero(acc);
#pragma unroll
      for (int i = 0; i < 2; i++) sx[i] = sxx[i] = sd[i] = (f32x4){0.f, 0.f, 0.f, 0.f};
      const u32x4 ones4 = {0x3F803F80u, 0x3F803F80u, 0x3F803F80u, 0x3F803F80u};
      bf16x8 ones = __builtin_bit_cast(bf16x8, ones4);
      asm volatile("" : "+v"(ones));
      bar();
      STAMP(1)
      C.first();
#define KA_HALF0(S) C.load<S, 1, 4>(); C.mma<0, 4>(acc); C.stats<0>(sx, sxx, sd, ones); C.interleave<22, 4>();
#define KA_STEP(S) KA_HALF0(S) wait_lgkm0(); bar(); __builtin_amdgcn_sched_barrier(0); \
      C.load<(S + 1) % NST, 0, 4>(); C.mma<1, 4>(acc); C.stats<1>(sx, sxx, sd, ones); C.interleave<22, 4>();
      for (int it = 0; it < 3; it++) {
        KA_STEP(0) KA_STEP(1) KA_STEP(2) KA_STEP(3)
      }
      if (wid == 0 && lane < 24) word_store(rall, (lane >> 2) * 65536 + mycell + (lane & 3) * 64, 0u);
      flag_request(rall, FL_TICK * 65536 + mycell, tick0);
      __builtin_amdgcn_sched_barrier(0);
      KA_STEP(0) KA_STEP(1) KA_STEP(2) KA_HALF0(3)
      C.stats<1>(sx, sxx, sd, ones);
      C.mma<1, 4>(acc);
      STAMP(2)
      const float sgn = half ? -1.f : 1.f;
#pragma unroll
      for (int i = 0; i < 2; i++) {
        const int e = c & 3;
        const float q = e == 0 ? sxx[i][0] : e == 1 ? sxx[i][1] : e == 2 ? sxx[i][2] : sxx[i][3];
        const float s = e == 0 ? sx[i][0] : e == 1 ? sx[i][1] : e == 2 ? sx[i][2] : sx[i][3];
        const float mean = s * (1.0f / D_), var = fmaxf(q * (1.0f / D_) - mean * mean, 0.f);
        const float rstd = sgn / sqrtf(var + LN_EPS);
        if ((c >> 2) == g) { tabA[64 * wP + 32 * wQ + 16 * i + c] = rstd; tabB[64 * wP + 32 * wQ + 16 * i + c] = -mean * rstd; }
        if (g == 0) tabD[64 * wQ + 32 * wP + 16 * i + c] = sd[i][0];
      }
      wait_lgkm0(); bar();
      rsrc_t ro = mk_rsrc(ws + WS_AB + ((size_t)((half * 8 + et) * 32 + 2 * tt + wP) * 16) * 1024, 16384);
      f32x4 ra[4], rb[4];
      float rs[4];
#pragma unroll
      for (int i = 0; i < 4; i++) {
        ra[i] = *(const LAS f32x4*)(tabA + 64 * wP + 16 * (i ^ (2 * wQ)) + 4 * g);
        rb[i] = *(const LAS f32x4*)(tabB + 64 * wP + 16 * (i ^ (2 * wQ)) + 4 * g);
        rs[i] = tabD[64 * wQ + 16 * (i ^ (2 * wP)) + c];
      }
#pragma unroll
      for (int u = 0; u < 2; u++)
#pragma unroll
        for (int j = 0; j < 4; j++) {
          const f32x4 a = ra[2 * u] * acc[2 * u][j] + rb[2 * u] * rs[j], b = ra[2 * u + 1] * acc[2 * u + 1][j] + rb[2 * u + 1] * rs[j];
          store_sc1(ro, ((u ^ wQ) * 8 + 4 * wQ + (j ^ (2 * wP))) * 1024 + lane * 16,
                    (u32x4){cvtpk(a[0], a[1]), cvtpk(a[2], a[3]), cvtpk(b[0], b[1]), cvtpk(b[2], b[3])});
        }
  bar();
      asm volatile("s_waitcnt vmcnt(0)" : "+v"(tick0)::"memory");
      if (lane == 0) word_store(rall, FL_TICK * 65536 + mycell, tick0 + 1);
      STAMP(3)
      bar();
      bar();
    }
    u32x4 hk[2][2];
    {
      Consumer<4, M_TILED, M_ROW> C;
      C.init(sm, wP, wQ, lane);
      f32x4 acc[4][4];
      C.zero(acc);
      bar();
      STAMP(4)
      C.first();
      for (int it = 0; it < 6; it++) {
        C.step4<4, 4>(acc);
      }
      C.step4<4, 2>(acc);
      STAMP(5)
      xch_send(ws + WS_X2, acc, wid, lane);
      __builtin_amdgcn_sched_barrier(0);
      rsrc_t rx = mk_rsrc(ws + WS_X2 + ((size_t)(blockIdx.x ^ 4) * 4 + wid) * 4096, 4096);
      rsrc_t rf = mk_rsrc(ws + WS_FL + ((size_t)(3 * 16 + tt) * 64 + (cell ^ 32)) * 64, 64);
      u32x4 rv[4];
      u32 fv;
      C.step<0, 2, 2>(acc); C.step<1, 2, 2>(acc); C.step<2, 2, 2>(acc);
      if (K_XF == 3) flag_set(ws, FL_X2, tt, cell);
      C.step<3, 2, 2>(acc);
      if (K_XF == 4) flag_set(ws, FL_X2, tt, cell);
      C.step<0, 2, 2>(acc); C.step<1, 2, 2>(acc);
      flag_request(rf, 0, fv);
      __builtin_amdgcn_sched_barrier(0);
      C.step<2, 2, 2>(acc);
      wait_vm<0>();
      flag_wait(rf, 0, fv);
      xch_request(rx, rv, lane);
      __builtin_amdgcn_sched_barrier(0);
      C.load<3, 1, 2>(); C.mma<0, 2>(acc); C.interleave<8, 2>();
      C.mma<1, 2>(acc);
      STAMP(6)
      xch_finish(rv);
            rsrc_t rh = mk_rsrc(ws + WS_H + ((size_t)(tt * 32 + 4 * et + 2 * wP) * 8 + m0) * 1024, 16384);
      rsrc_t rs = mk_rsrc(ws + WS_ST + ((size_t)(tt * 16 + 2 * et + wP) * 128 + 16 * m0) * 8, 256);
#pragma unroll
      for (int jj = 0; jj < 2; jj++) {
        float s = 0.f, q = 0.f;
#pragma unroll
        for (int u = 0; u < 2; u++) {
          const u32x4 o = rv[jj * 2 + u];
          const f32x4 a = acc[2 * u][jj], b = acc[2 * u + 1][jj];
          const int xrow = 32 * wQ + 16 * jj + c;
          const LAS char* xp = sm + (XOFF + wP * 8192 + xrow * 128 + (g & 1) * 8);
          const u32x2 xa = *(const LAS u32x2*)(xp + (((4 * u + (g >> 1)) ^ ((xrow >> 1) & 7)) * 16));
          const u32x2 xb = *(const LAS u32x2*)(xp + (((4 * u + 2 + (g >> 1)) ^ ((xrow >> 1) & 7)) * 16));
          u32x4 h;
          h[0] = cvtpk(a[0] + blo(o[0]) + blo(xa[0]), a[1] + bhi(o[0]) + bhi(xa[0]));
          h[1] = cvtpk(a[2] + blo(o[1]) + blo(xa[1]), a[3] + bhi(o[1]) + bhi(xa[1]));
          h[2] = cvtpk(b[0] + blo(o[2]) + blo(xb[0]), b[1] + bhi(o[2]) + bhi(xb[0]));
          h[3] = cvtpk(b[2] + blo(o[3]) + blo(xb[1]), b[3] + bhi(o[3]) + bhi(xb[1]));
          store_sc1(rh, (u * 8 + jj) * 1024 + lane * 16, h);
          hk[jj][u] = h;
#pragma unroll
          for (int e = 0; e < 4; e++) { const float lo = blo(h[e]), hi = bhi(h[e]); s += lo + hi; q += lo * lo + hi * hi; }
        }
        s = xsum32(xsum16(s)); q = xsum32(xsum16(q));
        if (g == 0) __builtin_amdgcn_raw_buffer_store_b64((u32x2){__float_as_uint(s), __float_as_uint(q)}, rs, (16 * jj + c) * 8, 0, 16);
      }
      flag_set(ws, FL_H, tt, cell);
      STAMP(8)
      bar();
      bar();
    }
    rsrc_t rr = mk_rsrc(ws + WS_R + ((size_t)(tt * 112 + 7 * ft) * 8 + 4 * wQ) * 1024, 7 * 8192);
    float ra[4], rb[4];
    auto act = [&](const f32x4& a, int col, int j, u32& lo, u32& hi) {
      const f32x4 cs = *(const LAS f32x4*)(tabC + col), bi = *(const LAS f32x4*)(tabBias + col);
      f32x4 v = ra[j] * a + (rb[j] * cs + bi);
#pragma unroll
      for (int e = 0; e < 4; e++) { v[e] = v[e] > 0.f ? v[e] : 0.f; v[e] *= v[e]; }
      lo = cvtpk(v[0], v[1]); hi = cvtpk(v[2], v[3]);
    };
    {
      Consumer<4, M_TR256, M_TILED> C;
      C.init(sm, wP, wQ, lane);
      f32x4 acc[4][4];
      C.zero(acc);
      bar();
      STAMP(9)
      C.first();
      for (int it = 0; it < 3; it++) {
        C.step4<4, 4>(acc);
      }
      C.last4<4>(acc);
      STAMP(10)
#pragma unroll
      for (int j = 0; j < 4; j++) { ra[j] = tabA[64 * wQ + 16 * j + c]; rb[j] = tabB[64 * wQ + 16 * j + c]; }
#pragma unroll
      for (int u = 0; u < 2; u++)
#pragma unroll
        for (int j = 0; j < 4; j++) {
          u32 a0, a1, b0, b1_;
          act(acc[2 * u][j], 64 * wP + 32 * u + 4 * g, j, a0, a1);
          act(acc[2 * u + 1][j], 64 * wP + 32 * u + 16 + 4 * g, j, b0, b1_);
          store_sc1(rr, ((2 * wP + u) * 8 + j) * 1024 + lane * 16, (u32x4){a0, a1, b0, b1_});
        }
    }
    {
      Consumer<3, M_TR192, M_TILED> C;
      C.init(sm, wP, wQ, lane);
      f32x4 acc[3][4];
      C.zero(acc);
      bar();
      STAMP(11)
      C.first();
      for (int it = 0; it < 3; it++) {
        C.step4<4, 4>(acc);
        if (it == K_FLAGIT) flag_set(ws, FL_RA, tt, cell);
      }
      C.last4<4>(acc);
      STAMP(12)
#pragma unroll
      for (int j = 0; j < 4; j++) {
        u32 a0, a1, b0, b1_;
        act(acc[0][j], 128 + 64 * wP + 4 * g, j, a0, a1);
        act(acc[1][j], 128 + 64 * wP + 16 + 4 * g, j, b0, b1_);
        store_sc1(rr, ((4 + 2 * wP) * 8 + j) * 1024 + lane * 16, (u32x4){a0, a1, b0, b1_});
        act(acc[2][j], 160 + 16 * wP + 4 * g, j, a0, a1);
        __builtin_amdgcn_raw_buffer_store_b64((u32x2){a0, a1}, rr, (5 * 8 + j) * 1024 + lane * 16 + 8 * wP, 0, 16);
      }
    }
    {
      Consumer<4, M_TR256, M_TILED> C;
      C.init(sm, wP, wQ, lane);
      f32x4 acc[4][4];
      C.zero(acc);
      bar();
      STAMP(13)
      C.first();
      for (int it = 0; it < 5; it++) {
        C.step4<4, 4>(acc);
        if (it == K_FLAGIT) flag_set(ws, FL_RB, tt, cell);
      }
      C.step4<4, 2>(acc);
      STAMP(14)
      xch_send(ws + WS_X4, acc, wid, lane);
      u32x2 br[4];
#pragma unroll
      for (int i = 0; i < 4; i++) br[i] = *(const u32x2*)(b2 + et * 128 + 64 * wP + 16 * i + 4 * g);
      __builtin_amdgcn_sched_barrier(0);
      rsrc_t rx = mk_rsrc(ws + WS_X4 + ((size_t)(blockIdx.x ^ 4) * 4 + wid) * 4096, 4096);
      rsrc_t rf = mk_rsrc(ws + WS_FL + ((size_t)(4 * 16 + tt) * 64 + (cell ^ 32)) * 64, 64);
      u32x4 rv[4];
      u32 fv;
      C.step<0, 2, 2>(acc); C.step<1, 2, 2>(acc); C.step<2, 2, 2>(acc);
      if (K_XF == 3) flag_set(ws, FL_X4, tt, cell);
      C.step<3, 2, 2>(acc);
      if (K_XF == 4) flag_set(ws, FL_X4, tt, cell);
      C.step<0, 2, 2>(acc); C.step<1, 2, 2>(acc);
      flag_request(rf, 0, fv);
      __builtin_amdgcn_sched_barrier(0);
      C.step<2, 2, 2>(acc);
      wait_vm<0>();
      flag_wait(rf, 0, fv);
      xch_request(rx, rv, lane);
      __builtin_amdgcn_sched_barrier(0);
      C.load<3, 1, 2>(); C.mma<0, 2>(acc); C.interleave<8, 2>();
      C.mma<1, 2>(acc);
      xch_finish(rv);

      rsrc_t ro = mk_rsrc(out + (size_t)(tt * 128 + 16 * m0) * D_ + et * 128 + 64 * wP, 31 * D_ * 2 + 128);
#pragma unroll
      for (int jj = 0; jj < 2; jj++)
#pragma unroll
        for (int u = 0; u < 2; u++) {
          const u32x4 o = rv[jj * 2 + u], h = hk[jj][u];
          const f32x4 a = acc[2 * u][jj], b = acc[2 * u + 1][jj];
          const u32x2 ba = br[2 * u], bb = br[2 * u + 1];
          const u32 a0 = cvtpk(a[0] + blo(o[0]) + blo(ba[0]) + blo(h[0]), a[1] + bhi(o[0]) + bhi(ba[0]) + bhi(h[0]));
          const u32 a1 = cvtpk(a[2] + blo(o[1]) + blo(ba[1]) + blo(h[1]), a[3] + bhi(o[1]) + bhi(ba[1]) + bhi(h[1]));
          const u32 b0 = cvtpk(b[0] + blo(o[2]) + blo(bb[0]) + blo(h[2]), b[1] + bhi(o[2]) + bhi(bb[0]) + bhi(h[2]));
          const u32 b1_ = cvtpk(b[2] + blo(o[3]) + blo(bb[1]) + blo(h[3]), b[3] + bhi(o[3]) + bhi(bb[1]) + bhi(h[3]));
          const auto s0 = __builtin_amdgcn_permlane16_swap(a0, b0, false, false);
          const auto s1 = __builtin_amdgcn_permlane16_swap(a1, b1_, false, false);
          __builtin_amdgcn_raw_buffer_store_b128((u32x4){s0[0], s1[0], s0[1], s1[1]}, ro,
                                                 (16 * jj + c) * (D_ * 2) + (32 * u + 16 * (g & 1) + 8 * (g >> 1)) * 2, 0, K_OUTSC1 ? 16 : 0);
        }
      DIAG_END(15)
    }
  }
}

}

extern "C" void kernel_launch(void* const* d_in, const int* in_sizes, int n_in, void* d_out, int out_size, void* d_ws, size_t ws_size,
                              hipStream_t stream) {
  const u16* const* I = (const u16* const*)d_in;
  (void)hipFuncSetAttribute((const void*)fused_kernel, hipFuncAttributeMaxDynamicSharedMemorySize, LDS_BYTES);
  fused_kernel<<<256, 512, LDS_BYTES, stream>>>(I[0], I[1], I[2], I[3], I[4], I[6], I[7], I[8], I[9], (char*)d_ws, (u16*)d_out);
}
```

```cpp
#include <hip/hip_runtime.h>
#include <hip/hip_bf16.h>

typedef unsigned short u16;
typedef unsigned int u32;
typedef unsigned long long u64;
typedef __bf16 bf16x8 __attribute__((ext_vector_type(8)));
typedef __bf16 bf16x2 __attribute__((ext_vector_type(2)));
typedef short s16x4 __attribute__((ext_vector_type(4)));
typedef short s16x8 __attribute__((ext_vector_type(8)));
typedef float f32x2 __attribute__((ext_vector_type(2)));
typedef float f32x4 __attribute__((ext_vector_type(4)));
typedef u32 u32x2 __attribute__((ext_vector_type(2)));
typedef u32 u32x4 __attribute__((ext_vector_type(4)));
typedef __amdgpu_buffer_rsrc_t rsrc_t;
#define LAS __attribute__((address_space(3)))
#define DI __device__ __forceinline__

#define K_FLAGIT 0
#define K_SIDE 1
#define K_PFC0 99
#define K_PFC1 99
#define K_ORD 3
#define K_PRIO 0
#define K_NOCS 0
#define K_CSN 12
#define K_WN 12
#define K_FQ 2
#define K_PFW 8
#define K_PFPHI 1
#define K_XF 4
#define K_SLEEP 1
#define K_OUTSC1 1
namespace {

constexpr int S_ = 2048, D_ = 1024, F_ = 3584;
constexpr float LN_EPS = 1e-5f;

constexpr size_t WS_AB = 0;
constexpr size_t WS_H = 8u << 20;
constexpr size_t WS_R = 12u << 20;
constexpr size_t WS_X2 = 27u << 20;
constexpr size_t WS_X4 = 31u << 20;
constexpr size_t WS_ST = 35u << 20;
constexpr size_t WS_CS = 36u << 20;
constexpr size_t WS_FL = 37u << 20;

#define DIAG_ARG
#define DIAG_BEGIN u64 rtA = 0, rtB = 0; const u64 rt0 = __builtin_amdgcn_s_memrealtime(); if (0 == 0) rtA = rt0; if (0 == 0) rtB = rt0;
#define STAMP(i) { if (i == 0) { __builtin_amdgcn_sched_barrier(0); rtA = __builtin_amdgcn_s_memrealtime(); __builtin_amdgcn_s_waitcnt(0xC07F); __builtin_amdgcn_sched_barrier(0); } if (i == 0) { __builtin_amdgcn_sched_barrier(0); rtB = __builtin_amdgcn_s_memrealtime(); __builtin_amdgcn_s_waitcnt(0xC07F); __builtin_amdgcn_sched_barrier(0); } }
#define PSTAMP(i)
#define PDIAG_END
#define DIAG_END(i)

DI u32 rfl(u32 x) { return __builtin_amdgcn_readfirstlane(x); }
DI u32 cvtpk(float lo, float hi) { f32x2 v = {lo, hi}; return __builtin_bit_cast(u32, __builtin_convertvector(v, bf16x2)); }
DI float blo(u32 v) { return __uint_as_float(v << 16); }
DI float bhi(u32 v) { return __uint_as_float(v & 0xFFFF0000u); }
DI rsrc_t mk_rsrc(const void* p, u32 bytes) { return __builtin_amdgcn_make_buffer_rsrc((void*)p, 0, bytes, 0x00020000); }
DI void bar() { asm volatile("s_barrier" ::: "memory"); }
DI void wait_lgkm0() { __builtin_amdgcn_s_waitcnt(0xC07F); }
template <int N> DI void wait_vm() { asm volatile("s_waitcnt vmcnt(%0)" ::"i"(N) : "memory"); }

template <int N> DI void dma(u32 m0, const u32* v, rsrc_t r, u32 so) {
  if constexpr (N == 4)
    asm volatile("s_mov_b32 m0, %0\n\ts_nop 0\n\t"
                 "buffer_load_dwordx4 %1, %5, %6 offen lds\n\t"
                 "buffer_load_dwordx4 %2, %5, %6 offen offset:1024 lds\n\t"
                 "buffer_load_dwordx4 %3, %5, %6 offen offset:2048 lds\n\t"
                 "buffer_load_dwordx4 %4, %5, %6 offen offset:3072 lds"
                 ::"s"(m0), "v"(v[0]), "v"(v[1]), "v"(v[2]), "v"(v[3]), "s"(r), "s"(so) : "memory");
  else if constexpr (N == 3)
    asm volatile("s_mov_b32 m0, %0\n\ts_nop 0\n\t"
                 "buffer_load_dwordx4 %1, %4, %5 offen lds\n\t"
                 "buffer_load_dwordx4 %2, %4, %5 offen offset:1024 lds\n\t"
                 "buffer_load_dwordx4 %3, %4, %5 offen offset:2048 lds"
                 ::"s"(m0), "v"(v[0]), "v"(v[1]), "v"(v[2]), "s"(r), "s"(so) : "memory");
  else
    asm volatile("s_mov_b32 m0, %0\n\ts_nop 0\n\t"
                 "buffer_load_dwordx4 %1, %3, %4 offen lds\n\t"
                 "buffer_load_dwordx4 %2, %3, %4 offen offset:1024 lds"
                 ::"s"(m0), "v"(v[0]), "v"(v[1]), "s"(r), "s"(so) : "memory");
}
template <int N> DI void dma_sc1(u32 m0, u32 v, rsrc_t r, u32 so) {
  if constexpr (N == 4)
    asm volatile("s_mov_b32 m0, %0\n\ts_nop 0\n\t"
                 "buffer_load_dwordx4 %1, %2, %3 offen sc1 lds\n\t"
                 "buffer_load_dwordx4 %1, %2, %3 offen offset:1024 sc1 lds\n\t"
                 "buffer_load_dwordx4 %1, %2, %3 offen offset:2048 sc1 lds\n\t"
                 "buffer_load_dwordx4 %1, %2, %3 offen offset:3072 sc1 lds"
                 ::"s"(m0), "v"(v), "s"(r), "s"(so) : "memory");
  else
    asm volatile("s_mov_b32 m0, %0\n\ts_nop 0\n\t"
                 "buffer_load_dwordx4 %1, %2, %3 offen sc1 lds\n\t"
                 "buffer_load_dwordx4 %1, %2, %3 offen offset:1024 sc1 lds"
                 ::"s"(m0), "v"(v), "s"(r), "s"(so) : "memory");
}

DI float xsum16(float v) { const auto r = __builtin_amdgcn_permlane16_swap(__float_as_uint(v), __float_as_uint(v), false, false); return __uint_as_float(r[0]) + __uint_as_float(r[1]); }
DI float xsum32(float v) { const auto r = __builtin_amdgcn_permlane32_swap(__float_as_uint(v), __float_as_uint(v), false, false); return __uint_as_float(r[0]) + __uint_as_float(r[1]); }

DI void pull_lines(u32 m0, u32 v, rsrc_t r, u32 so) {
  asm volatile("s_mov_b32 m0, %0\n\ts_nop 0\n\tbuffer_load_dword %1, %2, %3 offen lds" ::"s"(m0), "v"(v), "s"(r), "s"(so) : "memory");
}

DI int kstep(int t, int n, int phi) {
  int k = t;
  if ((t & 3) == phi) { k = t + 4; if (k >= n) k -= n; }
  return k;
}

DI int kstep2(int t, int n, int phi, int side) {
  const int k = kstep(t, n, phi), k2 = k ^ ((K_SIDE ? side : 0) << 2);
  return k2 < n ? k2 : k;
}

constexpr int NST = 4;
constexpr int SLOT = 16384;
constexpr int QOFF = NST * SLOT;
constexpr int TOFF = 2 * NST * SLOT;
constexpr int COFF = TOFF + 4096;
constexpr int XOFF = COFF + 4096;
constexpr int LDS_BYTES = XOFF + 16384;

enum { M_ROW = 0, M_TILED = 1, M_TR192 = 2, M_TR256 = 3 };

template <int PT, int PMODE, int QMODE>
struct Consumer {
  const LAS char* pb[4];
  const LAS char* qb[4];
  bf16x8 fp[2][PT], fq[2][4];

  DI void init(const LAS char* sm, int wP, int wQ, int lane, int prot = 0, int qrot = 0) {
    const int r = lane & 15, g = lane >> 4;
    if constexpr (PMODE == M_ROW) {
      const int a = (64 * wP + r) * 128 + ((g ^ (r >> 1)) * 16);
      pb[0] = sm + (a + prot * 4096); pb[1] = sm + ((a ^ 64) + prot * 4096);
      pb[2] = sm + (a + 4096 - prot * 4096); pb[3] = sm + ((a ^ 64) + 4096 - prot * 4096);
    } else if constexpr (PMODE == M_TILED) {
      pb[0] = sm + (4 * wP * 1024 + lane * 16);
    } else if constexpr (PMODE == M_TR192) {
      const int b = g & 1, a = (4 * g + (r >> 2)) * 192 + (r & 3) * 8;
      pb[0] = sm + (a + (4 * wP + b) * 32);
      pb[1] = sm + (a + (4 * wP + 1 - b) * 32);
      pb[2] = sm + (a + ((2 + wP) ^ b) * 32);
    } else {
      const int k = 4 * g + (r >> 2), a = k * 256 + (r & 3) * 8;
#pragma unroll
      for (int i = 0; i < 4; i++) pb[i] = sm + (a + (((4 * wP + i) ^ (k & 7)) * 32));
    }
    if constexpr (QMODE == M_ROW) {
      const int a = QOFF + (64 * wQ + r) * 128 + ((g ^ (r >> 1)) * 16);
      qb[0] = sm + (a + qrot * 4096); qb[1] = sm + ((a ^ 64) + qrot * 4096);
      qb[2] = sm + (a + 4096 - qrot * 4096); qb[3] = sm + ((a ^ 64) + 4096 - qrot * 4096);
      asm volatile("" : "+v"(qb[0]), "+v"(qb[1]), "+v"(qb[2]), "+v"(qb[3]));
    } else {
      qb[0] = sm + (QOFF + 4 * wQ * 1024 + lane * 16);
      asm volatile("" : "+v"(qb[0]));
    }
  }
  static DI bf16x8 tr2(const LAS char* p, int second) {
    s16x4 a = __builtin_amdgcn_ds_read_tr16_b64_v4i16((LAS s16x4*)p);
    s16x4 b = __builtin_amdgcn_ds_read_tr16_b64_v4i16((LAS s16x4*)(p + second));
    return __builtin_bit_cast(bf16x8, __builtin_shufflevector(a, b, 0, 1, 2, 3, 4, 5, 6, 7));
  }
  template <int S, int KS> DI bf16x8 ldp(int i) {
    if constexpr (PMODE == M_ROW) return *(const LAS bf16x8*)(pb[KS + (i & 2)] + (S * SLOT + (i & 1) * 2048));
    else if constexpr (PMODE == M_TILED) return *(const LAS bf16x8*)(pb[0] + (S * SLOT + KS * 8192 + i * 1024));
    else if constexpr (PMODE == M_TR192) return tr2(pb[i] + (S * SLOT + KS * 32 * 192), 16 * 192);
    else return tr2(pb[i] + (S * SLOT + KS * 32 * 256), 16 * 256);
  }
  template <int S, int KS> DI bf16x8 ldq(int j) {
    if constexpr (QMODE == M_ROW) return *(const LAS bf16x8*)(qb[KS + (j & 2)] + (S * SLOT + (j & 1) * 2048));
    else return *(const LAS bf16x8*)(qb[0] + (S * SLOT + KS * 8192 + j * 1024));
  }
  template <int S, int KS, int QN> DI void load() {
    fp[KS][0] = ldp<S, KS>(0);
#pragma unroll
    for (int j = 0; j < QN; j++) fq[KS][j] = ldq<S, KS>(j);
#pragma unroll
    for (int i = 1; i < PT; i++) fp[KS][i] = ldp<S, KS>(i);
  }
  template <int KS, int QN> DI void mma(f32x4 (&acc)[PT][4]) {
#pragma unroll
    for (int n = 0; n < PT * QN; n++) {
      int i, j;
      if (K_ORD == 0) { i = n / QN; j = n % QN; }
      else if (K_ORD == 1) { i = n / QN; j = (i & 1) ? QN - 1 - n % QN : n % QN; }
      else if (K_ORD == 2) { j = n / PT; i = n % PT; }
      else { j = n / PT; i = (j & 1) ? PT - 1 - n % PT : n % PT; }
      acc[i][j] = __builtin_amdgcn_mfma_f32_16x16x32_bf16(fp[KS][i], fq[KS][j], acc[i][j], 0, 0, 0);
    }
  }
  template <int KS> DI void stats(f32x4 (&sx)[2], f32x4 (&sxx)[2], f32x4 (&sd)[2], bf16x8 ones) {
#pragma unroll
    for (int i = 0; i < 2; i++) {
      sx[i] = __builtin_amdgcn_mfma_f32_16x16x32_bf16(fp[KS][i], ones, sx[i], 0, 0, 0);
      sxx[i] = __builtin_amdgcn_mfma_f32_16x16x32_bf16(fp[KS][i], fp[KS][i], sxx[i], 0, 0, 0);
      sd[i] = __builtin_amdgcn_mfma_f32_16x16x32_bf16(ones, fq[KS][i], sd[i], 0, 0, 0);
    }
  }
  template <int NM, int QN> static DI void interleave() {
    constexpr int NR = PT * (PMODE >= M_TR192 ? 2 : 1) + QN;
    if constexpr (NR <= NM) {
#pragma unroll
      for (int i = 0; i < NR; i++) { __builtin_amdgcn_sched_group_barrier(0x8, 1, 0); __builtin_amdgcn_sched_group_barrier(0x100, 1, 0); }
      __builtin_amdgcn_sched_group_barrier(0x8, NM - NR, 0);
    } else {
#pragma unroll
      for (int i = 0; i < NR / 2; i++) { __builtin_amdgcn_sched_group_barrier(0x8, 1, 0); __builtin_amdgcn_sched_group_barrier(0x100, 2, 0); }
      __builtin_amdgcn_sched_group_barrier(0x8, NM - NR / 2, 0);
    }
    __builtin_amdgcn_sched_barrier(0);
  }
  template <int S, int QC, int QN> DI void step(f32x4 (&acc)[PT][4]) {
    load<S, 1, QC>(); mma<0, QC>(acc); interleave<PT * QC, QC>();
    wait_lgkm0(); bar();
    __builtin_amdgcn_sched_barrier(0);
    load<(S + 1) % NST, 0, QN>(); mma<1, QC>(acc); interleave<PT * QC, QN>();
  }
  template <int QC, int QN> DI void step4(f32x4 (&acc)[PT][4]) {
    step<0, QC, QC>(acc); step<1, QC, QC>(acc); step<2, QC, QC>(acc); step<3, QC, QN>(acc);
  }
  template <int QC> DI void last4(f32x4 (&acc)[PT][4]) {
    step<0, QC, QC>(acc); step<1, QC, QC>(acc); step<2, QC, QC>(acc);
    load<3, 1, QC>(); mma<0, QC>(acc); interleave<PT * QC, QC>();
    mma<1, QC>(acc);
  }
  static DI void zero(f32x4 (&acc)[PT][4]) {
#pragma unroll
    for (int i = 0; i < PT; i++)
#pragma unroll
      for (int j = 0; j < 4; j++) { acc[i][j] = (f32x4){0.f, 0.f, 0.f, 0.f}; asm volatile("" : "+v"(acc[i][j])); }
  }
  DI void first() { load<0, 0, 4>(); }
};

DI u32 row_src(int imgrow, int srcrow, int lane, int ldbytes) {
  return srcrow * ldbytes + (((lane & 7) ^ ((imgrow >> 1) & 7)) * 16);
}
DI void store_sc1(rsrc_t r, u32 off, u32x4 v) { __builtin_amdgcn_raw_buffer_store_b128(v, r, off, 0, 16); }

DI void xch_send(char* x, const f32x4 (&acc)[4][4], int wid, int lane) {
  rsrc_t r = mk_rsrc(x + ((size_t)blockIdx.x * 4 + wid) * 4096, 4096);
#pragma unroll
  for (int jj = 0; jj < 2; jj++)
#pragma unroll
    for (int u = 0; u < 2; u++) {
      const f32x4 a = acc[2 * u][2 + jj], b = acc[2 * u + 1][2 + jj];
      u32x4 v = {cvtpk(a[0], a[1]), cvtpk(a[2], a[3]), cvtpk(b[0], b[1]), cvtpk(b[2], b[3])};
      store_sc1(r, lane * 16 + (jj * 2 + u) * 1024, v);
    }
}
DI void xch_request(rsrc_t r, u32x4 (&v)[4], int lane) {
  asm volatile("buffer_load_dwordx4 %0, %4, %5, 0 offen sc1\n\t"
               "buffer_load_dwordx4 %1, %4, %5, 0 offen offset:1024 sc1\n\t"
               "buffer_load_dwordx4 %2, %4, %5, 0 offen offset:2048 sc1\n\t"
               "buffer_load_dwordx4 %3, %4, %5, 0 offen offset:3072 sc1"
               : "=&v"(v[0]), "=&v"(v[1]), "=&v"(v[2]), "=&v"(v[3]) : "v"(lane * 16), "s"(r) : "memory");
}
DI void xch_finish(u32x4 (&v)[4]) { asm volatile("s_waitcnt vmcnt(0)" : "+v"(v[0]), "+v"(v[1]), "+v"(v[2]), "+v"(v[3])::"memory"); }

enum { FL_H = 0, FL_RA = 1, FL_RB = 2, FL_X2 = 3, FL_X4 = 4, FL_ACK = 5, FL_TICK = 6 };
DI void flag_set(char* ws, int which, int tt, int cell) {
  wait_vm<0>();
  if ((threadIdx.x & 63) == 0)
    __hip_atomic_store((u32*)(ws + WS_FL + ((size_t)(which * 16 + tt) * 64 + cell) * 64), 1u, __ATOMIC_RELAXED, __HIP_MEMORY_SCOPE_AGENT);
}
DI void flag_request(rsrc_t r, u32 off, u32& v) {
  asm volatile("buffer_load_dword %0, %1, %2, 0 offen sc1" : "=&v"(v) : "v"(off), "s"(r) : "memory");
}
DI void flag_wait(rsrc_t r, u32 off, u32& v) {
  asm volatile("" : "+v"(v)::"memory");
#pragma unroll 1
  for (int spin = 0; spin < (1 << 20); spin++) {
    if (__builtin_amdgcn_ballot_w64(v != 1u) == 0) break;
    __builtin_amdgcn_s_sleep(K_SLEEP);
    asm volatile("buffer_load_dword %0, %1, %2, 0 offen sc1\n\ts_waitcnt vmcnt(0)" : "=&v"(v) : "v"(off), "s"(r) : "memory");
  }
}

DI void word_store(rsrc_t r, u32 off, u32 v) { __builtin_amdgcn_raw_buffer_store_b32(v, r, off, 0, 16); }
template <class Done>
DI void watch(rsrc_t r, u32 off, u32& v, Done done, u32 ownoff, u32 ownval, u32& bump, bool& ticked, u64 ownmask) {
  asm volatile("" : "+v"(v)::"memory");
#pragma unroll 1
  for (int spin = 0; spin < (1 << 20); spin++) {
    const u64 miss = __builtin_amdgcn_ballot_w64(!done(v));
    if (miss == 0) break;
    ticked = ticked || (miss & ownmask) == 0;
    if (ticked && (spin & 63) == 63) {
      bump++;
      if ((ownmask >> (threadIdx.x & 63)) & 1) word_store(r, ownoff, ownval + bump);
    }
    __builtin_amdgcn_s_sleep(K_SLEEP);
    asm volatile("buffer_load_dword %0, %1, %2, 0 offen sc1\n\ts_waitcnt vmcnt(0)" : "=&v"(v) : "v"(off), "s"(r) : "memory");
  }
}

__global__ __launch_bounds__(512, 2) void fused_kernel(const u16* __restrict__ x, const u16* __restrict__ cre, const u16* __restrict__ cim,
                                                    const u16* __restrict__ dre, const u16* __restrict__ dim,
                                                    const u16* __restrict__ w1, const u16* __restrict__ b1, const u16* __restrict__ w2,
                                                    const u16* __restrict__ b2, char* __restrict__ ws, u16* __restrict__ out DIAG_ARG) {
  extern __shared__ __attribute__((aligned(16))) char smem[];
  DIAG_BEGIN
  asm volatile("" ::"s"(x), "s"(cre), "s"(cim), "s"(dre), "s"(dim), "s"(w1), "s"(b1), "s"(w2), "s"(b2), "s"(ws), "s"(out));
  const int lane = threadIdx.x & 63, wid = rfl(threadIdx.x >> 6);
  const int xcd = blockIdx.x & 7, loc = blockIdx.x >> 3, li = loc >> 2, lj = loc & 3;
  const int half = xcd >> 2, et = ((xcd >> 1) & 1) * 4 + lj, tt = (xcd & 1) * 8 + li, ft = (xcd >> 1) * 4 + lj;
  LAS char* sm = (LAS char*)smem;
  LAS float* tabA = (LAS float*)(sm + TOFF);
  LAS float* tabB = tabA + 128;
  LAS float* tabC = tabA + 256;
  LAS float* tabBias = tabA + 512;
  if (__builtin_expect(wid >= 4, 1)) {
    const int p = wid - 4, phi = (li + lj) & 3;
    const u32 lds0 = (u32)(size_t)smem, ldsb = lds0 + p * 4096, vlin = p * 4096 + lane * 16;
    rsrc_t rall = mk_rsrc(ws + WS_FL, 7 * 65536);
    const u32 colcell = ((lane >> 2) * 64 + ft * 4 + (lane & 3)) * 64, rowcell = (tt * 64 + (lane & 15) * 4) * 64;
    const u64 ownmask = 15ull << (4 * tt);
    u32 col0, row0, fv, bump = 1;
    bool ticked = false;
    {
      rsrc_t rp = mk_rsrc((const char*)x + (size_t)tt * 128 * D_ * 2 - 4096, 128 * D_ * 2 + 4096);
      rsrc_t rq = mk_rsrc((half ? dim : dre) + (size_t)et * 128 * D_, 128 * D_ * 2);
      u32 vp[4], vq[4], vx[2];
#pragma unroll
      for (int i = 0; i < 4; i++) {
        const int row = 32 * p + 8 * i + (lane >> 3), u = (row >> 4) & 1, ii = row & 15;
        vp[i] = row_src(row, 32 * p + 8 * (ii >> 2) + 4 * u + (ii & 3), lane, D_ * 2) + 4096 - i * 1024;
        vq[i] = row_src(row, row, lane, D_ * 2) - i * 1024;
      }
#pragma unroll
      for (int i = 0; i < 2; i++) {
        const int row = 16 * p + 8 * i + (lane >> 3);
        vx[i] = row_src(row, 64 * (row >> 5) + 32 * half + (row & 31), lane, D_ * 2) + 4096 - i * 1024;
      }
      auto issue = [&](int g) {
        const int k = kstep2(g, 16, phi, (xcd >> 1) ^ (xcd & 1));
        const u32 m = ldsb + (g & 3) * SLOT;
        dma<4>(m, vp, rp, k * 128);
        dma<4>(m + QOFF, vq, rq, k * 128);
        if ((k >> 1) == et) dma<2>(lds0 + XOFF + (k & 1) * 8192 + p * 2048, vx, rp, k * 128);
      };
      issue(0); issue(1); issue(2);
      wait_vm<16>(); bar();
#pragma unroll 1
      for (int g = 3; g < 6; g++) { issue(g); wait_vm<16>(); bar(); }
      flag_request(rall, FL_TICK * 65536 + colcell, col0);
      flag_request(rall, FL_TICK * 65536 + rowcell, row0);
#pragma unroll 1
      for (int g = 6; g < 16; g++) { issue(g); wait_vm<16>(); bar(); }
      asm volatile("" : "+v"(col0), "+v"(row0)::"memory");
    }
    {
      rsrc_t rp = mk_rsrc(ws + WS_AB + (size_t)et * 32 * 16384, 9 * 32 * 16384);
      rsrc_t rqo = mk_rsrc((const char*)(half ? cim : cre) + (size_t)tt * 128 * S_ * 2 - 4096, 128 * S_ * 2 + 4096);
      rsrc_t rq0 = mk_rsrc((const char*)cre + (size_t)tt * 128 * S_ * 2 - 4096, 128 * S_ * 2 + 4096);
      rsrc_t rq1 = mk_rsrc((const char*)cim + (size_t)tt * 128 * S_ * 2 - 4096, 128 * S_ * 2 + 4096);
      u32 vq[4], vh[2];
#pragma unroll
      for (int i = 0; i < 4; i++) {
        const int row = 32 * p + 8 * i + (lane >> 3);
        vq[i] = row_src(row, row ^ (half * 32), lane, S_ * 2) + 4096 - i * 1024;
      }
#pragma unroll
      for (int i = 0; i < 2; i++) {
        const int row = 64 * (p >> 1) + 16 * (p & 1) + 8 * i + (lane >> 3);
        vh[i] = row_src(row, row ^ (half * 32), lane, S_ * 2) + 4096 - i * 1024;
      }
      const u32 ldsh = lds0 + QOFF + (p >> 1) * 8192 + (p & 1) * 2048;
      auto fp = [&](int g) { dma_sc1<4>(ldsb + (g & 3) * SLOT, vlin, rp, (half * 256 + kstep2(g, 28, phi, (xcd >> 1) & 1)) * 16384); };
      auto fq = [&](int g) { dma<4>(ldsb + QOFF + (g & 3) * SLOT, vq, rqo, kstep2(g, 28, phi, (xcd >> 1) & 1) * 128); };
      auto halfs = [&](int g) {
        const int k = kstep2(g, 8, phi, (xcd >> 1) & 1), term = k >> 2, ks = 28 + (k & 3);
        dma_sc1<4>(ldsb + (g & 3) * SLOT, vlin, rp, (term * 256 + ks) * 16384);
        if (term) dma<2>(ldsh + (g & 3) * SLOT, vh, rq1, ks * 128);
        else dma<2>(ldsh + (g & 3) * SLOT, vh, rq0, ks * 128);
      };
      auto full = [&](int g) { fp(g); fq(g); };
      auto run = [&](int g0, int g1) {
#pragma unroll 1
        for (int g = g0; g < g1; g++) { full(g); wait_vm<16>(); bar(); }
      };
      if (K_FQ >= 1) fq(0);
      wait_vm<K_FQ >= 1 ? 12 : 8>(); bar();
      if (K_FQ >= 2) fq(1);
      wait_vm<K_FQ >= 2 ? 8 : K_FQ * 4>(); bar();
      bar();
      bar();
      PSTAMP(1)
      if (K_FQ < 1) fq(0);
      if (K_FQ < 2) fq(1);
      fq(2);
      bar();
      if (p == 0) {
        fv = col0;
        watch(rall, FL_TICK * 65536 + colcell, fv, [&](u32 w) { return w != col0; }, FL_TICK * 65536 + colcell, col0, bump, ticked, ownmask);
        ticked = true;
      }
      bar();
      PSTAMP(2)
      fp(0); fp(1); fp(2);
      wait_vm<8>();
      PSTAMP(9)
      bar();
      full(3); wait_vm<12>(); bar();
      const int g0 = 4 + loc % K_CSN;
      run(4, g0);
      {
        rsrc_t rw = mk_rsrc((const char*)w1 + (size_t)(tt * 64 + p * 16) * F_ * 2 + ft * 448, 15 * F_ * 2 + 448);
        u32x4 v[8];
        const int l28 = lane < 28 ? lane : lane < 56 ? lane - 28 : 27;
        const u32 vo = (lane >= 28 ? F_ * 2 : 0) + l28 * 16;
        full(g0);
#pragma unroll
        for (int i = 0; i < 8; i++)
          asm volatile("buffer_load_dwordx4 %0, %1, %2, %3 offen" : "=&v"(v[i]) : "v"(vo), "s"(rw), "s"(i * 2 * F_ * 2) : "memory");
        wait_vm<24>(); bar();
        full(g0 + 1); wait_vm<24>(); bar();
        full(g0 + 2); wait_vm<24>(); bar();
        full(g0 + 3);
        asm volatile("s_waitcnt vmcnt(16)" : "+v"(v[0]), "+v"(v[1]), "+v"(v[2]), "+v"(v[3]), "+v"(v[4]), "+v"(v[5]), "+v"(v[6]), "+v"(v[7])::"memory");
        bar();
        full(g0 + 4);
        float s[8] = {0, 0, 0, 0, 0, 0, 0, 0};
#pragma unroll
        for (int i = 0; i < 8; i++)
#pragma unroll
          for (int e = 0; e < 4; e++) { s[2 * e] += blo(v[i][e]); s[2 * e + 1] += bhi(v[i][e]); }
#pragma unroll
        for (int e = 0; e < 8; e++) s[e] += __shfl(s[e], lane + 28);
        if (lane < 28) {
          LAS float* d = (LAS float*)(sm + COFF) + p * 224 + lane * 8;
          *(LAS f32x4*)d = (f32x4){s[0], s[1], s[2], s[3]};
          *(LAS f32x4*)(d + 4) = (f32x4){s[4], s[5], s[6], s[7]};
        }
        wait_lgkm0();
        wait_vm<16>(); bar();
        if (p == 0 && lane < 56) {
          const LAS f32x4* d = (const LAS f32x4*)(sm + COFF) + lane;
          const f32x4 t = (d[0] + d[56]) + (d[112] + d[168]);
          store_sc1(mk_rsrc(ws + WS_CS + (size_t)tt * F_ * 4 + ft * 896, 896), lane * 16, __builtin_bit_cast(u32x4, t));
        }
      }
      run(g0 + 5, 20);
      flag_request(rall, FL_TICK * 65536 + rowcell, fv);
      full(20); wait_vm<16>(); bar();
      full(21); wait_vm<16>();
      asm volatile("" : "+v"(fv)::"memory");
      if (p == 0) {
        watch(rall, FL_TICK * 65536 + rowcell, fv, [&](u32 w) { return w != row0; }, FL_TICK * 65536 + colcell, col0, bump, ticked, ownmask);
        if (lane == 0) word_store(rall, FL_ACK * 65536 + (tt * 64 + ft * 4) * 64, 1u);
      }
      bar();
      run(22, 26);
      const u32 ackcell = FL_ACK * 65536 + ((lane & 16) ? rowcell : ((lane & 15) * 64 + ft * 4) * 64);
      flag_request(rall, ackcell, fv);
      full(26); wait_vm<16>(); bar();
      full(27); wait_vm<16>();
      PSTAMP(10)
      asm volatile("" : "+v"(fv)::"memory");
      if (p == 0) watch(rall, ackcell, fv, [&](u32 w) { return w == 1u; }, FL_TICK * 65536 + colcell, col0, bump, ticked, ownmask);
      PSTAMP(11)
      bar();
      halfs(0); wait_vm<14>(); bar();
#pragma unroll 1
      for (int g = 1; g < 8; g++) { halfs(g); wait_vm<12>(); bar(); }
    }
    rsrc_t rfl_ = mk_rsrc(ws + WS_FL + (size_t)tt * 4096, 3 * 16 * 4096);
    {
      rsrc_t rp = mk_rsrc((const char*)w1 + ft * 448, D_ * F_ * 2 - ft * 448);
      rsrc_t rq = mk_rsrc(ws + WS_H + (size_t)tt * 32 * 8192, 32 * 8192);
      u32 va[4], vb[3];
#pragma unroll
      for (int i = 0; i < 4; i++) {
        const int k = 16 * p + 4 * i + (lane >> 4);
        va[i] = k * (F_ * 2) + (((lane & 15) ^ ((k & 7) << 1)) * 16) - i * 1024;
      }
#pragma unroll
      for (int i = 0; i < 3; i++) {
        const int o = (p * 3 + i) * 64 + lane, k = o / 12, ch = o % 12;
        vb[i] = k * (F_ * 2) + 256 + ((ch ^ (((k >> 2) & 1) << 1)) * 16) - i * 1024;
      }
      rsrc_t rw = mk_rsrc(w2 + (size_t)(half * 1792 + tt * 112 + 28 * p) * D_ + et * 128, 27 * D_ * 2 + 256);
      const u32 vw = (lane >> 1) * (D_ * 2) + (lane & 1) * 128;
      auto pa = [&](int g) {
        if (g == 4 + loc % K_WN) pull_lines(lds0 + COFF + 3584, vw, rw, 0);
        dma<4>(ldsb + (g & 3) * SLOT, va, rp, kstep(g, 16, phi) * (64 * F_ * 2));
      };
      auto pbb = [&](int g) { dma<3>(lds0 + p * 3072 + (g & 3) * SLOT, vb, rp, kstep(g, 16, phi) * (64 * F_ * 2)); };
      auto q = [&](int g) { dma_sc1<4>(ldsb + QOFF + (g & 3) * SLOT, vlin, rq, kstep(g, 16, phi) * 16384); };
      wait_vm<6>(); bar();
      wait_vm<0>(); bar();
      bar();
      pa(0); pa(1); pa(2);
      const int pt = threadIdx.x - 256;
      rsrc_t rs = mk_rsrc(ws + WS_ST + (size_t)tt * 16384, 16384);
      rsrc_t rc = mk_rsrc(ws + WS_CS + ft * 896, 15 * F_ * 4 + 896);
      u32x2 sv[8];
      u32 cv[16], bv;
#pragma unroll
      for (int i = 0; i < 16; i++)
        asm volatile("buffer_load_dword %0, %1, %2, %3 offen sc1" : "=&v"(cv[i]) : "v"(min(pt, 223) * 4), "s"(rc), "s"(i * F_ * 4) : "memory");
      asm volatile("buffer_load_ushort %0, %1, %2, 0 offen" : "=&v"(bv) : "v"(min(pt, 223) * 2), "s"(mk_rsrc(b1 + ft * 224, 448)) : "memory");
      if (p == 0) { fv = 0; flag_wait(rfl_, lane * 64, fv); }
      bar();
      PSTAMP(3)
      q(0); q(1); q(2);
      const u32 so = (pt & 1) * 8192 + (pt >> 1) * 8;
#pragma unroll
      for (int i = 0; i < 8; i++)
        asm volatile("buffer_load_dwordx2 %0, %1, %2, %3 offen sc1" : "=&v"(sv[i]) : "v"(so), "s"(rs), "s"(i * 1024) : "memory");
      wait_vm<16>();
      PSTAMP(4)
      bar();
      pa(3); q(3); wait_vm<20>(); bar();
      pa(4); q(4); wait_vm<24>(); bar();
      pa(5); q(5); wait_vm<16>();
      asm volatile("" : "+v"(sv[0]), "+v"(sv[1]), "+v"(sv[2]), "+v"(sv[3]), "+v"(sv[4]), "+v"(sv[5]), "+v"(sv[6]), "+v"(sv[7]), "+v"(bv)::"memory");
      asm volatile("" : "+v"(cv[0]), "+v"(cv[1]), "+v"(cv[2]), "+v"(cv[3]), "+v"(cv[4]), "+v"(cv[5]), "+v"(cv[6]), "+v"(cv[7]), "+v"(cv[8]),
                   "+v"(cv[9]), "+v"(cv[10]), "+v"(cv[11]), "+v"(cv[12]), "+v"(cv[13]), "+v"(cv[14]), "+v"(cv[15])::"memory");
      bar();
      {
        float s = 0.f;
#pragma unroll
        for (int i = 0; i < 16; i++) s += __uint_as_float(cv[i]);
        if (pt < 224) { tabC[pt] = s; tabBias[pt] = __uint_as_float(bv << 16); }
      }
      {
        float s = 0.f, qq = 0.f;
#pragma unroll
        for (int i = 0; i < 8; i++) { s += __uint_as_float(sv[i][0]); qq += __uint_as_float(sv[i][1]); }
        s += __shfl_xor(s, 1); qq += __shfl_xor(qq, 1);
        const float mean = s * (1.0f / D_), var = fmaxf(qq * (1.0f / D_) - mean * mean, 0.f);
        const float rstd = 1.0f / sqrtf(var + LN_EPS);
        if ((pt & 1) == 0) { tabA[pt >> 1] = rstd; tabB[pt >> 1] = -mean * rstd; }
        wait_lgkm0();
      }
#pragma unroll 1
      for (int g = 6; g < 16; g++) { pa(g); q(g); wait_vm<16>(); bar(); }
      pbb(0); q(0); wait_vm<15>(); bar();
#pragma unroll 1
      for (int g = 1; g < 14; g++) { pbb(g); q(g); wait_vm<14>(); bar(); }
      flag_request(rfl_, 65536 + lane * 64, fv);
      pbb(14); q(14); wait_vm<14>(); bar();
      pbb(15); q(15); wait_vm<14>();
      PSTAMP(5)
      flag_wait(rfl_, 65536 + lane * 64, fv);
      PSTAMP(6)
      bar();
    }
    {
      rsrc_t rp = mk_rsrc(w2 + et * 128, F_ * D_ * 2 - et * 256);
      rsrc_t rq = mk_rsrc(ws + WS_R + (size_t)tt * 112 * 8192, 112 * 8192);
      u32 vp[4];
#pragma unroll
      for (int i = 0; i < 4; i++) {
        const int k = 16 * (p & 1) + 4 * i + (lane >> 4);
        vp[i] = k * (D_ * 2) + (((lane & 15) ^ ((k & 7) << 1)) * 16) - i * 1024;
      }
      const u32 vq0 = (4 * (p & 1) + 2 * half) * 1024 + lane * 16, vq1 = (4 * (p & 1) + 2 - 2 * half) * 1024 + lane * 16;
      const u32 ldsh = lds0 + QOFF + p * 4096;
      auto bblock = [&](int hf, int n) { return 7 * (8 * hf + n / 3) + 4 + n % 3; };
      auto full = [&](int g) {
        const int k = g < 16 ? kstep2(g, 16, phi, xcd & 1) : 16 + kstep2(g - 16, 8, phi, xcd & 1), n = 2 * k + (p >> 1);
        const int blk = k < 16 ? 7 * (8 * half + (n >> 2)) + (n & 3) : bblock(half, n - 32);
        const u32 m = ldsb + (g & 3) * SLOT;
        dma<4>(m, vp, rp, blk * (32 * D_ * 2));
        dma_sc1<2>(m + QOFF, vq0, rq, blk * 8192);
        dma_sc1<2>(m + QOFF + 2048, vq1, rq, blk * 8192);
      };
      auto halfs = [&](int g) {
        const int k = kstep2(g, 8, phi, xcd & 1), blk = bblock(k >> 2, 16 + 2 * (k & 3) + (p >> 1));
        dma<4>(ldsb + (g & 3) * SLOT, vp, rp, blk * (32 * D_ * 2));
        dma_sc1<2>(ldsh + (g & 3) * SLOT, vq0, rq, blk * 8192);
      };
      full(0); wait_vm<15>(); bar();
#pragma unroll 1
      for (int g = 1; g < 14; g++) { full(g); wait_vm<16>(); bar(); }
      flag_request(rfl_, 131072 + lane * 64, fv);
      full(14); wait_vm<16>(); bar();
      full(15); wait_vm<16>();
      PSTAMP(7)
      flag_wait(rfl_, 131072 + lane * 64, fv);
      PSTAMP(8)
      bar();
#pragma unroll 1
      for (int g = 16; g < 24; g++) { full(g); wait_vm<16>(); bar(); }
      halfs(0); wait_vm<14>(); bar();
#pragma unroll 1
      for (int g = 1; g < 8; g++) { halfs(g); wait_vm<12>(); bar(); }
      wait_vm<6>(); bar();
      wait_vm<0>(); bar();
      PDIAG_END
    }
  } else {
    if (K_PRIO) __builtin_amdgcn_s_setprio(K_PRIO);
    const int wP = wid >> 1, wQ = wid & 1, c = lane & 15, g = lane >> 4;
    const int m0 = 4 * wQ + 2 * half, cell = ft * 4 + wid;
    rsrc_t rall = mk_rsrc(ws + WS_FL, 7 * 65536);
    const u32 mycell = (tt * 64 + cell) * 64;
    u32 tick0;
    {
      LAS float* tabD = tabA + 256;
      Consumer<4, M_ROW, M_ROW> C;
      C.init(sm, wP, wQ, lane, wQ, wP);
      f32x4 acc[4][4], sx[2], sxx[2], sd[2];
      C.zero(acc);
#pragma unroll
      for (int i = 0; i < 2; i++) sx[i] = sxx[i] = sd[i] = (f32x4){0.f, 0.f, 0.f, 0.f};
      const u32x4 ones4 = {0x3F803F80u, 0x3F803F80u, 0x3F803F80u, 0x3F803F80u};
      bf16x8 ones = __builtin_bit_cast(bf16x8, ones4);
      asm volatile("" : "+v"(ones));
      bar();
      STAMP(1)
      C.first();
#define KA_HALF0(S) C.load<S, 1, 4>(); C.mma<0, 4>(acc); C.stats<0>(sx, sxx, sd, ones); C.interleave<22, 4>();
#define KA_STEP(S) KA_HALF0(S) wait_lgkm0(); bar(); __builtin_amdgcn_sched_barrier(0); \
      C.load<(S + 1) % NST, 0, 4>(); C.mma<1, 4>(acc); C.stats<1>(sx, sxx, sd, ones); C.interleave<22, 4>();
      for (int it = 0; it < 3; it++) {
        KA_STEP(0) KA_STEP(1) KA_STEP(2) KA_STEP(3)
      }
      if (wid == 0 && lane < 24) word_store(rall, (lane >> 2) * 65536 + mycell + (lane & 3) * 64, 0u);
      flag_request(rall, FL_TICK * 65536 + mycell, tick0);
      __builtin_amdgcn_sched_barrier(0);
      KA_STEP(0) KA_STEP(1) KA_STEP(2) KA_HALF0(3)
      C.stats<1>(sx, sxx, sd, ones);
      C.mma<1, 4>(acc);
      STAMP(2)
      const float sgn = half ? -1.f : 1.f;
#pragma unroll
      for (int i = 0; i < 2; i++) {
        const int e = c & 3;
        const float q = e == 0 ? sxx[i][0] : e == 1 ? sxx[i][1] : e == 2 ? sxx[i][2] : sxx[i][3];
        const float s = e == 0 ? sx[i][0] : e == 1 ? sx[i][1] : e == 2 ? sx[i][2] : sx[i][3];
        const float mean = s * (1.0f / D_), var = fmaxf(q * (1.0f / D_) - mean * mean, 0.f);
        const float rstd = sgn / sqrtf(var + LN_EPS);
        if ((c >> 2) == g) { tabA[64 * wP + 32 * wQ + 16 * i + c] = rstd; tabB[64 * wP + 32 * wQ + 16 * i + c] = -mean * rstd; }
        if (g == 0) tabD[64 * wQ + 32 * wP + 16 * i + c] = sd[i][0];
      }
      wait_lgkm0(); bar();
      rsrc_t ro = mk_rsrc(ws + WS_AB + ((size_t)((half * 8 + et) * 32 + 2 * tt + wP) * 16) * 1024, 16384);
      f32x4 ra[4], rb[4];
      float rs[4];
#pragma unroll
      for (int i = 0; i < 4; i++) {
        ra[i] = *(const LAS f32x4*)(tabA + 64 * wP + 16 * (i ^ (2 * wQ)) + 4 * g);
        rb[i] = *(const LAS f32x4*)(tabB + 64 * wP + 16 * (i ^ (2 * wQ)) + 4 * g);
        rs[i] = tabD[64 * wQ + 16 * (i ^ (2 * wP)) + c];
      }
#pragma unroll
      for (int u = 0; u < 2; u++)
#pragma unroll
        for (int j = 0; j < 4; j++) {
          const f32x4 a = ra[2 * u] * acc[2 * u][j] + rb[2 * u] * rs[j], b = ra[2 * u + 1] * acc[2 * u + 1][j] + rb[2 * u + 1] * rs[j];
          store_sc1(ro, ((u ^ wQ) * 8 + 4 * wQ + (j ^ (2 * wP))) * 1024 + lane * 16,
                    (u32x4){cvtpk(a[0], a[1]), cvtpk(a[2], a[3]), cvtpk(b[0], b[1]), cvtpk(b[2], b[3])});
        }
  bar();
      asm volatile("s_waitcnt vmcnt(0)" : "+v"(tick0)::"memory");
      if (lane == 0) word_store(rall, FL_TICK * 65536 + mycell, tick0 + 1);
      STAMP(3)
      bar();
      bar();
    }
    u32x4 hk[2][2];
    {
      Consumer<4, M_TILED, M_ROW> C;
      C.init(sm, wP, wQ, lane);
      f32x4 acc[4][4];
      C.zero(acc);
      bar();
      STAMP(4)
      C.first();
      for (int it = 0; it < 6; it++) {
        C.step4<4, 4>(acc);
      }
      C.step4<4, 2>(acc);
      STAMP(5)
      xch_send(ws + WS_X2, acc, wid, lane);
      __builtin_amdgcn_sched_barrier(0);
      rsrc_t rx = mk_rsrc(ws + WS_X2 + ((size_t)(blockIdx.x ^ 4) * 4 + wid) * 4096, 4096);
      rsrc_t rf = mk_rsrc(ws + WS_FL + ((size_t)(3 * 16 + tt) * 64 + (cell ^ 32)) * 64, 64);
      u32x4 rv[4];
      u32 fv;
      C.step<0, 2, 2>(acc); C.step<1, 2, 2>(acc); C.step<2, 2, 2>(acc);
      if (K_XF == 3) flag_set(ws, FL_X2, tt, cell);
      C.step<3, 2, 2>(acc);
      if (K_XF == 4) flag_set(ws, FL_X2, tt, cell);
      C.step<0, 2, 2>(acc); C.step<1, 2, 2>(acc);
      flag_request(rf, 0, fv);
      __builtin_amdgcn_sched_barrier(0);
      C.step<2, 2, 2>(acc);
      wait_vm<0>();
      flag_wait(rf, 0, fv);
      xch_request(rx, rv, lane);
      __builtin_amdgcn_sched_barrier(0);
      C.load<3, 1, 2>(); C.mma<0, 2>(acc); C.interleave<8, 2>();
      C.mma<1, 2>(acc);
      STAMP(6)
      xch_finish(rv);
            rsrc_t rh = mk_rsrc(ws + WS_H + ((size_t)(tt * 32 + 4 * et + 2 * wP) * 8 + m0) * 1024, 16384);
      rsrc_t rs = mk_rsrc(ws + WS_ST + ((size_t)(tt * 16 + 2 * et + wP) * 128 + 16 * m0) * 8, 256);
#pragma unroll
      for (int jj = 0; jj < 2; jj++) {
        float s = 0.f, q = 0.f;
#pragma unroll
        for (int u = 0; u < 2; u++) {
          const u32x4 o = rv[jj * 2 + u];
          const f32x4 a = acc[2 * u][jj], b = acc[2 * u + 1][jj];
          const int xrow = 32 * wQ + 16 * jj + c;
          const LAS char* xp = sm + (XOFF + wP * 8192 + xrow * 128 + (g & 1) * 8);
          const u32x2 xa = *(const LAS u32x2*)(xp + (((4 * u + (g >> 1)) ^ ((xrow >> 1) & 7)) * 16));
          const u32x2 xb = *(const LAS u32x2*)(xp + (((4 * u + 2 + (g >> 1)) ^ ((xrow >> 1) & 7)) * 16));
          u32x4 h;
          h[0] = cvtpk(a[0] + blo(o[0]) + blo(xa[0]), a[1] + bhi(o[0]) + bhi(xa[0]));
          h[1] = cvtpk(a[2] + blo(o[1]) + blo(xa[1]), a[3] + bhi(o[1]) + bhi(xa[1]));
          h[2] = cvtpk(b[0] + blo(o[2]) + blo(xb[0]), b[1] + bhi(o[2]) + bhi(xb[0]));
          h[3] = cvtpk(b[2] + blo(o[3]) + blo(xb[1]), b[3] + bhi(o[3]) + bhi(xb[1]));
          store_sc1(rh, (u * 8 + jj) * 1024 + lane * 16, h);
          hk[jj][u] = h;
#pragma unroll
          for (int e = 0; e < 4; e++) { const float lo = blo(h[e]), hi = bhi(h[e]); s += lo + hi; q += lo * lo + hi * hi; }
        }
        s = xsum32(xsum16(s)); q = xsum32(xsum16(q));
        if (g == 0) __builtin_amdgcn_raw_buffer_store_b64((u32x2){__float_as_uint(s), __float_as_uint(q)}, rs, (16 * jj + c) * 8, 0, 16);
      }
      flag_set(ws, FL_H, tt, cell);
      STAMP(8)
      bar();
      bar();
    }
    rsrc_t rr = mk_rsrc(ws + WS_R + ((size_t)(tt * 112 + 7 * ft) * 8 + 4 * wQ) * 1024, 7 * 8192);
    float ra[4], rb[4];
    auto act = [&](const f32x4& a, int col, int j, u32& lo, u32& hi) {
      const f32x4 cs = *(const LAS f32x4*)(tabC + col), bi = *(const LAS f32x4*)(tabBias + col);
      f32x4 v = ra[j] * a + (rb[j] * cs + bi);
#pragma unroll
      for (int e = 0; e < 4; e++) { v[e] = v[e] > 0.f ? v[e] : 0.f; v[e] *= v[e]; }
      lo = cvtpk(v[0], v[1]); hi = cvtpk(v[2], v[3]);
    };
    {
      Consumer<4, M_TR256, M_TILED> C;
      C.init(sm, wP, wQ, lane);
      f32x4 acc[4][4];
      C.zero(acc);
      bar();
      STAMP(9)
      C.first();
      for (int it = 0; it < 3; it++) {
        C.step4<4, 4>(acc);
      }
      C.last4<4>(acc);
      STAMP(10)
#pragma unroll
      for (int j = 0; j < 4; j++) { ra[j] = tabA[64 * wQ + 16 * j + c]; rb[j] = tabB[64 * wQ + 16 * j + c]; }
#pragma unroll
      for (int u = 0; u < 2; u++)
#pragma unroll
        for (int j = 0; j < 4; j++) {
          u32 a0, a1, b0, b1_;
          act(acc[2 * u][j], 64 * wP + 32 * u + 4 * g, j, a0, a1);
          act(acc[2 * u + 1][j], 64 * wP + 32 * u + 16 + 4 * g, j, b0, b1_);
          store_sc1(rr, ((2 * wP + u) * 8 + j) * 1024 + lane * 16, (u32x4){a0, a1, b0, b1_});
        }
    }
    {
      Consumer<3, M_TR192, M_TILED> C;
      C.init(sm, wP, wQ, lane);
      f32x4 acc[3][4];
      C.zero(acc);
      bar();
      STAMP(11)
      C.first();
      for (int it = 0; it < 3; it++) {
        C.step4<4, 4>(acc);
        if (it == K_FLAGIT) flag_set(ws, FL_RA, tt, cell);
      }
      C.last4<4>(acc);
      STAMP(12)
#pragma unroll
      for (int j = 0; j < 4; j++) {
        u32 a0, a1, b0, b1_;
        act(acc[0][j], 128 + 64 * wP + 4 * g, j, a0, a1);
        act(acc[1][j], 128 + 64 * wP + 16 + 4 * g, j, b0, b1_);
        store_sc1(rr, ((4 + 2 * wP) * 8 + j) * 1024 + lane * 16, (u32x4){a0, a1, b0, b1_});
        act(acc[2][j], 160 + 16 * wP + 4 * g, j, a0, a1);
        __builtin_amdgcn_raw_buffer_store_b64((u32x2){a0, a1}, rr, (5 * 8 + j) * 1024 + lane * 16 + 8 * wP, 0, 16);
      }
    }
    {
      Consumer<4, M_TR256, M_TILED> C;
      C.init(sm, wP, wQ, lane);
      f32x4 acc[4][4];
      C.zero(acc);
      bar();
      STAMP(13)
      C.first();
      for (int it = 0; it < 5; it++) {
        C.step4<4, 4>(acc);
        if (it == K_FLAGIT) flag_set(ws, FL_RB, tt, cell);
      }
      C.step4<4, 2>(acc);
      STAMP(14)
      xch_send(ws + WS_X4, acc, wid, lane);
      u32x2 br[4];
#pragma unroll
      for (int i = 0; i < 4; i++) br[i] = *(const u32x2*)(b2 + et * 128 + 64 * wP + 16 * i + 4 * g);
      __builtin_amdgcn_sched_barrier(0);
      rsrc_t rx = mk_rsrc(ws + WS_X4 + ((size_t)(blockIdx.x ^ 4) * 4 + wid) * 4096, 4096);
      rsrc_t rf = mk_rsrc(ws + WS_FL + ((size_t)(4 * 16 + tt) * 64 + (cell ^ 32)) * 64, 64);
      u32x4 rv[4];
      u32 fv;
      C.step<0, 2, 2>(acc); C.step<1, 2, 2>(acc); C.step<2, 2, 2>(acc);
      if (K_XF == 3) flag_set(ws, FL_X4, tt, cell);
      C.step<3, 2, 2>(acc);
      if (K_XF == 4) flag_set(ws, FL_X4, tt, cell);
      C.step<0, 2, 2>(acc); C.step<1, 2, 2>(acc);
      flag_request(rf, 0, fv);
      __builtin_amdgcn_sched_barrier(0);
      C.step<2, 2, 2>(acc);
      wait_vm<0>();
      flag_wait(rf, 0, fv);
      xch_request(rx, rv, lane);
      __builtin_amdgcn_sched_barrier(0);
      C.load<3, 1, 2>(); C.mma<0, 2>(acc); C.interleave<8, 2>();
      C.mma<1, 2>(acc);
      xch_finish(rv);

      rsrc_t ro = mk_rsrc(out + (size_t)(tt * 128 + 16 * m0) * D_ + et * 128 + 64 * wP, 31 * D_ * 2 + 128);
#pragma unroll
      for (int jj = 0; jj < 2; jj++)
#pragma unroll
        for (int u = 0; u < 2; u++) {
          const u32x4 o = rv[jj * 2 + u], h = hk[jj][u];
          const f32x4 a = acc[2 * u][jj], b = acc[2 * u + 1][jj];
          const u32x2 ba = br[2 * u], bb = br[2 * u + 1];
          const u32 a0 = cvtpk(a[0] + blo(o[0]) + blo(ba[0]) + blo(h[0]), a[1] + bhi(o[0]) + bhi(ba[0]) + bhi(h[0]));
          const u32 a1 = cvtpk(a[2] + blo(o[1]) + blo(ba[1]) + blo(h[1]), a[3] + bhi(o[1]) + bhi(ba[1]) + bhi(h[1]));
          const u32 b0 = cvtpk(b[0] + blo(o[2]) + blo(bb[0]) + blo(h[2]), b[1] + bhi(o[2]) + bhi(bb[0]) + bhi(h[2]));
          const u32 b1_ = cvtpk(b[2] + blo(o[3]) + blo(bb[1]) + blo(h[3]), b[3] + bhi(o[3]) + bhi(bb[1]) + bhi(h[3]));
          const auto s0 = __builtin_amdgcn_permlane16_swap(a0, b0, false, false);
          const auto s1 = __builtin_amdgcn_permlane16_swap(a1, b1_, false, false);
          __builtin_amdgcn_raw_buffer_store_b128((u32x4){s0[0], s1[0], s0[1], s1[1]}, ro,
                                                 (16 * jj + c) * (D_ * 2) + (32 * u + 16 * (g & 1) + 8 * (g >> 1)) * 2, 0, K_OUTSC1 ? 16 : 0);
        }
      DIAG_END(15)
    }
  }
}

__global__ void t2_kernel(char* ws) { const u64 lim = *(const volatile u64*)(ws + (60u << 20)) + 20000; while (__builtin_amdgcn_s_memrealtime() < lim) __builtin_amdgcn_s_sleep(2); }
__global__ void t1_kernel(char* ws) { if (threadIdx.x == 0) *(u64*)(ws + (60u << 20)) = __builtin_amdgcn_s_memrealtime(); }
}

extern "C" void kernel_launch(void* const* d_in, const int* in_sizes, int n_in, void* d_out, int out_size, void* d_ws, size_t ws_size,
                              hipStream_t stream) {
  const u16* const* I = (const u16* const*)d_in;
  (void)hipFuncSetAttribute((const void*)fused_kernel, hipFuncAttributeMaxDynamicSharedMemorySize, LDS_BYTES);
  t1_kernel<<<1, 64, 0, stream>>>((char*)d_ws);
  fused_kernel<<<256, 512, LDS_BYTES, stream>>>(I[0], I[1], I[2], I[3], I[4], I[6], I[7], I[8], I[9], (char*)d_ws, (u16*)d_out);
  t2_kernel<<<1, 64, 0, stream>>>((char*)d_ws);
}
```

```cpp
#include <hip/hip_runtime.h>
#include <hip/hip_bf16.h>

typedef unsigned short u16;
typedef unsigned int u32;
typedef unsigned long long u64;
typedef __bf16 bf16x8 __attribute__((ext_vector_type(8)));
typedef __bf16 bf16x2 __attribute__((ext_vector_type(2)));
typedef short s16x4 __attribute__((ext_vector_type(4)));
typedef short s16x8 __attribute__((ext_vector_type(8)));
typedef float f32x2 __attribute__((ext_vector_type(2)));
typedef float f32x4 __attribute__((ext_vector_type(4)));
typedef u32 u32x2 __attribute__((ext_vector_type(2)));
typedef u32 u32x4 __attribute__((ext_vector_type(4)));
typedef __amdgpu_buffer_rsrc_t rsrc_t;
#define LAS __attribute__((address_space(3)))
#define DI __device__ __forceinline__

#define K_FLAGIT 0
#define K_SIDE 1
#define K_PFC0 99
#define K_PFC1 99
#define K_ORD 3
#define K_PRIO 0
#define K_NOCS 0
#define K_CSN 12
#define K_WN 0
#define K_FQ 2
#define K_PFW 8
#define K_PFPHI 1
#define K_XF 4
#define K_SLEEP 1
#define K_OUTSC1 1
namespace {

constexpr int S_ = 2048, D_ = 1024, F_ = 3584;
constexpr float LN_EPS = 1e-5f;

constexpr size_t WS_AB = 0;
constexpr size_t WS_H = 8u << 20;
constexpr size_t WS_R = 12u << 20;
constexpr size_t WS_X2 = 27u << 20;
constexpr size_t WS_X4 = 31u << 20;
constexpr size_t WS_ST = 35u << 20;
constexpr size_t WS_CS = 36u << 20;
constexpr size_t WS_FL = 37u << 20;

#define DIAG_ARG
#define DIAG_BEGIN
#define STAMP(i)
#define PSTAMP(i)
#define PDIAG_END
#define DIAG_END(i)

DI u32 rfl(u32 x) { return __builtin_amdgcn_readfirstlane(x); }
DI u32 cvtpk(float lo, float hi) { f32x2 v = {lo, hi}; return __builtin_bit_cast(u32, __builtin_convertvector(v, bf16x2)); }
DI float blo(u32 v) { return __uint_as_float(v << 16); }
DI float bhi(u32 v) { return __uint_as_float(v & 0xFFFF0000u); }
DI rsrc_t mk_rsrc(const void* p, u32 bytes) { return __builtin_amdgcn_make_buffer_rsrc((void*)p, 0, bytes, 0x00020000); }
DI void bar() { asm volatile("s_barrier" ::: "memory"); }
DI void wait_lgkm0() { __builtin_amdgcn_s_waitcnt(0xC07F); }
template <int N> DI void wait_vm() { asm volatile("s_waitcnt vmcnt(%0)" ::"i"(N) : "memory"); }

template <int N> DI void dma(u32 m0, const u32* v, rsrc_t r, u32 so) {
  if constexpr (N == 4)
    asm volatile("s_mov_b32 m0, %0\n\ts_nop 0\n\t"
                 "buffer_load_dwordx4 %1, %5, %6 offen lds\n\t"
                 "buffer_load_dwordx4 %2, %5, %6 offen offset:1024 lds\n\t"
                 "buffer_load_dwordx4 %3, %5, %6 offen offset:2048 lds\n\t"
                 "buffer_load_dwordx4 %4, %5, %6 offen offset:3072 lds"
                 ::"s"(m0), "v"(v[0]), "v"(v[1]), "v"(v[2]), "v"(v[3]), "s"(r), "s"(so) : "memory");
  else if constexpr (N == 3)
    asm volatile("s_mov_b32 m0, %0\n\ts_nop 0\n\t"
                 "buffer_load_dwordx4 %1, %4, %5 offen lds\n\t"
                 "buffer_load_dwordx4 %2, %4, %5 offen offset:1024 lds\n\t"
                 "buffer_load_dwordx4 %3, %4, %5 offen offset:2048 lds"
                 ::"s"(m0), "v"(v[0]), "v"(v[1]), "v"(v[2]), "s"(r), "s"(so) : "memory");
  else
    asm volatile("s_mov_b32 m0, %0\n\ts_nop 0\n\t"
                 "buffer_load_dwordx4 %1, %3, %4 offen lds\n\t"
                 "buffer_load_dwordx4 %2, %3, %4 offen offset:1024 lds"
                 ::"s"(m0), "v"(v[0]), "v"(v[1]), "s"(r), "s"(so) : "memory");
}
template <int N> DI void dma_sc1(u32 m0, u32 v, rsrc_t r, u32 so) {
  if constexpr (N == 4)
    asm volatile("s_mov_b32 m0, %0\n\ts_nop 0\n\t"
                 "buffer_load_dwordx4 %1, %2, %3 offen sc1 lds\n\t"
                 "buffer_load_dwordx4 %1, %2, %3 offen offset:1024 sc1 lds\n\t"
                 "buffer_load_dwordx4 %1, %2, %3 offen offset:2048 sc1 lds\n\t"
                 "buffer_load_dwordx4 %1, %2, %3 offen offset:3072 sc1 lds"
                 ::"s"(m0), "v"(v), "s"(r), "s"(so) : "memory");
  else
    asm volatile("s_mov_b32 m0, %0\n\ts_nop 0\n\t"
                 "buffer_load_dwordx4 %1, %2, %3 offen sc1 lds\n\t"
                 "buffer_load_dwordx4 %1, %2, %3 offen offset:1024 sc1 lds"
                 ::"s"(m0), "v"(v), "s"(r), "s"(so) : "memory");
}

DI float xsum16(float v) { const auto r = __builtin_amdgcn_permlane16_swap(__float_as_uint(v), __float_as_uint(v), false, false); return __uint_as_float(r[0]) + __uint_as_float(r[1]); }
DI float xsum32(float v) { const auto r = __builtin_amdgcn_permlane32_swap(__float_as_uint(v), __float_as_uint(v), false, false); return __uint_as_float(r[0]) + __uint_as_float(r[1]); }

DI void pull_lines(u32 m0, u32 v, rsrc_t r, u32 so) {
  asm volatile("s_mov_b32 m0, %0\n\ts_nop 0\n\tbuffer_load_dword %1, %2, %3 offen lds" ::"s"(m0), "v"(v), "s"(r), "s"(so) : "memory");
}

DI int kstep(int t, int n, int phi) {
  int k = t;
  if ((t & 3) == phi) { k = t + 4; if (k >= n) k -= n; }
  return k;
}

DI int kstep2(int t, int n, int phi, int side) {
  const int k = kstep(t, n, phi), k2 = k ^ ((K_SIDE ? side : 0) << 2);
  return k2 < n ? k2 : k;
}

constexpr int NST = 4;
constexpr int SLOT = 16384;
constexpr int QOFF = NST * SLOT;
constexpr int TOFF = 2 * NST * SLOT;
constexpr int COFF = TOFF + 4096;
constexpr int XOFF = COFF + 4096;
constexpr int LDS_BYTES = XOFF + 16384;

enum { M_ROW = 0, M_TILED = 1, M_TR192 = 2, M_TR256 = 3 };

template <int PT, int PMODE, int QMODE>
struct Consumer {
  const LAS char* pb[4];
  const LAS char* qb[4];
  bf16x8 fp[2][PT], fq[2][4];

  DI void init(const LAS char* sm, int wP, int wQ, int lane, int prot = 0, int qrot = 0) {
    const int r = lane & 15, g = lane >> 4;
    if constexpr (PMODE == M_ROW) {
      const int a = (64 * wP + r) * 128 + ((g ^ (r >> 1)) * 16);
      pb[0] = sm + (a + prot * 4096); pb[1] = sm + ((a ^ 64) + prot * 4096);
      pb[2] = sm + (a + 4096 - prot * 4096); pb[3] = sm + ((a ^ 64) + 4096 - prot * 4096);
    } else if constexpr (PMODE == M_TILED) {
      pb[0] = sm + (4 * wP * 1024 + lane * 16);
    } else if constexpr (PMODE == M_TR192) {
      const int b = g & 1, a = (4 * g + (r >> 2)) * 192 + (r & 3) * 8;
      pb[0] = sm + (a + (4 * wP + b) * 32);
      pb[1] = sm + (a + (4 * wP + 1 - b) * 32);
      pb[2] = sm + (a + ((2 + wP) ^ b) * 32);
    } else {
      const int k = 4 * g + (r >> 2), a = k * 256 + (r & 3) * 8;
#pragma unroll
      for (int i = 0; i < 4; i++) pb[i] = sm + (a + (((4 * wP + i) ^ (k & 7)) * 32));
    }
    if constexpr (QMODE == M_ROW) {
      const int a = QOFF + (64 * wQ + r) * 128 + ((g ^ (r >> 1)) * 16);
      qb[0] = sm + (a + qrot * 4096); qb[1] = sm + ((a ^ 64) + qrot * 4096);
      qb[2] = sm + (a + 4096 - qrot * 4096); qb[3] = sm + ((a ^ 64) + 4096 - qrot * 4096);
      asm volatile("" : "+v"(qb[0]), "+v"(qb[1]), "+v"(qb[2]), "+v"(qb[3]));
    } else {
      qb[0] = sm + (QOFF + 4 * wQ * 1024 + lane * 16);
      asm volatile("" : "+v"(qb[0]));
    }
  }
  static DI bf16x8 tr2(const LAS char* p, int second) {
    s16x4 a = __builtin_amdgcn_ds_read_tr16_b64_v4i16((LAS s16x4*)p);
    s16x4 b = __builtin_amdgcn_ds_read_tr16_b64_v4i16((LAS s16x4*)(p + second));
    return __builtin_bit_cast(bf16x8, __builtin_shufflevector(a, b, 0, 1, 2, 3, 4, 5, 6, 7));
  }
  template <int S, int KS> DI bf16x8 ldp(int i) {
    if constexpr (PMODE == M_ROW) return *(const LAS bf16x8*)(pb[KS + (i & 2)] + (S * SLOT + (i & 1) * 2048));
    else if constexpr (PMODE == M_TILED) return *(const LAS bf16x8*)(pb[0] + (S * SLOT + KS * 8192 + i * 1024));
    else if constexpr (PMODE == M_TR192) return tr2(pb[i] + (S * SLOT + KS * 32 * 192), 16 * 192);
    else return tr2(pb[i] + (S * SLOT + KS * 32 * 256), 16 * 256);
  }
  template <int S, int KS> DI bf16x8 ldq(int j) {
    if constexpr (QMODE == M_ROW) return *(const LAS bf16x8*)(qb[KS + (j & 2)] + (S * SLOT + (j & 1) * 2048));
    else return *(const LAS bf16x8*)(qb[0] + (S * SLOT + KS * 8192 + j * 1024));
  }
  template <int S, int KS, int QN> DI void load() {
    fp[KS][0] = ldp<S, KS>(0);
#pragma unroll
    for (int j = 0; j < QN; j++) fq[KS][j] = ldq<S, KS>(j);
#pragma unroll
    for (int i = 1; i < PT; i++) fp[KS][i] = ldp<S, KS>(i);
  }
  template <int KS, int QN> DI void mma(f32x4 (&acc)[PT][4]) {
#pragma unroll
    for (int n = 0; n < PT * QN; n++) {
      int i, j;
      if (K_ORD == 0) { i = n / QN; j = n % QN; }
      else if (K_ORD == 1) { i = n / QN; j = (i & 1) ? QN - 1 - n % QN : n % QN; }
      else if (K_ORD == 2) { j = n / PT; i = n % PT; }
      else { j = n / PT; i = (j & 1) ? PT - 1 - n % PT : n % PT; }
      acc[i][j] = __builtin_amdgcn_mfma_f32_16x16x32_bf16(fp[KS][i], fq[KS][j], acc[i][j], 0, 0, 0);
    }
  }
  template <int KS> DI void stats(f32x4 (&sx)[2], f32x4 (&sxx)[2], f32x4 (&sd)[2], bf16x8 ones) {
#pragma unroll
    for (int i = 0; i < 2; i++) {
      sx[i] = __builtin_amdgcn_mfma_f32_16x16x32_bf16(fp[KS][i], ones, sx[i], 0, 0, 0);
      sxx[i] = __builtin_amdgcn_mfma_f32_16x16x32_bf16(fp[KS][i], fp[KS][i], sxx[i], 0, 0, 0);
      sd[i] = __builtin_amdgcn_mfma_f32_16x16x32_bf16(ones, fq[KS][i], sd[i], 0, 0, 0);
    }
  }
  template <int NM, int QN> static DI void interleave() {
    constexpr int NR = PT * (PMODE >= M_TR192 ? 2 : 1) + QN;
    if constexpr (NR <= NM) {
#pragma unroll
      for (int i = 0; i < NR; i++) { __builtin_amdgcn_sched_group_barrier(0x8, 1, 0); __builtin_amdgcn_sched_group_barrier(0x100, 1, 0); }
      __builtin_amdgcn_sched_group_barrier(0x8, NM - NR, 0);
    } else {
#pragma unroll
      for (int i = 0; i < NR / 2; i++) { __builtin_amdgcn_sched_group_barrier(0x8, 1, 0); __builtin_amdgcn_sched_group_barrier(0x100, 2, 0); }
      __builtin_amdgcn_sched_group_barrier(0x8, NM - NR / 2, 0);
    }
    __builtin_amdgcn_sched_barrier(0);
  }
  template <int S, int QC, int QN> DI void step(f32x4 (&acc)[PT][4]) {
    load<S, 1, QC>(); mma<0, QC>(acc); interleave<PT * QC, QC>();
    wait_lgkm0(); bar();
    __builtin_amdgcn_sched_barrier(0);
    load<(S + 1) % NST, 0, QN>(); mma<1, QC>(acc); interleave<PT * QC, QN>();
  }
  template <int QC, int QN> DI void step4(f32x4 (&acc)[PT][4]) {
    step<0, QC, QC>(acc); step<1, QC, QC>(acc); step<2, QC, QC>(acc); step<3, QC, QN>(acc);
  }
  template <int QC> DI void last4(f32x4 (&acc)[PT][4]) {
    step<0, QC, QC>(acc); step<1, QC, QC>(acc); step<2, QC, QC>(acc);
    load<3, 1, QC>(); mma<0, QC>(acc); interleave<PT * QC, QC>();
    mma<1, QC>(acc);
  }
  static DI void zero(f32x4 (&acc)[PT][4]) {
#pragma unroll
    for (int i = 0; i < PT; i++)
#pragma unroll
      for (int j = 0; j < 4; j++) { acc[i][j] = (f32x4){0.f, 0.f, 0.f, 0.f}; asm volatile("" : "+v"(acc[i][j])); }
  }
  DI void first() { load<0, 0, 4>(); }
};

DI u32 row_src(int imgrow, int srcrow, int lane, int ldbytes) {
  return srcrow * ldbytes + (((lane & 7) ^ ((imgrow >> 1) & 7)) * 16);
}
DI void store_sc1(rsrc_t r, u32 off, u32x4 v) { __builtin_amdgcn_raw_buffer_store_b128(v, r, off, 0, 16); }

DI void xch_send(char* x, const f32x4 (&acc)[4][4], int wid, int lane) {
  rsrc_t r = mk_rsrc(x + ((size_t)blockIdx.x * 4 + wid) * 4096, 4096);
#pragma unroll
  for (int jj = 0; jj < 2; jj++)
#pragma unroll
    for (int u = 0; u < 2; u++) {
      const f32x4 a = acc[2 * u][2 + jj], b = acc[2 * u + 1][2 + jj];
      u32x4 v = {cvtpk(a[0], a[1]), cvtpk(a[2], a[3]), cvtpk(b[0], b[1]), cvtpk(b[2], b[3])};
      store_sc1(r, lane * 16 + (jj * 2 + u) * 1024, v);
    }
}
DI void xch_request(rsrc_t r, u32x4 (&v)[4], int lane) {
  asm volatile("buffer_load_dwordx4 %0, %4, %5, 0 offen sc1\n\t"
               "buffer_load_dwordx4 %1, %4, %5, 0 offen offset:1024 sc1\n\t"
               "buffer_load_dwordx4 %2, %4, %5, 0 offen offset:2048 sc1\n\t"
               "buffer_load_dwordx4 %3, %4, %5, 0 offen offset:3072 sc1"
               : "=&v"(v[0]), "=&v"(v[1]), "=&v"(v[2]), "=&v"(v[3]) : "v"(lane * 16), "s"(r) : "memory");
}
DI void xch_finish(u32x4 (&v)[4]) { asm volatile("s_waitcnt vmcnt(0)" : "+v"(v[0]), "+v"(v[1]), "+v"(v[2]), "+v"(v[3])::"memory"); }

enum { FL_H = 0, FL_RA = 1, FL_RB = 2, FL_X2 = 3, FL_X4 = 4, FL_ACK = 5, FL_TICK = 6 };
DI void flag_set(char* ws, int which, int tt, int cell) {
  wait_vm<0>();
  if ((threadIdx.x & 63) == 0)
    __hip_atomic_store((u32*)(ws + WS_FL + ((size_t)(which * 16 + tt) * 64 + cell) * 64), 1u, __ATOMIC_RELAXED, __HIP_MEMORY_SCOPE_AGENT);
}
DI void flag_request(rsrc_t r, u32 off, u32& v) {
  asm volatile("buffer_load_dword %0, %1, %2, 0 offen sc1" : "=&v"(v) : "v"(off), "s"(r) : "memory");
}
DI void flag_wait(rsrc_t r, u32 off, u32& v) {
  asm volatile("" : "+v"(v)::"memory");
#pragma unroll 1
  for (int spin = 0; spin < (1 << 20); spin++) {
    if (__builtin_amdgcn_ballot_w64(v != 1u) == 0) break;
    __builtin_amdgcn_s_sleep(K_SLEEP);
    asm volatile("buffer_load_dword %0, %1, %2, 0 offen sc1\n\ts_waitcnt vmcnt(0)" : "=&v"(v) : "v"(off), "s"(r) : "memory");
  }
}

DI void word_store(rsrc_t r, u32 off, u32 v) { __builtin_amdgcn_raw_buffer_store_b32(v, r, off, 0, 16); }
template <class Done>
DI void watch(rsrc_t r, u32 off, u32& v, Done done, u32 ownoff, u32 ownval, u32& bump, bool& ticked, u64 ownmask) {
  asm volatile("" : "+v"(v)::"memory");
#pragma unroll 1
  for (int spin = 0; spin < (1 << 20); spin++) {
    const u64 miss = __builtin_amdgcn_ballot_w64(!done(v));
    if (miss == 0) break;
    ticked = ticked || (miss & ownmask) == 0;
    if (ticked && (spin & 63) == 63) {
      bump++;
      if ((ownmask >> (threadIdx.x & 63)) & 1) word_store(r, ownoff, ownval + bump);
    }
    __builtin_amdgcn_s_sleep(K_SLEEP);
    asm volatile("buffer_load_dword %0, %1, %2, 0 offen sc1\n\ts_waitcnt vmcnt(0)" : "=&v"(v) : "v"(off), "s"(r) : "memory");
  }
}

__global__ __launch_bounds__(512, 2) void fused_kernel(const u16* __restrict__ x, const u16* __restrict__ cre, const u16* __restrict__ cim,
                                                    const u16* __restrict__ dre, const u16* __restrict__ dim,
                                                    const u16* __restrict__ w1, const u16* __restrict__ b1, const u16* __restrict__ w2,
                                                    const u16* __restrict__ b2, char* __restrict__ ws, u16* __restrict__ out DIAG_ARG) {
  extern __shared__ __attribute__((aligned(16))) char smem[];
  DIAG_BEGIN
  asm volatile("" ::"s"(x), "s"(cre), "s"(cim), "s"(dre), "s"(dim), "s"(w1), "s"(b1), "s"(w2), "s"(b2), "s"(ws), "s"(out));
  const int lane = threadIdx.x & 63, wid = rfl(threadIdx.x >> 6);
  const int xcd = blockIdx.x & 7, loc = blockIdx.x >> 3, li = loc >> 2, lj = loc & 3;
  const int half = xcd >> 2, et = ((xcd >> 1) & 1) * 4 + lj, tt = (xcd & 1) * 8 + li, ft = (xcd >> 1) * 4 + lj;
  LAS char* sm = (LAS char*)smem;
  LAS float* tabA = (LAS float*)(sm + TOFF);
  LAS float* tabB = tabA + 128;
  LAS float* tabC = tabA + 256;
  LAS float* tabBias = tabA + 512;
  if (__builtin_expect(wid >= 4, 1)) {
    const int p = wid - 4, phi = (li + lj) & 3;
    const u32 lds0 = (u32)(size_t)smem, ldsb = lds0 + p * 4096, vlin = p * 4096 + lane * 16;
    rsrc_t rall = mk_rsrc(ws + WS_FL, 7 * 65536);
    const u32 colcell = ((lane >> 2) * 64 + ft * 4 + (lane & 3)) * 64, rowcell = (tt * 64 + (lane & 15) * 4) * 64;
    const u64 ownmask = 15ull << (4 * tt);
    u32 col0, row0, fv, bump = 1;
    bool ticked = false;
    {
      rsrc_t rp = mk_rsrc((const char*)x + (size_t)tt * 128 * D_ * 2 - 4096, 128 * D_ * 2 + 4096);
      rsrc_t rq = mk_rsrc((half ? dim : dre) + (size_t)et * 128 * D_, 128 * D_ * 2);
      u32 vp[4], vq[4], vx[2];
#pragma unroll
      for (int i = 0; i < 4; i++) {
        const int row = 32 * p + 8 * i + (lane >> 3), u = (row >> 4) & 1, ii = row & 15;
        vp[i] = row_src(row, 32 * p + 8 * (ii >> 2) + 4 * u + (ii & 3), lane, D_ * 2) + 4096 - i * 1024;
        vq[i] = row_src(row, row, lane, D_ * 2) - i * 1024;
      }
#pragma unroll
      for (int i = 0; i < 2; i++) {
        const int row = 16 * p + 8 * i + (lane >> 3);
        vx[i] = row_src(row, 64 * (row >> 5) + 32 * half + (row & 31), lane, D_ * 2) + 4096 - i * 1024;
      }
      auto issue = [&](int g) {
        const int k = kstep2(g, 16, phi, (xcd >> 1) ^ (xcd & 1));
        const u32 m = ldsb + (g & 3) * SLOT;
        dma<4>(m, vp, rp, k * 128);
        dma<4>(m + QOFF, vq, rq, k * 128);
        if ((k >> 1) == et) dma<2>(lds0 + XOFF + (k & 1) * 8192 + p * 2048, vx, rp, k * 128);
      };
      issue(0); issue(1); issue(2);
      wait_vm<16>(); bar();
#pragma unroll 1
      for (int g = 3; g < 6; g++) { issue(g); wait_vm<16>(); bar(); }
      flag_request(rall, FL_TICK * 65536 + colcell, col0);
      flag_request(rall, FL_TICK * 65536 + rowcell, row0);
#pragma unroll 1
      for (int g = 6; g < 16; g++) { issue(g); wait_vm<16>(); bar(); }
      asm volatile("" : "+v"(col0), "+v"(row0)::"memory");
    }
    {
      rsrc_t rp = mk_rsrc(ws + WS_AB + (size_t)et * 32 * 16384, 9 * 32 * 16384);
      rsrc_t rqo = mk_rsrc((const char*)(half ? cim : cre) + (size_t)tt * 128 * S_ * 2 - 4096, 128 * S_ * 2 + 4096);
      rsrc_t rq0 = mk_rsrc((const char*)cre + (size_t)tt * 128 * S_ * 2 - 4096, 128 * S_ * 2 + 4096);
      rsrc_t rq1 = mk_rsrc((const char*)cim + (size_t)tt * 128 * S_ * 2 - 4096, 128 * S_ * 2 + 4096);
      u32 vq[4], vh[2];
#pragma unroll
      for (int i = 0; i < 4; i++) {
        const int row = 32 * p + 8 * i + (lane >> 3);
        vq[i] = row_src(row, row ^ (half * 32), lane, S_ * 2) + 4096 - i * 1024;
      }
#pragma unroll
      for (int i = 0; i < 2; i++) {
        const int row = 64 * (p >> 1) + 16 * (p & 1) + 8 * i + (lane >> 3);
        vh[i] = row_src(row, row ^ (half * 32), lane, S_ * 2) + 4096 - i * 1024;
      }
      const u32 ldsh = lds0 + QOFF + (p >> 1) * 8192 + (p & 1) * 2048;
      auto fp = [&](int g) { dma_sc1<4>(ldsb + (g & 3) * SLOT, vlin, rp, (half * 256 + kstep2(g, 28, phi, (xcd >> 1) & 1)) * 16384); };
      auto fq = [&](int g) { dma<4>(ldsb + QOFF + (g & 3) * SLOT, vq, rqo, kstep2(g, 28, phi, (xcd >> 1) & 1) * 128); };
      auto halfs = [&](int g) {
        const int k = kstep2(g, 8, phi, (xcd >> 1) & 1), term = k >> 2, ks = 28 + (k & 3);
        dma_sc1<4>(ldsb + (g & 3) * SLOT, vlin, rp, (term * 256 + ks) * 16384);
        if (term) dma<2>(ldsh + (g & 3) * SLOT, vh, rq1, ks * 128);
        else dma<2>(ldsh + (g & 3) * SLOT, vh, rq0, ks * 128);
      };
      auto full = [&](int g) { fp(g); fq(g); };
      auto run = [&](int g0, int g1) {
#pragma unroll 1
        for (int g = g0; g < g1; g++) { full(g); wait_vm<16>(); bar(); }
      };
      if (K_FQ >= 1) fq(0);
      wait_vm<K_FQ >= 1 ? 12 : 8>(); bar();
      if (K_FQ >= 2) fq(1);
      wait_vm<K_FQ >= 2 ? 8 : K_FQ * 4>(); bar();
      bar();
      bar();
      PSTAMP(1)
      if (K_FQ < 1) fq(0);
      if (K_FQ < 2) fq(1);
      fq(2);
      bar();
      if (p == 0) {
        fv = col0;
        watch(rall, FL_TICK * 65536 + colcell, fv, [&](u32 w) { return w != col0; }, FL_TICK * 65536 + colcell, col0, bump, ticked, ownmask);
        ticked = true;
      }
      bar();
      PSTAMP(2)
      fp(0); fp(1); fp(2);
      wait_vm<8>();
      PSTAMP(9)
      bar();
      full(3); wait_vm<12>(); bar();
      const int g0 = 4 + loc % K_CSN;
      run(4, g0);
      {
        rsrc_t rw = mk_rsrc((const char*)w1 + (size_t)(tt * 64 + p * 16) * F_ * 2 + ft * 448, 15 * F_ * 2 + 448);
        u32x4 v[8];
        const int l28 = lane < 28 ? lane : lane < 56 ? lane - 28 : 27;
        const u32 vo = (lane >= 28 ? F_ * 2 : 0) + l28 * 16;
        full(g0);
#pragma unroll
        for (int i = 0; i < 8; i++)
          asm volatile("buffer_load_dwordx4 %0, %1, %2, %3 offen" : "=&v"(v[i]) : "v"(vo), "s"(rw), "s"(i * 2 * F_ * 2) : "memory");
        wait_vm<24>(); bar();
        full(g0 + 1); wait_vm<24>(); bar();
        full(g0 + 2); wait_vm<24>(); bar();
        full(g0 + 3);
        asm volatile("s_waitcnt vmcnt(16)" : "+v"(v[0]), "+v"(v[1]), "+v"(v[2]), "+v"(v[3]), "+v"(v[4]), "+v"(v[5]), "+v"(v[6]), "+v"(v[7])::"memory");
        bar();
        full(g0 + 4);
        float s[8] = {0, 0, 0, 0, 0, 0, 0, 0};
#pragma unroll
        for (int i = 0; i < 8; i++)
#pragma unroll
          for (int e = 0; e < 4; e++) { s[2 * e] += blo(v[i][e]); s[2 * e + 1] += bhi(v[i][e]); }
#pragma unroll
        for (int e = 0; e < 8; e++) s[e] += __shfl(s[e], lane + 28);
        if (lane < 28) {
          LAS float* d = (LAS float*)(sm + COFF) + p * 224 + lane * 8;
          *(LAS f32x4*)d = (f32x4){s[0], s[1], s[2], s[3]};
          *(LAS f32x4*)(d + 4) = (f32x4){s[4], s[5], s[6], s[7]};
        }
        wait_lgkm0();
        wait_vm<16>(); bar();
        if (p == 0 && lane < 56) {
          const LAS f32x4* d = (const LAS f32x4*)(sm + COFF) + lane;
          const f32x4 t = (d[0] + d[56]) + (d[112] + d[168]);
          store_sc1(mk_rsrc(ws + WS_CS + (size_t)tt * F_ * 4 + ft * 896, 896), lane * 16, __builtin_bit_cast(u32x4, t));
        }
      }
      run(g0 + 5, 20);
      flag_request(rall, FL_TICK * 65536 + rowcell, fv);
      full(20); wait_vm<16>(); bar();
      full(21); wait_vm<16>();
      asm volatile("" : "+v"(fv)::"memory");
      if (p == 0) {
        watch(rall, FL_TICK * 65536 + rowcell, fv, [&](u32 w) { return w != row0; }, FL_TICK * 65536 + colcell, col0, bump, ticked, ownmask);
        if (lane == 0) word_store(rall, FL_ACK * 65536 + (tt * 64 + ft * 4) * 64, 1u);
      }
      bar();
      run(22, 26);
      const u32 ackcell = FL_ACK * 65536 + ((lane & 16) ? rowcell : ((lane & 15) * 64 + ft * 4) * 64);
      flag_request(rall, ackcell, fv);
      full(26); wait_vm<16>(); bar();
      full(27); wait_vm<16>();
      PSTAMP(10)
      asm volatile("" : "+v"(fv)::"memory");
      if (p == 0) watch(rall, ackcell, fv, [&](u32 w) { return w == 1u; }, FL_TICK * 65536 + colcell, col0, bump, ticked, ownmask);
      PSTAMP(11)
      bar();
      halfs(0); wait_vm<14>(); bar();
#pragma unroll 1
      for (int g = 1; g < 8; g++) { halfs(g); wait_vm<12>(); bar(); }
    }
    rsrc_t rfl_ = mk_rsrc(ws + WS_FL + (size_t)tt * 4096, 3 * 16 * 4096);
    {
      rsrc_t rp = mk_rsrc((const char*)w1 + ft * 448, D_ * F_ * 2 - ft * 448);
      rsrc_t rq = mk_rsrc(ws + WS_H + (size_t)tt * 32 * 8192, 32 * 8192);
      u32 va[4], vb[3];
#pragma unroll
      for (int i = 0; i < 4; i++) {
        const int k = 16 * p + 4 * i + (lane >> 4);
        va[i] = k * (F_ * 2) + (((lane & 15) ^ ((k & 7) << 1)) * 16) - i * 1024;
      }
#pragma unroll
      for (int i = 0; i < 3; i++) {
        const int o = (p * 3 + i) * 64 + lane, k = o / 12, ch = o % 12;
        vb[i] = k * (F_ * 2) + 256 + ((ch ^ (((k >> 2) & 1) << 1)) * 16) - i * 1024;
      }
      rsrc_t rw = mk_rsrc(w2 + (size_t)(half * 1792 + tt * 112 + 28 * p) * D_ + et * 128, 27 * D_ * 2 + 256);
      const u32 vw = (lane >> 1) * (D_ * 2) + (lane & 1) * 128;
      auto pa = [&](int g) {
        if (K_WN > 0 && g == 4 + loc % (K_WN > 0 ? K_WN : 1)) pull_lines(lds0 + COFF + 3584, vw, rw, 0);
        dma<4>(ldsb + (g & 3) * SLOT, va, rp, kstep(g, 16, phi) * (64 * F_ * 2));
      };
      auto pbb = [&](int g) { dma<3>(lds0 + p * 3072 + (g & 3) * SLOT, vb, rp, kstep(g, 16, phi) * (64 * F_ * 2)); };
      auto q = [&](int g) { dma_sc1<4>(ldsb + QOFF + (g & 3) * SLOT, vlin, rq, kstep(g, 16, phi) * 16384); };
      wait_vm<6>(); bar();
      wait_vm<0>(); bar();
      bar();
      pa(0); pa(1); pa(2);
      const int pt = threadIdx.x - 256;
      rsrc_t rs = mk_rsrc(ws + WS_ST + (size_t)tt * 16384, 16384);
      rsrc_t rc = mk_rsrc(ws + WS_CS + ft * 896, 15 * F_ * 4 + 896);
      u32x2 sv[8];
      u32 cv[16], bv;
#pragma unroll
      for (int i = 0; i < 16; i++)
        asm volatile("buffer_load_dword %0, %1, %2, %3 offen sc1" : "=&v"(cv[i]) : "v"(min(pt, 223) * 4), "s"(rc), "s"(i * F_ * 4) : "memory");
      asm volatile("buffer_load_ushort %0, %1, %2, 0 offen" : "=&v"(bv) : "v"(min(pt, 223) * 2), "s"(mk_rsrc(b1 + ft * 224, 448)) : "memory");
      if (p == 0) { fv = 0; flag_wait(rfl_, lane * 64, fv); }
      bar();
      PSTAMP(3)
      q(0); q(1); q(2);
      const u32 so = (pt & 1) * 8192 + (pt >> 1) * 8;
#pragma unroll
      for (int i = 0; i < 8; i++)
        asm volatile("buffer_load_dwordx2 %0, %1, %2, %3 offen sc1" : "=&v"(sv[i]) : "v"(so), "s"(rs), "s"(i * 1024) : "memory");
      wait_vm<16>();
      PSTAMP(4)
      bar();
      pa(3); q(3); wait_vm<20>(); bar();
      pa(4); q(4); wait_vm<24>(); bar();
      pa(5); q(5); wait_vm<16>();
      asm volatile("" : "+v"(sv[0]), "+v"(sv[1]), "+v"(sv[2]), "+v"(sv[3]), "+v"(sv[4]), "+v"(sv[5]), "+v"(sv[6]), "+v"(sv[7]), "+v"(bv)::"memory");
      asm volatile("" : "+v"(cv[0]), "+v"(cv[1]), "+v"(cv[2]), "+v"(cv[3]), "+v"(cv[4]), "+v"(cv[5]), "+v"(cv[6]), "+v"(cv[7]), "+v"(cv[8]),
                   "+v"(cv[9]), "+v"(cv[10]), "+v"(cv[11]), "+v"(cv[12]), "+v"(cv[13]), "+v"(cv[14]), "+v"(cv[15])::"memory");
      bar();
      {
        float s = 0.f;
#pragma unroll
        for (int i = 0; i < 16; i++) s += __uint_as_float(cv[i]);
        if (pt < 224) { tabC[pt] = s; tabBias[pt] = __uint_as_float(bv << 16); }
      }
      {
        float s = 0.f, qq = 0.f;
#pragma unroll
        for (int i = 0; i < 8; i++) { s += __uint_as_float(sv[i][0]); qq += __uint_as_float(sv[i][1]); }
        s += __shfl_xor(s, 1); qq += __shfl_xor(qq, 1);
        const float mean = s * (1.0f / D_), var = fmaxf(qq * (1.0f / D_) - mean * mean, 0.f);
        const float rstd = 1.0f / sqrtf(var + LN_EPS);
        if ((pt & 1) == 0) { tabA[pt >> 1] = rstd; tabB[pt >> 1] = -mean * rstd; }
        wait_lgkm0();
      }
#pragma unroll 1
      for (int g = 6; g < 16; g++) { pa(g); q(g); wait_vm<16>(); bar(); }
      pbb(0); q(0); wait_vm<15>(); bar();
#pragma unroll 1
      for (int g = 1; g < 14; g++) { pbb(g); q(g); wait_vm<14>(); bar(); }
      flag_request(rfl_, 65536 + lane * 64, fv);
      pbb(14); q(14); wait_vm<14>(); bar();
      pbb(15); q(15); wait_vm<14>();
      PSTAMP(5)
      flag_wait(rfl_, 65536 + lane * 64, fv);
      PSTAMP(6)
      bar();
    }
    {
      rsrc_t rp = mk_rsrc(w2 + et * 128, F_ * D_ * 2 - et * 256);
      rsrc_t rq = mk_rsrc(ws + WS_R + (size_t)tt * 112 * 8192, 112 * 8192);
      u32 vp[4];
#pragma unroll
      for (int i = 0; i < 4; i++) {
        const int k = 16 * (p & 1) + 4 * i + (lane >> 4);
        vp[i] = k * (D_ * 2) + (((lane & 15) ^ ((k & 7) << 1)) * 16) - i * 1024;
      }
      const u32 vq0 = (4 * (p & 1) + 2 * half) * 1024 + lane * 16, vq1 = (4 * (p & 1) + 2 - 2 * half) * 1024 + lane * 16;
      const u32 ldsh = lds0 + QOFF + p * 4096;
      auto bblock = [&](int hf, int n) { return 7 * (8 * hf + n / 3) + 4 + n % 3; };
      auto full = [&](int g) {
        const int k = g < 16 ? kstep2(g, 16, phi, xcd & 1) : 16 + kstep2(g - 16, 8, phi, xcd & 1), n = 2 * k + (p >> 1);
        const int blk = k < 16 ? 7 * (8 * half + (n >> 2)) + (n & 3) : bblock(half, n - 32);
        const u32 m = ldsb + (g & 3) * SLOT;
        dma<4>(m, vp, rp, blk * (32 * D_ * 2));
        dma_sc1<2>(m + QOFF, vq0, rq, blk * 8192);
        dma_sc1<2>(m + QOFF + 2048, vq1, rq, blk * 8192);
      };
      auto halfs = [&](int g) {
        const int k = kstep2(g, 8, phi, xcd & 1), blk = bblock(k >> 2, 16 + 2 * (k & 3) + (p >> 1));
        dma<4>(ldsb + (g & 3) * SLOT, vp, rp, blk * (32 * D_ * 2));
        dma_sc1<2>(ldsh + (g & 3) * SLOT, vq0, rq, blk * 8192);
      };
      full(0); wait_vm<15>(); bar();
#pragma unroll 1
      for (int g = 1; g < 14; g++) { full(g); wait_vm<16>(); bar(); }
      flag_request(rfl_, 131072 + lane * 64, fv);
      full(14); wait_vm<16>(); bar();
      full(15); wait_vm<16>();
      PSTAMP(7)
      flag_wait(rfl_, 131072 + lane * 64, fv);
      PSTAMP(8)
      bar();
#pragma unroll 1
      for (int g = 16; g < 24; g++) { full(g); wait_vm<16>(); bar(); }
      halfs(0); wait_vm<14>(); bar();
#pragma unroll 1
      for (int g = 1; g < 8; g++) { halfs(g); wait_vm<12>(); bar(); }
      wait_vm<6>(); bar();
      wait_vm<0>(); bar();
      PDIAG_END
    }
  } else {
    if (K_PRIO) __builtin_amdgcn_s_setprio(K_PRIO);
    const int wP = wid >> 1, wQ = wid & 1, c = lane & 15, g = lane >> 4;
    const int m0 = 4 * wQ + 2 * half, cell = ft * 4 + wid;
    rsrc_t rall = mk_rsrc(ws + WS_FL, 7 * 65536);
    const u32 mycell = (tt * 64 + cell) * 64;
    u32 tick0;
    {
      LAS float* tabD = tabA + 256;
      Consumer<4, M_ROW, M_ROW> C;
      C.init(sm, wP, wQ, lane, wQ, wP);
      f32x4 acc[4][4], sx[2], sxx[2], sd[2];
      C.zero(acc);
#pragma unroll
      for (int i = 0; i < 2; i++) sx[i] = sxx[i] = sd[i] = (f32x4){0.f, 0.f, 0.f, 0.f};
      const u32x4 ones4 = {0x3F803F80u, 0x3F803F80u, 0x3F803F80u, 0x3F803F80u};
      bf16x8 ones = __builtin_bit_cast(bf16x8, ones4);
      asm volatile("" : "+v"(ones));
      bar();
      STAMP(1)
      C.first();
#define KA_HALF0(S) C.load<S, 1, 4>(); C.mma<0, 4>(acc); C.stats<0>(sx, sxx, sd, ones); C.interleave<22, 4>();
#define KA_STEP(S) KA_HALF0(S) wait_lgkm0(); bar(); __builtin_amdgcn_sched_barrier(0); \
      C.load<(S + 1) % NST, 0, 4>(); C.mma<1, 4>(acc); C.stats<1>(sx, sxx, sd, ones); C.interleave<22, 4>();
      for (int it = 0; it < 3; it++) {
        KA_STEP(0) KA_STEP(1) KA_STEP(2) KA_STEP(3)
      }
      if (wid == 0 && lane < 24) word_store(rall, (lane >> 2) * 65536 + mycell + (lane & 3) * 64, 0u);
      flag_request(rall, FL_TICK * 65536 + mycell, tick0);
      __builtin_amdgcn_sched_barrier(0);
      KA_STEP(0) KA_STEP(1) KA_STEP(2) KA_HALF0(3)
      C.stats<1>(sx, sxx, sd, ones);
      C.mma<1, 4>(acc);
      STAMP(2)
      const float sgn = half ? -1.f : 1.f;
#pragma unroll
      for (int i = 0; i < 2; i++) {
        const int e = c & 3;
        const float q = e == 0 ? sxx[i][0] : e == 1 ? sxx[i][1] : e == 2 ? sxx[i][2] : sxx[i][3];
        const float s = e == 0 ? sx[i][0] : e == 1 ? sx[i][1] : e == 2 ? sx[i][2] : sx[i][3];
        const float mean = s * (1.0f / D_), var = fmaxf(q * (1.0f / D_) - mean * mean, 0.f);
        const float rstd = sgn / sqrtf(var + LN_EPS);
        if ((c >> 2) == g) { tabA[64 * wP + 32 * wQ + 16 * i + c] = rstd; tabB[64 * wP + 32 * wQ + 16 * i + c] = -mean * rstd; }
        if (g == 0) tabD[64 * wQ + 32 * wP + 16 * i + c] = sd[i][0];
      }
      wait_lgkm0(); bar();
      rsrc_t ro = mk_rsrc(ws + WS_AB + ((size_t)((half * 8 + et) * 32 + 2 * tt + wP) * 16) * 1024, 16384);
      f32x4 ra[4], rb[4];
      float rs[4];
#pragma unroll
      for (int i = 0; i < 4; i++) {
        ra[i] = *(const LAS f32x4*)(tabA + 64 * wP + 16 * (i ^ (2 * wQ)) + 4 * g);
        rb[i] = *(const LAS f32x4*)(tabB + 64 * wP + 16 * (i ^ (2 * wQ)) + 4 * g);
        rs[i] = tabD[64 * wQ + 16 * (i ^ (2 * wP)) + c];
      }
#pragma unroll
      for (int u = 0; u < 2; u++)
#pragma unroll
        for (int j = 0; j < 4; j++) {
          const f32x4 a = ra[2 * u] * acc[2 * u][j] + rb[2 * u] * rs[j], b = ra[2 * u + 1] * acc[2 * u + 1][j] + rb[2 * u + 1] * rs[j];
          store_sc1(ro, ((u ^ wQ) * 8 + 4 * wQ + (j ^ (2 * wP))) * 1024 + lane * 16,
                    (u32x4){cvtpk(a[0], a[1]), cvtpk(a[2], a[3]), cvtpk(b[0], b[1]), cvtpk(b[2], b[3])});
        }
  bar();
      asm volatile("s_waitcnt vmcnt(0)" : "+v"(tick0)::"memory");
      if (lane == 0) word_store(rall, FL_TICK * 65536 + mycell, tick0 + 1);
      STAMP(3)
      bar();
      bar();
    }
    u32x4 hk[2][2];
    {
      Consumer<4, M_TILED, M_ROW> C;
      C.init(sm, wP, wQ, lane);
      f32x4 acc[4][4];
      C.zero(acc);
      bar();
      STAMP(4)
      C.first();
      for (int it = 0; it < 6; it++) {
        C.step4<4, 4>(acc);
      }
      C.step4<4, 2>(acc);
      STAMP(5)
      xch_send(ws + WS_X2, acc, wid, lane);
      __builtin_amdgcn_sched_barrier(0);
      rsrc_t rx = mk_rsrc(ws + WS_X2 + ((size_t)(blockIdx.x ^ 4) * 4 + wid) * 4096, 4096);
      rsrc_t rf = mk_rsrc(ws + WS_FL + ((size_t)(3 * 16 + tt) * 64 + (cell ^ 32)) * 64, 64);
      u32x4 rv[4];
      u32 fv;
      C.step<0, 2, 2>(acc); C.step<1, 2, 2>(acc); C.step<2, 2, 2>(acc);
      if (K_XF == 3) flag_set(ws, FL_X2, tt, cell);
      C.step<3, 2, 2>(acc);
      if (K_XF == 4) flag_set(ws, FL_X2, tt, cell);
      C.step<0, 2, 2>(acc); C.step<1, 2, 2>(acc);
      flag_request(rf, 0, fv);
      __builtin_amdgcn_sched_barrier(0);
      C.step<2, 2, 2>(acc);
      wait_vm<0>();
      flag_wait(rf, 0, fv);
      xch_request(rx, rv, lane);
      __builtin_amdgcn_sched_barrier(0);
      C.load<3, 1, 2>(); C.mma<0, 2>(acc); C.interleave<8, 2>();
      C.mma<1, 2>(acc);
      STAMP(6)
      xch_finish(rv);
            rsrc_t rh = mk_rsrc(ws + WS_H + ((size_t)(tt * 32 + 4 * et + 2 * wP) * 8 + m0) * 1024, 16384);
      rsrc_t rs = mk_rsrc(ws + WS_ST + ((size_t)(tt * 16 + 2 * et + wP) * 128 + 16 * m0) * 8, 256);
#pragma unroll
      for (int jj = 0; jj < 2; jj++) {
        float s = 0.f, q = 0.f;
#pragma unroll
        for (int u = 0; u < 2; u++) {
          const u32x4 o = rv[jj * 2 + u];
          const f32x4 a = acc[2 * u][jj], b = acc[2 * u + 1][jj];
          const int xrow = 32 * wQ + 16 * jj + c;
          const LAS char* xp = sm + (XOFF + wP * 8192 + xrow * 128 + (g & 1) * 8);
          const u32x2 xa = *(const LAS u32x2*)(xp + (((4 * u + (g >> 1)) ^ ((xrow >> 1) & 7)) * 16));
          const u32x2 xb = *(const LAS u32x2*)(xp + (((4 * u + 2 + (g >> 1)) ^ ((xrow >> 1) & 7)) * 16));
          u32x4 h;
          h[0] = cvtpk(a[0] + blo(o[0]) + blo(xa[0]), a[1] + bhi(o[0]) + bhi(xa[0]));
          h[1] = cvtpk(a[2] + blo(o[1]) + blo(xa[1]), a[3] + bhi(o[1]) + bhi(xa[1]));
          h[2] = cvtpk(b[0] + blo(o[2]) + blo(xb[0]), b[1] + bhi(o[2]) + bhi(xb[0]));
          h[3] = cvtpk(b[2] + blo(o[3]) + blo(xb[1]), b[3] + bhi(o[3]) + bhi(xb[1]));
          store_sc1(rh, (u * 8 + jj) * 1024 + lane * 16, h);
          hk[jj][u] = h;
#pragma unroll
          for (int e = 0; e < 4; e++) { const float lo = blo(h[e]), hi = bhi(h[e]); s += lo + hi; q += lo * lo + hi * hi; }
        }
        s = xsum32(xsum16(s)); q = xsum32(xsum16(q));
        if (g == 0) __builtin_amdgcn_raw_buffer_store_b64((u32x2){__float_as_uint(s), __float_as_uint(q)}, rs, (16 * jj + c) * 8, 0, 16);
      }
      flag_set(ws, FL_H, tt, cell);
      STAMP(8)
      bar();
      bar();
    }
    rsrc_t rr = mk_rsrc(ws + WS_R + ((size_t)(tt * 112 + 7 * ft) * 8 + 4 * wQ) * 1024, 7 * 8192);
    float ra[4], rb[4];
    auto act = [&](const f32x4& a, int col, int j, u32& lo, u32& hi) {
      const f32x4 cs = *(const LAS f32x4*)(tabC + col), bi = *(const LAS f32x4*)(tabBias + col);
      f32x4 v = ra[j] * a + (rb[j] * cs + bi);
#pragma unroll
      for (int e = 0; e < 4; e++) { v[e] = v[e] > 0.f ? v[e] : 0.f; v[e] *= v[e]; }
      lo = cvtpk(v[0], v[1]); hi = cvtpk(v[2], v[3]);
    };
    {
      Consumer<4, M_TR256, M_TILED> C;
      C.init(sm, wP, wQ, lane);
      f32x4 acc[4][4];
      C.zero(acc);
      bar();
      STAMP(9)
      C.first();
      for (int it = 0; it < 3; it++) {
        C.step4<4, 4>(acc);
      }
      C.last4<4>(acc);
      STAMP(10)
#pragma unroll
      for (int j = 0; j < 4; j++) { ra[j] = tabA[64 * wQ + 16 * j + c]; rb[j] = tabB[64 * wQ + 16 * j + c]; }
#pragma unroll
      for (int u = 0; u < 2; u++)
#pragma unroll
        for (int j = 0; j < 4; j++) {
          u32 a0, a1, b0, b1_;
          act(acc[2 * u][j], 64 * wP + 32 * u + 4 * g, j, a0, a1);
          act(acc[2 * u + 1][j], 64 * wP + 32 * u + 16 + 4 * g, j, b0, b1_);
          store_sc1(rr, ((2 * wP + u) * 8 + j) * 1024 + lane * 16, (u32x4){a0, a1, b0, b1_});
        }
    }
    {
      Consumer<3, M_TR192, M_TILED> C;
      C.init(sm, wP, wQ, lane);
      f32x4 acc[3][4];
      C.zero(acc);
      bar();
      STAMP(11)
      C.first();
      for (int it = 0; it < 3; it++) {
        C.step4<4, 4>(acc);
        if (it == K_FLAGIT) flag_set(ws, FL_RA, tt, cell);
      }
      C.last4<4>(acc);
      STAMP(12)
#pragma unroll
      for (int j = 0; j < 4; j++) {
        u32 a0, a1, b0, b1_;
        act(acc[0][j], 128 + 64 * wP + 4 * g, j, a0, a1);
        act(acc[1][j], 128 + 64 * wP + 16 + 4 * g, j, b0, b1_);
        store_sc1(rr, ((4 + 2 * wP) * 8 + j) * 1024 + lane * 16, (u32x4){a0, a1, b0, b1_});
        act(acc[2][j], 160 + 16 * wP + 4 * g, j, a0, a1);
        __builtin_amdgcn_raw_buffer_store_b64((u32x2){a0, a1}, rr, (5 * 8 + j) * 1024 + lane * 16 + 8 * wP, 0, 16);
      }
    }
    {
      Consumer<4, M_TR256, M_TILED> C;
      C.init(sm, wP, wQ, lane);
      f32x4 acc[4][4];
      C.zero(acc);
      bar();
      STAMP(13)
      C.first();
      for (int it = 0; it < 5; it++) {
        C.step4<4, 4>(acc);
        if (it == K_FLAGIT) flag_set(ws, FL_RB, tt, cell);
      }
      C.step4<4, 2>(acc);
      STAMP(14)
      xch_send(ws + WS_X4, acc, wid, lane);
      u32x2 br[4];
#pragma unroll
      for (int i = 0; i < 4; i++) br[i] = *(const u32x2*)(b2 + et * 128 + 64 * wP + 16 * i + 4 * g);
      __builtin_amdgcn_sched_barrier(0);
      rsrc_t rx = mk_rsrc(ws + WS_X4 + ((size_t)(blockIdx.x ^ 4) * 4 + wid) * 4096, 4096);
      rsrc_t rf = mk_rsrc(ws + WS_FL + ((size_t)(4 * 16 + tt) * 64 + (cell ^ 32)) * 64, 64);
      u32x4 rv[4];
      u32 fv;
      C.step<0, 2, 2>(acc); C.step<1, 2, 2>(acc); C.step<2, 2, 2>(acc);
      if (K_XF == 3) flag_set(ws, FL_X4, tt, cell);
      C.step<3, 2, 2>(acc);
      if (K_XF == 4) flag_set(ws, FL_X4, tt, cell);
      C.step<0, 2, 2>(acc); C.step<1, 2, 2>(acc);
      flag_request(rf, 0, fv);
      __builtin_amdgcn_sched_barrier(0);
      C.step<2, 2, 2>(acc);
      wait_vm<0>();
      flag_wait(rf, 0, fv);
      xch_request(rx, rv, lane);
      __builtin_amdgcn_sched_barrier(0);
      C.load<3, 1, 2>(); C.mma<0, 2>(acc); C.interleave<8, 2>();
      C.mma<1, 2>(acc);
      xch_finish(rv);

      rsrc_t ro = mk_rsrc(out + (size_t)(tt * 128 + 16 * m0) * D_ + et * 128 + 64 * wP, 31 * D_ * 2 + 128);
#pragma unroll
      for (int jj = 0; jj < 2; jj++)
#pragma unroll
        for (int u = 0; u < 2; u++) {
          const u32x4 o = rv[jj * 2 + u], h = hk[jj][u];
          const f32x4 a = acc[2 * u][jj], b = acc[2 * u + 1][jj];
          const u32x2 ba = br[2 * u], bb = br[2 * u + 1];
          const u32 a0 = cvtpk(a[0] + blo(o[0]) + blo(ba[0]) + blo(h[0]), a[1] + bhi(o[0]) + bhi(ba[0]) + bhi(h[0]));
          const u32 a1 = cvtpk(a[2] + blo(o[1]) + blo(ba[1]) + blo(h[1]), a[3] + bhi(o[1]) + bhi(ba[1]) + bhi(h[1]));
          const u32 b0 = cvtpk(b[0] + blo(o[2]) + blo(bb[0]) + blo(h[2]), b[1] + bhi(o[2]) + bhi(bb[0]) + bhi(h[2]));
          const u32 b1_ = cvtpk(b[2] + blo(o[3]) + blo(bb[1]) + blo(h[3]), b[3] + bhi(o[3]) + bhi(bb[1]) + bhi(h[3]));
          const auto s0 = __builtin_amdgcn_permlane16_swap(a0, b0, false, false);
          const auto s1 = __builtin_amdgcn_permlane16_swap(a1, b1_, false, false);
          __builtin_amdgcn_raw_buffer_store_b128((u32x4){s0[0], s1[0], s0[1], s1[1]}, ro,
                                                 (16 * jj + c) * (D_ * 2) + (32 * u + 16 * (g & 1) + 8 * (g >> 1)) * 2, 0, K_OUTSC1 ? 16 : 0);
        }
      DIAG_END(15)
    }
  }
}

}

extern "C" void kernel_launch(void* const* d_in, const int* in_sizes, int n_in, void* d_out, int out_size, void* d_ws, size_t ws_size,
                              hipStream_t stream) {
  const u16* const* I = (const u16* const*)d_in;
  (void)hipFuncSetAttribute((const void*)fused_kernel, hipFuncAttributeMaxDynamicSharedMemorySize, LDS_BYTES);
  fused_kernel<<<256, 512, LDS_BYTES, stream>>>(I[0], I[1], I[2], I[3], I[4], I[6], I[7], I[8], I[9], (char*)d_ws, (u16*)d_out);
}
```

```cpp
#include <hip/hip_runtime.h>
#include <hip/hip_bf16.h>
#include <type_traits>

typedef unsigned short u16;
typedef unsigned int u32;
typedef unsigned long long u64;
typedef __bf16 bf16x8 __attribute__((ext_vector_type(8)));
typedef __bf16 bf16x2 __attribute__((ext_vector_type(2)));
typedef short s16x4 __attribute__((ext_vector_type(4)));
typedef short s16x8 __attribute__((ext_vector_type(8)));
typedef float f32x2 __attribute__((ext_vector_type(2)));
typedef float f32x4 __attribute__((ext_vector_type(4)));
typedef u32 u32x2 __attribute__((ext_vector_type(2)));
typedef u32 u32x4 __attribute__((ext_vector_type(4)));
typedef __amdgpu_buffer_rsrc_t rsrc_t;
#define LAS __attribute__((address_space(3)))
#define DI __device__ __forceinline__

#define K_WN 12
#define K_SLEEP 1
#define K_SPIN (1 << 18)
#define K_GAPS(NM, NR) ((NR) < (NM) ? (NR) : (NM))
#define K_PPRIO 0
#define K_BASE 1
#define K_XF 4
namespace {

constexpr int S_ = 2048, D_ = 1024, F_ = 3584;
constexpr float LN_EPS = 1e-5f;

constexpr size_t WS_AB = 0;
constexpr size_t WS_H = 8u << 20;
constexpr size_t WS_R = 12u << 20;
constexpr size_t WS_X2 = 27u << 20;
constexpr size_t WS_X4 = 31u << 20;
constexpr size_t WS_ST = 35u << 20;
constexpr size_t WS_CS = 36u << 20;
constexpr size_t WS_FL = 37u << 20;

#define DIAG_ARG
#define DIAG_BEGIN
#define STAMP(i)
#define PSTAMP(i)
#define PDIAG_END
#define DIAG_END(i)

DI u32 rfl(u32 x) { return __builtin_amdgcn_readfirstlane(x); }
DI u32 cvtpk(float lo, float hi) { f32x2 v = {lo, hi}; return __builtin_bit_cast(u32, __builtin_convertvector(v, bf16x2)); }
DI float blo(u32 v) { return __uint_as_float(v << 16); }
DI float bhi(u32 v) { return __uint_as_float(v & 0xFFFF0000u); }
DI rsrc_t mk_rsrc(const void* p, u32 bytes) { return __builtin_amdgcn_make_buffer_rsrc((void*)p, 0, bytes, 0x00020000); }
DI void bar() { asm volatile("s_barrier" ::: "memory"); }
DI void wait_lgkm0() { __builtin_amdgcn_s_waitcnt(0xC07F); }
template <int N> DI void wait_vm() { asm volatile("s_waitcnt vmcnt(%0)" ::"i"(N) : "memory"); }

template <int N> DI void dma(u32 m0, const u32* v, rsrc_t r, u32 so) {
  if constexpr (N == 4)
    asm volatile("s_mov_b32 m0, %0\n\ts_nop 0\n\t"
                 "buffer_load_dwordx4 %1, %5, %6 offen lds\n\t"
                 "buffer_load_dwordx4 %2, %5, %6 offen offset:1024 lds\n\t"
                 "buffer_load_dwordx4 %3, %5, %6 offen offset:2048 lds\n\t"
                 "buffer_load_dwordx4 %4, %5, %6 offen offset:3072 lds"
                 ::"s"(m0), "v"(v[0]), "v"(v[1]), "v"(v[2]), "v"(v[3]), "s"(r), "s"(so) : "memory");
  else if constexpr (N == 3)
    asm volatile("s_mov_b32 m0, %0\n\ts_nop 0\n\t"
                 "buffer_load_dwordx4 %1, %4, %5 offen lds\n\t"
                 "buffer_load_dwordx4 %2, %4, %5 offen offset:1024 lds\n\t"
                 "buffer_load_dwordx4 %3, %4, %5 offen offset:2048 lds"
                 ::"s"(m0), "v"(v[0]), "v"(v[1]), "v"(v[2]), "s"(r), "s"(so) : "memory");
  else
    asm volatile("s_mov_b32 m0, %0\n\ts_nop 0\n\t"
                 "buffer_load_dwordx4 %1, %3, %4 offen lds\n\t"
                 "buffer_load_dwordx4 %2, %3, %4 offen offset:1024 lds"
                 ::"s"(m0), "v"(v[0]), "v"(v[1]), "s"(r), "s"(so) : "memory");
}
template <int N> DI void dma_sc1(u32 m0, u32 v, rsrc_t r, u32 so) {
  if constexpr (N == 4)
    asm volatile("s_mov_b32 m0, %0\n\ts_nop 0\n\t"
                 "buffer_load_dwordx4 %1, %2, %3 offen sc1 lds\n\t"
                 "buffer_load_dwordx4 %1, %2, %3 offen offset:1024 sc1 lds\n\t"
                 "buffer_load_dwordx4 %1, %2, %3 offen offset:2048 sc1 lds\n\t"
                 "buffer_load_dwordx4 %1, %2, %3 offen offset:3072 sc1 lds"
                 ::"s"(m0), "v"(v), "s"(r), "s"(so) : "memory");
  else
    asm volatile("s_mov_b32 m0, %0\n\ts_nop 0\n\t"
                 "buffer_load_dwordx4 %1, %2, %3 offen sc1 lds\n\t"
                 "buffer_load_dwordx4 %1, %2, %3 offen offset:1024 sc1 lds"
                 ::"s"(m0), "v"(v), "s"(r), "s"(so) : "memory");
}

DI float xsum16(float v) { const auto r = __builtin_amdgcn_permlane16_swap(__float_as_uint(v), __float_as_uint(v), false, false); return __uint_as_float(r[0]) + __uint_as_float(r[1]); }
DI float xsum32(float v) { const auto r = __builtin_amdgcn_permlane32_swap(__float_as_uint(v), __float_as_uint(v), false, false); return __uint_as_float(r[0]) + __uint_as_float(r[1]); }

template <int A, int B, class F> DI void static_for(F f) {
  if constexpr (A < B) { f(std::integral_constant<int, A>{}); static_for<A + 1, B>(f); }
}

DI void pull_lines(u32 m0, u32 v, rsrc_t r, u32 so) {
  asm volatile("s_mov_b32 m0, %0\n\ts_nop 0\n\tbuffer_load_dword %1, %2, %3 offen lds" ::"s"(m0), "v"(v), "s"(r), "s"(so) : "memory");
}

DI int kstep(int t, int n, int phi) {
  int k = t;
  if ((t & 3) == phi) { k = t + 4; if (k >= n) k = phi; }
  return k;
}

DI int kstep2(int t, int n, int phi, int side) {
  const int k = kstep(t, n, phi), k2 = k ^ (side << 2);
  return k2 < n ? k2 : k;
}

constexpr int NST = 4;
constexpr int SLOT = 16384;
constexpr int QOFF = NST * SLOT;
constexpr int TOFF = 2 * NST * SLOT;
constexpr int COFF = TOFF + 4096;
constexpr int XOFF = COFF + 4096;
constexpr int LDS_BYTES = XOFF + 16384;

enum { M_ROW = 0, M_TILED = 1, M_TR192 = 2, M_TR256 = 3 };

template <int PT, int PMODE, int QMODE>
struct Consumer {
  const LAS char* pb[4];
  const LAS char* qb[4];
  bf16x8 fp[2][PT], fq[2][4];

  DI void init(const LAS char* sm, int wP, int wQ, int lane, int prot = 0, int qrot = 0) {
    const int r = lane & 15, g = lane >> 4;
    if constexpr (PMODE == M_ROW) {
      const int a = (64 * wP + r) * 128 + ((g ^ (r >> 1)) * 16);
      pb[0] = sm + (a + prot * 4096); pb[1] = sm + ((a ^ 64) + prot * 4096);
      pb[2] = sm + (a + 4096 - prot * 4096); pb[3] = sm + ((a ^ 64) + 4096 - prot * 4096);
    } else if constexpr (PMODE == M_TILED) {
      pb[0] = sm + (4 * wP * 1024 + lane * 16);
    } else if constexpr (PMODE == M_TR192) {
      const int b = g & 1, a = (4 * g + (r >> 2)) * 192 + (r & 3) * 8;
      pb[0] = sm + (a + (4 * wP + b) * 32);
      pb[1] = sm + (a + (4 * wP + 1 - b) * 32);
      pb[2] = sm + (a + ((2 + wP) ^ b) * 32);
    } else {
      const int k = 4 * g + (r >> 2), a = k * 256 + (r & 3) * 8;
#pragma unroll
      for (int i = 0; i < 4; i++) pb[i] = sm + (a + (((4 * wP + i) ^ (k & 7)) * 32));
    }
    if constexpr (QMODE == M_ROW) {
      const int a = QOFF + (64 * wQ + r) * 128 + ((g ^ (r >> 1)) * 16);
      qb[0] = sm + (a + qrot * 4096); qb[1] = sm + ((a ^ 64) + qrot * 4096);
      qb[2] = sm + (a + 4096 - qrot * 4096); qb[3] = sm + ((a ^ 64) + 4096 - qrot * 4096);
      asm volatile("" : "+v"(qb[0]), "+v"(qb[1]), "+v"(qb[2]), "+v"(qb[3]));
    } else {
      qb[0] = sm + (QOFF + 4 * wQ * 1024 + lane * 16);
      asm volatile("" : "+v"(qb[0]));
    }
  }
  static DI bf16x8 tr2(const LAS char* p, int second) {
    s16x4 a = __builtin_amdgcn_ds_read_tr16_b64_v4i16((LAS s16x4*)p);
    s16x4 b = __builtin_amdgcn_ds_read_tr16_b64_v4i16((LAS s16x4*)(p + second));
    return __builtin_bit_cast(bf16x8, __builtin_shufflevector(a, b, 0, 1, 2, 3, 4, 5, 6, 7));
  }
  template <int S, int KS> DI bf16x8 ldp(int i) {
    if constexpr (PMODE == M_ROW) return *(const LAS bf16x8*)(pb[KS + (i & 2)] + (S * SLOT + (i & 1) * 2048));
    else if constexpr (PMODE == M_TILED) return *(const LAS bf16x8*)(pb[0] + (S * SLOT + KS * 8192 + i * 1024));
    else if constexpr (PMODE == M_TR192) return tr2(pb[i] + (S * SLOT + KS * 32 * 192), 16 * 192);
    else return tr2(pb[i] + (S * SLOT + KS * 32 * 256), 16 * 256);
  }
  template <int S, int KS> DI bf16x8 ldq(int j) {
    if constexpr (QMODE == M_ROW) return *(const LAS bf16x8*)(qb[KS + (j & 2)] + (S * SLOT + (j & 1) * 2048));
    else return *(const LAS bf16x8*)(qb[0] + (S * SLOT + KS * 8192 + j * 1024));
  }
  template <int S, int KS, int QN> DI void load() {
    fq[KS][0] = ldq<S, KS>(0);
#pragma unroll
    for (int i = 0; i < PT; i++) fp[KS][i] = ldp<S, KS>(i);
#pragma unroll
    for (int j = 1; j < QN; j++) fq[KS][j] = ldq<S, KS>(j);
  }
  template <int KS, int QN, bool ZERO = false> DI void mma(f32x4 (&acc)[PT][4]) {
#pragma unroll
    for (int n = 0; n < PT * QN; n++) {
      const int j = n / PT, i = (j & 1) ? PT - 1 - n % PT : n % PT;
      acc[i][j] = __builtin_amdgcn_mfma_f32_16x16x32_bf16(fp[KS][i], fq[KS][j], ZERO ? (f32x4){0.f, 0.f, 0.f, 0.f} : acc[i][j], 0, 0, 0);
    }
  }
  template <int KS> DI void stats(f32x4 (&sx)[2], f32x4 (&sxx)[2], f32x4 (&sd)[2], bf16x8 ones) {
#pragma unroll
    for (int i = 0; i < 2; i++) {
      sx[i] = __builtin_amdgcn_mfma_f32_16x16x32_bf16(fp[KS][i], ones, sx[i], 0, 0, 0);
      sxx[i] = __builtin_amdgcn_mfma_f32_16x16x32_bf16(fp[KS][i], fp[KS][i], sxx[i], 0, 0, 0);
      sd[i] = __builtin_amdgcn_mfma_f32_16x16x32_bf16(ones, fq[KS][i], sd[i], 0, 0, 0);
    }
  }
  template <int NM, int QN> static DI void interleave() {
    constexpr int NR = PT * (PMODE >= M_TR192 ? 2 : 1) + QN, G = K_GAPS(NM, NR);
    static_for<0, G>([](auto ic) {
      __builtin_amdgcn_sched_group_barrier(0x8, 1, 0);
      __builtin_amdgcn_sched_group_barrier(0x100, NR / G + (ic.value < NR % G ? 1 : 0), 0);
    });
    __builtin_amdgcn_sched_group_barrier(0x8, NM - G, 0);
    __builtin_amdgcn_sched_barrier(0);
  }
  template <int S, int QC, int QN> DI void step(f32x4 (&acc)[PT][4]) {
    load<S, 1, QC>(); mma<0, QC>(acc); interleave<PT * QC, QC>();
    wait_lgkm0(); bar();
    __builtin_amdgcn_sched_barrier(0);
    load<(S + 1) % NST, 0, QN>(); mma<1, QC>(acc); interleave<PT * QC, QN>();
  }
  template <int S, int LS, bool ZERO, class W> DI void half_with(f32x4 (&acc)[PT][4], W w) {
    static_for<0, PT * 4>([&](auto nc) {
      constexpr int n = nc.value, j = n / PT, i = (j & 1) ? PT - 1 - n % PT : n % PT;
      acc[i][j] = __builtin_amdgcn_mfma_f32_16x16x32_bf16(fp[1 - LS][i], fq[1 - LS][j], ZERO ? (f32x4){0.f, 0.f, 0.f, 0.f} : acc[i][j], 0, 0, 0);
      if constexpr (n == 0) fq[LS][0] = ldq<S, LS>(0);
      else if constexpr (n <= PT) fp[LS][n - 1] = ldp<S, LS>(n - 1);
      else if constexpr (n < PT + 4) fq[LS][n - PT] = ldq<S, LS>(n - PT);
      w(nc);
      __builtin_amdgcn_sched_barrier(0);
    });
  }
  template <int S, bool ZERO, class W0, class W1> DI void step_with(f32x4 (&acc)[PT][4], W0 w0, W1 w1) {
    half_with<S, 1, ZERO>(acc, w0);
    wait_lgkm0(); bar();
    __builtin_amdgcn_sched_barrier(0);
    half_with<(S + 1) % NST, 0, false>(acc, w1);
  }
  template <int QC, int QN> DI void step4(f32x4 (&acc)[PT][4]) {
    step<0, QC, QC>(acc); step<1, QC, QC>(acc); step<2, QC, QC>(acc); step<3, QC, QN>(acc);
  }
  template <int QC> DI void last4(f32x4 (&acc)[PT][4]) {
    step<0, QC, QC>(acc); step<1, QC, QC>(acc); step<2, QC, QC>(acc);
    load<3, 1, QC>(); mma<0, QC>(acc); interleave<PT * QC, QC>();
    wait_lgkm0();
    mma<1, QC>(acc);
  }
  static DI void zero(f32x4 (&acc)[PT][4]) {
#pragma unroll
    for (int i = 0; i < PT; i++)
#pragma unroll
      for (int j = 0; j < 4; j++) { acc[i][j] = (f32x4){0.f, 0.f, 0.f, 0.f}; asm volatile("" : "+v"(acc[i][j])); }
  }
  DI void first() { load<0, 0, 4>(); }
};

DI u32 row_src(int imgrow, int srcrow, int lane, int ldbytes) {
  return srcrow * ldbytes + (((lane & 7) ^ ((imgrow >> 1) & 7)) * 16);
}
DI void store_sc1(rsrc_t r, u32 off, u32x4 v) { __builtin_amdgcn_raw_buffer_store_b128(v, r, off, 0, 16); }

DI void xch_send(char* x, const f32x4 (&acc)[4][4], int wid, int lane) {
  rsrc_t r = mk_rsrc(x + ((size_t)blockIdx.x * 4 + wid) * 4096, 4096);
#pragma unroll
  for (int jj = 0; jj < 2; jj++)
#pragma unroll
    for (int u = 0; u < 2; u++) {
      const f32x4 a = acc[2 * u][2 + jj], b = acc[2 * u + 1][2 + jj];
      u32x4 v = {cvtpk(a[0], a[1]), cvtpk(a[2], a[3]), cvtpk(b[0], b[1]), cvtpk(b[2], b[3])};
      store_sc1(r, lane * 16 + (jj * 2 + u) * 1024, v);
    }
}
DI void xch_request(rsrc_t r, u32x4 (&v)[4], int lane) {
  asm volatile("buffer_load_dwordx4 %0, %4, %5, 0 offen sc1\n\t"
               "buffer_load_dwordx4 %1, %4, %5, 0 offen offset:1024 sc1\n\t"
               "buffer_load_dwordx4 %2, %4, %5, 0 offen offset:2048 sc1\n\t"
               "buffer_load_dwordx4 %3, %4, %5, 0 offen offset:3072 sc1"
               : "=&v"(v[0]), "=&v"(v[1]), "=&v"(v[2]), "=&v"(v[3]) : "v"(lane * 16), "s"(r) : "memory");
}
DI void xch_finish(u32x4 (&v)[4]) { asm volatile("s_waitcnt vmcnt(0)" : "+v"(v[0]), "+v"(v[1]), "+v"(v[2]), "+v"(v[3])::"memory"); }

enum { FL_RA = 0, FL_RB = 1, FL_X2 = 2, FL_X4 = 3, FL_ACK = 4, FL_TICK = 5, FL_CNT = 6 };
enum { CN_H = 0, CN_AB = 128 };
constexpr u32 NOWHERE = 0xFFFFFF00u;
DI void flag_set(char* ws, int which, int tt, int cell) {
  wait_vm<0>();
  if ((threadIdx.x & 63) == 0)
    __hip_atomic_store((u32*)(ws + WS_FL + ((size_t)(which * 16 + tt) * 64 + cell) * 64), 1u, __ATOMIC_RELAXED, __HIP_MEMORY_SCOPE_AGENT);
}
DI void flag_request(rsrc_t r, u32 off, u32& v) {
  asm volatile("buffer_load_dword %0, %1, %2, 0 offen sc1" : "=&v"(v) : "v"(off), "s"(r) : "memory");
}
DI void flag_wait(rsrc_t r, u32 off, u32& v) {
  asm volatile("" : "+v"(v)::"memory");
#pragma unroll 1
  for (int spin = 0; spin < K_SPIN; spin++) {
    if (__builtin_amdgcn_ballot_w64(v != 1u) == 0) break;
    __builtin_amdgcn_s_sleep(K_SLEEP);
    asm volatile("buffer_load_dword %0, %1, %2, 0 offen sc1\n\ts_waitcnt vmcnt(0)" : "=&v"(v) : "v"(off), "s"(r) : "memory");
  }
}

DI void word_store(rsrc_t r, u32 off, u32 v) { __builtin_amdgcn_raw_buffer_store_b32(v, r, off, 0, 16); }
DI void count_up(char* ws, u32 off) {
  if ((threadIdx.x & 63) == 0) __hip_atomic_fetch_add((u32*)(ws + WS_FL + off), 1u, __ATOMIC_RELAXED, __HIP_MEMORY_SCOPE_AGENT);
}
template <class Done> DI void watch(rsrc_t r, u32 off, u32& v, Done done, u32 tickoff, u32& tick) {
#pragma unroll 1
  for (int spin = 0; spin < K_SPIN; spin++) {
    if (__builtin_amdgcn_ballot_w64(!done(v)) == 0) break;
    if ((spin & 63) == 63) { tick++; word_store(r, tickoff, tick); }
    __builtin_amdgcn_s_sleep(K_SLEEP);
    asm volatile("buffer_load_dword %0, %1, %2, 0 offen sc1\n\ts_waitcnt vmcnt(0)" : "=&v"(v) : "v"(off), "s"(r) : "memory");
  }
}
__global__ __launch_bounds__(512, 2) void fused_kernel(const u16* __restrict__ x, const u16* __restrict__ cre, const u16* __restrict__ cim,
                                                    const u16* __restrict__ dre, const u16* __restrict__ dim,
                                                    const u16* __restrict__ w1, const u16* __restrict__ b1, const u16* __restrict__ w2,
                                                    const u16* __restrict__ b2, char* __restrict__ ws, u16* __restrict__ out DIAG_ARG) {
  extern __shared__ __attribute__((aligned(16))) char smem[];
  DIAG_BEGIN
  asm volatile("" ::"s"(x), "s"(cre), "s"(cim), "s"(dre), "s"(dim), "s"(w1), "s"(b1), "s"(w2), "s"(b2), "s"(ws), "s"(out));
  const int lane = threadIdx.x & 63, wid = rfl(threadIdx.x >> 6);
  const int xcd = blockIdx.x & 7, loc = blockIdx.x >> 3, li = loc >> 2, lj = loc & 3;
  const int half = xcd >> 2, et = ((xcd >> 1) & 1) * 4 + lj, tt = (xcd & 1) * 8 + li, ft = (xcd >> 1) * 4 + lj;
  LAS char* sm = (LAS char*)smem;
  LAS float* tabA = (LAS float*)(sm + TOFF);
  LAS float* tabB = tabA + 128;
  LAS float* tabC = tabA + 256;
  LAS float* tabBias = tabA + 512;
  if (__builtin_expect(wid >= 4, 1)) {
    if (K_PPRIO) __builtin_amdgcn_s_setprio(K_PPRIO);
    const int p = wid - 4, phi = (li + lj) & 3;
    const u32 lds0 = (u32)(size_t)smem, ldsb = lds0 + p * 4096, vlin = p * 4096 + lane * 16;
    rsrc_t rall = mk_rsrc(ws + WS_FL, 7 * 65536);
    const u32 grpcell = ((lane & 16) ? tt * 64 + (lane & 15) * 4 : (lane & 15) * 64 + ft * 4) * 64;
    const u32 tickoff = lane == 0 ? FL_TICK * 65536 + (tt * 64 + ft * 4) * 64 : NOWHERE;
    const u32 ackoff = lane == 0 ? FL_ACK * 65536 + (tt * 64 + ft * 4) * 64 : NOWHERE;
    const u32 rowcnt = FL_CNT * 65536 + tt * 4096, colcnt = FL_CNT * 65536 + ft * 4096;
    const int pt = threadIdx.x - 256;
    u32 base, fv, tick;
    const int side2 = (xcd >> 1) & 1;
    auto skip = [&](int r) { return r + (r >= 2 * tt ? 2 : 0); };
    auto g2k = [&](int q) { return q < 2 ? 2 * tt + q : skip(kstep2(q - 2, 26, phi, side2)); };
    rsrc_t rco = mk_rsrc((const char*)(half ? cim : cre) + (size_t)tt * 128 * S_ * 2 - 4096, 128 * S_ * 2 + 4096);
    u32 vc[4];
#pragma unroll
    for (int i = 0; i < 4; i++) {
      const int row = 32 * p + 8 * i + (lane >> 3);
      vc[i] = row_src(row, row ^ (half * 32), lane, S_ * 2) + 4096 - i * 1024;
    }
    auto g2q = [&](int q) { dma<4>(ldsb + QOFF + (q & 3) * SLOT, vc, rco, g2k(q) * 128); };
    {
      rsrc_t rp = mk_rsrc((const char*)x + (size_t)tt * 128 * D_ * 2 - 4096, 128 * D_ * 2 + 4096);
      rsrc_t rq = mk_rsrc((half ? dim : dre) + (size_t)et * 128 * D_, 128 * D_ * 2);
      u32 vp[4], vq[4], vx[2];
#pragma unroll
      for (int i = 0; i < 4; i++) {
        const int row = 32 * p + 8 * i + (lane >> 3), u = (row >> 4) & 1, ii = row & 15;
        vp[i] = row_src(row, 32 * p + 8 * (ii >> 2) + 4 * u + (ii & 3), lane, D_ * 2) + 4096 - i * 1024;
        vq[i] = row_src(row, row, lane, D_ * 2) - i * 1024;
      }
#pragma unroll
      for (int i = 0; i < 2; i++) {
        const int row = 16 * p + 8 * i + (lane >> 3);
        vx[i] = row_src(row, 64 * (row >> 5) + 32 * half + (row & 31), lane, D_ * 2) + 4096 - i * 1024;
      }
      const int side = (xcd >> 1) ^ (xcd & 1);
      auto issue = [&](int g) {
        const int k = kstep2(g, 16, phi, side);
        const u32 m = ldsb + (g & 3) * SLOT;
        dma<4>(m, vp, rp, k * 128);
        dma<4>(m + QOFF, vq, rq, k * 128);
        if ((k >> 1) == et) dma<2>(lds0 + XOFF + (k & 1) * 8192 + p * 2048, vx, rp, k * 128);
      };
      auto run = [&](int g0, int g1) {
#pragma unroll 1
        for (int g = g0; g < g1; g++) { issue(g); wait_vm<16>(); bar(); }
      };
      issue(0); issue(1); issue(2);
      flag_request(rall, p == 0 ? FL_TICK * 65536 + grpcell : NOWHERE, base);
      wait_vm<17>(); bar();
      issue(3); wait_vm<17>(); bar();
      {
        u32 z = lane < 16 ? (lane >> 2) * 65536 + (tt * 64 + ft * 4 + (lane & 3)) * 64 : FL_ACK * 65536 + (tt * 64 + ft * 4) * 64;
        if (lane == 17) z = ft == 0 ? rowcnt + CN_H : NOWHERE;
        if (lane == 18) z = tt == 0 ? colcnt + CN_AB : NOWHERE;
        word_store(rall, p == 0 && lane < 19 ? z : NOWHERE, 0u);
      }
      issue(4); wait_vm<17>(); bar();
      run(5, 7);
      issue(7);
      asm volatile("s_waitcnt vmcnt(16)" : "+v"(base)::"memory");
      tick = __builtin_amdgcn_readlane(base, tt) + 1;
      if (p == 0) word_store(rall, tickoff, tick);
      bar();
      run(8, 12);
      flag_request(rall, p == 0 ? FL_TICK * 65536 + grpcell : NOWHERE, fv);
      issue(12); wait_vm<16>(); bar();
      issue(13);
      asm volatile("s_waitcnt vmcnt(16)" : "+v"(fv)::"memory");
      PSTAMP(9)
      if (p == 0) {
        watch(rall, FL_TICK * 65536 + grpcell, fv, [&](u32 w) { return w != base; }, tickoff, tick);
        word_store(rall, ackoff, 1u);
      }
      PSTAMP(10)
      bar();
      run(14, 16);
      g2q(0); wait_vm<12>(); bar();
      g2q(1); wait_vm<8>(); bar();
    }
    {
      rsrc_t rp = mk_rsrc(ws + WS_AB + (size_t)et * 32 * 16384, 9 * 32 * 16384);
      rsrc_t rq0 = mk_rsrc((const char*)cre + (size_t)tt * 128 * S_ * 2 - 4096, 128 * S_ * 2 + 4096);
      rsrc_t rq1 = mk_rsrc((const char*)cim + (size_t)tt * 128 * S_ * 2 - 4096, 128 * S_ * 2 + 4096);
      u32 vh[2];
#pragma unroll
      for (int i = 0; i < 2; i++) {
        const int row = 64 * (p >> 1) + 16 * (p & 1) + 8 * i + (lane >> 3);
        vh[i] = row_src(row, row ^ (half * 32), lane, S_ * 2) + 4096 - i * 1024;
      }
      const u32 ldsh = lds0 + QOFF + (p >> 1) * 8192 + (p & 1) * 2048;
      auto fp = [&](int q) { dma_sc1<4>(ldsb + (q & 3) * SLOT, vlin, rp, (half * 256 + g2k(q)) * 16384); };
      auto full = [&](int q) { fp(q); g2q(q); };
      auto halfs = [&](int g) {
        const int k = kstep2(g, 8, phi, side2), term = k >> 2, ks = skip(26 + (k & 3));
        dma_sc1<4>(ldsb + (g & 3) * SLOT, vlin, rp, (term * 256 + ks) * 16384);
        if (term) dma<2>(ldsh + (g & 3) * SLOT, vh, rq1, ks * 128);
        else dma<2>(ldsh + (g & 3) * SLOT, vh, rq0, ks * 128);
      };
      g2q(2);
      bar();
      wait_vm<8>(); bar();
      g2q(3);
      rsrc_t rw = mk_rsrc((const char*)w1 + (size_t)(tt * 64 + p * 16) * F_ * 2 + ft * 448, 15 * F_ * 2 + 448);
      u32x4 v[8];
      const int l28 = lane < 28 ? lane : lane < 56 ? lane - 28 : 27;
      const u32 vo = (lane >= 28 ? F_ * 2 : 0) + l28 * 16;
#pragma unroll
      for (int i = 0; i < 8; i++)
        asm volatile("buffer_load_dwordx4 %0, %1, %2, %3 offen" : "=&v"(v[i]) : "v"(vo), "s"(rw), "s"(i * 2 * F_ * 2) : "memory");
      wait_vm<16>(); bar();
      bar();
      PSTAMP(12)
      if (p == 0) {
        count_up(ws, colcnt + CN_AB);
        fv = 0;
        watch(rall, colcnt + CN_AB, fv, [&](u32 w) { return w >= 16u; }, tickoff, tick);
        word_store(rall, ackoff, 2u);
      }
      bar();
      PSTAMP(2)
      fp(2); fp(3); full(4);
      asm volatile("s_waitcnt vmcnt(12)" : "+v"(v[0]), "+v"(v[1]), "+v"(v[2]), "+v"(v[3]), "+v"(v[4]), "+v"(v[5]), "+v"(v[6]), "+v"(v[7])::"memory");
      bar();
      full(5);
      {
        float s[8] = {0, 0, 0, 0, 0, 0, 0, 0};
#pragma unroll
        for (int i = 0; i < 8; i++)
#pragma unroll
          for (int e = 0; e < 4; e++) { s[2 * e] += blo(v[i][e]); s[2 * e + 1] += bhi(v[i][e]); }
#pragma unroll
        for (int e = 0; e < 8; e++) s[e] += __shfl(s[e], lane + 28);
        if (lane < 28) {
          LAS float* d = (LAS float*)(sm + COFF) + p * 224 + lane * 8;
          *(LAS f32x4*)d = (f32x4){s[0], s[1], s[2], s[3]};
          *(LAS f32x4*)(d + 4) = (f32x4){s[4], s[5], s[6], s[7]};
        }
      }
      wait_vm<16>(); wait_lgkm0(); bar();
      full(6);
      if (p == 0 && lane < 56) {
        const LAS f32x4* d = (const LAS f32x4*)(sm + COFF) + lane;
        const f32x4 t = (d[0] + d[56]) + (d[112] + d[168]);
        store_sc1(mk_rsrc(ws + WS_CS + (size_t)tt * F_ * 4 + ft * 896, 896), lane * 16, __builtin_bit_cast(u32x4, t));
      }
      wait_vm<16>(); bar();
      full(7); wait_vm<16>(); bar();
      full(8); wait_vm<16>();
      if (p == 0) count_up(ws, colcnt + CN_AB);
      bar();
#pragma unroll 1
      for (int q = 9; q < 26; q++) { full(q); wait_vm<16>(); bar(); }
      const u32 lastoff = lane < 32 ? FL_ACK * 65536 + grpcell : colcnt + CN_AB;
      const u32 lastmin = lane >= 32 ? 32u : lane == 16 + (ft ^ 8) ? 2u : 1u;
      flag_request(rall, p == 0 ? lastoff : NOWHERE, fv);
      full(26); wait_vm<16>(); bar();
      full(27);
      asm volatile("s_waitcnt vmcnt(16)" : "+v"(fv)::"memory");
      if (p == 0) watch(rall, lastoff, fv, [&](u32 w) { return w >= lastmin; }, tickoff, tick);
      bar();
      halfs(0); wait_vm<14>(); bar();
#pragma unroll 1
      for (int g = 1; g < 8; g++) { halfs(g); wait_vm<12>(); bar(); }
    }
    rsrc_t rfl_ = mk_rsrc(ws + WS_FL + (size_t)tt * 4096, 2 * 16 * 4096);
    {
      rsrc_t rp = mk_rsrc((const char*)w1 + ft * 448, D_ * F_ * 2 - ft * 448);
      const int cola = (ft & 1) * 192, colb = (ft & 1) ? 0 : 256;
      rsrc_t rq = mk_rsrc(ws + WS_H + (size_t)tt * 32 * 8192, 32 * 8192);
      u32 va[4], vb[3];
#pragma unroll
      for (int i = 0; i < 4; i++) {
        const int k = 16 * p + 4 * i + (lane >> 4);
        va[i] = k * (F_ * 2) + cola + (((lane & 15) ^ ((k & 7) << 1)) * 16) - i * 1024;
      }
#pragma unroll
      for (int i = 0; i < 3; i++) {
        const int o = (p * 3 + i) * 64 + lane, k = o / 12, ch = o % 12;
        vb[i] = k * (F_ * 2) + colb + ((ch ^ (((k >> 2) & 1) << 1)) * 16) - i * 1024;
      }
      rsrc_t rw = mk_rsrc(w2 + (size_t)(half * 1792 + tt * 112 + 28 * p) * D_ + et * 128, 27 * D_ * 2 + 256);
      const u32 vw = (lane >> 1) * (D_ * 2) + (lane & 1) * 128;
      auto pa = [&](int g) {
        if (K_WN > 0 && g == 4 + loc % (K_WN > 0 ? K_WN : 1)) pull_lines(lds0 + COFF + 3584, vw, rw, 0);
        dma<4>(ldsb + (g & 3) * SLOT, va, rp, kstep(g, 16, phi) * (64 * F_ * 2));
      };
      auto pbb = [&](int g) { dma<3>(lds0 + p * 3072 + (g & 3) * SLOT, vb, rp, kstep(g, 16, phi) * (64 * F_ * 2)); };
      auto q = [&](int g) { dma_sc1<4>(ldsb + QOFF + (g & 3) * SLOT, vlin, rq, kstep(g, 16, phi) * 16384); };
      wait_vm<6>(); bar();
      wait_vm<0>(); bar();
      bar();
      PSTAMP(13)
      if (p == 0) count_up(ws, rowcnt + CN_H);
      pa(0); pa(1); pa(2);
      rsrc_t rs = mk_rsrc(ws + WS_ST + (size_t)tt * 16384, 16384);
      rsrc_t rc = mk_rsrc(ws + WS_CS + ft * 896, 15 * F_ * 4 + 896);
      u32x2 sv[8];
      u32 cv[16], bv;
#pragma unroll
      for (int i = 0; i < 16; i++)
        asm volatile("buffer_load_dword %0, %1, %2, %3 offen sc1" : "=&v"(cv[i]) : "v"(min(pt, 223) * 4), "s"(rc), "s"(i * F_ * 4) : "memory");
      asm volatile("buffer_load_ushort %0, %1, %2, 0 offen" : "=&v"(bv) : "v"(min(pt, 223) * 2), "s"(mk_rsrc(b1 + ft * 224, 448)) : "memory");
      if (p == 0) { fv = 0; watch(rall, rowcnt + CN_H, fv, [&](u32 w) { return w >= 16u; }, NOWHERE, tick); }
      bar();
      PSTAMP(3)
      q(0); q(1); q(2);
      const u32 so = (pt & 1) * 8192 + (pt >> 1) * 8;
#pragma unroll
      for (int i = 0; i < 8; i++)
        asm volatile("buffer_load_dwordx2 %0, %1, %2, %3 offen sc1" : "=&v"(sv[i]) : "v"(so), "s"(rs), "s"(i * 1024) : "memory");
      wait_vm<16>();
      PSTAMP(4)
      bar();
      pa(3); q(3); wait_vm<20>(); bar();
      pa(4); q(4); wait_vm<24>(); bar();
      pa(5); q(5); wait_vm<16>();
      asm volatile("" : "+v"(sv[0]), "+v"(sv[1]), "+v"(sv[2]), "+v"(sv[3]), "+v"(sv[4]), "+v"(sv[5]), "+v"(sv[6]), "+v"(sv[7]), "+v"(bv)::"memory");
      asm volatile("" : "+v"(cv[0]), "+v"(cv[1]), "+v"(cv[2]), "+v"(cv[3]), "+v"(cv[4]), "+v"(cv[5]), "+v"(cv[6]), "+v"(cv[7]), "+v"(cv[8]),
                   "+v"(cv[9]), "+v"(cv[10]), "+v"(cv[11]), "+v"(cv[12]), "+v"(cv[13]), "+v"(cv[14]), "+v"(cv[15])::"memory");
      bar();
      {
        float s = 0.f;
#pragma unroll
        for (int i = 0; i < 16; i++) s += __uint_as_float(cv[i]);
        if (pt < 224) { tabC[pt] = s; tabBias[pt] = __uint_as_float(bv << 16); }
      }
      {
        float s = 0.f, qq = 0.f;
#pragma unroll
        for (int i = 0; i < 8; i++) { s += __uint_as_float(sv[i][0]); qq += __uint_as_float(sv[i][1]); }
        s += __shfl_xor(s, 1); qq += __shfl_xor(qq, 1);
        const float mean = s * (1.0f / D_), var = fmaxf(qq * (1.0f / D_) - mean * mean, 0.f);
        const float rstd = 1.0f / sqrtf(var + LN_EPS);
        if ((pt & 1) == 0) { tabA[pt >> 1] = rstd; tabB[pt >> 1] = -mean * rstd; }
        wait_lgkm0();
      }
#pragma unroll 1
      for (int g = 6; g < 16; g++) { pa(g); q(g); wait_vm<16>(); bar(); }
      pbb(0); q(0); wait_vm<15>(); bar();
#pragma unroll 1
      for (int g = 1; g < 14; g++) { pbb(g); q(g); wait_vm<14>(); bar(); }
      flag_request(rfl_, FL_RA * 65536 + lane * 64, fv);
      pbb(14); q(14); wait_vm<14>(); bar();
      pbb(15); q(15); wait_vm<14>();
      PSTAMP(5)
      flag_wait(rfl_, FL_RA * 65536 + lane * 64, fv);
      PSTAMP(6)
      bar();
    }
    {
      rsrc_t rp = mk_rsrc(w2 + et * 128, F_ * D_ * 2 - et * 256);
      rsrc_t rq = mk_rsrc(ws + WS_R + (size_t)tt * 112 * 8192, 112 * 8192);
      u32 vp[4];
#pragma unroll
      for (int i = 0; i < 4; i++) {
        const int k = 16 * (p & 1) + 4 * i + (lane >> 4);
        vp[i] = k * (D_ * 2) + (((lane & 15) ^ ((k & 7) << 1)) * 16) - i * 1024;
      }
      const u32 vq0 = (4 * (p & 1) + 2 * half) * 1024 + lane * 16, vq1 = (4 * (p & 1) + 2 - 2 * half) * 1024 + lane * 16;
      const u32 ldsh = lds0 + QOFF + p * 4096;
      auto bblock = [&](int hf, int n) { return 7 * (8 * hf + n / 3) + ((n / 3) & 1 ? 0 : 4) + n % 3; };
      auto full = [&](int g) {
        const int k = g < 16 ? kstep2(g, 16, phi, xcd & 1) : 16 + kstep2(g - 16, 8, phi, xcd & 1), n = 2 * k + (p >> 1);
        const int blk = k < 16 ? 7 * (8 * half + (n >> 2)) + ((n >> 2) & 1) * 3 + (n & 3) : bblock(half, n - 32);
        const u32 m = ldsb + (g & 3) * SLOT;
        dma<4>(m, vp, rp, blk * (32 * D_ * 2));
        dma_sc1<2>(m + QOFF, vq0, rq, blk * 8192);
        dma_sc1<2>(m + QOFF + 2048, vq1, rq, blk * 8192);
      };
      auto halfs = [&](int g) {
        const int k = kstep2(g, 8, phi, xcd & 1), blk = bblock(k >> 2, 16 + 2 * (k & 3) + (p >> 1));
        dma<4>(ldsb + (g & 3) * SLOT, vp, rp, blk * (32 * D_ * 2));
        dma_sc1<2>(ldsh + (g & 3) * SLOT, vq0, rq, blk * 8192);
      };
      full(0); wait_vm<15>(); bar();
#pragma unroll 1
      for (int g = 1; g < 14; g++) { full(g); wait_vm<16>(); bar(); }
      flag_request(rfl_, FL_RB * 65536 + lane * 64, fv);
      full(14); wait_vm<16>(); bar();
      full(15); wait_vm<16>();
      PSTAMP(7)
      flag_wait(rfl_, FL_RB * 65536 + lane * 64, fv);
      PSTAMP(8)
      bar();
#pragma unroll 1
      for (int g = 16; g < 24; g++) { full(g); wait_vm<16>(); bar(); }
      halfs(0); wait_vm<14>(); bar();
#pragma unroll 1
      for (int g = 1; g < 8; g++) { halfs(g); wait_vm<12>(); bar(); }
      wait_vm<6>(); bar();
      wait_vm<0>(); bar();
      PDIAG_END
    }
  } else {
    const int wP = wid >> 1, wQ = wid & 1, c = lane & 15, g = lane >> 4;
    const int m0 = 4 * wQ + 2 * half, cell = ft * 4 + wid;
    {
      Consumer<4, M_ROW, M_ROW> C;
      C.init(sm, wP, wQ, lane, wQ, wP);
      f32x4 acc[4][4], sx[2], sxx[2], sd[2];
      C.zero(acc);
#pragma unroll
      for (int i = 0; i < 2; i++) sx[i] = sxx[i] = sd[i] = (f32x4){0.f, 0.f, 0.f, 0.f};
      const u32x4 ones4 = {0x3F803F80u, 0x3F803F80u, 0x3F803F80u, 0x3F803F80u};
      bf16x8 ones = __builtin_bit_cast(bf16x8, ones4);
      asm volatile("" : "+v"(ones));
      bar();
      STAMP(1)
      C.first();
#define G1_HALF0(S) C.load<S, 1, 4>(); C.mma<0, 4>(acc); C.stats<0>(sx, sxx, sd, ones); C.interleave<22, 4>();
#define G1_STEP(S) G1_HALF0(S) wait_lgkm0(); bar(); __builtin_amdgcn_sched_barrier(0); \
      C.load<(S + 1) % NST, 0, 4>(); C.mma<1, 4>(acc); C.stats<1>(sx, sxx, sd, ones); C.interleave<22, 4>();
      for (int it = 0; it < 3; it++) { G1_STEP(0) G1_STEP(1) G1_STEP(2) G1_STEP(3) }
      G1_STEP(0) G1_STEP(1) G1_STEP(2) G1_HALF0(3)
      wait_lgkm0();
      C.stats<1>(sx, sxx, sd, ones);
      __builtin_amdgcn_sched_barrier(0);
      C.mma<1, 4>(acc);
#pragma unroll
      for (int i = 0; i < 2; i++) {
        const int e = c & 3;
        const float q = e == 0 ? sxx[i][0] : e == 1 ? sxx[i][1] : e == 2 ? sxx[i][2] : sxx[i][3];
        const float s = e == 0 ? sx[i][0] : e == 1 ? sx[i][1] : e == 2 ? sx[i][2] : sx[i][3];
        const float mean = s * (1.0f / D_), var = fmaxf(q * (1.0f / D_) - mean * mean, 0.f);
        const float rstd = (half ? -1.f : 1.f) * __builtin_amdgcn_rsqf(var + LN_EPS);
        const int at = (c >> 2) == g ? 64 * wP + 32 * wQ + 16 * i + c : 384 + lane;
        tabA[at] = rstd; tabB[at] = -mean * rstd;
      }
      tabC[g < 2 ? 64 * wQ + 32 * wP + 16 * g + c : 128 + lane] = g ? sd[1][0] : sd[0][0];
#pragma unroll
      for (int i = 0; i < 16; i++) { __builtin_amdgcn_sched_group_barrier(0x8, 1, 0); __builtin_amdgcn_sched_group_barrier(0x2, 3, 0); }
      __builtin_amdgcn_sched_barrier(0);
      STAMP(2)
      wait_lgkm0(); bar();
      rsrc_t ro = mk_rsrc(ws + WS_AB + ((size_t)((half * 8 + et) * 32 + 2 * tt + wP) * 16 + 4 * wQ) * 1024, 12288);
      f32x4 ra[4], rb[4];
      float rs[4];
#pragma unroll
      for (int i = 0; i < 4; i++) {
        ra[i] = *(const LAS f32x4*)(tabA + 64 * wP + 16 * (i ^ (2 * wQ)) + 4 * g);
        rb[i] = *(const LAS f32x4*)(tabB + 64 * wP + 16 * (i ^ (2 * wQ)) + 4 * g);
        rs[i] = tabC[64 * wQ + 16 * (i ^ (2 * wP)) + c];
      }
#pragma unroll
      for (int j = 0; j < 4; j++)
#pragma unroll
        for (int u = 0; u < 2; u++) {
          const f32x4 a = ra[2 * u] * acc[2 * u][j] + rb[2 * u] * rs[j], b = ra[2 * u + 1] * acc[2 * u + 1][j] + rb[2 * u + 1] * rs[j];
          const u32x4 o = {cvtpk(a[0], a[1]), cvtpk(a[2], a[3]), cvtpk(b[0], b[1]), cvtpk(b[2], b[3])};
          const int at = ((u ^ wQ) * 8 + (j ^ (2 * wP))) * 1024 + lane * 16;
          store_sc1(ro, at, o);
          *(LAS u32x4*)(sm + (wP * SLOT + wQ * 4096 + at)) = o;
        }
    }
    u32x4 hk[2][2];
    {
      Consumer<4, M_TILED, M_ROW> C;
      C.init(sm, wP, wQ, lane);
      f32x4 acc[4][4];
      wait_lgkm0(); bar();
      STAMP(3)
      C.first();
      C.load<0, 1, 4>(); C.mma<0, 4, true>(acc); C.interleave<16, 4>();
      wait_lgkm0(); bar();
      __builtin_amdgcn_sched_barrier(0);
      C.load<1, 0, 4>(); C.mma<1, 4>(acc); C.interleave<16, 4>();
      C.load<1, 1, 4>(); C.mma<0, 4>(acc); C.interleave<16, 4>();
      wait_lgkm0();
      C.mma<1, 4>(acc);
      __builtin_amdgcn_sched_barrier(0);
      wait_vm<0>();
      STAMP(4)
      bar();
      bar();
      bar();
      STAMP(5)
      C.load<2, 0, 4>();
      C.step<2, 4, 4>(acc); C.step<3, 4, 4>(acc);
      for (int it = 0; it < 5; it++) C.step4<4, 4>(acc);
      C.step4<4, 2>(acc);
      STAMP(6)
      xch_send(ws + WS_X2, acc, wid, lane);
      __builtin_amdgcn_sched_barrier(0);
      rsrc_t rx = mk_rsrc(ws + WS_X2 + ((size_t)(blockIdx.x ^ 4) * 4 + wid) * 4096, 4096);
      rsrc_t rf = mk_rsrc(ws + WS_FL + ((size_t)(FL_X2 * 16 + tt) * 64 + (cell ^ 32)) * 64, 64);
      u32x4 rv[4];
      u32 fv;
      C.step<0, 2, 2>(acc); C.step<1, 2, 2>(acc); C.step<2, 2, 2>(acc);
      if (K_XF == 3) flag_set(ws, FL_X2, tt, cell);
      C.step<3, 2, 2>(acc);
      if (K_XF == 4) flag_set(ws, FL_X2, tt, cell);
      C.step<0, 2, 2>(acc); C.step<1, 2, 2>(acc);
      flag_request(rf, 0, fv);
      __builtin_amdgcn_sched_barrier(0);
      C.step<2, 2, 2>(acc);
      wait_vm<0>();
      flag_wait(rf, 0, fv);
      xch_request(rx, rv, lane);
      __builtin_amdgcn_sched_barrier(0);
      C.load<3, 1, 2>(); C.mma<0, 2>(acc); C.interleave<8, 2>();
      C.mma<1, 2>(acc);
      STAMP(7)
      xch_finish(rv);
            rsrc_t rh = mk_rsrc(ws + WS_H + ((size_t)(tt * 32 + 4 * et + 2 * wP) * 8 + m0) * 1024, 16384);
      rsrc_t rs = mk_rsrc(ws + WS_ST + ((size_t)(tt * 16 + 2 * et + wP) * 128 + 16 * m0) * 8, 256);
#pragma unroll
      for (int jj = 0; jj < 2; jj++) {
        float s = 0.f, q = 0.f;
#pragma unroll
        for (int u = 0; u < 2; u++) {
          const u32x4 o = rv[jj * 2 + u];
          const f32x4 a = acc[2 * u][jj], b = acc[2 * u + 1][jj];
          const int xrow = 32 * wQ + 16 * jj + c;
          const LAS char* xp = sm + (XOFF + wP * 8192 + xrow * 128 + (g & 1) * 8);
          const u32x2 xa = *(const LAS u32x2*)(xp + (((4 * u + (g >> 1)) ^ ((xrow >> 1) & 7)) * 16));
          const u32x2 xb = *(const LAS u32x2*)(xp + (((4 * u + 2 + (g >> 1)) ^ ((xrow >> 1) & 7)) * 16));
          u32x4 h;
          h[0] = cvtpk(a[0] + blo(o[0]) + blo(xa[0]), a[1] + bhi(o[0]) + bhi(xa[0]));
          h[1] = cvtpk(a[2] + blo(o[1]) + blo(xa[1]), a[3] + bhi(o[1]) + bhi(xa[1]));
          h[2] = cvtpk(b[0] + blo(o[2]) + blo(xb[0]), b[1] + bhi(o[2]) + bhi(xb[0]));
          h[3] = cvtpk(b[2] + blo(o[3]) + blo(xb[1]), b[3] + bhi(o[3]) + bhi(xb[1]));
          store_sc1(rh, (u * 8 + jj) * 1024 + lane * 16, h);
          hk[jj][u] = h;
#pragma unroll
          for (int e = 0; e < 4; e++) { const float lo = blo(h[e]), hi = bhi(h[e]); s += lo + hi; q += lo * lo + hi * hi; }
        }
        s = xsum32(xsum16(s)); q = xsum32(xsum16(q));
        if (g == 0) __builtin_amdgcn_raw_buffer_store_b64((u32x2){__float_as_uint(s), __float_as_uint(q)}, rs, (16 * jj + c) * 8, 0, 16);
      }
      wait_vm<0>();
      STAMP(8)
      bar();
#pragma unroll
      for (int q = 0; q < 4; q++) *(LAS u32x4*)(sm + XOFF + wid * 4096 + q * 1024 + lane * 16) = hk[q >> 1][q & 1];
      bar();
    }
    rsrc_t rr = mk_rsrc(ws + WS_R + ((size_t)(tt * 112 + 7 * ft) * 8 + 4 * wQ) * 1024, 7 * 8192);
    float ra[4], rb[4];
    f32x4 cs[3], bi[3];
    u32 pk[6];
    auto act2 = [&](const f32x4& a, int i, int j, int e) {
      const f32x2 t = (f32x2){rb[j], rb[j]} * (f32x2){cs[i][2 * e], cs[i][2 * e + 1]} + (f32x2){bi[i][2 * e], bi[i][2 * e + 1]};
      const f32x2 v = (f32x2){ra[j], ra[j]} * (f32x2){a[2 * e], a[2 * e + 1]} + t;
      return cvtpk(v[0] * fmaxf(v[0], 0.f), v[1] * fmaxf(v[1], 0.f));
    };
    const int ca = (ft & 1) * 96, cb = (ft & 1) ? 0 : 128;
    const u32 sta = (ca / 32 + 2 * wP) * 8192 + lane * 16, stb = (cb / 32) * 8192 + lane * 16;
    auto tables = [&](int i, int col) { cs[i] = *(const LAS f32x4*)(tabC + col); bi[i] = *(const LAS f32x4*)(tabBias + col); };
    f32x4 accA[4][4], accB[3][4];
    {
      Consumer<4, M_TR256, M_TILED> C;
      C.init(sm, wP, wQ, lane);
      C.zero(accA);
      bar();
      STAMP(9)
      C.first();
      for (int it = 0; it < 3; it++) C.step4<4, 4>(accA);
#pragma unroll
      for (int j = 0; j < 4; j++) { ra[j] = tabA[64 * wQ + 16 * j + c]; rb[j] = tabB[64 * wQ + 16 * j + c]; }
      tables(0, ca + 64 * wP + 4 * g); tables(1, ca + 64 * wP + 16 + 4 * g);
      __builtin_amdgcn_sched_barrier(0);
      C.last4<4>(accA);
      STAMP(10)
    }
    {
      auto outA = [&](int u, int j0, int n) {
        if (n < 8) {
          const int j = j0 + (n >> 2), t = (n >> 1) & 1;
          pk[n & 3] = act2(accA[2 * u + t][j], t, j, n & 1);
          if ((n & 3) == 3) store_sc1(rr, sta + (u * 8 + j) * 1024, (u32x4){pk[0], pk[1], pk[2], pk[3]});
        } else if (n == 8 && u == 0 && j0 == 2) {
          tables(0, ca + 64 * wP + 32 + 4 * g); tables(1, ca + 64 * wP + 48 + 4 * g);
        }
      };
      Consumer<3, M_TR192, M_TILED> C;
      C.init(sm, wP, wQ, lane);
      bar();
      STAMP(11)
      C.first();
      C.step_with<0, true>(accB, [&](int n) { outA(0, 0, n); }, [&](int n) { outA(0, 2, n); });
      C.step_with<1, false>(accB, [&](int n) { outA(1, 0, n); }, [&](int n) { outA(1, 2, n); });
      C.step<2, 4, 4>(accB); C.step<3, 4, 4>(accB);
      flag_set(ws, FL_RA, tt, cell);
      C.step<0, 4, 4>(accB);
      C.step<1, 4, 4>(accB);
      C.step<2, 4, 4>(accB); C.step<3, 4, 4>(accB);
      C.step4<4, 4>(accB);
      tables(0, cb + 64 * wP + 4 * g); tables(1, cb + 16 + 64 * wP + 4 * g); tables(2, cb + 32 + 16 * wP + 4 * g);
      __builtin_amdgcn_sched_barrier(0);
      C.last4<4>(accB);
      STAMP(12)
    }
    {
      auto outB = [&](int j, int n) {
        if ((n & 1) == 0 && n < 12) {
          const int q = n >> 1;
          pk[q] = act2(accB[q >> 1][j], q >> 1, j, q & 1);
          if (q == 3) store_sc1(rr, stb + (16 * wP + j) * 1024, (u32x4){pk[0], pk[1], pk[2], pk[3]});
          if (q == 5) __builtin_amdgcn_raw_buffer_store_b64((u32x2){pk[4], pk[5]}, rr, stb + (8 + j) * 1024 + 8 * wP, 0, 16);
        }
      };
      Consumer<4, M_TR256, M_TILED> C;
      C.init(sm, wP, wQ, lane);
      f32x4 acc[4][4];
      bar();
      STAMP(13)
      C.first();
      C.step_with<0, true>(acc, [&](int n) { outB(0, n); }, [&](int n) { outB(1, n); });
      C.step_with<1, false>(acc, [&](int n) { outB(2, n); }, [&](int n) { outB(3, n); });
      C.step<2, 4, 4>(acc); C.step<3, 4, 4>(acc);
      flag_set(ws, FL_RB, tt, cell);
      C.step<0, 4, 4>(acc);
      C.step<1, 4, 4>(acc);
      C.step<2, 4, 4>(acc); C.step<3, 4, 4>(acc);
      for (int it = 0; it < 3; it++) C.step4<4, 4>(acc);
      C.step4<4, 2>(acc);
      STAMP(14)
      xch_send(ws + WS_X4, acc, wid, lane);
      u32x2 br[4];
#pragma unroll
      for (int i = 0; i < 4; i++) br[i] = *(const u32x2*)(b2 + et * 128 + 64 * wP + 16 * i + 4 * g);
      __builtin_amdgcn_sched_barrier(0);
      rsrc_t rx = mk_rsrc(ws + WS_X4 + ((size_t)(blockIdx.x ^ 4) * 4 + wid) * 4096, 4096);
      rsrc_t rf = mk_rsrc(ws + WS_FL + ((size_t)(FL_X4 * 16 + tt) * 64 + (cell ^ 32)) * 64, 64);
      u32x4 rv[4];
      u32 fv;
      C.step<0, 2, 2>(acc); C.step<1, 2, 2>(acc); C.step<2, 2, 2>(acc);
      if (K_XF == 3) flag_set(ws, FL_X4, tt, cell);
      C.step<3, 2, 2>(acc);
      if (K_XF == 4) flag_set(ws, FL_X4, tt, cell);
      C.step<0, 2, 2>(acc); C.step<1, 2, 2>(acc);
      flag_request(rf, 0, fv);
      __builtin_amdgcn_sched_barrier(0);
      C.step<2, 2, 2>(acc);
      wait_vm<0>();
      flag_wait(rf, 0, fv);
      xch_request(rx, rv, lane);
      __builtin_amdgcn_sched_barrier(0);
      C.load<3, 1, 2>(); C.mma<0, 2>(acc); C.interleave<8, 2>();
      C.mma<1, 2>(acc);
      xch_finish(rv);

      rsrc_t ro = mk_rsrc(out + (size_t)(tt * 128 + 16 * m0) * D_ + et * 128 + 64 * wP, 31 * D_ * 2 + 128);
#pragma unroll
      for (int jj = 0; jj < 2; jj++)
#pragma unroll
        for (int u = 0; u < 2; u++) {
          const u32x4 o = rv[jj * 2 + u], h = *(const LAS u32x4*)(sm + XOFF + wid * 4096 + (jj * 2 + u) * 1024 + lane * 16);
          const f32x4 a = acc[2 * u][jj], b = acc[2 * u + 1][jj];
          const u32x2 ba = br[2 * u], bb = br[2 * u + 1];
          const u32 a0 = cvtpk(a[0] + blo(o[0]) + blo(ba[0]) + blo(h[0]), a[1] + bhi(o[0]) + bhi(ba[0]) + bhi(h[0]));
          const u32 a1 = cvtpk(a[2] + blo(o[1]) + blo(ba[1]) + blo(h[1]), a[3] + bhi(o[1]) + bhi(ba[1]) + bhi(h[1]));
          const u32 b0 = cvtpk(b[0] + blo(o[2]) + blo(bb[0]) + blo(h[2]), b[1] + bhi(o[2]) + bhi(bb[0]) + bhi(h[2]));
          const u32 b1_ = cvtpk(b[2] + blo(o[3]) + blo(bb[1]) + blo(h[3]), b[3] + bhi(o[3]) + bhi(bb[1]) + bhi(h[3]));
          const auto s0 = __builtin_amdgcn_permlane16_swap(a0, b0, false, false);
          const auto s1 = __builtin_amdgcn_permlane16_swap(a1, b1_, false, false);
          __builtin_amdgcn_raw_buffer_store_b128((u32x4){s0[0], s1[0], s0[1], s1[1]}, ro,
                                                 (16 * jj + c) * (D_ * 2) + (32 * u + 16 * (g & 1) + 8 * (g >> 1)) * 2, 0, 16);
        }
      DIAG_END(15)
    }
  }
}

}

extern "C" void kernel_launch(void* const* d_in, const int* in_sizes, int n_in, void* d_out, int out_size, void* d_ws, size_t ws_size,
                              hipStream_t stream) {
  const u16* const* I = (const u16* const*)d_in;
  (void)hipFuncSetAttribute((const void*)fused_kernel, hipFuncAttributeMaxDynamicSharedMemorySize, LDS_BYTES);
  fused_kernel<<<256, 512, LDS_BYTES, stream>>>(I[0], I[1], I[2], I[3], I[4], I[6], I[7], I[8], I[9], (char*)d_ws, (u16*)d_out);
}
```

```cpp
#include <hip/hip_runtime.h>
#include <hip/hip_bf16.h>
#include <type_traits>

typedef unsigned short u16;
typedef unsigned int u32;
typedef unsigned long long u64;
typedef __bf16 bf16x8 __attribute__((ext_vector_type(8)));
typedef __bf16 bf16x2 __attribute__((ext_vector_type(2)));
typedef short s16x4 __attribute__((ext_vector_type(4)));
typedef short s16x8 __attribute__((ext_vector_type(8)));
typedef float f32x2 __attribute__((ext_vector_type(2)));
typedef float f32x4 __attribute__((ext_vector_type(4)));
typedef u32 u32x2 __attribute__((ext_vector_type(2)));
typedef u32 u32x4 __attribute__((ext_vector_type(4)));
typedef __amdgpu_buffer_rsrc_t rsrc_t;
#define LAS __attribute__((address_space(3)))
#define DI __device__ __forceinline__

#define K_WN 8
#define K_SLEEP 1
#define K_SPIN (1 << 18)
#define K_GAPS(NM, NR) ((NR) < (NM) ? (NR) : (NM))
#define K_PPRIO 0
#define K_CSP 0
#define K_WB 1
#define K_REV 1
#define K_LEAD 0
#define K_XF 3
namespace {

constexpr int S_ = 2048, D_ = 1024, F_ = 3584;
constexpr float LN_EPS = 1e-5f;

constexpr size_t WS_AB = 0;
constexpr size_t WS_H = 8u << 20;
constexpr size_t WS_R = 12u << 20;
constexpr size_t WS_X2 = 27u << 20;
constexpr size_t WS_X4 = 31u << 20;
constexpr size_t WS_ST = 35u << 20;
constexpr size_t WS_CS = 36u << 20;
constexpr size_t WS_SX = 38u << 20;
constexpr size_t WS_FL = 37u << 20;

#define DIAG_ARG
#define DIAG_BEGIN
#define STAMP(i)
#define PSTAMP(i)
#define PDIAG_END
#define DIAG_END(i)

DI u32 rfl(u32 x) { return __builtin_amdgcn_readfirstlane(x); }
DI u32 cvtpk(float lo, float hi) { f32x2 v = {lo, hi}; return __builtin_bit_cast(u32, __builtin_convertvector(v, bf16x2)); }
DI float blo(u32 v) { return __uint_as_float(v << 16); }
DI float bhi(u32 v) { return __uint_as_float(v & 0xFFFF0000u); }
DI rsrc_t mk_rsrc(const void* p, u32 bytes) { return __builtin_amdgcn_make_buffer_rsrc((void*)p, 0, bytes, 0x00020000); }
DI void bar() { asm volatile("s_barrier" ::: "memory"); }
DI void wait_lgkm0() { __builtin_amdgcn_s_waitcnt(0xC07F); }
template <int N> DI void wait_vm() { asm volatile("s_waitcnt vmcnt(%0)" ::"i"(N) : "memory"); }

template <int N> DI void dma(u32 m0, const u32* v, rsrc_t r, u32 so) {
  if constexpr (N == 4)
    asm volatile("s_mov_b32 m0, %0\n\ts_nop 3\n\t"
                 "buffer_load_dwordx4 %1, %5, %6 offen lds\n\t"
                 "buffer_load_dwordx4 %2, %5, %6 offen offset:1024 lds\n\t"
                 "buffer_load_dwordx4 %3, %5, %6 offen offset:2048 lds\n\t"
                 "buffer_load_dwordx4 %4, %5, %6 offen offset:3072 lds"
                 ::"s"(m0), "v"(v[0]), "v"(v[1]), "v"(v[2]), "v"(v[3]), "s"(r), "s"(so) : "memory");
  else if constexpr (N == 3)
    asm volatile("s_mov_b32 m0, %0\n\ts_nop 3\n\t"
                 "buffer_load_dwordx4 %1, %4, %5 offen lds\n\t"
                 "buffer_load_dwordx4 %2, %4, %5 offen offset:1024 lds\n\t"
                 "buffer_load_dwordx4 %3, %4, %5 offen offset:2048 lds"
                 ::"s"(m0), "v"(v[0]), "v"(v[1]), "v"(v[2]), "s"(r), "s"(so) : "memory");
  else
    asm volatile("s_mov_b32 m0, %0\n\ts_nop 3\n\t"
                 "buffer_load_dwordx4 %1, %3, %4 offen lds\n\t"
                 "buffer_load_dwordx4 %2, %3, %4 offen offset:1024 lds"
                 ::"s"(m0), "v"(v[0]), "v"(v[1]), "s"(r), "s"(so) : "memory");
}
template <int N> DI void dma_sc1(u32 m0, u32 v, rsrc_t r, u32 so) {
  if constexpr (N == 4)
    asm volatile("s_mov_b32 m0, %0\n\ts_nop 3\n\t"
                 "buffer_load_dwordx4 %1, %2, %3 offen sc1 lds\n\t"
                 "buffer_load_dwordx4 %1, %2, %3 offen offset:1024 sc1 lds\n\t"
                 "buffer_load_dwordx4 %1, %2, %3 offen offset:2048 sc1 lds\n\t"
                 "buffer_load_dwordx4 %1, %2, %3 offen offset:3072 sc1 lds"
                 ::"s"(m0), "v"(v), "s"(r), "s"(so) : "memory");
  else
    asm volatile("s_mov_b32 m0, %0\n\ts_nop 3\n\t"
                 "buffer_load_dwordx4 %1, %2, %3 offen sc1 lds\n\t"
                 "buffer_load_dwordx4 %1, %2, %3 offen offset:1024 sc1 lds"
                 ::"s"(m0), "v"(v), "s"(r), "s"(so) : "memory");
}

DI float xsum16(float v) { const auto r = __builtin_amdgcn_permlane16_swap(__float_as_uint(v), __float_as_uint(v), false, false); return __uint_as_float(r[0]) + __uint_as_float(r[1]); }
DI float xsum32(float v) { const auto r = __builtin_amdgcn_permlane32_swap(__float_as_uint(v), __float_as_uint(v), false, false); return __uint_as_float(r[0]) + __uint_as_float(r[1]); }

template <int A, int B, class F> DI void static_for(F f) {
  if constexpr (A < B) { f(std::integral_constant<int, A>{}); static_for<A + 1, B>(f); }
}

DI void pull_lines(u32 m0, u32 v, rsrc_t r, u32 so) {
  asm volatile("s_mov_b32 m0, %0\n\ts_nop 3\n\tbuffer_load_dword %1, %2, %3 offen lds" ::"s"(m0), "v"(v), "s"(r), "s"(so) : "memory");
}

DI int kstep(int t, int n, int phi) {
  int k = t;
  if ((t & 3) == phi) { k = t + 4; if (k >= n) k = phi; }
  return k;
}

DI int kstep2(int t, int n, int phi, int side) {
  const int k = kstep(t, n, phi), k2 = k ^ (side << 2);
  return k2 < n ? k2 : k;
}

constexpr int NST = 4;
constexpr int SLOT = 16384;
constexpr int QOFF = NST * SLOT;
constexpr int TOFF = 2 * NST * SLOT;
constexpr int COFF = TOFF + 4096;
constexpr int XOFF = COFF + 4096;
constexpr int LDS_BYTES = XOFF + 16384;

enum { M_ROW = 0, M_TILED = 1, M_TR192 = 2, M_TR256 = 3 };

template <int PT, int PMODE, int QMODE>
struct Consumer {
  const LAS char* pb[4];
  const LAS char* qb[4];
  bf16x8 fp[2][PT], fq[2][4];

  DI void init(const LAS char* sm, int wP, int wQ, int lane, int prot = 0, int qrot = 0) {
    const int r = lane & 15, g = lane >> 4;
    if constexpr (PMODE == M_ROW) {
      const int a = (64 * wP + r) * 128 + ((g ^ (r >> 1)) * 16);
      pb[0] = sm + (a + prot * 4096); pb[1] = sm + ((a ^ 64) + prot * 4096);
      pb[2] = sm + (a + 4096 - prot * 4096); pb[3] = sm + ((a ^ 64) + 4096 - prot * 4096);
    } else if constexpr (PMODE == M_TILED) {
      pb[0] = sm + (4 * wP * 1024 + lane * 16);
    } else if constexpr (PMODE == M_TR192) {
      const int b = g & 1, a = (4 * g + (r >> 2)) * 192 + (r & 3) * 8;
      pb[0] = sm + (a + (4 * wP + b) * 32);
      pb[1] = sm + (a + (4 * wP + 1 - b) * 32);
      pb[2] = sm + (a + ((2 + wP) ^ b) * 32);
    } else {
      const int k = 4 * g + (r >> 2), a = k * 256 + (r & 3) * 8;
#pragma unroll
      for (int i = 0; i < 4; i++) pb[i] = sm + (a + (((4 * wP + i) ^ (k & 7)) * 32));
    }
    if constexpr (QMODE == M_ROW) {
      const int a = QOFF + (64 * wQ + r) * 128 + ((g ^ (r >> 1)) * 16);
      qb[0] = sm + (a + qrot * 4096); qb[1] = sm + ((a ^ 64) + qrot * 4096);
      qb[2] = sm + (a + 4096 - qrot * 4096); qb[3] = sm + ((a ^ 64) + 4096 - qrot * 4096);
      asm volatile("" : "+v"(qb[0]), "+v"(qb[1]), "+v"(qb[2]), "+v"(qb[3]));
    } else {
      qb[0] = sm + (QOFF + 4 * wQ * 1024 + lane * 16);
      asm volatile("" : "+v"(qb[0]));
    }
  }
  static DI bf16x8 tr2(const LAS char* p, int second) {
    s16x4 a = __builtin_amdgcn_ds_read_tr16_b64_v4i16((LAS s16x4*)p);
    s16x4 b = __builtin_amdgcn_ds_read_tr16_b64_v4i16((LAS s16x4*)(p + second));
    return __builtin_bit_cast(bf16x8, __builtin_shufflevector(a, b, 0, 1, 2, 3, 4, 5, 6, 7));
  }
  template <int S, int KS> DI bf16x8 ldp(int i) {
    if constexpr (PMODE == M_ROW) return *(const LAS bf16x8*)(pb[KS + (i & 2)] + (S * SLOT + (i & 1) * 2048));
    else if constexpr (PMODE == M_TILED) return *(const LAS bf16x8*)(pb[0] + (S * SLOT + KS * 8192 + i * 1024));
    else if constexpr (PMODE == M_TR192) return tr2(pb[i] + (S * SLOT + KS * 32 * 192), 16 * 192);
    else return tr2(pb[i] + (S * SLOT + KS * 32 * 256), 16 * 256);
  }
  template <int S, int KS> DI bf16x8 ldq(int j) {
    if constexpr (QMODE == M_ROW) return *(const LAS bf16x8*)(qb[KS + (j & 2)] + (S * SLOT + (j & 1) * 2048));
    else return *(const LAS bf16x8*)(qb[0] + (S * SLOT + KS * 8192 + j * 1024));
  }
  template <int S, int KS, int QN> DI void load() {
    fq[KS][0] = ldq<S, KS>(0);
#pragma unroll
    for (int i = 0; i < PT; i++) fp[KS][i] = ldp<S, KS>(i);
#pragma unroll
    for (int j = 1; j < QN; j++) fq[KS][j] = ldq<S, KS>(j);
  }
  template <int KS, int QN, bool ZERO = false> DI void mma(f32x4 (&acc)[PT][4]) {
#pragma unroll
    for (int n = 0; n < PT * QN; n++) {
      const int j = n / PT, i = (j & 1) ? PT - 1 - n % PT : n % PT;
      acc[i][j] = __builtin_amdgcn_mfma_f32_16x16x32_bf16(fp[KS][i], fq[KS][j], ZERO ? (f32x4){0.f, 0.f, 0.f, 0.f} : acc[i][j], 0, 0, 0);
    }
  }
  template <int KS> DI void qsums(f32x4 (&sd)[2], bf16x8 ones) {
#pragma unroll
    for (int i = 0; i < 2; i++) sd[i] = __builtin_amdgcn_mfma_f32_16x16x32_bf16(ones, fq[KS][i], sd[i], 0, 0, 0);
  }
  template <int NM, int QN> static DI void interleave() {
    constexpr int NR = PT * (PMODE >= M_TR192 ? 2 : 1) + QN, G = K_GAPS(NM, NR);
    static_for<0, G>([](auto ic) {
      __builtin_amdgcn_sched_group_barrier(0x8, 1, 0);
      __builtin_amdgcn_sched_group_barrier(0x100, NR / G + (ic.value < NR % G ? 1 : 0), 0);
    });
    __builtin_amdgcn_sched_group_barrier(0x8, NM - G, 0);
    __builtin_amdgcn_sched_barrier(0);
  }
  template <int S, int QC, int QN> DI void step(f32x4 (&acc)[PT][4]) {
    load<S, 1, QC>(); mma<0, QC>(acc); interleave<PT * QC, QC>();
    wait_lgkm0(); bar();
    __builtin_amdgcn_sched_barrier(0);
    load<(S + 1) % NST, 0, QN>(); mma<1, QC>(acc); interleave<PT * QC, QN>();
  }
  template <int S, int LS, bool ZERO, class W> DI void half_with(f32x4 (&acc)[PT][4], W w) {
    static_for<0, PT * 4>([&](auto nc) {
      constexpr int n = nc.value, j = n / PT, i = (j & 1) ? PT - 1 - n % PT : n % PT;
      acc[i][j] = __builtin_amdgcn_mfma_f32_16x16x32_bf16(fp[1 - LS][i], fq[1 - LS][j], ZERO ? (f32x4){0.f, 0.f, 0.f, 0.f} : acc[i][j], 0, 0, 0);
      if constexpr (n == 0) fq[LS][0] = ldq<S, LS>(0);
      else if constexpr (n <= PT) fp[LS][n - 1] = ldp<S, LS>(n - 1);
      else if constexpr (n < PT + 4) fq[LS][n - PT] = ldq<S, LS>(n - PT);
      w(nc);
      __builtin_amdgcn_sched_barrier(0);
    });
  }
  template <int S, bool ZERO, class W0, class W1> DI void step_with(f32x4 (&acc)[PT][4], W0 w0, W1 w1) {
    half_with<S, 1, ZERO>(acc, w0);
    wait_lgkm0(); bar();
    __builtin_amdgcn_sched_barrier(0);
    half_with<(S + 1) % NST, 0, false>(acc, w1);
  }
  template <int QC, int QN> DI void step4(f32x4 (&acc)[PT][4]) {
    step<0, QC, QC>(acc); step<1, QC, QC>(acc); step<2, QC, QC>(acc); step<3, QC, QN>(acc);
  }
  template <int QC> DI void last4(f32x4 (&acc)[PT][4]) {
    step<0, QC, QC>(acc); step<1, QC, QC>(acc); step<2, QC, QC>(acc);
    load<3, 1, QC>(); mma<0, QC>(acc); interleave<PT * QC, QC>();
    wait_lgkm0();
    mma<1, QC>(acc);
  }
  static DI void zero(f32x4 (&acc)[PT][4]) {
#pragma unroll
    for (int i = 0; i < PT; i++)
#pragma unroll
      for (int j = 0; j < 4; j++) { acc[i][j] = (f32x4){0.f, 0.f, 0.f, 0.f}; asm volatile("" : "+v"(acc[i][j])); }
  }
  DI void first() { load<0, 0, 4>(); }
};

DI u32 row_src(int imgrow, int srcrow, int lane, int ldbytes) {
  return srcrow * ldbytes + (((lane & 7) ^ ((imgrow >> 1) & 7)) * 16);
}
DI void store_sc1(rsrc_t r, u32 off, u32x4 v) { __builtin_amdgcn_raw_buffer_store_b128(v, r, off, 0, 16); }

DI void xch_send(char* x, const f32x4 (&acc)[4][4], int wid, int lane) {
  rsrc_t r = mk_rsrc(x + ((size_t)blockIdx.x * 4 + wid) * 4096, 4096);
#pragma unroll
  for (int jj = 0; jj < 2; jj++)
#pragma unroll
    for (int u = 0; u < 2; u++) {
      const f32x4 a = acc[2 * u][2 + jj], b = acc[2 * u + 1][2 + jj];
      u32x4 v = {cvtpk(a[0], a[1]), cvtpk(a[2], a[3]), cvtpk(b[0], b[1]), cvtpk(b[2], b[3])};
      store_sc1(r, lane * 16 + (jj * 2 + u) * 1024, v);
    }
}
DI void xch_request(rsrc_t r, u32x4 (&v)[4], int lane) {
  asm volatile("s_nop 4\n\tbuffer_load_dwordx4 %0, %4, %5, 0 offen sc1\n\t"
               "buffer_load_dwordx4 %1, %4, %5, 0 offen offset:1024 sc1\n\t"
               "buffer_load_dwordx4 %2, %4, %5, 0 offen offset:2048 sc1\n\t"
               "buffer_load_dwordx4 %3, %4, %5, 0 offen offset:3072 sc1"
               : "=&v"(v[0]), "=&v"(v[1]), "=&v"(v[2]), "=&v"(v[3]) : "v"(lane * 16), "s"(r) : "memory");
}
DI void xch_finish(u32x4 (&v)[4]) { asm volatile("s_waitcnt vmcnt(0)" : "+v"(v[0]), "+v"(v[1]), "+v"(v[2]), "+v"(v[3])::"memory"); }

enum { FL_RA = 0, FL_RB = 1, FL_X2 = 2, FL_X4 = 3, FL_ACK = 4, FL_TICK = 5, FL_CNT = 6 };
enum { CN_H = 0, CN_AB = 128, CN_SX = 256 };
constexpr u32 NOWHERE = 0xFFFFFF00u;
DI void flag_set(char* ws, int which, int tt, int cell) {
  wait_vm<0>();
  if ((threadIdx.x & 63) == 0)
    __hip_atomic_store((u32*)(ws + WS_FL + ((size_t)(which * 16 + tt) * 64 + cell) * 64), 1u, __ATOMIC_RELAXED, __HIP_MEMORY_SCOPE_AGENT);
}
DI void flag_request(rsrc_t r, u32 off, u32& v) {
  asm volatile("s_nop 4\n\tbuffer_load_dword %0, %1, %2, 0 offen sc1" : "=&v"(v) : "v"(off), "s"(r) : "memory");
}
DI void flag_wait(rsrc_t r, u32 off, u32& v) {
  asm volatile("" : "+v"(v)::"memory");
#pragma unroll 1
  for (int spin = 0; spin < K_SPIN; spin++) {
    if (__builtin_amdgcn_ballot_w64(v != 1u) == 0) break;
    __builtin_amdgcn_s_sleep(K_SLEEP);
    asm volatile("s_nop 4\n\tbuffer_load_dword %0, %1, %2, 0 offen sc1\n\ts_waitcnt vmcnt(0)" : "=&v"(v) : "v"(off), "s"(r) : "memory");
  }
}

DI void word_store(rsrc_t r, u32 off, u32 v) { __builtin_amdgcn_raw_buffer_store_b32(v, r, off, 0, 16); }
DI void count_up(char* ws, u32 off) {
  if ((threadIdx.x & 63) == 0) __hip_atomic_fetch_add((u32*)(ws + WS_FL + off), 1u, __ATOMIC_RELAXED, __HIP_MEMORY_SCOPE_AGENT);
}
template <class Done> DI void watch(rsrc_t r, u32 off, u32& v, Done done, u32 tickoff, u32& tick) {
#pragma unroll 1
  for (int spin = 0; spin < K_SPIN; spin++) {
    if (__builtin_amdgcn_ballot_w64(!done(v)) == 0) break;
    if ((spin & 63) == 63) { tick++; word_store(r, tickoff, tick); }
    __builtin_amdgcn_s_sleep(K_SLEEP);
    asm volatile("s_nop 4\n\tbuffer_load_dword %0, %1, %2, 0 offen sc1\n\ts_waitcnt vmcnt(0)" : "=&v"(v) : "v"(off), "s"(r) : "memory");
  }
}
__global__ __launch_bounds__(512, 2) void fused_kernel(const u16* __restrict__ x, const u16* __restrict__ cre, const u16* __restrict__ cim,
                                                    const u16* __restrict__ dre, const u16* __restrict__ dim,
                                                    const u16* __restrict__ w1, const u16* __restrict__ b1, const u16* __restrict__ w2,
                                                    const u16* __restrict__ b2, char* __restrict__ ws, u16* __restrict__ out DIAG_ARG) {
  extern __shared__ __attribute__((aligned(16))) char smem[];
  DIAG_BEGIN
  asm volatile("" ::"s"(x), "s"(cre), "s"(cim), "s"(dre), "s"(dim), "s"(w1), "s"(b1), "s"(w2), "s"(b2), "s"(ws), "s"(out));
  const int lane0 = threadIdx.x & 63, wid = rfl(threadIdx.x >> 6);
  const int xcd = blockIdx.x & 7, loc = blockIdx.x >> 3, li = loc >> 2, lj = loc & 3;
  const int half = xcd >> 2, et = ((xcd >> 1) & 1) * 4 + lj, tt = (xcd & 1) * 8 + li, ft = (xcd >> 1) * 4 + lj;
  LAS char* sm = (LAS char*)smem;
  LAS float* tabA = (LAS float*)(sm + TOFF);
  LAS float* tabB = tabA + 128;
  LAS float* tabC = tabA + 256;
  LAS float* tabBias = tabA + 512;
  if (__builtin_expect(wid >= 4, 1)) {
    if (K_PPRIO) __builtin_amdgcn_s_setprio(K_PPRIO);
    const int p = wid - 4, phi = (li + lj) & 3;
    const u32 lds0 = (u32)(size_t)smem, ldsb = lds0 + p * 4096;
    rsrc_t rall = mk_rsrc(ws + WS_FL, 7 * 65536);
    rsrc_t rp1 = mk_rsrc((const char*)x + (size_t)tt * 128 * D_ * 2 - 4096, 128 * D_ * 2 + 4096);
    rsrc_t rq1_ = mk_rsrc((half ? dim : dre) + (size_t)et * 128 * D_, 128 * D_ * 2);
    u32 vp1[4], vq1[4];
#pragma unroll
    for (int i = 0; i < 4; i++) {
      const int row = 32 * p + 8 * i + (lane0 >> 3), u = (row >> 4) & 1, ii = row & 15;
      vp1[i] = row_src(row, 32 * p + 8 * (ii >> 2) + 4 * u + (ii & 3), lane0, D_ * 2) + 4096 - i * 1024;
      vq1[i] = row_src(row, row, lane0, D_ * 2) - i * 1024;
    }
    const int side1 = (xcd >> 1) ^ (xcd & 1);
    dma<4>(ldsb, vp1, rp1, kstep2(0, 16, phi, side1) * 128);
    dma<4>(ldsb + QOFF, vq1, rq1_, kstep2(0, 16, phi, side1) * 128);
    int lane = lane0;
    asm volatile("" : "+v"(lane)::"memory");
    __builtin_amdgcn_sched_barrier(0);
    const u32 vlin = p * 4096 + lane * 16;
    const u32 grpcell = ((lane & 16) ? tt * 64 + (lane & 15) * 4 : (lane & 15) * 64 + ft * 4) * 64;
    const u32 tickoff = lane == 0 ? FL_TICK * 65536 + (tt * 64 + ft * 4) * 64 : NOWHERE;
    const u32 ackoff = lane == 0 ? FL_ACK * 65536 + (tt * 64 + ft * 4) * 64 : NOWHERE;
    const u32 rowcnt = FL_CNT * 65536 + tt * 4096, colcnt = FL_CNT * 65536 + ft * 4096;
    const int pt = threadIdx.x - 256;
    u32 base, fv, tick;
    const int side2 = (xcd >> 1) & 1;
    auto skip = [&](int r) { return r + (r >= 2 * tt ? 2 : 0); };
    auto g2k = [&](int q) { return q < 2 ? 2 * tt + q : skip(kstep2(q - 2, 26, phi, side2)); };
    rsrc_t rco = mk_rsrc((const char*)(half ? cim : cre) + (size_t)tt * 128 * S_ * 2 - 4096, 128 * S_ * 2 + 4096);
    u32 vc[4];
#pragma unroll
    for (int i = 0; i < 4; i++) {
      const int row = 32 * p + 8 * i + (lane >> 3);
      vc[i] = row_src(row, row ^ (half * 32), lane, S_ * 2) + 4096 - i * 1024;
    }
    auto g2q = [&](int q) { dma<4>(ldsb + QOFF + (q & 3) * SLOT, vc, rco, g2k(q) * 128); };
    {
      u32 vx[2];
#pragma unroll
      for (int i = 0; i < 2; i++) {
        const int row = 16 * p + 8 * i + (lane >> 3);
        vx[i] = row_src(row, 64 * (row >> 5) + 32 * half + (row & 31), lane, D_ * 2) + 4096 - i * 1024;
      }
      auto issue = [&](int g) {
        const int k = kstep2(g, 16, phi, side1);
        const u32 m = ldsb + (g & 3) * SLOT;
        dma<4>(m, vp1, rp1, k * 128);
        dma<4>(m + QOFF, vq1, rq1_, k * 128);
      };
      auto run = [&](int g0, int g1) {
#pragma unroll 1
        for (int g = g0; g < g1; g++) { issue(g); wait_vm<16>(); bar(); }
      };
      flag_request(rall, p == 0 ? FL_TICK * 65536 + grpcell : NOWHERE, base);
      issue(1); issue(2);
      {
        u32 z = lane < 16 ? (lane >> 2) * 65536 + (tt * 64 + ft * 4 + (lane & 3)) * 64 : FL_ACK * 65536 + (tt * 64 + ft * 4) * 64;
        if (lane == 17) z = ft == 0 ? rowcnt + CN_H : NOWHERE;
        if (lane == 18) z = tt == 0 ? colcnt + CN_AB : NOWHERE;
        if (lane == 19) z = ft == 0 ? rowcnt + CN_SX : NOWHERE;
        word_store(rall, p == 0 && lane < 20 ? z : NOWHERE, 0u);
      }
      wait_vm<18>(); bar();
      const int js = (phi + 1) & 3;
      auto sums = [&]() {
        const int c = lane & 15, g = lane >> 4, e = c & 3;
        const u32x4 ones4 = {0x3F803F80u, 0x3F803F80u, 0x3F803F80u, 0x3F803F80u};
        const bf16x8 ones = __builtin_bit_cast(bf16x8, ones4);
        const int a = js * SLOT + (32 * p + c) * 128 + ((g ^ (c >> 1)) * 16);
        rsrc_t rx = mk_rsrc(ws + WS_SX + (size_t)(tt * 16 + (js ^ (side1 << 2))) * 1024 + p * 256, 256);
#pragma unroll
        for (int i = 0; i < 2; i++) {
          const bf16x8 x0 = *(const LAS bf16x8*)(sm + (a + i * 2048)), x1 = *(const LAS bf16x8*)(sm + ((a ^ 64) + i * 2048));
          const f32x4 z = {0.f, 0.f, 0.f, 0.f};
          const f32x4 sx = __builtin_amdgcn_mfma_f32_16x16x32_bf16(x1, ones, __builtin_amdgcn_mfma_f32_16x16x32_bf16(x0, ones, z, 0, 0, 0), 0, 0, 0);
          const f32x4 sq = __builtin_amdgcn_mfma_f32_16x16x32_bf16(x1, x1, __builtin_amdgcn_mfma_f32_16x16x32_bf16(x0, x0, z, 0, 0, 0), 0, 0, 0);
          const float q = e == 0 ? sq[0] : e == 1 ? sq[1] : e == 2 ? sq[2] : sq[3];
          const float t = e == 0 ? sx[0] : e == 1 ? sx[1] : e == 2 ? sx[2] : sx[3];
          __builtin_amdgcn_raw_buffer_store_b64((u32x2){__float_as_uint(t), __float_as_uint(q)}, rx, (c >> 2) == g ? (16 * i + c) * 8 : NOWHERE, 0, 16);
        }
      };
      issue(3);
      if (js == 0) sums();
      asm volatile("s_waitcnt vmcnt(16)" : "+v"(base)::"memory");
      tick = __builtin_amdgcn_readlane(base, tt) + 1;
      bar();
#pragma unroll 1
      for (int g = 2; g < 6; g++) {
        issue(g + 2);
        if (g == js + 1) sums();
        wait_vm<16>();
        if (g == 3 && p == 0) word_store(rall, tickoff, tick);
        bar();
      }
      issue(8); wait_vm<16>(); bar();
      flag_request(rall, p == 0 ? FL_TICK * 65536 + grpcell : NOWHERE, fv);
      issue(9); wait_vm<16>(); bar();
      issue(10);
      asm volatile("s_waitcnt vmcnt(16)" : "+v"(fv)::"memory");
      PSTAMP(14)
      if (p == 0) {
        watch(rall, FL_TICK * 65536 + grpcell, fv, [&](u32 w) { return w != base; }, tickoff, tick);
        count_up(ws, rowcnt + CN_SX);
        word_store(rall, ackoff, 1u);
      }
      PSTAMP(15)
      bar();
      run(11, 14);
      dma<2>(lds0 + XOFF + p * 2048, vx, rp1, et * 256);
      dma<2>(lds0 + XOFF + 8192 + p * 2048, vx, rp1, et * 256 + 128);
      flag_request(rall, p == 0 ? rowcnt + CN_SX : NOWHERE, fv);
      issue(14); wait_vm<21>(); bar();
      issue(15);
      asm volatile("s_waitcnt vmcnt(16)" : "+v"(fv)::"memory");
      PSTAMP(9)
      if (p == 0) watch(rall, rowcnt + CN_SX, fv, [&](u32 w) { return w >= 16u; }, tickoff, tick);
      PSTAMP(10)
      bar();
      u32x2 sv[8];
      rsrc_t rsx = mk_rsrc(ws + WS_SX + (size_t)tt * 16384, 16384);
#pragma unroll
      for (int i = 0; i < 8; i++)
        asm volatile("s_nop 4\n\tbuffer_load_dwordx2 %0, %1, %2, %3 offen sc1" : "=&v"(sv[i]) : "v"((pt & 1) * 8192 + (pt >> 1) * 8), "s"(rsx), "s"(i * 1024) : "memory");
      g2q(0); wait_vm<20>(); bar();
      g2q(1); wait_vm<16>(); bar();
      g2q(2);
      asm volatile("s_waitcnt vmcnt(12)" : "+v"(sv[0]), "+v"(sv[1]), "+v"(sv[2]), "+v"(sv[3]), "+v"(sv[4]), "+v"(sv[5]), "+v"(sv[6]), "+v"(sv[7])::"memory");
      float a = 0.f, q = 0.f;
#pragma unroll
      for (int i = 0; i < 8; i++) { a += __uint_as_float(sv[i][0]); q += __uint_as_float(sv[i][1]); }
      a += __builtin_bit_cast(float, __builtin_amdgcn_update_dpp(0, __builtin_bit_cast(int, a), 0xB1, 0xF, 0xF, true));
      q += __builtin_bit_cast(float, __builtin_amdgcn_update_dpp(0, __builtin_bit_cast(int, q), 0xB1, 0xF, 0xF, true));
      const float mean = a * (1.0f / D_), var = fmaxf(q * (1.0f / D_) - mean * mean, 0.f);
      const float rstd = (half ? -1.f : 1.f) * __builtin_amdgcn_rsqf(var + LN_EPS);
      tabA[(pt & 1) * 128 + (pt >> 1)] = (pt & 1) ? -mean * rstd : rstd;
      wait_lgkm0();
    }
    {
      rsrc_t rp = mk_rsrc(ws + WS_AB + (size_t)et * 32 * 16384, 9 * 32 * 16384);
      rsrc_t rq0 = mk_rsrc((const char*)cre + (size_t)tt * 128 * S_ * 2 - 4096, 128 * S_ * 2 + 4096);
      rsrc_t rq1 = mk_rsrc((const char*)cim + (size_t)tt * 128 * S_ * 2 - 4096, 128 * S_ * 2 + 4096);
      u32 vh[2];
#pragma unroll
      for (int i = 0; i < 2; i++) {
        const int row = 64 * (p >> 1) + 16 * (p & 1) + 8 * i + (lane >> 3);
        vh[i] = row_src(row, row ^ (half * 32), lane, S_ * 2) + 4096 - i * 1024;
      }
      const u32 ldsh = lds0 + QOFF + (p >> 1) * 8192 + (p & 1) * 2048;
      auto fp = [&](int q) { dma_sc1<4>(ldsb + (q & 3) * SLOT, vlin, rp, (half * 256 + g2k(q)) * 16384); };
      auto full = [&](int q) { fp(q); g2q(q); };
      auto halfs = [&](int g) {
        const int k = kstep2(g, 8, phi, side2), term = k >> 2, ks = skip(26 + (k & 3));
        dma_sc1<4>(ldsb + (g & 3) * SLOT, vlin, rp, (term * 256 + ks) * 16384);
        if (term) dma<2>(ldsh + (g & 3) * SLOT, vh, rq1, ks * 128);
        else dma<2>(ldsh + (g & 3) * SLOT, vh, rq0, ks * 128);
      };
      bar();
      wait_vm<8>(); bar();
      g2q(3);
      rsrc_t rw = mk_rsrc((const char*)w1 + (size_t)(tt * 64 + p * 16) * F_ * 2 + ft * 448, 15 * F_ * 2 + 448);
      u32x4 v[8];
      const int l28 = lane < 28 ? lane : lane < 56 ? lane - 28 : 27;
      const u32 vo = (lane >= 28 ? F_ * 2 : 0) + l28 * 16;
      auto csload = [&]() {
#pragma unroll
      for (int i = 0; i < 8; i++)
        asm volatile("s_nop 4\n\tbuffer_load_dwordx4 %0, %1, %2, %3 offen" : "=&v"(v[i]) : "v"(vo), "s"(rw), "s"(i * 2 * F_ * 2) : "memory");
      };
      if (K_CSP == 0) csload();
      wait_vm<K_CSP == 0 ? 16 : 8>(); bar();
      if (K_CSP == 1) csload();
      bar();
      PSTAMP(12)
      if (p == 0) {
        count_up(ws, colcnt + CN_AB);
        fv = 0;
        watch(rall, colcnt + CN_AB, fv, [&](u32 w) { return w >= 16u; }, tickoff, tick);
        word_store(rall, ackoff, 2u);
      }
      bar();
      PSTAMP(2)
      fp(2); fp(3); full(4);
      asm volatile("s_waitcnt vmcnt(12)" : "+v"(v[0]), "+v"(v[1]), "+v"(v[2]), "+v"(v[3]), "+v"(v[4]), "+v"(v[5]), "+v"(v[6]), "+v"(v[7])::"memory");
      bar();
      full(5);
      {
        float s[8] = {0, 0, 0, 0, 0, 0, 0, 0};
#pragma unroll
        for (int i = 0; i < 8; i++)
#pragma unroll
          for (int e = 0; e < 4; e++) { s[2 * e] += blo(v[i][e]); s[2 * e + 1] += bhi(v[i][e]); }
#pragma unroll
        for (int e = 0; e < 8; e++) s[e] += __shfl(s[e], lane + 28);
        if (lane < 28) {
          LAS float* d = (LAS float*)(sm + COFF) + p * 224 + lane * 8;
          *(LAS f32x4*)d = (f32x4){s[0], s[1], s[2], s[3]};
          *(LAS f32x4*)(d + 4) = (f32x4){s[4], s[5], s[6], s[7]};
        }
      }
      wait_vm<16>(); wait_lgkm0(); bar();
      full(6);
      if (p == 0 && lane < 56) {
        const LAS f32x4* d = (const LAS f32x4*)(sm + COFF) + lane;
        const f32x4 t = (d[0] + d[56]) + (d[112] + d[168]);
        store_sc1(mk_rsrc(ws + WS_CS + (size_t)tt * F_ * 4 + ft * 896, 896), lane * 16, __builtin_bit_cast(u32x4, t));
      }
      wait_vm<16>(); bar();
      full(7); wait_vm<16>(); bar();
      full(8); wait_vm<16>();
      if (p == 0) count_up(ws, colcnt + CN_AB);
      bar();
#pragma unroll 1
      for (int q = 9; q < 26; q++) { full(q); wait_vm<16>(); bar(); }
      const u32 lastoff = lane < 32 ? FL_ACK * 65536 + grpcell : colcnt + CN_AB;
      const u32 lastmin = lane >= 32 ? 32u : lane == 16 + (ft ^ 8) ? 2u : 1u;
      flag_request(rall, p == 0 ? lastoff : NOWHERE, fv);
      full(26); wait_vm<16>(); bar();
      full(27);
      asm volatile("s_waitcnt vmcnt(16)" : "+v"(fv)::"memory");
      if (p == 0) watch(rall, lastoff, fv, [&](u32 w) { return w >= lastmin; }, tickoff, tick);
      bar();
      halfs(0); wait_vm<14>(); bar();
#pragma unroll 1
      for (int g = 1; g < 8; g++) { halfs(g); wait_vm<12>(); bar(); }
    }
    rsrc_t rfl_ = mk_rsrc(ws + WS_FL + (size_t)tt * 4096, 2 * 16 * 4096);
    {
      rsrc_t rp = mk_rsrc((const char*)w1 + ft * 448, D_ * F_ * 2 - ft * 448);
      const int cola = (ft & 1) * 192, colb = (ft & 1) ? 0 : 256;
      rsrc_t rq = mk_rsrc(ws + WS_H + (size_t)tt * 32 * 8192, 32 * 8192);
      u32 va[4], vb[3];
#pragma unroll
      for (int i = 0; i < 4; i++) {
        const int k = 16 * p + 4 * i + (lane >> 4);
        va[i] = k * (F_ * 2) + cola + (((lane & 15) ^ ((k & 7) << 1)) * 16) - i * 1024;
      }
#pragma unroll
      for (int i = 0; i < 3; i++) {
        const int o = (p * 3 + i) * 64 + lane, k = o / 12, ch = o % 12;
        vb[i] = k * (F_ * 2) + colb + ((ch ^ (((k >> 2) & 1) << 1)) * 16) - i * 1024;
      }
      rsrc_t rw = mk_rsrc(w2 + (size_t)(half * 1792 + tt * 112 + 28 * p) * D_ + et * 128, 27 * D_ * 2 + 256);
      const u32 vw = (lane >> 1) * (D_ * 2) + (lane & 1) * 128;
      auto pa = [&](int g) {
        if (K_WN > 0 && !K_WB && g == 4 + loc % (K_WN > 0 ? K_WN : 1)) pull_lines(lds0 + COFF + 3584, vw, rw, 0);
        dma<4>(ldsb + (g & 3) * SLOT, va, rp, kstep(g, 16, phi) * (64 * F_ * 2));
      };
      auto kb = [&](int g) { return K_REV ? 15 - kstep(g, 16, phi) : kstep(g, 16, phi); };
      auto pbb = [&](int g) {
        if (K_WN > 0 && K_WB && g == loc % (K_WN > 0 ? K_WN : 1)) pull_lines(lds0 + COFF + 3584, vw, rw, 0);
        dma<3>(lds0 + p * 3072 + (g & 3) * SLOT, vb, rp, kb(g) * (64 * F_ * 2));
        dma_sc1<4>(ldsb + QOFF + (g & 3) * SLOT, vlin, rq, kb(g) * 16384);
      };
      auto q = [&](int g) { dma_sc1<4>(ldsb + QOFF + (g & 3) * SLOT, vlin, rq, kstep(g, 16, phi) * 16384); };
      wait_vm<6>(); bar();
      wait_vm<0>(); bar();
      bar();
      PSTAMP(13)
      if (p == 0) count_up(ws, rowcnt + CN_H);
      pa(0); pa(1); pa(2);
      rsrc_t rs = mk_rsrc(ws + WS_ST + (size_t)tt * 16384, 16384);
      rsrc_t rc = mk_rsrc(ws + WS_CS + ft * 896, 15 * F_ * 4 + 896);
      u32x2 sv[8];
      u32 cv[16], bv;
#pragma unroll
      for (int i = 0; i < 16; i++)
        asm volatile("s_nop 4\n\tbuffer_load_dword %0, %1, %2, %3 offen sc1" : "=&v"(cv[i]) : "v"(min(pt, 223) * 4), "s"(rc), "s"(i * F_ * 4) : "memory");
      asm volatile("s_nop 4\n\tbuffer_load_ushort %0, %1, %2, 0 offen" : "=&v"(bv) : "v"(min(pt, 223) * 2), "s"(mk_rsrc(b1 + ft * 224, 448)) : "memory");
      if (p == 0) { fv = 0; watch(rall, rowcnt + CN_H, fv, [&](u32 w) { return w >= 16u; }, NOWHERE, tick); }
      bar();
      PSTAMP(3)
      q(0); q(1); q(2);
      const u32 so = (pt & 1) * 8192 + (pt >> 1) * 8;
#pragma unroll
      for (int i = 0; i < 8; i++)
        asm volatile("s_nop 4\n\tbuffer_load_dwordx2 %0, %1, %2, %3 offen sc1" : "=&v"(sv[i]) : "v"(so), "s"(rs), "s"(i * 1024) : "memory");
      wait_vm<16>();
      PSTAMP(4)
      bar();
      pa(3); q(3); wait_vm<20>(); bar();
      pa(4); q(4); wait_vm<24>(); bar();
      pa(5); q(5); wait_vm<16>();
      asm volatile("" : "+v"(sv[0]), "+v"(sv[1]), "+v"(sv[2]), "+v"(sv[3]), "+v"(sv[4]), "+v"(sv[5]), "+v"(sv[6]), "+v"(sv[7]), "+v"(bv)::"memory");
      asm volatile("" : "+v"(cv[0]), "+v"(cv[1]), "+v"(cv[2]), "+v"(cv[3]), "+v"(cv[4]), "+v"(cv[5]), "+v"(cv[6]), "+v"(cv[7]), "+v"(cv[8]),
                   "+v"(cv[9]), "+v"(cv[10]), "+v"(cv[11]), "+v"(cv[12]), "+v"(cv[13]), "+v"(cv[14]), "+v"(cv[15])::"memory");
      bar();
      {
        float s = 0.f;
#pragma unroll
        for (int i = 0; i < 16; i++) s += __uint_as_float(cv[i]);
        if (pt < 224) { tabC[pt] = s; tabBias[pt] = __uint_as_float(bv << 16); }
      }
      {
        float s = 0.f, qq = 0.f;
#pragma unroll
        for (int i = 0; i < 8; i++) { s += __uint_as_float(sv[i][0]); qq += __uint_as_float(sv[i][1]); }
        s += __shfl_xor(s, 1); qq += __shfl_xor(qq, 1);
        const float mean = s * (1.0f / D_), var = fmaxf(qq * (1.0f / D_) - mean * mean, 0.f);
        const float rstd = 1.0f / sqrtf(var + LN_EPS);
        if ((pt & 1) == 0) { tabA[pt >> 1] = rstd; tabB[pt >> 1] = -mean * rstd; }
        wait_lgkm0();
      }
#pragma unroll 1
      for (int g = 6; g < 16; g++) { pa(g); q(g); wait_vm<16>(); bar(); }
      pbb(0); wait_vm<15>(); bar();
#pragma unroll 1
      for (int g = 1; g < 14; g++) { pbb(g); wait_vm<14>(); bar(); }
      flag_request(rfl_, FL_RA * 65536 + lane * 64, fv);
      pbb(14); wait_vm<14>(); bar();
      pbb(15); wait_vm<14>();
      PSTAMP(5)
      flag_wait(rfl_, FL_RA * 65536 + lane * 64, fv);
      PSTAMP(6)
      bar();
    }
    {
      rsrc_t rp = mk_rsrc(w2 + et * 128, F_ * D_ * 2 - et * 256);
      rsrc_t rq = mk_rsrc(ws + WS_R + (size_t)tt * 112 * 8192, 112 * 8192);
      u32 vp[4];
#pragma unroll
      for (int i = 0; i < 4; i++) {
        const int k = 16 * (p & 1) + 4 * i + (lane >> 4);
        vp[i] = k * (D_ * 2) + (((lane & 15) ^ ((k & 7) << 1)) * 16) - i * 1024;
      }
      const u32 vq0 = (4 * (p & 1) + 2 * half) * 1024 + lane * 16, vq1 = (4 * (p & 1) + 2 - 2 * half) * 1024 + lane * 16;
      const u32 ldsh = lds0 + QOFF + p * 4096;
      auto bblock = [&](int hf, int n) { return 7 * (8 * hf + n / 3) + ((n / 3) & 1 ? 0 : 4) + n % 3; };
      auto full = [&](int g) {
        const int k = g < 16 ? kstep2(g, 16, phi, xcd & 1) : 16 + kstep2(g - 16, 8, phi, xcd & 1), n = 2 * k + (p >> 1);
        const int blk = k < 16 ? 7 * (8 * half + (n >> 2)) + ((n >> 2) & 1) * 3 + (n & 3) : bblock(half, n - 32);
        const u32 m = ldsb + (g & 3) * SLOT;
        dma<4>(m, vp, rp, blk * (32 * D_ * 2));
        dma_sc1<2>(m + QOFF, vq0, rq, blk * 8192);
        dma_sc1<2>(m + QOFF + 2048, vq1, rq, blk * 8192);
      };
      auto halfs = [&](int g) {
        const int k = kstep2(g, 8, phi, xcd & 1), blk = bblock(k >> 2, 16 + 2 * (k & 3) + (p >> 1));
        dma<4>(ldsb + (g & 3) * SLOT, vp, rp, blk * (32 * D_ * 2));
        dma_sc1<2>(ldsh + (g & 3) * SLOT, vq0, rq, blk * 8192);
      };
      full(0); wait_vm<15>(); bar();
#pragma unroll 1
      for (int g = 1; g < 14; g++) { full(g); wait_vm<16>(); bar(); }
      flag_request(rfl_, FL_RB * 65536 + lane * 64, fv);
      full(14); wait_vm<16>(); bar();
      full(15); wait_vm<16>();
      PSTAMP(7)
      flag_wait(rfl_, FL_RB * 65536 + lane * 64, fv);
      PSTAMP(8)
      bar();
#pragma unroll 1
      for (int g = 16; g < 24; g++) { full(g); wait_vm<16>(); bar(); }
      halfs(0); wait_vm<14>(); bar();
#pragma unroll 1
      for (int g = 1; g < 8; g++) { halfs(g); wait_vm<12>(); bar(); }
      wait_vm<6>(); bar();
      wait_vm<0>(); bar();
      PDIAG_END
    }
  } else {
    const int lane = lane0;
    const int wP = wid >> 1, wQ = wid & 1, c = lane & 15, g = lane >> 4;
    const int m0 = 4 * wQ + 2 * half, cell = ft * 4 + wid;
    {
      Consumer<4, M_ROW, M_ROW> C;
      C.init(sm, wP, wQ, lane, 0, wP);
      f32x4 acc[4][4], sd[2];
      C.zero(acc);
#pragma unroll
      for (int i = 0; i < 2; i++) sd[i] = (f32x4){0.f, 0.f, 0.f, 0.f};
      const u32x4 ones4 = {0x3F803F80u, 0x3F803F80u, 0x3F803F80u, 0x3F803F80u};
      bf16x8 ones = __builtin_bit_cast(bf16x8, ones4);
      asm volatile("" : "+v"(ones));
      bar();
      STAMP(1)
      C.first();
#define G1_HALF0(S) C.load<S, 1, 4>(); C.mma<0, 4>(acc); C.qsums<0>(sd, ones); C.interleave<18, 4>();
#define G1_STEP(S) G1_HALF0(S) wait_lgkm0(); bar(); __builtin_amdgcn_sched_barrier(0); \
      C.load<(S + 1) % NST, 0, 4>(); C.mma<1, 4>(acc); C.qsums<1>(sd, ones); C.interleave<18, 4>();
      for (int it = 0; it < 3; it++) { G1_STEP(0) G1_STEP(1) G1_STEP(2) G1_STEP(3) }
      G1_STEP(0) G1_STEP(1) G1_STEP(2) G1_HALF0(3)
      wait_lgkm0();
      C.qsums<1>(sd, ones);
      __builtin_amdgcn_sched_barrier(0);
      C.mma<1, 4>(acc);
      tabC[g < 2 ? 64 * wQ + 32 * wP + 16 * g + c : 128 + lane] = g ? sd[1][0] : sd[0][0];
      __builtin_amdgcn_sched_group_barrier(0x8, 4, 0);
      __builtin_amdgcn_sched_group_barrier(0x2 | 0x200, 8, 0);
      __builtin_amdgcn_sched_group_barrier(0x8, 12, 0);
      __builtin_amdgcn_sched_barrier(0);
      STAMP(2)
      wait_lgkm0(); bar();
      rsrc_t ro = mk_rsrc(ws + WS_AB + ((size_t)((half * 8 + et) * 32 + 2 * tt + wP) * 16 + 4 * wQ) * 1024, 12288);
      f32x4 ra[4], rb[4];
      float rs[4];
#pragma unroll
      for (int i = 0; i < 4; i++) {
        ra[i] = *(const LAS f32x4*)(tabA + 64 * wP + 16 * i + 4 * g);
        rb[i] = *(const LAS f32x4*)(tabB + 64 * wP + 16 * i + 4 * g);
        rs[i] = tabC[64 * wQ + 16 * (i ^ (2 * wP)) + c];
      }
#pragma unroll
      for (int j = 0; j < 4; j++)
#pragma unroll
        for (int u = 0; u < 2; u++) {
          const f32x4 a = ra[2 * u] * acc[2 * u][j] + rb[2 * u] * rs[j], b = ra[2 * u + 1] * acc[2 * u + 1][j] + rb[2 * u + 1] * rs[j];
          const u32x4 o = {cvtpk(a[0], a[1]), cvtpk(a[2], a[3]), cvtpk(b[0], b[1]), cvtpk(b[2], b[3])};
          const int at = (u * 8 + (j ^ (2 * wP))) * 1024 + lane * 16;
          store_sc1(ro, at, o);
          *(LAS u32x4*)(sm + (wP * SLOT + wQ * 4096 + at)) = o;
        }
    }
    u32x4 hk[2][2];
    {
      Consumer<4, M_TILED, M_ROW> C;
      C.init(sm, wP, wQ, lane);
      f32x4 acc[4][4];
      wait_lgkm0(); bar();
      STAMP(3)
      C.first();
      C.load<0, 1, 4>(); C.mma<0, 4, true>(acc); C.interleave<16, 4>();
      wait_lgkm0(); bar();
      __builtin_amdgcn_sched_barrier(0);
      C.load<1, 0, 4>(); C.mma<1, 4>(acc); C.interleave<16, 4>();
      C.load<1, 1, 4>(); C.mma<0, 4>(acc); C.interleave<16, 4>();
      wait_lgkm0();
      C.mma<1, 4>(acc);
      __builtin_amdgcn_sched_barrier(0);
      wait_vm<0>();
      STAMP(4)
      bar();
      bar();
      bar();
      STAMP(5)
      C.load<2, 0, 4>();
      C.step<2, 4, 4>(acc); C.step<3, 4, 4>(acc);
      for (int it = 0; it < 5; it++) C.step4<4, 4>(acc);
      C.step4<4, 2>(acc);
      STAMP(6)
      xch_send(ws + WS_X2, acc, wid, lane);
      __builtin_amdgcn_sched_barrier(0);
      rsrc_t rx = mk_rsrc(ws + WS_X2 + ((size_t)(blockIdx.x ^ 4) * 4 + wid) * 4096, 4096);
      rsrc_t rf = mk_rsrc(ws + WS_FL + ((size_t)(FL_X2 * 16 + tt) * 64 + (cell ^ 32)) * 64, 64);
      u32x4 rv[4];
      u32 fv;
      C.step<0, 2, 2>(acc); C.step<1, 2, 2>(acc); C.step<2, 2, 2>(acc);
      if (K_XF == 3) flag_set(ws, FL_X2, tt, cell);
      C.step<3, 2, 2>(acc);
      if (K_XF == 4) flag_set(ws, FL_X2, tt, cell);
      C.step<0, 2, 2>(acc); C.step<1, 2, 2>(acc);
      flag_request(rf, 0, fv);
      __builtin_amdgcn_sched_barrier(0);
      C.step<2, 2, 2>(acc);
      wait_vm<0>();
      flag_wait(rf, 0, fv);
      xch_request(rx, rv, lane);
      __builtin_amdgcn_sched_barrier(0);
      C.load<3, 1, 2>(); C.mma<0, 2>(acc); C.interleave<8, 2>();
      C.mma<1, 2>(acc);
      STAMP(7)
      xch_finish(rv);
            rsrc_t rh = mk_rsrc(ws + WS_H + ((size_t)(tt * 32 + 4 * et + 2 * wP) * 8 + m0) * 1024, 16384);
      rsrc_t rs = mk_rsrc(ws + WS_ST + ((size_t)(tt * 16 + 2 * et + wP) * 128 + 16 * m0) * 8, 256);
#pragma unroll
      for (int jj = 0; jj < 2; jj++) {
        float s = 0.f, q = 0.f;
#pragma unroll
        for (int u = 0; u < 2; u++) {
          const u32x4 o = rv[jj * 2 + u];
          const f32x4 a = acc[2 * u][jj], b = acc[2 * u + 1][jj];
          const int xrow = 32 * wQ + 16 * jj + c;
          const LAS char* xp = sm + (XOFF + wP * 8192 + xrow * 128 + (g & 1) * 8);
          const u32x2 xa = *(const LAS u32x2*)(xp + (((4 * u + (g >> 1)) ^ ((xrow >> 1) & 7)) * 16));
          const u32x2 xb = *(const LAS u32x2*)(xp + (((4 * u + 2 + (g >> 1)) ^ ((xrow >> 1) & 7)) * 16));
          u32x4 h;
          h[0] = cvtpk(a[0] + blo(o[0]) + blo(xa[0]), a[1] + bhi(o[0]) + bhi(xa[0]));
          h[1] = cvtpk(a[2] + blo(o[1]) + blo(xa[1]), a[3] + bhi(o[1]) + bhi(xa[1]));
          h[2] = cvtpk(b[0] + blo(o[2]) + blo(xb[0]), b[1] + bhi(o[2]) + bhi(xb[0]));
          h[3] = cvtpk(b[2] + blo(o[3]) + blo(xb[1]), b[3] + bhi(o[3]) + bhi(xb[1]));
          store_sc1(rh, (u * 8 + jj) * 1024 + lane * 16, h);
          hk[jj][u] = h;
#pragma unroll
          for (int e = 0; e < 4; e++) { const float lo = blo(h[e]), hi = bhi(h[e]); s += lo + hi; q += lo * lo + hi * hi; }
        }
        s = xsum32(xsum16(s)); q = xsum32(xsum16(q));
        if (g == 0) __builtin_amdgcn_raw_buffer_store_b64((u32x2){__float_as_uint(s), __float_as_uint(q)}, rs, (16 * jj + c) * 8, 0, 16);
      }
      wait_vm<0>();
      STAMP(8)
      bar();
#pragma unroll
      for (int q = 0; q < 4; q++) *(LAS u32x4*)(sm + XOFF + wid * 4096 + q * 1024 + lane * 16) = hk[q >> 1][q & 1];
      bar();
    }
    rsrc_t rr = mk_rsrc(ws + WS_R + ((size_t)(tt * 112 + 7 * ft) * 8 + 4 * wQ) * 1024, 7 * 8192);
    float ra[4], rb[4];
    f32x4 cs[3], bi[3];
    u32 pk[6];
    auto act2 = [&](const f32x4& a, int i, int j, int e) {
      const f32x2 t = (f32x2){rb[j], rb[j]} * (f32x2){cs[i][2 * e], cs[i][2 * e + 1]} + (f32x2){bi[i][2 * e], bi[i][2 * e + 1]};
      const f32x2 v = (f32x2){ra[j], ra[j]} * (f32x2){a[2 * e], a[2 * e + 1]} + t;
      const f32x2 m = {fmaxf(v[0], 0.f), fmaxf(v[1], 0.f)};
      return cvtpk(m[0] * m[0], m[1] * m[1]);
    };
    const int ca = (ft & 1) * 96, cb = (ft & 1) ? 0 : 128;
    const u32 sta = (ca / 32 + 2 * wP) * 8192 + lane * 16, stb = (cb / 32) * 8192 + lane * 16;
    auto tables = [&](int i, int col) { cs[i] = *(const LAS f32x4*)(tabC + col); bi[i] = *(const LAS f32x4*)(tabBias + col); };
    f32x4 accA[4][4], accB[3][4];
    {
      Consumer<4, M_TR256, M_TILED> C;
      C.init(sm, wP, wQ, lane);
      C.zero(accA);
      bar();
      STAMP(9)
      C.first();
      for (int it = 0; it < 3; it++) C.step4<4, 4>(accA);
#pragma unroll
      for (int j = 0; j < 4; j++) { ra[j] = tabA[64 * wQ + 16 * j + c]; rb[j] = tabB[64 * wQ + 16 * j + c]; }
      tables(0, ca + 64 * wP + 4 * g); tables(1, ca + 64 * wP + 16 + 4 * g);
      __builtin_amdgcn_sched_barrier(0);
      C.last4<4>(accA);
      STAMP(10)
    }
    {
      auto outA = [&](int u, int j0, int n) {
        if (n < 8) {
          const int j = j0 + (n >> 2), t = (n >> 1) & 1;
          pk[n & 3] = act2(accA[2 * u + t][j], t, j, n & 1);
          if ((n & 3) == 3) store_sc1(rr, sta + (u * 8 + j) * 1024, (u32x4){pk[0], pk[1], pk[2], pk[3]});
        } else if (n == 8 && u == 0 && j0 == 2) {
          tables(0, ca + 64 * wP + 32 + 4 * g); tables(1, ca + 64 * wP + 48 + 4 * g);
        }
      };
      Consumer<3, M_TR192, M_TILED> C;
      C.init(sm, wP, wQ, lane);
      bar();
      STAMP(11)
      C.first();
      C.step_with<0, true>(accB, [&](int n) { outA(0, 0, n); }, [&](int n) { outA(0, 2, n); });
      C.step_with<1, false>(accB, [&](int n) { outA(1, 0, n); }, [&](int n) { outA(1, 2, n); });
      C.step<2, 4, 4>(accB); C.step<3, 4, 4>(accB);
      flag_set(ws, FL_RA, tt, cell);
      C.step<0, 4, 4>(accB);
      C.step<1, 4, 4>(accB);
      C.step<2, 4, 4>(accB); C.step<3, 4, 4>(accB);
      C.step4<4, 4>(accB);
      tables(0, cb + 64 * wP + 4 * g); tables(1, cb + 16 + 64 * wP + 4 * g); tables(2, cb + 32 + 16 * wP + 4 * g);
      __builtin_amdgcn_sched_barrier(0);
      C.last4<4>(accB);
      STAMP(12)
    }
    {
      auto outB = [&](int j, int n) {
        if ((n & 1) == 0 && n < 12) {
          const int q = n >> 1;
          pk[q] = act2(accB[q >> 1][j], q >> 1, j, q & 1);
          if (q == 3) store_sc1(rr, stb + (16 * wP + j) * 1024, (u32x4){pk[0], pk[1], pk[2], pk[3]});
          if (q == 5) __builtin_amdgcn_raw_buffer_store_b64((u32x2){pk[4], pk[5]}, rr, stb + (8 + j) * 1024 + 8 * wP, 0, 16);
        }
      };
      Consumer<4, M_TR256, M_TILED> C;
      C.init(sm, wP, wQ, lane);
      f32x4 acc[4][4];
      bar();
      STAMP(13)
      C.first();
      C.step_with<0, true>(acc, [&](int n) { outB(0, n); }, [&](int n) { outB(1, n); });
      C.step_with<1, false>(acc, [&](int n) { outB(2, n); }, [&](int n) { outB(3, n); });
      C.step<2, 4, 4>(acc); C.step<3, 4, 4>(acc);
      flag_set(ws, FL_RB, tt, cell);
      C.step<0, 4, 4>(acc);
      C.step<1, 4, 4>(acc);
      C.step<2, 4, 4>(acc); C.step<3, 4, 4>(acc);
      for (int it = 0; it < 3; it++) C.step4<4, 4>(acc);
      C.step4<4, 2>(acc);
      STAMP(14)
      xch_send(ws + WS_X4, acc, wid, lane);
      u32x2 br[4];
#pragma unroll
      for (int i = 0; i < 4; i++) br[i] = *(const u32x2*)(b2 + et * 128 + 64 * wP + 16 * i + 4 * g);
      __builtin_amdgcn_sched_barrier(0);
      rsrc_t rx = mk_rsrc(ws + WS_X4 + ((size_t)(blockIdx.x ^ 4) * 4 + wid) * 4096, 4096);
      rsrc_t rf = mk_rsrc(ws + WS_FL + ((size_t)(FL_X4 * 16 + tt) * 64 + (cell ^ 32)) * 64, 64);
      u32x4 rv[4];
      u32 fv;
      C.step<0, 2, 2>(acc); C.step<1, 2, 2>(acc); C.step<2, 2, 2>(acc);
      if (K_XF == 3) flag_set(ws, FL_X4, tt, cell);
      C.step<3, 2, 2>(acc);
      if (K_XF == 4) flag_set(ws, FL_X4, tt, cell);
      C.step<0, 2, 2>(acc); C.step<1, 2, 2>(acc);
      flag_request(rf, 0, fv);
      __builtin_amdgcn_sched_barrier(0);
      C.step<2, 2, 2>(acc);
      wait_vm<0>();
      flag_wait(rf, 0, fv);
      xch_request(rx, rv, lane);
      __builtin_amdgcn_sched_barrier(0);
      C.load<3, 1, 2>(); C.mma<0, 2>(acc); C.interleave<8, 2>();
      C.mma<1, 2>(acc);
      xch_finish(rv);

      rsrc_t ro = mk_rsrc(out + (size_t)(tt * 128 + 16 * m0) * D_ + et * 128 + 64 * wP, 31 * D_ * 2 + 128);
#pragma unroll
      for (int jj = 0; jj < 2; jj++)
#pragma unroll
        for (int u = 0; u < 2; u++) {
          const u32x4 o = rv[jj * 2 + u], h = *(const LAS u32x4*)(sm + XOFF + wid * 4096 + (jj * 2 + u) * 1024 + lane * 16);
          const f32x4 a = acc[2 * u][jj], b = acc[2 * u + 1][jj];
          const u32x2 ba = br[2 * u], bb = br[2 * u + 1];
          const u32 a0 = cvtpk(a[0] + blo(o[0]) + blo(ba[0]) + blo(h[0]), a[1] + bhi(o[0]) + bhi(ba[0]) + bhi(h[0]));
          const u32 a1 = cvtpk(a[2] + blo(o[1]) + blo(ba[1]) + blo(h[1]), a[3] + bhi(o[1]) + bhi(ba[1]) + bhi(h[1]));
          const u32 b0 = cvtpk(b[0] + blo(o[2]) + blo(bb[0]) + blo(h[2]), b[1] + bhi(o[2]) + bhi(bb[0]) + bhi(h[2]));
          const u32 b1_ = cvtpk(b[2] + blo(o[3]) + blo(bb[1]) + blo(h[3]), b[3] + bhi(o[3]) + bhi(bb[1]) + bhi(h[3]));
          const auto s0 = __builtin_amdgcn_permlane16_swap(a0, b0, false, false);
          const auto s1 = __builtin_amdgcn_permlane16_swap(a1, b1_, false, false);
          __builtin_amdgcn_raw_buffer_store_b128((u32x4){s0[0], s1[0], s0[1], s1[1]}, ro,
                                                 (16 * jj + c) * (D_ * 2) + (32 * u + 16 * (g & 1) + 8 * (g >> 1)) * 2, 0, 16);
        }
      DIAG_END(15)
    }
  }
}

}

extern "C" void kernel_launch(void* const* d_in, const int* in_sizes, int n_in, void* d_out, int out_size, void* d_ws, size_t ws_size,
                              hipStream_t stream) {
  const u16* const* I = (const u16* const*)d_in;
  (void)hipFuncSetAttribute((const void*)fused_kernel, hipFuncAttributeMaxDynamicSharedMemorySize, LDS_BYTES);
  fused_kernel<<<256, 512, LDS_BYTES, stream>>>(I[0], I[1], I[2], I[3], I[4], I[6], I[7], I[8], I[9], (char*)d_ws, (u16*)d_out);
}
```

```cpp
#include <hip/hip_runtime.h>
#include <hip/hip_bf16.h>
#include <type_traits>

typedef unsigned short u16;
typedef unsigned int u32;
typedef unsigned long long u64;
typedef __bf16 bf16x8 __attribute__((ext_vector_type(8)));
typedef __bf16 bf16x2 __attribute__((ext_vector_type(2)));
typedef short s16x4 __attribute__((ext_vector_type(4)));
typedef short s16x8 __attribute__((ext_vector_type(8)));
typedef float f32x2 __attribute__((ext_vector_type(2)));
typedef float f32x4 __attribute__((ext_vector_type(4)));
typedef u32 u32x2 __attribute__((ext_vector_type(2)));
typedef u32 u32x4 __attribute__((ext_vector_type(4)));
typedef u32 u32x16 __attribute__((ext_vector_type(16)));
typedef __amdgpu_buffer_rsrc_t rsrc_t;
#define GAS __attribute__((address_space(1)))
#define LAS __attribute__((address_space(3)))
#define DI __device__ __forceinline__

#define K_WN 8
#define K_SLEEP 1
#define K_SPIN (1 << 18)
#define K_GAPS(NM, NR) ((NR) < (NM) ? (NR) : (NM))
#define K_PPRIO 0
#define K_CSP 0
#define K_WB 1
#define K_REV 1
#define K_LEAD 3
#define K_NS1 6
#define K_XF 3
namespace {

constexpr int S_ = 2048, D_ = 1024, F_ = 3584;
constexpr float LN_EPS = 1e-5f;

constexpr size_t WS_AB = 0;
constexpr size_t WS_H = 8u << 20;
constexpr size_t WS_R = 12u << 20;
constexpr size_t WS_X2 = 27u << 20;
constexpr size_t WS_X4 = 31u << 20;
constexpr size_t WS_ST = 35u << 20;
constexpr size_t WS_CS = 36u << 20;
constexpr size_t WS_SX = 38u << 20;
constexpr size_t WS_FL = 37u << 20;

#define DIAG_ARG
#define DIAG_BEGIN
#define STAMP(i)
#define PSTAMP(i)
#define PDIAG_END
#define DIAG_END(i)

DI u32 rfl(u32 x) { return __builtin_amdgcn_readfirstlane(x); }
DI u32 cvtpk(float lo, float hi) { f32x2 v = {lo, hi}; return __builtin_bit_cast(u32, __builtin_convertvector(v, bf16x2)); }
DI float blo(u32 v) { return __uint_as_float(v << 16); }
DI float bhi(u32 v) { return __uint_as_float(v & 0xFFFF0000u); }
DI rsrc_t mk_rsrc(const void* p, u32 bytes) { return __builtin_amdgcn_make_buffer_rsrc((void*)p, 0, bytes, 0x00020000); }
DI void bar() { asm volatile("s_barrier" ::: "memory"); }
DI void wait_lgkm0() { __builtin_amdgcn_s_waitcnt(0xC07F); }
template <int N> DI void wait_vm() { asm volatile("s_waitcnt vmcnt(%0)" ::"i"(N) : "memory"); }

template <int N> DI void dma(u32 m0, const u32* v, rsrc_t r, u32 so) {
  if constexpr (N == 4)
    asm volatile("s_mov_b32 m0, %0\n\ts_nop 3\n\t"
                 "buffer_load_dwordx4 %1, %5, %6 offen lds\n\t"
                 "buffer_load_dwordx4 %2, %5, %6 offen offset:1024 lds\n\t"
                 "buffer_load_dwordx4 %3, %5, %6 offen offset:2048 lds\n\t"
                 "buffer_load_dwordx4 %4, %5, %6 offen offset:3072 lds"
                 ::"s"(m0), "v"(v[0]), "v"(v[1]), "v"(v[2]), "v"(v[3]), "s"(r), "s"(so) : "memory");
  else if constexpr (N == 3)
    asm volatile("s_mov_b32 m0, %0\n\ts_nop 3\n\t"
                 "buffer_load_dwordx4 %1, %4, %5 offen lds\n\t"
                 "buffer_load_dwordx4 %2, %4, %5 offen offset:1024 lds\n\t"
                 "buffer_load_dwordx4 %3, %4, %5 offen offset:2048 lds"
                 ::"s"(m0), "v"(v[0]), "v"(v[1]), "v"(v[2]), "s"(r), "s"(so) : "memory");
  else
    asm volatile("s_mov_b32 m0, %0\n\ts_nop 3\n\t"
                 "buffer_load_dwordx4 %1, %3, %4 offen lds\n\t"
                 "buffer_load_dwordx4 %2, %3, %4 offen offset:1024 lds"
                 ::"s"(m0), "v"(v[0]), "v"(v[1]), "s"(r), "s"(so) : "memory");
}
template <int N> DI void dma_sc1(u32 m0, u32 v, rsrc_t r, u32 so) {
  if constexpr (N == 4)
    asm volatile("s_mov_b32 m0, %0\n\ts_nop 3\n\t"
                 "buffer_load_dwordx4 %1, %2, %3 offen sc1 lds\n\t"
                 "buffer_load_dwordx4 %1, %2, %3 offen offset:1024 sc1 lds\n\t"
                 "buffer_load_dwordx4 %1, %2, %3 offen offset:2048 sc1 lds\n\t"
                 "buffer_load_dwordx4 %1, %2, %3 offen offset:3072 sc1 lds"
                 ::"s"(m0), "v"(v), "s"(r), "s"(so) : "memory");
  else
    asm volatile("s_mov_b32 m0, %0\n\ts_nop 3\n\t"
                 "buffer_load_dwordx4 %1, %2, %3 offen sc1 lds\n\t"
                 "buffer_load_dwordx4 %1, %2, %3 offen offset:1024 sc1 lds"
                 ::"s"(m0), "v"(v), "s"(r), "s"(so) : "memory");
}

DI float xsum16(float v) { const auto r = __builtin_amdgcn_permlane16_swap(__float_as_uint(v), __float_as_uint(v), false, false); return __uint_as_float(r[0]) + __uint_as_float(r[1]); }
DI float xsum32(float v) { const auto r = __builtin_amdgcn_permlane32_swap(__float_as_uint(v), __float_as_uint(v), false, false); return __uint_as_float(r[0]) + __uint_as_float(r[1]); }

template <int A, int B, class F> DI void static_for(F f) {
  if constexpr (A < B) { f(std::integral_constant<int, A>{}); static_for<A + 1, B>(f); }
}

DI void pull_lines(u32 m0, u32 v, rsrc_t r, u32 so) {
  asm volatile("s_mov_b32 m0, %0\n\ts_nop 3\n\tbuffer_load_dword %1, %2, %3 offen lds" ::"s"(m0), "v"(v), "s"(r), "s"(so) : "memory");
}

DI int kstep(int t, int n, int phi) {
  int k = t;
  if ((t & 3) == phi) { k = t + 4; if (k >= n) k = phi; }
  return k;
}

DI int kstep2(int t, int n, int phi, int side) {
  const int k = kstep(t, n, phi), k2 = k ^ (side << 2);
  return k2 < n ? k2 : k;
}

constexpr int NST = 4;
constexpr int SLOT = 16384;
constexpr int QOFF = NST * SLOT;
constexpr int P3 = 64 * 448;
constexpr int Q3OFF = 3 * P3;
constexpr int TOFF = Q3OFF + 3 * SLOT;
constexpr int COFF = TOFF + 4096;
constexpr int XOFF = COFF + 4096;
constexpr int LDS_BYTES = XOFF + 16384;

enum { M_ROW = 0, M_TILED = 1, M_TR448 = 2, M_TR256 = 3 };

template <int PT, int PMODE, int QMODE>
struct Consumer {
  static constexpr int NS = PMODE == M_TR448 ? 3 : NST, QB = PMODE == M_TR448 ? Q3OFF : QOFF;
  const LAS char* pb[6];
  const LAS char* qb[4];
  bf16x8 fp[2][PT], fq[2][4];

  DI void init(const LAS char* sm, int wP, int wQ, int lane, int prot = 0, int qrot = 0) {
    const int r = lane & 15, g = lane >> 4;
    if constexpr (PMODE == M_ROW) {
      const int a = (64 * wP + r) * 128 + ((g ^ (r >> 1)) * 16);
      pb[0] = sm + (a + prot * 4096); pb[1] = sm + ((a ^ 64) + prot * 4096);
      pb[2] = sm + (a + 4096 - prot * 4096); pb[3] = sm + ((a ^ 64) + 4096 - prot * 4096);
    } else if constexpr (PMODE == M_TILED) {
      pb[0] = sm + (4 * wP * 1024 + lane * 16);
    } else if constexpr (PMODE == M_TR448) {
      const int b = g & 1, a = (4 * g + (r >> 2)) * 448 + (r & 3) * 8;
      pb[0] = sm + (a + (8 * wP + b) * 32);
      pb[1] = sm + (a + (8 * wP - b) * 32);
      pb[2] = sm + (a + ((6 + wP) ^ b) * 32);
#pragma unroll
      for (int i = 0; i < 3; i++) { pb[3 + i] = pb[i] + 2 * P3; asm volatile("" : "+v"(pb[3 + i])); }
    } else {
      const int k = 4 * g + (r >> 2), a = k * 256 + (r & 3) * 8;
#pragma unroll
      for (int i = 0; i < 4; i++) pb[i] = sm + (a + (((4 * wP + i) ^ (k & 7)) * 32));
    }
    if constexpr (QMODE == M_ROW) {
      const int a = QOFF + (64 * wQ + r) * 128 + ((g ^ (r >> 1)) * 16);
      qb[0] = sm + (a + qrot * 4096); qb[1] = sm + ((a ^ 64) + qrot * 4096);
      qb[2] = sm + (a + 4096 - qrot * 4096); qb[3] = sm + ((a ^ 64) + 4096 - qrot * 4096);
      asm volatile("" : "+v"(qb[0]), "+v"(qb[1]), "+v"(qb[2]), "+v"(qb[3]));
    } else {
      qb[0] = sm + (QB + 4 * wQ * 1024 + lane * 16);
      asm volatile("" : "+v"(qb[0]));
    }
  }
  static DI bf16x8 tr2(const LAS char* p, int second) {
    s16x4 a = __builtin_amdgcn_ds_read_tr16_b64_v4i16((LAS s16x4*)p);
    s16x4 b = __builtin_amdgcn_ds_read_tr16_b64_v4i16((LAS s16x4*)(p + second));
    return __builtin_bit_cast(bf16x8, __builtin_shufflevector(a, b, 0, 1, 2, 3, 4, 5, 6, 7));
  }
  template <int S, int KS> DI bf16x8 ldp(int i) {
    if constexpr (PMODE == M_ROW) return *(const LAS bf16x8*)(pb[KS + (i & 2)] + (S * SLOT + (i & 1) * 2048));
    else if constexpr (PMODE == M_TILED) return *(const LAS bf16x8*)(pb[0] + (S * SLOT + KS * 8192 + i * 1024));
    else if constexpr (PMODE == M_TR448)
      return tr2(pb[(S == 2 ? 3 : 0) + (i == 6 ? 2 : i & 1)] + ((S & 1) * P3 + KS * 32 * 448 + (i == 6 ? 0 : i * 32)), 16 * 448);
    else return tr2(pb[i] + (S * SLOT + KS * 32 * 256), 16 * 256);
  }
  template <int S, int KS> DI bf16x8 ldq(int j) {
    if constexpr (QMODE == M_ROW) return *(const LAS bf16x8*)(qb[KS + (j & 2)] + (S * SLOT + (j & 1) * 2048));
    else return *(const LAS bf16x8*)(qb[0] + (S * SLOT + KS * 8192 + j * 1024));
  }
  template <int S, int KS, int QN> DI void load() {
    fq[KS][0] = ldq<S, KS>(0);
#pragma unroll
    for (int i = 0; i < PT; i++) fp[KS][i] = ldp<S, KS>(i);
#pragma unroll
    for (int j = 1; j < QN; j++) fq[KS][j] = ldq<S, KS>(j);
  }
  template <int KS, int QN, bool ZERO = false> DI void mma(f32x4 (&acc)[PT][4]) {
#pragma unroll
    for (int n = 0; n < PT * QN; n++) {
      const int j = n / PT, i = (j & 1) ? PT - 1 - n % PT : n % PT;
      acc[i][j] = __builtin_amdgcn_mfma_f32_16x16x32_bf16(fp[KS][i], fq[KS][j], ZERO ? (f32x4){0.f, 0.f, 0.f, 0.f} : acc[i][j], 0, 0, 0);
    }
  }
  template <int KS> DI void qsums(f32x4 (&sd)[2], bf16x8 ones) {
#pragma unroll
    for (int i = 0; i < 2; i++) sd[i] = __builtin_amdgcn_mfma_f32_16x16x32_bf16(ones, fq[KS][i], sd[i], 0, 0, 0);
  }
  template <int NM, int QN> static DI void interleave() {
    constexpr int NR = PT * (PMODE >= M_TR448 ? 2 : 1) + QN, G = K_GAPS(NM, NR);
    static_for<0, G>([](auto ic) {
      __builtin_amdgcn_sched_group_barrier(0x8, 1, 0);
      __builtin_amdgcn_sched_group_barrier(0x100, NR / G + (ic.value < NR % G ? 1 : 0), 0);
    });
    __builtin_amdgcn_sched_group_barrier(0x8, NM - G, 0);
    __builtin_amdgcn_sched_barrier(0);
  }
  template <int S, int QC, int QN> DI void step(f32x4 (&acc)[PT][4]) {
    load<S, 1, QC>(); mma<0, QC>(acc); interleave<PT * QC, QC>();
    wait_lgkm0(); bar();
    __builtin_amdgcn_sched_barrier(0);
    load<(S + 1) % NS, 0, QN>(); mma<1, QC>(acc); interleave<PT * QC, QN>();
  }
  template <int S, int LS, bool ZERO, class W> DI void half_with(f32x4 (&acc)[PT][4], W w) {
    static_for<0, PT * 4>([&](auto nc) {
      constexpr int n = nc.value, j = n / PT, i = (j & 1) ? PT - 1 - n % PT : n % PT;
      acc[i][j] = __builtin_amdgcn_mfma_f32_16x16x32_bf16(fp[1 - LS][i], fq[1 - LS][j], ZERO ? (f32x4){0.f, 0.f, 0.f, 0.f} : acc[i][j], 0, 0, 0);
      if constexpr (S >= 0) {
        if constexpr (n == 0) fq[LS][0] = ldq<S, LS>(0);
        else if constexpr (n <= PT) fp[LS][n - 1] = ldp<S, LS>(n - 1);
        else if constexpr (n < PT + 4) fq[LS][n - PT] = ldq<S, LS>(n - PT);
      }
      w(nc);
      __builtin_amdgcn_sched_barrier(0);
    });
  }
  template <int S, int QC> DI void last(f32x4 (&acc)[PT][4]) {
    load<S, 1, QC>(); mma<0, QC>(acc); interleave<PT * QC, QC>();
    wait_lgkm0();
    mma<1, QC>(acc);
  }
  template <int QC, int QN> DI void step4(f32x4 (&acc)[PT][4]) {
    step<0, QC, QC>(acc); step<1, QC, QC>(acc); step<2, QC, QC>(acc); step<3, QC, QN>(acc);
  }
  static DI void zero(f32x4 (&acc)[PT][4]) {
#pragma unroll
    for (int i = 0; i < PT; i++)
#pragma unroll
      for (int j = 0; j < 4; j++) { acc[i][j] = (f32x4){0.f, 0.f, 0.f, 0.f}; asm volatile("" : "+v"(acc[i][j])); }
  }
  DI void first() { load<0, 0, 4>(); }
};

DI u32 row_src(int imgrow, int srcrow, int lane, int ldbytes) {
  return srcrow * ldbytes + (((lane & 7) ^ ((imgrow >> 1) & 7)) * 16);
}
DI void store_sc1(rsrc_t r, u32 off, u32x4 v) { __builtin_amdgcn_raw_buffer_store_b128(v, r, off, 0, 16); }

DI void xch_send(char* x, const f32x4 (&acc)[4][4], int wid, int lane) {
  rsrc_t r = mk_rsrc(x + ((size_t)blockIdx.x * 4 + wid) * 4096, 4096);
#pragma unroll
  for (int jj = 0; jj < 2; jj++)
#pragma unroll
    for (int u = 0; u < 2; u++) {
      const f32x4 a = acc[2 * u][2 + jj], b = acc[2 * u + 1][2 + jj];
      u32x4 v = {cvtpk(a[0], a[1]), cvtpk(a[2], a[3]), cvtpk(b[0], b[1]), cvtpk(b[2], b[3])};
      store_sc1(r, lane * 16 + (jj * 2 + u) * 1024, v);
    }
}
DI void xch_request(rsrc_t r, u32x4 (&v)[4], int lane) {
  asm volatile("s_nop 4\n\tbuffer_load_dwordx4 %0, %4, %5, 0 offen sc1\n\t"
               "buffer_load_dwordx4 %1, %4, %5, 0 offen offset:1024 sc1\n\t"
               "buffer_load_dwordx4 %2, %4, %5, 0 offen offset:2048 sc1\n\t"
               "buffer_load_dwordx4 %3, %4, %5, 0 offen offset:3072 sc1"
               : "=&v"(v[0]), "=&v"(v[1]), "=&v"(v[2]), "=&v"(v[3]) : "v"(lane * 16), "s"(r) : "memory");
}
DI void xch_finish(u32x4 (&v)[4]) { asm volatile("s_waitcnt vmcnt(0)" : "+v"(v[0]), "+v"(v[1]), "+v"(v[2]), "+v"(v[3])::"memory"); }

enum { FL_X2 = 0, FL_X4 = 1, FL_ACK = 2, FL_TICK = 3, FL_CNT = 4 };
enum { CN_H = 0, CN_AB = 128, CN_SX = 256, CN_R = 384 };
constexpr u32 NOWHERE = 0xFFFFFF00u;
DI void flag_set(char* ws, int which, int tt, int cell) {
  wait_vm<0>();
  if ((threadIdx.x & 63) == 0)
    __hip_atomic_store((u32*)(ws + WS_FL + ((size_t)(which * 16 + tt) * 64 + cell) * 64), 1u, __ATOMIC_RELAXED, __HIP_MEMORY_SCOPE_AGENT);
}
DI void flag_request(rsrc_t r, u32 off, u32& v) {
  asm volatile("s_nop 4\n\tbuffer_load_dword %0, %1, %2, 0 offen sc1" : "=&v"(v) : "v"(off), "s"(r) : "memory");
}
DI void flag_wait(rsrc_t r, u32 off, u32& v) {
  asm volatile("" : "+v"(v)::"memory");
#pragma unroll 1
  for (int spin = 0; spin < K_SPIN; spin++) {
    if (__builtin_amdgcn_ballot_w64(v != 1u) == 0) break;
    __builtin_amdgcn_s_sleep(K_SLEEP);
    asm volatile("s_nop 4\n\tbuffer_load_dword %0, %1, %2, 0 offen sc1\n\ts_waitcnt vmcnt(0)" : "=&v"(v) : "v"(off), "s"(r) : "memory");
  }
}

DI void word_store(rsrc_t r, u32 off, u32 v) { __builtin_amdgcn_raw_buffer_store_b32(v, r, off, 0, 16); }
DI void count_up(char* ws, u32 off) {
  if ((threadIdx.x & 63) == 0) __hip_atomic_fetch_add((u32*)(ws + WS_FL + off), 1u, __ATOMIC_RELAXED, __HIP_MEMORY_SCOPE_AGENT);
}
template <class Done> DI void watch(rsrc_t r, u32 off, u32& v, Done done, u32 tickoff, u32& tick) {
#pragma unroll 1
  for (int spin = 0; spin < K_SPIN; spin++) {
    if (__builtin_amdgcn_ballot_w64(!done(v)) == 0) break;
    if ((spin & 63) == 63) { tick++; word_store(r, tickoff, tick); }
    __builtin_amdgcn_s_sleep(K_SLEEP);
    asm volatile("s_nop 4\n\tbuffer_load_dword %0, %1, %2, 0 offen sc1\n\ts_waitcnt vmcnt(0)" : "=&v"(v) : "v"(off), "s"(r) : "memory");
  }
}
__global__ __launch_bounds__(512, 2) void fused_kernel(const u16*, const u16*, const u16*, const u16*, const u16*, const u16*, const u16*,
                                                       const u16*, const u16*, char*, u16* DIAG_ARG) {
  u32x16 ka; u32x4 kb4; u32x2 kc;
  asm volatile("s_load_dwordx16 %0, %3, 0x0\n\ts_load_dwordx4 %1, %3, 0x40\n\ts_load_dwordx2 %2, %3, 0x50"
               : "=&s"(ka), "=&s"(kb4), "=&s"(kc) : "s"(__builtin_amdgcn_kernarg_segment_ptr()));
  __builtin_amdgcn_sched_barrier(0);
  extern __shared__ __attribute__((aligned(16))) char smem[];
  DIAG_BEGIN
  const int lane0 = threadIdx.x & 63, wid = rfl(threadIdx.x >> 6);
  const int xcd = blockIdx.x & 7, loc = blockIdx.x >> 3, li = loc >> 2, lj = loc & 3;
  const int half = xcd >> 2, et = ((xcd >> 1) & 1) * 4 + lj, tt = (xcd & 1) * 8 + li, ft = (xcd >> 1) * 4 + lj;
  LAS char* sm = (LAS char*)smem;
  LAS float* tabA = (LAS float*)(sm + TOFF);
  LAS float* tabB = tabA + 128;
  LAS float* tabC = tabA + 256;
  LAS float* tabBias = tabA + 512;
  u32 vp1[4], vq1[4];
#pragma unroll
  for (int i = 0; i < 4; i++) {
    const int row = 32 * (wid & 3) + 8 * i + (lane0 >> 3), u = (row >> 4) & 1, ii = row & 15;
    vp1[i] = row_src(row, 32 * (wid & 3) + 8 * (ii >> 2) + 4 * u + (ii & 3), lane0, D_ * 2) + 4096 - i * 1024;
    vq1[i] = row_src(row, row, lane0, D_ * 2) - i * 1024;
  }
  asm volatile("" : "+v"(vp1[0]), "+v"(vp1[1]), "+v"(vp1[2]), "+v"(vp1[3]), "+v"(vq1[0]), "+v"(vq1[1]), "+v"(vq1[2]), "+v"(vq1[3]));
  __builtin_amdgcn_sched_barrier(0);
  asm volatile("s_waitcnt lgkmcnt(0)" : "+s"(ka), "+s"(kb4), "+s"(kc));
  auto ptr = [](u32 lo, u32 hi) { return (u16*)(GAS u16*)(((u64)hi << 32) | lo); };
  const u16 *x = ptr(ka[0], ka[1]), *cre = ptr(ka[2], ka[3]), *cim = ptr(ka[4], ka[5]), *dre = ptr(ka[6], ka[7]), *dim = ptr(ka[8], ka[9]);
  const u16 *w1 = ptr(ka[10], ka[11]), *b1 = ptr(ka[12], ka[13]), *w2 = ptr(ka[14], ka[15]), *b2 = ptr(kb4[0], kb4[1]);
  char* ws = (char*)ptr(kb4[2], kb4[3]);
  u16* out = ptr(kc[0], kc[1]);
  if (__builtin_expect(wid >= 4, 1)) {
    if (K_PPRIO) __builtin_amdgcn_s_setprio(K_PPRIO);
    const int p = wid - 4, phi = (li + lj) & 3;
    const u32 lds0 = (u32)(size_t)smem, ldsb = lds0 + p * 4096;
    rsrc_t rall = mk_rsrc(ws + WS_FL, 5 * 65536);
    rsrc_t rp1 = mk_rsrc((const char*)x + (size_t)tt * 128 * D_ * 2 - 4096, 128 * D_ * 2 + 4096);
    rsrc_t rq1_ = mk_rsrc((half ? dim : dre) + (size_t)et * 128 * D_, 128 * D_ * 2);
    const int side1 = (xcd >> 1) ^ (xcd & 1);
    dma<4>(ldsb, vp1, rp1, kstep2(0, 16, phi, side1) * 128);
    dma<4>(ldsb + QOFF, vq1, rq1_, kstep2(0, 16, phi, side1) * 128);
    int lane = lane0;
    asm volatile("" : "+v"(lane)::"memory");
    __builtin_amdgcn_sched_barrier(0);
    const u32 vlin = p * 4096 + lane * 16;
    const u32 grpcell = ((lane & 16) ? tt * 64 + (lane & 15) * 4 : (lane & 15) * 64 + ft * 4) * 64;
    const u32 tickoff = lane == 0 ? FL_TICK * 65536 + (tt * 64 + ft * 4) * 64 : NOWHERE;
    const u32 ackoff = lane == 0 ? FL_ACK * 65536 + (tt * 64 + ft * 4) * 64 : NOWHERE;
    const u32 rowcnt = FL_CNT * 65536 + tt * 4096, colcnt = FL_CNT * 65536 + ft * 4096;
    const int pt = threadIdx.x - 256;
    u32 base, fv, tick;
    const int side2 = (xcd >> 1) & 1;
    auto skip = [&](int r) { return r + (r >= 2 * tt ? 2 : 0); };
    auto g2k = [&](int q) { return q < 2 ? 2 * tt + q : skip(kstep2(q - 2, 26, phi, side2)); };
    rsrc_t rco = mk_rsrc((const char*)(half ? cim : cre) + (size_t)tt * 128 * S_ * 2 - 4096, 128 * S_ * 2 + 4096);
    u32 vc[4];
#pragma unroll
    for (int i = 0; i < 4; i++) {
      const int row = 32 * p + 8 * i + (lane >> 3);
      vc[i] = row_src(row, row ^ (half * 32), lane, S_ * 2) + 4096 - i * 1024;
    }
    auto g2q = [&](int q) { dma<4>(ldsb + QOFF + (q & 3) * SLOT, vc, rco, g2k(q) * 128); };
    {
      u32 vx[2];
#pragma unroll
      for (int i = 0; i < 2; i++) {
        const int row = 16 * p + 8 * i + (lane >> 3);
        vx[i] = row_src(row, 64 * (row >> 5) + 32 * half + (row & 31), lane, D_ * 2) + 4096 - i * 1024;
      }
      auto issue = [&](int g) {
        const int k = kstep2(g, 16, phi, side1);
        const u32 m = ldsb + (g & 3) * SLOT;
        dma<4>(m, vp1, rp1, k * 128);
        dma<4>(m + QOFF, vq1, rq1_, k * 128);
      };
      auto run = [&](int g0, int g1) {
#pragma unroll 1
        for (int g = g0; g < g1; g++) { issue(g); wait_vm<16>(); bar(); }
      };
      flag_request(rall, p == 0 ? FL_TICK * 65536 + grpcell : NOWHERE, base);
      issue(1); issue(2);
      {
        u32 z = lane < 8 ? (lane >> 2) * 65536 + (tt * 64 + ft * 4 + (lane & 3)) * 64 : FL_ACK * 65536 + (tt * 64 + ft * 4) * 64;
        if (lane == 9) z = tt == 0 ? colcnt + CN_AB : NOWHERE;
        if (lane >= 10) z = ft == 0 ? rowcnt + (lane == 10 ? CN_H : lane == 11 ? CN_SX : CN_R) : NOWHERE;
        word_store(rall, p == 0 && lane < 13 ? z : NOWHERE, 0u);
      }
      wait_vm<18>();
      PSTAMP(1)
      bar();
      const int js = (phi + 1) & 3;
      auto sums = [&]() {
        const int c = lane & 15, g = lane >> 4, e = c & 3;
        const u32x4 ones4 = {0x3F803F80u, 0x3F803F80u, 0x3F803F80u, 0x3F803F80u};
        const bf16x8 ones = __builtin_bit_cast(bf16x8, ones4);
        const int a = js * SLOT + (32 * p + c) * 128 + ((g ^ (c >> 1)) * 16);
        rsrc_t rx = mk_rsrc(ws + WS_SX + (size_t)(tt * 16 + (js ^ (side1 << 2))) * 1024 + p * 256, 256);
#pragma unroll
        for (int i = 0; i < 2; i++) {
          const bf16x8 x0 = *(const LAS bf16x8*)(sm + (a + i * 2048)), x1 = *(const LAS bf16x8*)(sm + ((a ^ 64) + i * 2048));
          const f32x4 z = {0.f, 0.f, 0.f, 0.f};
          const f32x4 sx = __builtin_amdgcn_mfma_f32_16x16x32_bf16(x1, ones, __builtin_amdgcn_mfma_f32_16x16x32_bf16(x0, ones, z, 0, 0, 0), 0, 0, 0);
          const f32x4 sq = __builtin_amdgcn_mfma_f32_16x16x32_bf16(x1, x1, __builtin_amdgcn_mfma_f32_16x16x32_bf16(x0, x0, z, 0, 0, 0), 0, 0, 0);
          const float q = e == 0 ? sq[0] : e == 1 ? sq[1] : e == 2 ? sq[2] : sq[3];
          const float t = e == 0 ? sx[0] : e == 1 ? sx[1] : e == 2 ? sx[2] : sx[3];
          __builtin_amdgcn_raw_buffer_store_b64((u32x2){__float_as_uint(t), __float_as_uint(q)}, rx, (c >> 2) == g ? (16 * i + c) * 8 : NOWHERE, 0, 16);
        }
      };
      issue(3);
      if (js == 0) sums();
      asm volatile("s_waitcnt vmcnt(16)" : "+v"(base)::"memory");
      tick = __builtin_amdgcn_readlane(base, tt) + 1;
      bar();
#pragma unroll 1
      for (int g = 2; g < 6; g++) {
        issue(g + 2);
        if (g == js + 1) sums();
        wait_vm<16>();
        if (g == 3 && p == 0) word_store(rall, tickoff, tick);
        bar();
      }
      issue(8); wait_vm<16>(); bar();
      flag_request(rall, p == 0 ? FL_TICK * 65536 + grpcell : NOWHERE, fv);
      issue(9); wait_vm<16>(); bar();
      issue(10);
      asm volatile("s_waitcnt vmcnt(16)" : "+v"(fv)::"memory");
      PSTAMP(14)
      if (p == 0) {
        watch(rall, FL_TICK * 65536 + grpcell, fv, [&](u32 w) { return w != base; }, tickoff, tick);
        count_up(ws, rowcnt + CN_SX);
        word_store(rall, ackoff, 1u);
      }
      PSTAMP(15)
      bar();
      run(11, 14);
      dma<2>(lds0 + XOFF + p * 2048, vx, rp1, et * 256);
      dma<2>(lds0 + XOFF + 8192 + p * 2048, vx, rp1, et * 256 + 128);
      flag_request(rall, p == 0 ? rowcnt + CN_SX : NOWHERE, fv);
      issue(14); wait_vm<21>(); bar();
      issue(15);
      asm volatile("s_waitcnt vmcnt(16)" : "+v"(fv)::"memory");
      PSTAMP(9)
      if (p == 0) watch(rall, rowcnt + CN_SX, fv, [&](u32 w) { return w >= 16u; }, tickoff, tick);
      PSTAMP(10)
      bar();
      u32x2 sv[8];
      rsrc_t rsx = mk_rsrc(ws + WS_SX + (size_t)tt * 16384, 16384);
#pragma unroll
      for (int i = 0; i < 8; i++)
        asm volatile("s_nop 4\n\tbuffer_load_dwordx2 %0, %1, %2, %3 offen sc1" : "=&v"(sv[i]) : "v"((pt & 1) * 8192 + (pt >> 1) * 8), "s"(rsx), "s"(i * 1024) : "memory");
      g2q(0); wait_vm<20>(); bar();
      g2q(1); wait_vm<16>(); bar();
      g2q(2);
      asm volatile("s_waitcnt vmcnt(12)" : "+v"(sv[0]), "+v"(sv[1]), "+v"(sv[2]), "+v"(sv[3]), "+v"(sv[4]), "+v"(sv[5]), "+v"(sv[6]), "+v"(sv[7])::"memory");
      float a = 0.f, q = 0.f;
#pragma unroll
      for (int i = 0; i < 8; i++) { a += __uint_as_float(sv[i][0]); q += __uint_as_float(sv[i][1]); }
      a += __builtin_bit_cast(float, __builtin_amdgcn_update_dpp(0, __builtin_bit_cast(int, a), 0xB1, 0xF, 0xF, true));
      q += __builtin_bit_cast(float, __builtin_amdgcn_update_dpp(0, __builtin_bit_cast(int, q), 0xB1, 0xF, 0xF, true));
      const float mean = a * (1.0f / D_), var = fmaxf(q * (1.0f / D_) - mean * mean, 0.f);
      const float rstd = (half ? -1.f : 1.f) * __builtin_amdgcn_rsqf(var + LN_EPS);
      tabA[(pt & 1) * 128 + (pt >> 1)] = (pt & 1) ? -mean * rstd : rstd;
      wait_lgkm0();
    }
    {
      rsrc_t rp = mk_rsrc(ws + WS_AB + (size_t)et * 32 * 16384, 9 * 32 * 16384);
      rsrc_t rq0 = mk_rsrc((const char*)cre + (size_t)tt * 128 * S_ * 2 - 4096, 128 * S_ * 2 + 4096);
      rsrc_t rq1 = mk_rsrc((const char*)cim + (size_t)tt * 128 * S_ * 2 - 4096, 128 * S_ * 2 + 4096);
      u32 vh[2];
#pragma unroll
      for (int i = 0; i < 2; i++) {
        const int row = 64 * (p >> 1) + 16 * (p & 1) + 8 * i + (lane >> 3);
        vh[i] = row_src(row, row ^ (half * 32), lane, S_ * 2) + 4096 - i * 1024;
      }
      const u32 ldsh = lds0 + QOFF + (p >> 1) * 8192 + (p & 1) * 2048;
      auto fp = [&](int q) { dma_sc1<4>(ldsb + (q & 3) * SLOT, vlin, rp, (half * 256 + g2k(q)) * 16384); };
      auto full = [&](int q) { fp(q); g2q(q); };
      auto halfs = [&](int g) {
        const int k = kstep2(g, 8, phi, side2), term = k >> 2, ks = skip(26 + (k & 3));
        dma_sc1<4>(ldsb + (g & 3) * SLOT, vlin, rp, (term * 256 + ks) * 16384);
        if (term) dma<2>(ldsh + (g & 3) * SLOT, vh, rq1, ks * 128);
        else dma<2>(ldsh + (g & 3) * SLOT, vh, rq0, ks * 128);
      };
      bar();
      wait_vm<8>(); bar();
      g2q(3);
      rsrc_t rw = mk_rsrc((const char*)w1 + (size_t)(tt * 64 + p * 16) * F_ * 2 + ft * 448, 15 * F_ * 2 + 448);
      u32x4 v[8];
      const int l28 = lane < 28 ? lane : lane < 56 ? lane - 28 : 27;
      const u32 vo = (lane >= 28 ? F_ * 2 : 0) + l28 * 16;
      auto csload = [&]() {
#pragma unroll
      for (int i = 0; i < 8; i++)
        asm volatile("s_nop 4\n\tbuffer_load_dwordx4 %0, %1, %2, %3 offen" : "=&v"(v[i]) : "v"(vo), "s"(rw), "s"(i * 2 * F_ * 2) : "memory");
      };
      if (K_CSP == 0) csload();
      wait_vm<K_CSP == 0 ? 16 : 8>(); bar();
      if (K_CSP == 1) csload();
      bar();
      PSTAMP(12)
      if (p == 0) {
        count_up(ws, colcnt + CN_AB);
        fv = 0;
        watch(rall, colcnt + CN_AB, fv, [&](u32 w) { return w >= 16u; }, tickoff, tick);
        word_store(rall, ackoff, 2u);
      }
      bar();
      PSTAMP(2)
      fp(2); fp(3); full(4);
      asm volatile("s_waitcnt vmcnt(12)" : "+v"(v[0]), "+v"(v[1]), "+v"(v[2]), "+v"(v[3]), "+v"(v[4]), "+v"(v[5]), "+v"(v[6]), "+v"(v[7])::"memory");
      bar();
      full(5);
      {
        float s[8] = {0, 0, 0, 0, 0, 0, 0, 0};
#pragma unroll
        for (int i = 0; i < 8; i++)
#pragma unroll
          for (int e = 0; e < 4; e++) { s[2 * e] += blo(v[i][e]); s[2 * e + 1] += bhi(v[i][e]); }
#pragma unroll
        for (int e = 0; e < 8; e++) s[e] += __shfl(s[e], lane + 28);
        if (lane < 28) {
          LAS float* d = (LAS float*)(sm + COFF) + p * 224 + lane * 8;
          *(LAS f32x4*)d = (f32x4){s[0], s[1], s[2], s[3]};
          *(LAS f32x4*)(d + 4) = (f32x4){s[4], s[5], s[6], s[7]};
        }
      }
      wait_vm<16>(); wait_lgkm0(); bar();
      full(6);
      if (p == 0 && lane < 56) {
        const LAS f32x4* d = (const LAS f32x4*)(sm + COFF) + lane;
        const f32x4 t = (d[0] + d[56]) + (d[112] + d[168]);
        store_sc1(mk_rsrc(ws + WS_CS + (size_t)tt * F_ * 4 + ft * 896, 896), lane * 16, __builtin_bit_cast(u32x4, t));
      }
      wait_vm<16>(); bar();
      full(7); wait_vm<16>(); bar();
      full(8); wait_vm<16>();
      if (p == 0) count_up(ws, colcnt + CN_AB);
      bar();
#pragma unroll 1
      for (int q = 9; q < 26; q++) { full(q); wait_vm<16>(); bar(); }
      const u32 lastoff = lane < 32 ? FL_ACK * 65536 + grpcell : colcnt + CN_AB;
      const u32 lastmin = lane >= 32 ? 32u : lane == 16 + (ft ^ 8) ? 2u : 1u;
      flag_request(rall, p == 0 ? lastoff : NOWHERE, fv);
      full(26); wait_vm<16>(); bar();
      full(27);
      asm volatile("s_waitcnt vmcnt(16)" : "+v"(fv)::"memory");
      if (p == 0) watch(rall, lastoff, fv, [&](u32 w) { return w >= lastmin; }, tickoff, tick);
      bar();
      halfs(0); wait_vm<14>(); bar();
#pragma unroll 1
      for (int g = 1; g < 8; g++) { halfs(g); wait_vm<12>(); bar(); }
    }
    rsrc_t rp4 = mk_rsrc(w2 + et * 128, F_ * D_ * 2 - et * 256);
    u32 vp4[4];
#pragma unroll
    for (int i = 0; i < 4; i++) {
      const int k = 16 * (p & 1) + 4 * i + (lane >> 4);
      vp4[i] = k * (D_ * 2) + (((lane & 15) ^ ((k & 7) << 1)) * 16) - i * 1024;
    }
    auto g4blk = [&](int q) {
      const int r = q < 3 ? q : kstep2(q - 3, 21, phi, xcd & 1), pr = r % 3, hb = p >> 1;
      const int tl = q < 3 ? et : r / 3 + (r / 3 >= et ? 1 : 0);
      return 7 * (8 * half + tl) + 2 * pr + hb + (2 * pr + hb >= 3 ? 1 : 0);
    };
    auto g4p = [&](int q) { dma<4>(ldsb + ((q + 2) & 3) * SLOT, vp4, rp4, g4blk(q) * (32 * D_ * 2)); };
    {
      rsrc_t rp = mk_rsrc((const char*)w1 + ft * 448 - 4096, D_ * F_ * 2 - ft * 448 + 4096);
      rsrc_t rq = mk_rsrc(ws + WS_H + (size_t)tt * 32 * 8192, 32 * 8192);
      u32 vp[7];
#pragma unroll
      for (int i = 0; i < 7; i++) {
        const int o = (p * 7 + i) * 64 + lane, k = o / 28, ch = o % 28;
        vp[i] = k * (F_ * 2) + ((ch ^ (((k >> 2) & 1) << 1)) * 16) + 4096 - (i & 3) * 1024;
      }
      rsrc_t rw = mk_rsrc(w2 + (size_t)(half * 1792 + tt * 112 + 28 * p) * D_ + et * 128, 27 * D_ * 2 + 256);
      const u32 vw = (lane >> 1) * (D_ * 2) + (lane & 1) * 128;
      auto pa = [&](int g, int sl) {
        const u32 so = kstep(g, 16, phi) * (64 * F_ * 2), m = lds0 + sl * P3 + p * 7168;
        dma<4>(m, vp, rp, so);
        dma<3>(m + 4096, vp + 4, rp, so);
      };
      auto q = [&](int g, int sl) { dma_sc1<4>(ldsb + Q3OFF + sl * SLOT, vlin, rq, kstep(g, 16, phi) * 16384); };
      wait_vm<6>(); bar();
      wait_vm<0>(); bar();
      bar();
      PSTAMP(13)
      if (p == 0) count_up(ws, rowcnt + CN_H);
      pa(0, 0); pa(1, 1);
      if (K_LEAD & 1) pull_lines(lds0 + COFF + 3584, p == 1 ? (128 + 16 * li + (lane >> 2)) * (F_ * 2) + (lane & 3) * 128 + 4096 : NOWHERE, rp, 0);
      rsrc_t rs = mk_rsrc(ws + WS_ST + (size_t)tt * 16384, 16384);
      rsrc_t rc = mk_rsrc(ws + WS_CS + ft * 896, 15 * F_ * 4 + 896);
      u32x2 sv[8];
      u32 cv[16], bv;
#pragma unroll
      for (int i = 0; i < 16; i++)
        asm volatile("s_nop 4\n\tbuffer_load_dword %0, %1, %2, %3 offen sc1" : "=&v"(cv[i]) : "v"(min(pt, 223) * 4), "s"(rc), "s"(i * F_ * 4) : "memory");
      asm volatile("s_nop 4\n\tbuffer_load_ushort %0, %1, %2, 0 offen" : "=&v"(bv) : "v"(min(pt, 223) * 2), "s"(mk_rsrc(b1 + ft * 224, 448)) : "memory");
      if (p == 0) { fv = 0; watch(rall, rowcnt + CN_H, fv, [&](u32 w) { return w >= 16u; }, NOWHERE, tick); }
      bar();
      PSTAMP(3)
      q(0, 0); q(1, 1);
      wait_vm<4>();
      PSTAMP(4)
      bar();
      pa(2, 2); q(2, 2);
#pragma unroll
      for (int i = 0; i < 8; i++)
        asm volatile("s_nop 4\n\tbuffer_load_dwordx2 %0, %1, %2, %3 offen sc1" : "=&v"(sv[i]) : "v"((pt & 1) * 8192 + (pt >> 1) * 8), "s"(rs), "s"(i * 1024) : "memory");
      wait_vm<19>(); bar();
      pa(3, 0); q(3, 0); wait_vm<19>(); bar();
      pa(4, 1); q(4, 1); wait_vm<11>();
      asm volatile("" : "+v"(sv[0]), "+v"(sv[1]), "+v"(sv[2]), "+v"(sv[3]), "+v"(sv[4]), "+v"(sv[5]), "+v"(sv[6]), "+v"(sv[7]), "+v"(bv)::"memory");
      asm volatile("" : "+v"(cv[0]), "+v"(cv[1]), "+v"(cv[2]), "+v"(cv[3]), "+v"(cv[4]), "+v"(cv[5]), "+v"(cv[6]), "+v"(cv[7]), "+v"(cv[8]),
                   "+v"(cv[9]), "+v"(cv[10]), "+v"(cv[11]), "+v"(cv[12]), "+v"(cv[13]), "+v"(cv[14]), "+v"(cv[15])::"memory");
      bar();
      pa(5, 2); q(5, 2);
      {
        float s = 0.f, a = 0.f, qq = 0.f;
#pragma unroll
        for (int i = 0; i < 16; i++) s += __uint_as_float(cv[i]);
        if (pt < 224) { tabC[pt] = s; tabBias[pt] = __uint_as_float(bv << 16); }
#pragma unroll
        for (int i = 0; i < 8; i++) { a += __uint_as_float(sv[i][0]); qq += __uint_as_float(sv[i][1]); }
        a += __builtin_bit_cast(float, __builtin_amdgcn_update_dpp(0, __builtin_bit_cast(int, a), 0xB1, 0xF, 0xF, true));
        qq += __builtin_bit_cast(float, __builtin_amdgcn_update_dpp(0, __builtin_bit_cast(int, qq), 0xB1, 0xF, 0xF, true));
        const float mean = a * (1.0f / D_), var = fmaxf(qq * (1.0f / D_) - mean * mean, 0.f);
        const float rstd = __builtin_amdgcn_rsqf(var + LN_EPS);
        tabA[(pt & 1) * 128 + (pt >> 1)] = (pt & 1) ? -mean * rstd : rstd;
      }
      wait_vm<11>(); wait_lgkm0(); bar();
      int sl = 0;
#pragma unroll 1
      for (int g = 6; g < 16; g++) {
        pa(g, sl); q(g, sl);
        const int gp = K_WN > 0 ? 6 + loc % (K_WN > 0 ? K_WN : 1) : 99;
        if (g == gp) pull_lines(lds0 + COFF + 3584, vw, rw, 0);
        if ((unsigned)(g - gp) < 2u) wait_vm<12>(); else wait_vm<11>();
        bar();
        sl = sl == 2 ? 0 : sl + 1;
      }
      g4p(0); wait_vm<4>(); bar();
      g4p(1);
      bar();
    }
    {
      rsrc_t rq = mk_rsrc(ws + WS_R + (size_t)tt * 112 * 8192, 112 * 8192);
      const u32 vq0 = (4 * (p & 1) + 2 * half) * 1024 + lane * 16, vq1 = (4 * (p & 1) + 2 - 2 * half) * 1024 + lane * 16;
      auto g4q = [&](int q) {
        const u32 m = ldsb + QOFF + ((q + 2) & 3) * SLOT, so = g4blk(q) * 8192;
        dma_sc1<2>(m, vq0, rq, so);
        dma_sc1<2>(m + 2048, vq1, rq, so);
      };
      auto halfs = [&](int g) {
        const int k = kstep2(g, 8, phi, xcd & 1), blk = 7 * (2 * k + (p >> 1)) + 3;
        dma<4>(ldsb + ((g + 2) & 3) * SLOT, vp4, rp4, blk * (32 * D_ * 2));
        dma_sc1<2>(ldsb + QOFF + ((g + 2) & 3) * SLOT, vq0, rq, blk * 8192);
      };
      wait_vm<4>(); bar();
      g4p(2); wait_vm<4>(); bar();
      g4p(3); wait_vm<4>(); bar();
      if (K_LEAD & 2) {
        const int qq = 4 + min(lane >> 3, 2);
        const u32 b4 = g4blk(4), b5 = g4blk(5), b6 = g4blk(6), bb = qq == 4 ? b4 : qq == 5 ? b5 : b6;
        pull_lines(lds0 + COFF + 3584, (p & 1) == 0 && lane < 24 ? (bb * 32 + 4 * li + ((lane >> 1) & 3)) * (D_ * 2) + (lane & 1) * 128 : NOWHERE, rp4, 0);
      }
      bar();
      PSTAMP(5)
      if (p == 0) {
        count_up(ws, rowcnt + CN_R);
        fv = 0;
        watch(rall, rowcnt + CN_R, fv, [&](u32 w) { return w >= 16u; }, NOWHERE, tick);
      }
      bar();
      PSTAMP(6)
      g4q(3); g4p(4); g4q(4); g4p(5); g4q(5);
      wait_vm<16>(); bar();
#pragma unroll 1
      for (int q = 6; q < 24; q++) { g4p(q); g4q(q); wait_vm<16>(); bar(); }
      halfs(0); wait_vm<14>(); bar();
#pragma unroll 1
      for (int g = 1; g < 8; g++) { halfs(g); wait_vm<12>(); bar(); }
      wait_vm<6>(); bar();
      wait_vm<0>(); bar();
      PDIAG_END
    }
  } else {
    const int lane = lane0;
    const int wP = wid >> 1, wQ = wid & 1, c = lane & 15, g = lane >> 4;
    const int m0 = 4 * wQ + 2 * half, cell = ft * 4 + wid;
    {
      Consumer<4, M_ROW, M_ROW> C;
      C.init(sm, wP, wQ, lane, 0, wP);
      f32x4 acc[4][4], sd[2];
      C.zero(acc);
#pragma unroll
      for (int i = 0; i < 2; i++) sd[i] = (f32x4){0.f, 0.f, 0.f, 0.f};
      const u32x4 ones4 = {0x3F803F80u, 0x3F803F80u, 0x3F803F80u, 0x3F803F80u};
      bf16x8 ones = __builtin_bit_cast(bf16x8, ones4);
      asm volatile("" : "+v"(ones));
      bar();
      STAMP(1)
      C.first();
#define G1_HALF0(S) C.load<S, 1, 4>(); C.mma<0, 4>(acc); C.qsums<0>(sd, ones); C.interleave<18, 4>();
#define G1_STEP(S) G1_HALF0(S) wait_lgkm0(); bar(); __builtin_amdgcn_sched_barrier(0); \
      C.load<(S + 1) % NST, 0, 4>(); C.mma<1, 4>(acc); C.qsums<1>(sd, ones); C.interleave<18, 4>();
      for (int it = 0; it < 3; it++) { G1_STEP(0) G1_STEP(1) G1_STEP(2) G1_STEP(3) }
      G1_STEP(0) G1_STEP(1) G1_STEP(2) G1_HALF0(3)
      wait_lgkm0();
      C.qsums<1>(sd, ones);
      __builtin_amdgcn_sched_barrier(0);
      C.mma<1, 4>(acc);
      tabC[g < 2 ? 64 * wQ + 32 * wP + 16 * g + c : 128 + lane] = g ? sd[1][0] : sd[0][0];
      __builtin_amdgcn_sched_group_barrier(0x8, 4, 0);
      __builtin_amdgcn_sched_group_barrier(0x2 | 0x200, 8, 0);
      __builtin_amdgcn_sched_group_barrier(0x8, 12, 0);
      __builtin_amdgcn_sched_barrier(0);
      STAMP(2)
      wait_lgkm0(); bar();
      rsrc_t ro = mk_rsrc(ws + WS_AB + ((size_t)((half * 8 + et) * 32 + 2 * tt + wP) * 16 + 4 * wQ) * 1024, 12288);
      f32x4 ra[4], rb[4];
      float rs[4];
#pragma unroll
      for (int i = 0; i < 4; i++) {
        ra[i] = *(const LAS f32x4*)(tabA + 64 * wP + 16 * i + 4 * g);
        rb[i] = *(const LAS f32x4*)(tabB + 64 * wP + 16 * i + 4 * g);
        rs[i] = tabC[64 * wQ + 16 * (i ^ (2 * wP)) + c];
      }
#pragma unroll
      for (int j = 0; j < 4; j++)
#pragma unroll
        for (int u = 0; u < 2; u++) {
          const f32x4 a = ra[2 * u] * acc[2 * u][j] + rb[2 * u] * rs[j], b = ra[2 * u + 1] * acc[2 * u + 1][j] + rb[2 * u + 1] * rs[j];
          const u32x4 o = {cvtpk(a[0], a[1]), cvtpk(a[2], a[3]), cvtpk(b[0], b[1]), cvtpk(b[2], b[3])};
          const int at = (u * 8 + (j ^ (2 * wP))) * 1024 + lane * 16;
          store_sc1(ro, at, o);
          *(LAS u32x4*)(sm + (wP * SLOT + wQ * 4096 + at)) = o;
        }
    }
    u32x4 hk[2][2];
    {
      Consumer<4, M_TILED, M_ROW> C;
      C.init(sm, wP, wQ, lane);
      f32x4 acc[4][4];
      wait_lgkm0(); bar();
      STAMP(3)
      C.first();
      C.load<0, 1, 4>(); C.mma<0, 4, true>(acc); C.interleave<16, 4>();
      wait_lgkm0(); bar();
      __builtin_amdgcn_sched_barrier(0);
      C.load<1, 0, 4>(); C.mma<1, 4>(acc); C.interleave<16, 4>();
      C.load<1, 1, 4>(); C.mma<0, 4>(acc); C.interleave<16, 4>();
      wait_lgkm0();
      C.mma<1, 4>(acc);
      __builtin_amdgcn_sched_barrier(0);
      wait_vm<0>();
      STAMP(4)
      bar();
      bar();
      bar();
      STAMP(5)
      C.load<2, 0, 4>();
      C.step<2, 4, 4>(acc); C.step<3, 4, 4>(acc);
      for (int it = 0; it < 5; it++) C.step4<4, 4>(acc);
      C.step4<4, 2>(acc);
      STAMP(6)
      xch_send(ws + WS_X2, acc, wid, lane);
      __builtin_amdgcn_sched_barrier(0);
      rsrc_t rx = mk_rsrc(ws + WS_X2 + ((size_t)(blockIdx.x ^ 4) * 4 + wid) * 4096, 4096);
      rsrc_t rf = mk_rsrc(ws + WS_FL + ((size_t)(FL_X2 * 16 + tt) * 64 + (cell ^ 32)) * 64, 64);
      u32x4 rv[4];
      u32 fv;
      C.step<0, 2, 2>(acc); C.step<1, 2, 2>(acc); C.step<2, 2, 2>(acc);
      if (K_XF == 3) flag_set(ws, FL_X2, tt, cell);
      C.step<3, 2, 2>(acc);
      if (K_XF == 4) flag_set(ws, FL_X2, tt, cell);
      C.step<0, 2, 2>(acc); C.step<1, 2, 2>(acc);
      flag_request(rf, 0, fv);
      __builtin_amdgcn_sched_barrier(0);
      C.step<2, 2, 2>(acc);
      wait_vm<0>();
      flag_wait(rf, 0, fv);
      xch_request(rx, rv, lane);
      __builtin_amdgcn_sched_barrier(0);
      C.load<3, 1, 2>(); C.mma<0, 2>(acc); C.interleave<8, 2>();
      C.mma<1, 2>(acc);
      STAMP(7)
      xch_finish(rv);
            rsrc_t rh = mk_rsrc(ws + WS_H + ((size_t)(tt * 32 + 4 * et + 2 * wP) * 8 + m0) * 1024, 16384);
      rsrc_t rs = mk_rsrc(ws + WS_ST + ((size_t)(tt * 16 + 2 * et + wP) * 128 + 16 * m0) * 8, 256);
#pragma unroll
      for (int jj = 0; jj < 2; jj++) {
        float s = 0.f, q = 0.f;
#pragma unroll
        for (int u = 0; u < 2; u++) {
          const u32x4 o = rv[jj * 2 + u];
          const f32x4 a = acc[2 * u][jj], b = acc[2 * u + 1][jj];
          const int xrow = 32 * wQ + 16 * jj + c;
          const LAS char* xp = sm + (XOFF + wP * 8192 + xrow * 128 + (g & 1) * 8);
          const u32x2 xa = *(const LAS u32x2*)(xp + (((4 * u + (g >> 1)) ^ ((xrow >> 1) & 7)) * 16));
          const u32x2 xb = *(const LAS u32x2*)(xp + (((4 * u + 2 + (g >> 1)) ^ ((xrow >> 1) & 7)) * 16));
          u32x4 h;
          h[0] = cvtpk(a[0] + blo(o[0]) + blo(xa[0]), a[1] + bhi(o[0]) + bhi(xa[0]));
          h[1] = cvtpk(a[2] + blo(o[1]) + blo(xa[1]), a[3] + bhi(o[1]) + bhi(xa[1]));
          h[2] = cvtpk(b[0] + blo(o[2]) + blo(xb[0]), b[1] + bhi(o[2]) + bhi(xb[0]));
          h[3] = cvtpk(b[2] + blo(o[3]) + blo(xb[1]), b[3] + bhi(o[3]) + bhi(xb[1]));
          store_sc1(rh, (u * 8 + jj) * 1024 + lane * 16, h);
          hk[jj][u] = h;
#pragma unroll
          for (int e = 0; e < 4; e++) { const float lo = blo(h[e]), hi = bhi(h[e]); s += lo + hi; q += lo * lo + hi * hi; }
        }
        s = xsum32(xsum16(s)); q = xsum32(xsum16(q));
        if (g == 0) __builtin_amdgcn_raw_buffer_store_b64((u32x2){__float_as_uint(s), __float_as_uint(q)}, rs, (16 * jj + c) * 8, 0, 16);
      }
      wait_vm<0>();
      STAMP(8)
      bar();
#pragma unroll
      for (int q = 0; q < 4; q++) *(LAS u32x4*)(sm + XOFF + wid * 4096 + q * 1024 + lane * 16) = hk[q >> 1][q & 1];
      bar();
    }
    rsrc_t rr = mk_rsrc(ws + WS_R + ((size_t)(tt * 112 + 7 * ft) * 8 + 4 * wQ) * 1024, 7 * 8192);
    u32x4 ro[14];
    {
      Consumer<7, M_TR448, M_TILED> C;
      C.init(sm, wP, wQ, lane);
      f32x4 acc[7][4];
      C.zero(acc);
      bar();
      STAMP(9)
      C.first();
      for (int it = 0; it < 5; it++) { C.step<0, 4, 4>(acc); C.step<1, 4, 4>(acc); C.step<2, 4, 4>(acc); }
      C.last<0, 4>(acc);
      bar();
      STAMP(10)
      float ra[4], rb[4];
#pragma unroll
      for (int j = 0; j < 4; j++) { ra[j] = tabA[64 * wQ + 16 * j + c]; rb[j] = tabB[64 * wQ + 16 * j + c]; }
      f32x4 cs[7], bi[7];
#pragma unroll
      for (int i = 0; i < 7; i++) {
        const int col = (i < 6 ? 128 * wP + 16 * i : 96 + 16 * wP) + 4 * g;
        cs[i] = *(const LAS f32x4*)(tabC + col); bi[i] = *(const LAS f32x4*)(tabBias + col);
      }
      auto act = [&](int i, int j) {
        f32x4 v = ra[j] * acc[i][j] + (rb[j] * cs[i] + bi[i]);
#pragma unroll
        for (int e = 0; e < 4; e++) v[e] = fmaxf(v[e], 0.f);
        v = v * v;
        return (u32x2){cvtpk(v[0], v[1]), cvtpk(v[2], v[3])};
      };
#pragma unroll
      for (int u = 0; u < 3; u++) {
        const int n = 3 * wP + u;
        LAS char* own = sm + (QOFF + (((n >> 1) + 2) & 3) * SLOT + (n & 1) * 8192 + 4 * wQ * 1024 + lane * 16);
#pragma unroll
        for (int j = 0; j < 4; j++) {
          const u32x2 a = act(2 * u, j), b = act(2 * u + 1, j);
          ro[4 * u + j] = (u32x4){a[0], a[1], b[0], b[1]};
          if (4 * u + j < K_NS1) store_sc1(rr, ((4 * wP + u) * 8 + j) * 1024 + lane * 16, ro[4 * u + j]);
          *(LAS u32x4*)(own + (j ^ (2 * half)) * 1024) = ro[4 * u + j];
        }
      }
#pragma unroll
      for (int j = 0; j < 4; j++)
        *(LAS u32x2*)(sm + (QOFF + SLOT + (4 * wQ + j) * 1024 + lane * 16 + 8 * wP)) = act(6, j);
    }
    {
      Consumer<4, M_TR256, M_TILED> C;
      C.init(sm, wP, wQ, lane);
      f32x4 acc[4][4];
      wait_lgkm0(); bar();
      STAMP(11)
      C.load<2, 0, 4>();
#pragma unroll
      for (int j = 0; j < 2; j++) ro[12 + j] = *(const LAS u32x4*)(sm + (QOFF + SLOT + (4 * wQ + 2 * wP + j) * 1024 + lane * 16));
      auto put = [&](int hs, int n) {
        constexpr int LEFT = 14 - K_NS1;
        for (int q = 0; q < LEFT; q++)
          if (q * 88 / LEFT == hs * 16 + n) {
            const int t = K_NS1 + q;
            store_sc1(rr, (t < 12 ? (4 * wP + (t >> 2)) * 8 + (t & 3) : 24 + 2 * wP + (t & 1)) * 1024 + lane * 16, ro[t]);
          }
      };
      C.half_with<2, 1, true>(acc, [&](int n) { put(0, n); });
      wait_lgkm0(); bar();
      C.half_with<3, 0, false>(acc, [&](int n) { put(1, n); });
      C.half_with<3, 1, false>(acc, [&](int n) { put(2, n); });
      wait_lgkm0(); bar();
      C.half_with<0, 0, false>(acc, [&](int n) { put(3, n); });
      C.half_with<0, 1, false>(acc, [&](int n) { put(4, n); });
      wait_lgkm0();
      C.half_with<-1, 0, false>(acc, [&](int n) { put(5, n); });
      __builtin_amdgcn_sched_barrier(0);
      wait_vm<0>();
      STAMP(12)
      bar();
      bar();
      bar();
      STAMP(13)
      C.load<1, 0, 4>();
      C.step<1, 4, 4>(acc); C.step<2, 4, 4>(acc); C.step<3, 4, 4>(acc);
      for (int it = 0; it < 4; it++) C.step4<4, 4>(acc);
      C.step<0, 4, 4>(acc); C.step<1, 4, 2>(acc);
      STAMP(14)
      xch_send(ws + WS_X4, acc, wid, lane);
      u32x2 br[4];
#pragma unroll
      for (int i = 0; i < 4; i++) br[i] = *(const u32x2*)(b2 + et * 128 + 64 * wP + 16 * i + 4 * g);
      __builtin_amdgcn_sched_barrier(0);
      rsrc_t rx = mk_rsrc(ws + WS_X4 + ((size_t)(blockIdx.x ^ 4) * 4 + wid) * 4096, 4096);
      rsrc_t rf = mk_rsrc(ws + WS_FL + ((size_t)(FL_X4 * 16 + tt) * 64 + (cell ^ 32)) * 64, 64);
      u32x4 rv[4];
      u32 fv;
      C.step<2, 2, 2>(acc); C.step<3, 2, 2>(acc); C.step<0, 2, 2>(acc);
      flag_set(ws, FL_X4, tt, cell);
      C.step<1, 2, 2>(acc); C.step<2, 2, 2>(acc); C.step<3, 2, 2>(acc);
      flag_request(rf, 0, fv);
      __builtin_amdgcn_sched_barrier(0);
      C.step<0, 2, 2>(acc);
      wait_vm<0>();
      flag_wait(rf, 0, fv);
      xch_request(rx, rv, lane);
      __builtin_amdgcn_sched_barrier(0);
      C.last<1, 2>(acc);
      xch_finish(rv);

      rsrc_t ro = mk_rsrc(out + (size_t)(tt * 128 + 16 * m0) * D_ + et * 128 + 64 * wP, 31 * D_ * 2 + 128);
#pragma unroll
      for (int jj = 0; jj < 2; jj++)
#pragma unroll
        for (int u = 0; u < 2; u++) {
          const u32x4 o = rv[jj * 2 + u], h = *(const LAS u32x4*)(sm + XOFF + wid * 4096 + (jj * 2 + u) * 1024 + lane * 16);
          const f32x4 a = acc[2 * u][jj], b = acc[2 * u + 1][jj];
          const u32x2 ba = br[2 * u], bb = br[2 * u + 1];
          const u32 a0 = cvtpk(a[0] + blo(o[0]) + blo(ba[0]) + blo(h[0]), a[1] + bhi(o[0]) + bhi(ba[0]) + bhi(h[0]));
          const u32 a1 = cvtpk(a[2] + blo(o[1]) + blo(ba[1]) + blo(h[1]), a[3] + bhi(o[1]) + bhi(ba[1]) + bhi(h[1]));
          const u32 b0 = cvtpk(b[0] + blo(o[2]) + blo(bb[0]) + blo(h[2]), b[1] + bhi(o[2]) + bhi(bb[0]) + bhi(h[2]));
          const u32 b1_ = cvtpk(b[2] + blo(o[3]) + blo(bb[1]) + blo(h[3]), b[3] + bhi(o[3]) + bhi(bb[1]) + bhi(h[3]));
          const auto s0 = __builtin_amdgcn_permlane16_swap(a0, b0, false, false);
          const auto s1 = __builtin_amdgcn_permlane16_swap(a1, b1_, false, false);
          __builtin_amdgcn_raw_buffer_store_b128((u32x4){s0[0], s1[0], s0[1], s1[1]}, ro,
                                                 (16 * jj + c) * (D_ * 2) + (32 * u + 16 * (g & 1) + 8 * (g >> 1)) * 2, 0, 16);
        }
      DIAG_END(15)
    }
  }
}

}

extern "C" void kernel_launch(void* const* d_in, const int* in_sizes, int n_in, void* d_out, int out_size, void* d_ws, size_t ws_size,
                              hipStream_t stream) {
  const u16* const* I = (const u16* const*)d_in;
  (void)hipFuncSetAttribute((const void*)fused_kernel, hipFuncAttributeMaxDynamicSharedMemorySize, LDS_BYTES);
  fused_kernel<<<256, 512, LDS_BYTES, stream>>>(I[0], I[1], I[2], I[3], I[4], I[6], I[7], I[8], I[9], (char*)d_ws, (u16*)d_out);
}
```

```cpp
#include <hip/hip_runtime.h>
#include <hip/hip_bf16.h>
#include <type_traits>

typedef unsigned short u16;
typedef unsigned int u32;
typedef unsigned long long u64;
typedef __bf16 bf16x8 __attribute__((ext_vector_type(8)));
typedef __bf16 bf16x2 __attribute__((ext_vector_type(2)));
typedef short s16x4 __attribute__((ext_vector_type(4)));
typedef float f32x2 __attribute__((ext_vector_type(2)));
typedef float f32x4 __attribute__((ext_vector_type(4)));
typedef u32 u32x2 __attribute__((ext_vector_type(2)));
typedef u32 u32x4 __attribute__((ext_vector_type(4)));
typedef u32 u32x16 __attribute__((ext_vector_type(16)));
typedef __amdgpu_buffer_rsrc_t rsrc_t;
#define GAS __attribute__((address_space(1)))
#define LAS __attribute__((address_space(3)))
#define DI __device__ __forceinline__

namespace {

constexpr int S_ = 2048, D_ = 1024, F_ = 3584;
constexpr float LN_EPS = 1e-5f;
constexpr int SPIN_LIMIT = 1 << 18;
constexpr int EARLY = 6;

constexpr size_t WS_AB = 0;
constexpr size_t WS_H = 8u << 20;
constexpr size_t WS_R = 12u << 20;
constexpr size_t WS_X2 = 27u << 20;
constexpr size_t WS_X4 = 31u << 20;
constexpr size_t WS_ST = 35u << 20;
constexpr size_t WS_CS = 36u << 20;
constexpr size_t WS_FL = 37u << 20;
constexpr size_t WS_SX = 38u << 20;

DI u32 rfl(u32 x) { return __builtin_amdgcn_readfirstlane(x); }
DI u32 cvtpk(float lo, float hi) { f32x2 v = {lo, hi}; return __builtin_bit_cast(u32, __builtin_convertvector(v, bf16x2)); }
DI float blo(u32 v) { return __uint_as_float(v << 16); }
DI float bhi(u32 v) { return __uint_as_float(v & 0xFFFF0000u); }
DI rsrc_t mk_rsrc(const void* p, u32 bytes) { return __builtin_amdgcn_make_buffer_rsrc((void*)p, 0, bytes, 0x00020000); }
DI void bar() { asm volatile("s_barrier" ::: "memory"); }
DI void wait_lgkm0() { __builtin_amdgcn_s_waitcnt(0xC07F); }
template <int N> DI void wait_vm() { asm volatile("s_waitcnt vmcnt(%0)" ::"i"(N) : "memory"); }

template <int N> DI void dma(u32 m0, const u32* v, rsrc_t r, u32 so) {
  if constexpr (N == 4)
    asm volatile("s_mov_b32 m0, %0\n\ts_nop 3\n\t"
                 "buffer_load_dwordx4 %1, %5, %6 offen lds\n\t"
                 "buffer_load_dwordx4 %2, %5, %6 offen offset:1024 lds\n\t"
                 "buffer_load_dwordx4 %3, %5, %6 offen offset:2048 lds\n\t"
                 "buffer_load_dwordx4 %4, %5, %6 offen offset:3072 lds"
                 ::"s"(m0), "v"(v[0]), "v"(v[1]), "v"(v[2]), "v"(v[3]), "s"(r), "s"(so) : "memory");
  else if constexpr (N == 3)
    asm volatile("s_mov_b32 m0, %0\n\ts_nop 3\n\t"
                 "buffer_load_dwordx4 %1, %4, %5 offen lds\n\t"
                 "buffer_load_dwordx4 %2, %4, %5 offen offset:1024 lds\n\t"
                 "buffer_load_dwordx4 %3, %4, %5 offen offset:2048 lds"
                 ::"s"(m0), "v"(v[0]), "v"(v[1]), "v"(v[2]), "s"(r), "s"(so) : "memory");
  else
    asm volatile("s_mov_b32 m0, %0\n\ts_nop 3\n\t"
                 "buffer_load_dwordx4 %1, %3, %4 offen lds\n\t"
                 "buffer_load_dwordx4 %2, %3, %4 offen offset:1024 lds"
                 ::"s"(m0), "v"(v[0]), "v"(v[1]), "s"(r), "s"(so) : "memory");
}
template <int N> DI void dma_sc1(u32 m0, u32 v, rsrc_t r, u32 so) {
  if constexpr (N == 4)
    asm volatile("s_mov_b32 m0, %0\n\ts_nop 3\n\t"
                 "buffer_load_dwordx4 %1, %2, %3 offen sc1 lds\n\t"
                 "buffer_load_dwordx4 %1, %2, %3 offen offset:1024 sc1 lds\n\t"
                 "buffer_load_dwordx4 %1, %2, %3 offen offset:2048 sc1 lds\n\t"
                 "buffer_load_dwordx4 %1, %2, %3 offen offset:3072 sc1 lds"
                 ::"s"(m0), "v"(v), "s"(r), "s"(so) : "memory");
  else
    asm volatile("s_mov_b32 m0, %0\n\ts_nop 3\n\t"
                 "buffer_load_dwordx4 %1, %2, %3 offen sc1 lds\n\t"
                 "buffer_load_dwordx4 %1, %2, %3 offen offset:1024 sc1 lds"
                 ::"s"(m0), "v"(v), "s"(r), "s"(so) : "memory");
}

DI float xsum16(float v) { const auto r = __builtin_amdgcn_permlane16_swap(__float_as_uint(v), __float_as_uint(v), false, false); return __uint_as_float(r[0]) + __uint_as_float(r[1]); }
DI float xsum32(float v) { const auto r = __builtin_amdgcn_permlane32_swap(__float_as_uint(v), __float_as_uint(v), false, false); return __uint_as_float(r[0]) + __uint_as_float(r[1]); }

template <int A, int B, class F> DI void static_for(F f) {
  if constexpr (A < B) { f(std::integral_constant<int, A>{}); static_for<A + 1, B>(f); }
}

DI void pull_lines(u32 m0, u32 v, rsrc_t r, u32 so) {
  asm volatile("s_mov_b32 m0, %0\n\ts_nop 3\n\tbuffer_load_dword %1, %2, %3 offen lds" ::"s"(m0), "v"(v), "s"(r), "s"(so) : "memory");
}

DI int kstep(int t, int n, int phi) {
  int k = t;
  if ((t & 3) == phi) { k = t + 4; if (k >= n) k = phi; }
  return k;
}

DI int kstep2(int t, int n, int phi, int side) {
  const int k = kstep(t, n, phi), k2 = k ^ (side << 2);
  return k2 < n ? k2 : k;
}

constexpr int NST = 4;
constexpr int SLOT = 16384;
constexpr int QOFF = NST * SLOT;
constexpr int P3 = 64 * 448;
constexpr int Q3OFF = 3 * P3;
constexpr int TOFF = Q3OFF + 3 * SLOT;
constexpr int COFF = TOFF + 4096;
constexpr int XOFF = COFF + 4096;
constexpr int LDS_BYTES = XOFF + 16384;

enum { M_ROW = 0, M_TILED = 1, M_TR448 = 2, M_TR256 = 3 };

template <int PT, int PMODE, int QMODE>
struct Consumer {
  static constexpr int NS = PMODE == M_TR448 ? 3 : NST, QB = PMODE == M_TR448 ? Q3OFF : QOFF;
  const LAS char* pb[6];
  const LAS char* qb[4];
  bf16x8 fp[2][PT], fq[2][4];

  DI void init(const LAS char* sm, int wP, int wQ, int lane, int qrot = 0) {
    const int r = lane & 15, g = lane >> 4;
    if constexpr (PMODE == M_ROW) {
      const int a = (64 * wP + r) * 128 + ((g ^ (r >> 1)) * 16);
      pb[0] = sm + a; pb[1] = sm + (a ^ 64);
    } else if constexpr (PMODE == M_TILED) {
      pb[0] = sm + (4 * wP * 1024 + lane * 16);
    } else if constexpr (PMODE == M_TR448) {
      const int b = g & 1, a = (4 * g + (r >> 2)) * 448 + (r & 3) * 8;
      pb[0] = sm + (a + (8 * wP + b) * 32);
      pb[1] = sm + (a + (8 * wP - b) * 32);
      pb[2] = sm + (a + ((6 + wP) ^ b) * 32);
#pragma unroll
      for (int i = 0; i < 3; i++) { pb[3 + i] = pb[i] + 2 * P3; asm volatile("" : "+v"(pb[3 + i])); }
    } else {
      const int k = 4 * g + (r >> 2), a = k * 256 + (r & 3) * 8;
#pragma unroll
      for (int i = 0; i < 4; i++) pb[i] = sm + (a + (((4 * wP + i) ^ (k & 7)) * 32));
    }
    if constexpr (QMODE == M_ROW) {
      const int a = QOFF + (64 * wQ + r) * 128 + ((g ^ (r >> 1)) * 16);
      qb[0] = sm + (a + qrot * 4096); qb[1] = sm + ((a ^ 64) + qrot * 4096);
      qb[2] = sm + (a + 4096 - qrot * 4096); qb[3] = sm + ((a ^ 64) + 4096 - qrot * 4096);
      asm volatile("" : "+v"(qb[0]), "+v"(qb[1]), "+v"(qb[2]), "+v"(qb[3]));
    } else {
      qb[0] = sm + (QB + 4 * wQ * 1024 + lane * 16);
      asm volatile("" : "+v"(qb[0]));
    }
  }
  static DI bf16x8 tr2(const LAS char* p, int second) {
    s16x4 a = __builtin_amdgcn_ds_read_tr16_b64_v4i16((LAS s16x4*)p);
    s16x4 b = __builtin_amdgcn_ds_read_tr16_b64_v4i16((LAS s16x4*)(p + second));
    return __builtin_bit_cast(bf16x8, __builtin_shufflevector(a, b, 0, 1, 2, 3, 4, 5, 6, 7));
  }
  template <int S, int KS> DI bf16x8 ldp(int i) {
    if constexpr (PMODE == M_ROW) return *(const LAS bf16x8*)(pb[KS] + (S * SLOT + i * 2048));
    else if constexpr (PMODE == M_TILED) return *(const LAS bf16x8*)(pb[0] + (S * SLOT + KS * 8192 + i * 1024));
    else if constexpr (PMODE == M_TR448)
      return tr2(pb[(S == 2 ? 3 : 0) + (i == 6 ? 2 : i & 1)] + ((S & 1) * P3 + KS * 32 * 448 + (i == 6 ? 0 : i * 32)), 16 * 448);
    else return tr2(pb[i] + (S * SLOT + KS * 32 * 256), 16 * 256);
  }
  template <int S, int KS> DI bf16x8 ldq(int j) {
    if constexpr (QMODE == M_ROW) return *(const LAS bf16x8*)(qb[KS + (j & 2)] + (S * SLOT + (j & 1) * 2048));
    else return *(const LAS bf16x8*)(qb[0] + (S * SLOT + KS * 8192 + j * 1024));
  }
  template <int S, int KS, int QN> DI void load() {
    fq[KS][0] = ldq<S, KS>(0);
#pragma unroll
    for (int i = 0; i < PT; i++) fp[KS][i] = ldp<S, KS>(i);
#pragma unroll
    for (int j = 1; j < QN; j++) fq[KS][j] = ldq<S, KS>(j);
  }
  template <int KS, int QN, bool ZERO = false> DI void mma(f32x4 (&acc)[PT][4]) {
#pragma unroll
    for (int n = 0; n < PT * QN; n++) {
      const int j = n / PT, i = (j & 1) ? PT - 1 - n % PT : n % PT;
      acc[i][j] = __builtin_amdgcn_mfma_f32_16x16x32_bf16(fp[KS][i], fq[KS][j], ZERO ? (f32x4){0.f, 0.f, 0.f, 0.f} : acc[i][j], 0, 0, 0);
    }
  }
  template <int KS> DI void qsums(f32x4 (&sd)[2], bf16x8 ones) {
#pragma unroll
    for (int i = 0; i < 2; i++) sd[i] = __builtin_amdgcn_mfma_f32_16x16x32_bf16(ones, fq[KS][i], sd[i], 0, 0, 0);
  }
  template <int NM, int QN> static DI void interleave() {
    constexpr int NR = PT * (PMODE >= M_TR448 ? 2 : 1) + QN, G = NR < NM ? NR : NM;
    static_for<0, G>([](auto ic) {
      __builtin_amdgcn_sched_group_barrier(0x8, 1, 0);
      __builtin_amdgcn_sched_group_barrier(0x100, NR / G + (ic.value < NR % G ? 1 : 0), 0);
    });
    __builtin_amdgcn_sched_group_barrier(0x8, NM - G, 0);
    __builtin_amdgcn_sched_barrier(0);
  }
  template <int S, int QC, int QN> DI void step(f32x4 (&acc)[PT][4]) {
    load<S, 1, QC>(); mma<0, QC>(acc); interleave<PT * QC, QC>();
    wait_lgkm0(); bar();
    __builtin_amdgcn_sched_barrier(0);
    load<(S + 1) % NS, 0, QN>(); mma<1, QC>(acc); interleave<PT * QC, QN>();
  }
  template <int S, int LS, bool ZERO, class W> DI void half_with(f32x4 (&acc)[PT][4], W w) {
    static_for<0, PT * 4>([&](auto nc) {
      constexpr int n = nc.value, j = n / PT, i = (j & 1) ? PT - 1 - n % PT : n % PT;
      acc[i][j] = __builtin_amdgcn_mfma_f32_16x16x32_bf16(fp[1 - LS][i], fq[1 - LS][j], ZERO ? (f32x4){0.f, 0.f, 0.f, 0.f} : acc[i][j], 0, 0, 0);
      if constexpr (S >= 0) {
        if constexpr (n == 0) fq[LS][0] = ldq<S, LS>(0);
        else if constexpr (n <= PT) fp[LS][n - 1] = ldp<S, LS>(n - 1);
        else if constexpr (n < PT + 4) fq[LS][n - PT] = ldq<S, LS>(n - PT);
      }
      w(nc);
      __builtin_amdgcn_sched_barrier(0);
    });
  }
  template <int S, int QC> DI void last(f32x4 (&acc)[PT][4]) {
    load<S, 1, QC>(); mma<0, QC>(acc); interleave<PT * QC, QC>();
    wait_lgkm0();
    mma<1, QC>(acc);
  }
  template <int QC, int QN> DI void step4(f32x4 (&acc)[PT][4]) {
    step<0, QC, QC>(acc); step<1, QC, QC>(acc); step<2, QC, QC>(acc); step<3, QC, QN>(acc);
  }
  static DI void zero(f32x4 (&acc)[PT][4]) {
#pragma unroll
    for (int i = 0; i < PT; i++)
#pragma unroll
      for (int j = 0; j < 4; j++) { acc[i][j] = (f32x4){0.f, 0.f, 0.f, 0.f}; asm volatile("" : "+v"(acc[i][j])); }
  }
  DI void first() { load<0, 0, 4>(); }
};

DI u32 row_src(int imgrow, int srcrow, int lane, int ldbytes) {
  return srcrow * ldbytes + (((lane & 7) ^ ((imgrow >> 1) & 7)) * 16);
}
DI void store_sc1(rsrc_t r, u32 off, u32x4 v) { __builtin_amdgcn_raw_buffer_store_b128(v, r, off, 0, 16); }

DI void xch_send(char* x, const f32x4 (&acc)[4][4], int wid, int lane) {
  rsrc_t r = mk_rsrc(x + ((size_t)blockIdx.x * 4 + wid) * 4096, 4096);
#pragma unroll
  for (int jj = 0; jj < 2; jj++)
#pragma unroll
    for (int u = 0; u < 2; u++) {
      const f32x4 a = acc[2 * u][2 + jj], b = acc[2 * u + 1][2 + jj];
      u32x4 v = {cvtpk(a[0], a[1]), cvtpk(a[2], a[3]), cvtpk(b[0], b[1]), cvtpk(b[2], b[3])};
      store_sc1(r, lane * 16 + (jj * 2 + u) * 1024, v);
    }
}
DI void xch_request(rsrc_t r, u32x4 (&v)[4], int lane) {
  asm volatile("s_nop 4\n\tbuffer_load_dwordx4 %0, %4, %5, 0 offen sc1\n\t"
               "buffer_load_dwordx4 %1, %4, %5, 0 offen offset:1024 sc1\n\t"
               "buffer_load_dwordx4 %2, %4, %5, 0 offen offset:2048 sc1\n\t"
               "buffer_load_dwordx4 %3, %4, %5, 0 offen offset:3072 sc1"
               : "=&v"(v[0]), "=&v"(v[1]), "=&v"(v[2]), "=&v"(v[3]) : "v"(lane * 16), "s"(r) : "memory");
}
DI void xch_finish(u32x4 (&v)[4]) { asm volatile("s_waitcnt vmcnt(0)" : "+v"(v[0]), "+v"(v[1]), "+v"(v[2]), "+v"(v[3])::"memory"); }

enum { FL_X2 = 0, FL_X4 = 1, FL_ACK = 2, FL_TICK = 3, FL_CNT = 4 };
enum { CN_H = 0, CN_AB = 128, CN_SX = 256, CN_R = 384 };
constexpr u32 NOWHERE = 0xFFFFFF00u;
DI void flag_set(char* ws, int which, int tt, int cell) {
  wait_vm<0>();
  if ((threadIdx.x & 63) == 0)
    __hip_atomic_store((u32*)(ws + WS_FL + ((size_t)(which * 16 + tt) * 64 + cell) * 64), 1u, __ATOMIC_RELAXED, __HIP_MEMORY_SCOPE_AGENT);
}
DI void flag_request(rsrc_t r, u32 off, u32& v) {
  asm volatile("s_nop 4\n\tbuffer_load_dword %0, %1, %2, 0 offen sc1" : "=&v"(v) : "v"(off), "s"(r) : "memory");
}
DI void flag_wait(rsrc_t r, u32 off, u32& v) {
  asm volatile("" : "+v"(v)::"memory");
#pragma unroll 1
  for (int spin = 0; spin < SPIN_LIMIT; spin++) {
    if (__builtin_amdgcn_ballot_w64(v != 1u) == 0) break;
    __builtin_amdgcn_s_sleep(1);
    asm volatile("s_nop 4\n\tbuffer_load_dword %0, %1, %2, 0 offen sc1\n\ts_waitcnt vmcnt(0)" : "=&v"(v) : "v"(off), "s"(r) : "memory");
  }
}

DI void word_store(rsrc_t r, u32 off, u32 v) { __builtin_amdgcn_raw_buffer_store_b32(v, r, off, 0, 16); }
DI void count_up(char* ws, u32 off) {
  if ((threadIdx.x & 63) == 0) __hip_atomic_fetch_add((u32*)(ws + WS_FL + off), 1u, __ATOMIC_RELAXED, __HIP_MEMORY_SCOPE_AGENT);
}
template <class Done> DI void watch(rsrc_t r, u32 off, u32& v, Done done, u32 tickoff, u32& tick) {
#pragma unroll 1
  for (int spin = 0; spin < SPIN_LIMIT; spin++) {
    if (__builtin_amdgcn_ballot_w64(!done(v)) == 0) break;
    if ((spin & 63) == 63) { tick++; word_store(r, tickoff, tick); }
    __builtin_amdgcn_s_sleep(1);
    asm volatile("s_nop 4\n\tbuffer_load_dword %0, %1, %2, 0 offen sc1\n\ts_waitcnt vmcnt(0)" : "=&v"(v) : "v"(off), "s"(r) : "memory");
  }
}

__global__ __launch_bounds__(512, 2) void fused_kernel(const u16*, const u16*, const u16*, const u16*, const u16*, const u16*, const u16*,
                                                       const u16*, const u16*, char*, u16*) {
  u32x16 ka; u32x4 kb4; u32x2 kc;
  asm volatile("s_load_dwordx16 %0, %3, 0x0\n\ts_load_dwordx4 %1, %3, 0x40\n\ts_load_dwordx2 %2, %3, 0x50"
               : "=&s"(ka), "=&s"(kb4), "=&s"(kc) : "s"(__builtin_amdgcn_kernarg_segment_ptr()));
  __builtin_amdgcn_sched_barrier(0);
  extern __shared__ __attribute__((aligned(16))) char smem[];
  const int lane0 = threadIdx.x & 63, wid = rfl(threadIdx.x >> 6);
  const int xcd = blockIdx.x & 7, loc = blockIdx.x >> 3, li = loc >> 2, lj = loc & 3;
  const int half = xcd >> 2, et = ((xcd >> 1) & 1) * 4 + lj, tt = (xcd & 1) * 8 + li, ft = (xcd >> 1) * 4 + lj;
  LAS char* sm = (LAS char*)smem;
  LAS float* tabA = (LAS float*)(sm + TOFF);
  LAS float* tabB = tabA + 128;
  LAS float* tabC = tabA + 256;
  LAS float* tabBias = tabA + 512;
  u32 vp1[4], vq1[4];
#pragma unroll
  for (int i = 0; i < 4; i++) {
    const int row = 32 * (wid & 3) + 8 * i + (lane0 >> 3), u = (row >> 4) & 1, ii = row & 15;
    vp1[i] = row_src(row, 32 * (wid & 3) + 8 * (ii >> 2) + 4 * u + (ii & 3), lane0, D_ * 2) + 4096 - i * 1024;
    vq1[i] = row_src(row, row, lane0, D_ * 2) - i * 1024;
  }
  asm volatile("" : "+v"(vp1[0]), "+v"(vp1[1]), "+v"(vp1[2]), "+v"(vp1[3]), "+v"(vq1[0]), "+v"(vq1[1]), "+v"(vq1[2]), "+v"(vq1[3]));
  __builtin_amdgcn_sched_barrier(0);
  asm volatile("s_waitcnt lgkmcnt(0)" : "+s"(ka), "+s"(kb4), "+s"(kc));
  auto ptr = [](u32 lo, u32 hi) { return (u16*)(GAS u16*)(((u64)hi << 32) | lo); };
  const u16 *x = ptr(ka[0], ka[1]), *cre = ptr(ka[2], ka[3]), *cim = ptr(ka[4], ka[5]), *dre = ptr(ka[6], ka[7]), *dim = ptr(ka[8], ka[9]);
  const u16 *w1 = ptr(ka[10], ka[11]), *b1 = ptr(ka[12], ka[13]), *w2 = ptr(ka[14], ka[15]), *b2 = ptr(kb4[0], kb4[1]);
  char* ws = (char*)ptr(kb4[2], kb4[3]);
  u16* out = ptr(kc[0], kc[1]);
  if (__builtin_expect(wid >= 4, 1)) {
    const int p = wid - 4, phi = (li + lj) & 3;
    const u32 lds0 = (u32)(size_t)smem, ldsb = lds0 + p * 4096;
    rsrc_t rall = mk_rsrc(ws + WS_FL, 5 * 65536);
    rsrc_t rp1 = mk_rsrc((const char*)x + (size_t)tt * 128 * D_ * 2 - 4096, 128 * D_ * 2 + 4096);
    rsrc_t rq1d = mk_rsrc((half ? dim : dre) + (size_t)et * 128 * D_, 128 * D_ * 2);
    const int side1 = (xcd >> 1) ^ (xcd & 1);
    dma<4>(ldsb, vp1, rp1, kstep2(0, 16, phi, side1) * 128);
    dma<4>(ldsb + QOFF, vq1, rq1d, kstep2(0, 16, phi, side1) * 128);
    int lane = lane0;
    asm volatile("" : "+v"(lane)::"memory");
    __builtin_amdgcn_sched_barrier(0);
    const u32 vlin = p * 4096 + lane * 16;
    const u32 grpcell = ((lane & 16) ? tt * 64 + (lane & 15) * 4 : (lane & 15) * 64 + ft * 4) * 64;
    const u32 tickoff = lane == 0 ? FL_TICK * 65536 + (tt * 64 + ft * 4) * 64 : NOWHERE;
    const u32 ackoff = lane == 0 ? FL_ACK * 65536 + (tt * 64 + ft * 4) * 64 : NOWHERE;
    const u32 rowcnt = FL_CNT * 65536 + tt * 4096, colcnt = FL_CNT * 65536 + ft * 4096;
    const int pt = threadIdx.x - 256;
    u32 base, fv, tick;
    const int side2 = (xcd >> 1) & 1;
    auto skip = [&](int r) { return r + (r >= 2 * tt ? 2 : 0); };
    auto g2k = [&](int q) { return q < 2 ? 2 * tt + q : skip(kstep2(q - 2, 26, phi, side2)); };
    rsrc_t rco = mk_rsrc((const char*)(half ? cim : cre) + (size_t)tt * 128 * S_ * 2 - 4096, 128 * S_ * 2 + 4096);
    u32 vc[4];
#pragma unroll
    for (int i = 0; i < 4; i++) {
      const int row = 32 * p + 8 * i + (lane >> 3);
      vc[i] = row_src(row, row ^ (half * 32), lane, S_ * 2) + 4096 - i * 1024;
    }
    auto g2q = [&](int q) { dma<4>(ldsb + QOFF + (q & 3) * SLOT, vc, rco, g2k(q) * 128); };
    {
      u32 vx[2];
#pragma unroll
      for (int i = 0; i < 2; i++) {
        const int row = 16 * p + 8 * i + (lane >> 3);
        vx[i] = row_src(row, 64 * (row >> 5) + 32 * half + (row & 31), lane, D_ * 2) + 4096 - i * 1024;
      }
      auto issue = [&](int g) {
        const int k = kstep2(g, 16, phi, side1);
        const u32 m = ldsb + (g & 3) * SLOT;
        dma<4>(m, vp1, rp1, k * 128);
        dma<4>(m + QOFF, vq1, rq1d, k * 128);
      };
      auto run = [&](int g0, int g1) {
#pragma unroll 1
        for (int g = g0; g < g1; g++) { issue(g); wait_vm<16>(); bar(); }
      };
      flag_request(rall, p == 0 ? FL_TICK * 65536 + grpcell : NOWHERE, base);
      issue(1); issue(2);
      {
        u32 z = lane < 8 ? (lane >> 2) * 65536 + (tt * 64 + ft * 4 + (lane & 3)) * 64 : FL_ACK * 65536 + (tt * 64 + ft * 4) * 64;
        if (lane == 9) z = tt == 0 ? colcnt + CN_AB : NOWHERE;
        if (lane >= 10) z = ft == 0 ? rowcnt + (lane == 10 ? CN_H : lane == 11 ? CN_SX : CN_R) : NOWHERE;
        word_store(rall, p == 0 && lane < 13 ? z : NOWHERE, 0u);
      }
      wait_vm<18>();
      bar();
      const int js = (phi + 1) & 3;
      auto sums = [&]() {
        const int c = lane & 15, g = lane >> 4, e = c & 3;
        const u32x4 ones4 = {0x3F803F80u, 0x3F803F80u, 0x3F803F80u, 0x3F803F80u};
        const bf16x8 ones = __builtin_bit_cast(bf16x8, ones4);
        const int a = js * SLOT + (32 * p + c) * 128 + ((g ^ (c >> 1)) * 16);
        rsrc_t rx = mk_rsrc(ws + WS_SX + (size_t)(tt * 16 + (js ^ (side1 << 2))) * 1024 + p * 256, 256);
#pragma unroll
        for (int i = 0; i < 2; i++) {
          const bf16x8 x0 = *(const LAS bf16x8*)(sm + (a + i * 2048)), x1 = *(const LAS bf16x8*)(sm + ((a ^ 64) + i * 2048));
          const f32x4 z = {0.f, 0.f, 0.f, 0.f};
          const f32x4 sx = __builtin_amdgcn_mfma_f32_16x16x32_bf16(x1, ones, __builtin_amdgcn_mfma_f32_16x16x32_bf16(x0, ones, z, 0, 0, 0), 0, 0, 0);
          const f32x4 sq = __builtin_amdgcn_mfma_f32_16x16x32_bf16(x1, x1, __builtin_amdgcn_mfma_f32_16x16x32_bf16(x0, x0, z, 0, 0, 0), 0, 0, 0);
          const float q = e == 0 ? sq[0] : e == 1 ? sq[1] : e == 2 ? sq[2] : sq[3];
          const float t = e == 0 ? sx[0] : e == 1 ? sx[1] : e == 2 ? sx[2] : sx[3];
          __builtin_amdgcn_raw_buffer_store_b64((u32x2){__float_as_uint(t), __float_as_uint(q)}, rx, (c >> 2) == g ? (16 * i + c) * 8 : NOWHERE, 0, 16);
        }
      };
      issue(3);
      if (js == 0) sums();
      asm volatile("s_waitcnt vmcnt(16)" : "+v"(base)::"memory");
      tick = __builtin_amdgcn_readlane(base, tt) + 1;
      bar();
#pragma unroll 1
      for (int g = 2; g < 6; g++) {
        issue(g + 2);
        if (g == js + 1) sums();
        wait_vm<16>();
        if (g == 3 && p == 0) word_store(rall, tickoff, tick);
        bar();
      }
      issue(8); wait_vm<16>(); bar();
      flag_request(rall, p == 0 ? FL_TICK * 65536 + grpcell : NOWHERE, fv);
      issue(9); wait_vm<16>(); bar();
      issue(10);
      asm volatile("s_waitcnt vmcnt(16)" : "+v"(fv)::"memory");
      if (p == 0) {
        watch(rall, FL_TICK * 65536 + grpcell, fv, [&](u32 w) { return w != base; }, tickoff, tick);
        count_up(ws, rowcnt + CN_SX);
        word_store(rall, ackoff, 1u);
      }
      bar();
      run(11, 14);
      dma<2>(lds0 + XOFF + p * 2048, vx, rp1, et * 256);
      dma<2>(lds0 + XOFF + 8192 + p * 2048, vx, rp1, et * 256 + 128);
      flag_request(rall, p == 0 ? rowcnt + CN_SX : NOWHERE, fv);
      issue(14); wait_vm<21>(); bar();
      issue(15);
      asm volatile("s_waitcnt vmcnt(16)" : "+v"(fv)::"memory");
      if (p == 0) watch(rall, rowcnt + CN_SX, fv, [&](u32 w) { return w >= 16u; }, tickoff, tick);
      bar();
      u32x2 sv[8];
      rsrc_t rsx = mk_rsrc(ws + WS_SX + (size_t)tt * 16384, 16384);
#pragma unroll
      for (int i = 0; i < 8; i++)
        asm volatile("s_nop 4\n\tbuffer_load_dwordx2 %0, %1, %2, %3 offen sc1" : "=&v"(sv[i]) : "v"((pt & 1) * 8192 + (pt >> 1) * 8), "s"(rsx), "s"(i * 1024) : "memory");
      g2q(0); wait_vm<20>(); bar();
      g2q(1); wait_vm<16>(); bar();
      g2q(2);
      asm volatile("s_waitcnt vmcnt(12)" : "+v"(sv[0]), "+v"(sv[1]), "+v"(sv[2]), "+v"(sv[3]), "+v"(sv[4]), "+v"(sv[5]), "+v"(sv[6]), "+v"(sv[7])::"memory");
      float a = 0.f, q = 0.f;
#pragma unroll
      for (int i = 0; i < 8; i++) { a += __uint_as_float(sv[i][0]); q += __uint_as_float(sv[i][1]); }
      a += __builtin_bit_cast(float, __builtin_amdgcn_update_dpp(0, __builtin_bit_cast(int, a), 0xB1, 0xF, 0xF, true));
      q += __builtin_bit_cast(float, __builtin_amdgcn_update_dpp(0, __builtin_bit_cast(int, q), 0xB1, 0xF, 0xF, true));
      const float mean = a * (1.0f / D_), var = fmaxf(q * (1.0f / D_) - mean * mean, 0.f);
      const float rstd = (half ? -1.f : 1.f) * __builtin_amdgcn_rsqf(var + LN_EPS);
      tabA[(pt & 1) * 128 + (pt >> 1)] = (pt & 1) ? -mean * rstd : rstd;
      wait_lgkm0();
    }
    {
      rsrc_t rp = mk_rsrc(ws + WS_AB + (size_t)et * 32 * 16384, 9 * 32 * 16384);
      rsrc_t rq0 = mk_rsrc((const char*)cre + (size_t)tt * 128 * S_ * 2 - 4096, 128 * S_ * 2 + 4096);
      rsrc_t rq1 = mk_rsrc((const char*)cim + (size_t)tt * 128 * S_ * 2 - 4096, 128 * S_ * 2 + 4096);
      u32 vh[2];
#pragma unroll
      for (int i = 0; i < 2; i++) {
        const int row = 64 * (p >> 1) + 16 * (p & 1) + 8 * i + (lane >> 3);
        vh[i] = row_src(row, row ^ (half * 32), lane, S_ * 2) + 4096 - i * 1024;
      }
      const u32 ldsh = lds0 + QOFF + (p >> 1) * 8192 + (p & 1) * 2048;
      auto fp = [&](int q) { dma_sc1<4>(ldsb + (q & 3) * SLOT, vlin, rp, (half * 256 + g2k(q)) * 16384); };
      auto full = [&](int q) { fp(q); g2q(q); };
      auto halfs = [&](int g) {
        const int k = kstep2(g, 8, phi, side2), term = k >> 2, ks = skip(26 + (k & 3));
        dma_sc1<4>(ldsb + (g & 3) * SLOT, vlin, rp, (term * 256 + ks) * 16384);
        if (term) dma<2>(ldsh + (g & 3) * SLOT, vh, rq1, ks * 128);
        else dma<2>(ldsh + (g & 3) * SLOT, vh, rq0, ks * 128);
      };
      bar();
      wait_vm<8>(); bar();
      g2q(3);
      rsrc_t rw = mk_rsrc((const char*)w1 + (size_t)(tt * 64 + p * 16) * F_ * 2 + ft * 448, 15 * F_ * 2 + 448);
      u32x4 v[8];
      const int l28 = lane < 28 ? lane : lane < 56 ? lane - 28 : 27;
      const u32 vo = (lane >= 28 ? F_ * 2 : 0) + l28 * 16;
#pragma unroll
      for (int i = 0; i < 8; i++)
        asm volatile("s_nop 4\n\tbuffer_load_dwordx4 %0, %1, %2, %3 offen" : "=&v"(v[i]) : "v"(vo), "s"(rw), "s"(i * 2 * F_ * 2) : "memory");
      wait_vm<16>(); bar();
      bar();
      if (p == 0) {
        count_up(ws, colcnt + CN_AB);
        fv = 0;
        watch(rall, colcnt + CN_AB, fv, [&](u32 w) { return w >= 16u; }, tickoff, tick);
        word_store(rall, ackoff, 2u);
      }
      bar();
      fp(2); fp(3); full(4);
      asm volatile("s_waitcnt vmcnt(12)" : "+v"(v[0]), "+v"(v[1]), "+v"(v[2]), "+v"(v[3]), "+v"(v[4]), "+v"(v[5]), "+v"(v[6]), "+v"(v[7])::"memory");
      bar();
      full(5);
      {
        float s[8] = {0, 0, 0, 0, 0, 0, 0, 0};
#pragma unroll
        for (int i = 0; i < 8; i++)
#pragma unroll
          for (int e = 0; e < 4; e++) { s[2 * e] += blo(v[i][e]); s[2 * e + 1] += bhi(v[i][e]); }
#pragma unroll
        for (int e = 0; e < 8; e++) s[e] += __shfl(s[e], lane + 28);
        if (lane < 28) {
          LAS float* d = (LAS float*)(sm + COFF) + p * 224 + lane * 8;
          *(LAS f32x4*)d = (f32x4){s[0], s[1], s[2], s[3]};
          *(LAS f32x4*)(d + 4) = (f32x4){s[4], s[5], s[6], s[7]};
        }
      }
      wait_vm<16>(); wait_lgkm0(); bar();
      full(6);
      if (p == 0 && lane < 56) {
        const LAS f32x4* d = (const LAS f32x4*)(sm + COFF) + lane;
        const f32x4 t = (d[0] + d[56]) + (d[112] + d[168]);
        store_sc1(mk_rsrc(ws + WS_CS + (size_t)tt * F_ * 4 + ft * 896, 896), lane * 16, __builtin_bit_cast(u32x4, t));
      }
      wait_vm<16>(); bar();
      full(7); wait_vm<16>(); bar();
      full(8); wait_vm<16>();
      if (p == 0) count_up(ws, colcnt + CN_AB);
      bar();
#pragma unroll 1
      for (int q = 9; q < 26; q++) { full(q); wait_vm<16>(); bar(); }
      const u32 lastoff = lane < 32 ? FL_ACK * 65536 + grpcell : colcnt + CN_AB;
      const u32 lastmin = lane >= 32 ? 32u : lane == 16 + (ft ^ 8) ? 2u : 1u;
      flag_request(rall, p == 0 ? lastoff : NOWHERE, fv);
      full(26); wait_vm<16>(); bar();
      full(27);
      asm volatile("s_waitcnt vmcnt(16)" : "+v"(fv)::"memory");
      if (p == 0) watch(rall, lastoff, fv, [&](u32 w) { return w >= lastmin; }, tickoff, tick);
      bar();
      halfs(0); wait_vm<14>(); bar();
#pragma unroll 1
      for (int g = 1; g < 8; g++) { halfs(g); wait_vm<12>(); bar(); }
    }
    rsrc_t rp4 = mk_rsrc(w2 + et * 128, F_ * D_ * 2 - et * 256);
    u32 vp4[4];
#pragma unroll
    for (int i = 0; i < 4; i++) {
      const int k = 16 * (p & 1) + 4 * i + (lane >> 4);
      vp4[i] = k * (D_ * 2) + (((lane & 15) ^ ((k & 7) << 1)) * 16) - i * 1024;
    }
    auto g4blk = [&](int q) {
      const int r = q < 3 ? q : kstep2(q - 3, 21, phi, xcd & 1), pr = r % 3, hb = p >> 1;
      const int tl = q < 3 ? et : r / 3 + (r / 3 >= et ? 1 : 0);
      return 7 * (8 * half + tl) + 2 * pr + hb + (2 * pr + hb >= 3 ? 1 : 0);
    };
    auto g4p = [&](int q) { dma<4>(ldsb + ((q + 2) & 3) * SLOT, vp4, rp4, g4blk(q) * (32 * D_ * 2)); };
    {
      rsrc_t rp = mk_rsrc((const char*)w1 + ft * 448 - 4096, D_ * F_ * 2 - ft * 448 + 4096);
      rsrc_t rq = mk_rsrc(ws + WS_H + (size_t)tt * 32 * 8192, 32 * 8192);
      u32 vp[7];
#pragma unroll
      for (int i = 0; i < 7; i++) {
        const int o = (p * 7 + i) * 64 + lane, k = o / 28, ch = o % 28;
        vp[i] = k * (F_ * 2) + ((ch ^ (((k >> 2) & 1) << 1)) * 16) + 4096 - (i & 3) * 1024;
      }
      rsrc_t rw = mk_rsrc(w2 + (size_t)(half * 1792 + tt * 112 + 28 * p) * D_ + et * 128, 27 * D_ * 2 + 256);
      const u32 vw = (lane >> 1) * (D_ * 2) + (lane & 1) * 128;
      auto pa = [&](int g, int sl) {
        const u32 so = kstep(g, 16, phi) * (64 * F_ * 2), m = lds0 + sl * P3 + p * 7168;
        dma<4>(m, vp, rp, so);
        dma<3>(m + 4096, vp + 4, rp, so);
      };
      auto q = [&](int g, int sl) { dma_sc1<4>(ldsb + Q3OFF + sl * SLOT, vlin, rq, kstep(g, 16, phi) * 16384); };
      wait_vm<6>(); bar();
      wait_vm<0>(); bar();
      bar();
      if (p == 0) count_up(ws, rowcnt + CN_H);
      pa(0, 0); pa(1, 1);
      pull_lines(lds0 + COFF + 3584, p == 1 ? (128 + 16 * li + (lane >> 2)) * (F_ * 2) + (lane & 3) * 128 + 4096 : NOWHERE, rp, 0);
      rsrc_t rs = mk_rsrc(ws + WS_ST + (size_t)tt * 16384, 16384);
      rsrc_t rc = mk_rsrc(ws + WS_CS + ft * 896, 15 * F_ * 4 + 896);
      u32x2 sv[8];
      u32 cv[16], bv;
#pragma unroll
      for (int i = 0; i < 16; i++)
        asm volatile("s_nop 4\n\tbuffer_load_dword %0, %1, %2, %3 offen sc1" : "=&v"(cv[i]) : "v"(min(pt, 223) * 4), "s"(rc), "s"(i * F_ * 4) : "memory");
      asm volatile("s_nop 4\n\tbuffer_load_ushort %0, %1, %2, 0 offen" : "=&v"(bv) : "v"(min(pt, 223) * 2), "s"(mk_rsrc(b1 + ft * 224, 448)) : "memory");
      if (p == 0) { fv = 0; watch(rall, rowcnt + CN_H, fv, [&](u32 w) { return w >= 16u; }, NOWHERE, tick); }
      bar();
      q(0, 0); q(1, 1);
      wait_vm<4>();
      bar();
      pa(2, 2); q(2, 2);
#pragma unroll
      for (int i = 0; i < 8; i++)
        asm volatile("s_nop 4\n\tbuffer_load_dwordx2 %0, %1, %2, %3 offen sc1" : "=&v"(sv[i]) : "v"((pt & 1) * 8192 + (pt >> 1) * 8), "s"(rs), "s"(i * 1024) : "memory");
      wait_vm<19>(); bar();
      pa(3, 0); q(3, 0); wait_vm<19>(); bar();
      pa(4, 1); q(4, 1); wait_vm<11>();
      asm volatile("" : "+v"(sv[0]), "+v"(sv[1]), "+v"(sv[2]), "+v"(sv[3]), "+v"(sv[4]), "+v"(sv[5]), "+v"(sv[6]), "+v"(sv[7]), "+v"(bv)::"memory");
      asm volatile("" : "+v"(cv[0]), "+v"(cv[1]), "+v"(cv[2]), "+v"(cv[3]), "+v"(cv[4]), "+v"(cv[5]), "+v"(cv[6]), "+v"(cv[7]), "+v"(cv[8]),
                   "+v"(cv[9]), "+v"(cv[10]), "+v"(cv[11]), "+v"(cv[12]), "+v"(cv[13]), "+v"(cv[14]), "+v"(cv[15])::"memory");
      bar();
      pa(5, 2); q(5, 2);
      {
        float s = 0.f, a = 0.f, qq = 0.f;
#pragma unroll
        for (int i = 0; i < 16; i++) s += __uint_as_float(cv[i]);
        if (pt < 224) { tabC[pt] = s; tabBias[pt] = __uint_as_float(bv << 16); }
#pragma unroll
        for (int i = 0; i < 8; i++) { a += __uint_as_float(sv[i][0]); qq += __uint_as_float(sv[i][1]); }
        a += __builtin_bit_cast(float, __builtin_amdgcn_update_dpp(0, __builtin_bit_cast(int, a), 0xB1, 0xF, 0xF, true));
        qq += __builtin_bit_cast(float, __builtin_amdgcn_update_dpp(0, __builtin_bit_cast(int, qq), 0xB1, 0xF, 0xF, true));
        const float mean = a * (1.0f / D_), var = fmaxf(qq * (1.0f / D_) - mean * mean, 0.f);
        const float rstd = __builtin_amdgcn_rsqf(var + LN_EPS);
        tabA[(pt & 1) * 128 + (pt >> 1)] = (pt & 1) ? -mean * rstd : rstd;
      }
      wait_vm<11>(); wait_lgkm0(); bar();
      int sl = 0;
#pragma unroll 1
      for (int g = 6; g < 16; g++) {
        pa(g, sl); q(g, sl);
        const int gp = 6 + (loc & 7);
        if (g == gp) pull_lines(lds0 + COFF + 3584, vw, rw, 0);
        if ((unsigned)(g - gp) < 2u) wait_vm<12>(); else wait_vm<11>();
        bar();
        sl = sl == 2 ? 0 : sl + 1;
      }
      g4p(0); wait_vm<4>(); bar();
      g4p(1);
      bar();
    }
    {
      rsrc_t rq = mk_rsrc(ws + WS_R + (size_t)tt * 112 * 8192, 112 * 8192);
      const u32 vq0 = (4 * (p & 1) + 2 * half) * 1024 + lane * 16, vq1 = (4 * (p & 1) + 2 - 2 * half) * 1024 + lane * 16;
      auto g4q = [&](int q) {
        const u32 m = ldsb + QOFF + ((q + 2) & 3) * SLOT, so = g4blk(q) * 8192;
        dma_sc1<2>(m, vq0, rq, so);
        dma_sc1<2>(m + 2048, vq1, rq, so);
      };
      auto halfs = [&](int g) {
        const int k = kstep2(g, 8, phi, xcd & 1), blk = 7 * (2 * k + (p >> 1)) + 3;
        dma<4>(ldsb + ((g + 2) & 3) * SLOT, vp4, rp4, blk * (32 * D_ * 2));
        dma_sc1<2>(ldsb + QOFF + ((g + 2) & 3) * SLOT, vq0, rq, blk * 8192);
      };
      wait_vm<4>(); bar();
      g4p(2); wait_vm<4>(); bar();
      g4p(3); wait_vm<4>(); bar();
      {
        const int qq = 4 + min(lane >> 3, 2);
        const u32 b4 = g4blk(4), b5 = g4blk(5), b6 = g4blk(6), bb = qq == 4 ? b4 : qq == 5 ? b5 : b6;
        pull_lines(lds0 + COFF + 3584, (p & 1) == 0 && lane < 24 ? (bb * 32 + 4 * li + ((lane >> 1) & 3)) * (D_ * 2) + (lane & 1) * 128 : NOWHERE, rp4, 0);
      }
      bar();
      if (p == 0) {
        count_up(ws, rowcnt + CN_R);
        fv = 0;
        watch(rall, rowcnt + CN_R, fv, [&](u32 w) { return w >= 16u; }, NOWHERE, tick);
      }
      bar();
      g4q(3); g4p(4); g4q(4); g4p(5); g4q(5);
      wait_vm<16>(); bar();
#pragma unroll 1
      for (int q = 6; q < 24; q++) { g4p(q); g4q(q); wait_vm<16>(); bar(); }
      halfs(0); wait_vm<14>(); bar();
#pragma unroll 1
      for (int g = 1; g < 8; g++) { halfs(g); wait_vm<12>(); bar(); }
      wait_vm<6>(); bar();
      wait_vm<0>(); bar();
    }
  } else {
    const int lane = lane0;
    const int wP = wid >> 1, wQ = wid & 1, c = lane & 15, g = lane >> 4;
    const int m0 = 4 * wQ + 2 * half, cell = ft * 4 + wid;
    {
      Consumer<4, M_ROW, M_ROW> C;
      C.init(sm, wP, wQ, lane, wP);
      f32x4 acc[4][4], sd[2];
      C.zero(acc);
#pragma unroll
      for (int i = 0; i < 2; i++) sd[i] = (f32x4){0.f, 0.f, 0.f, 0.f};
      const u32x4 ones4 = {0x3F803F80u, 0x3F803F80u, 0x3F803F80u, 0x3F803F80u};
      bf16x8 ones = __builtin_bit_cast(bf16x8, ones4);
      asm volatile("" : "+v"(ones));
      bar();
      C.first();
#define G1_HALF0(S) C.load<S, 1, 4>(); C.mma<0, 4>(acc); C.qsums<0>(sd, ones); C.interleave<18, 4>();
#define G1_STEP(S) G1_HALF0(S) wait_lgkm0(); bar(); __builtin_amdgcn_sched_barrier(0); \
      C.load<(S + 1) % NST, 0, 4>(); C.mma<1, 4>(acc); C.qsums<1>(sd, ones); C.interleave<18, 4>();
      for (int it = 0; it < 3; it++) { G1_STEP(0) G1_STEP(1) G1_STEP(2) G1_STEP(3) }
      G1_STEP(0) G1_STEP(1) G1_STEP(2) G1_HALF0(3)
      wait_lgkm0();
      C.qsums<1>(sd, ones);
      __builtin_amdgcn_sched_barrier(0);
      C.mma<1, 4>(acc);
      tabC[g < 2 ? 64 * wQ + 32 * wP + 16 * g + c : 128 + lane] = g ? sd[1][0] : sd[0][0];
      __builtin_amdgcn_sched_group_barrier(0x8, 4, 0);
      __builtin_amdgcn_sched_group_barrier(0x2 | 0x200, 8, 0);
      __builtin_amdgcn_sched_group_barrier(0x8, 12, 0);
      __builtin_amdgcn_sched_barrier(0);
      wait_lgkm0(); bar();
      rsrc_t ro = mk_rsrc(ws + WS_AB + ((size_t)((half * 8 + et) * 32 + 2 * tt + wP) * 16 + 4 * wQ) * 1024, 12288);
      f32x4 ra[4], rb[4];
      float rs[4];
#pragma unroll
      for (int i = 0; i < 4; i++) {
        ra[i] = *(const LAS f32x4*)(tabA + 64 * wP + 16 * i + 4 * g);
        rb[i] = *(const LAS f32x4*)(tabB + 64 * wP + 16 * i + 4 * g);
        rs[i] = tabC[64 * wQ + 16 * (i ^ (2 * wP)) + c];
      }
#pragma unroll
      for (int j = 0; j < 4; j++)
#pragma unroll
        for (int u = 0; u < 2; u++) {
          const f32x4 a = ra[2 * u] * acc[2 * u][j] + rb[2 * u] * rs[j], b = ra[2 * u + 1] * acc[2 * u + 1][j] + rb[2 * u + 1] * rs[j];
          const u32x4 o = {cvtpk(a[0], a[1]), cvtpk(a[2], a[3]), cvtpk(b[0], b[1]), cvtpk(b[2], b[3])};
          const int at = (u * 8 + (j ^ (2 * wP))) * 1024 + lane * 16;
          store_sc1(ro, at, o);
          *(LAS u32x4*)(sm + (wP * SLOT + wQ * 4096 + at)) = o;
        }
    }
    u32x4 hk[2][2];
    {
      Consumer<4, M_TILED, M_ROW> C;
      C.init(sm, wP, wQ, lane);
      f32x4 acc[4][4];
      wait_lgkm0(); bar();
      C.first();
      C.load<0, 1, 4>(); C.mma<0, 4, true>(acc); C.interleave<16, 4>();
      wait_lgkm0(); bar();
      __builtin_amdgcn_sched_barrier(0);
      C.load<1, 0, 4>(); C.mma<1, 4>(acc); C.interleave<16, 4>();
      C.load<1, 1, 4>(); C.mma<0, 4>(acc); C.interleave<16, 4>();
      wait_lgkm0();
      C.mma<1, 4>(acc);
      __builtin_amdgcn_sched_barrier(0);
      wait_vm<0>();
      bar();
      bar();
      bar();
      C.load<2, 0, 4>();
      C.step<2, 4, 4>(acc); C.step<3, 4, 4>(acc);
      for (int it = 0; it < 5; it++) C.step4<4, 4>(acc);
      C.step4<4, 2>(acc);
      xch_send(ws + WS_X2, acc, wid, lane);
      __builtin_amdgcn_sched_barrier(0);
      rsrc_t rx = mk_rsrc(ws + WS_X2 + ((size_t)(blockIdx.x ^ 4) * 4 + wid) * 4096, 4096);
      rsrc_t rf = mk_rsrc(ws + WS_FL + ((size_t)(FL_X2 * 16 + tt) * 64 + (cell ^ 32)) * 64, 64);
      u32x4 rv[4];
      u32 fv;
      C.step<0, 2, 2>(acc); C.step<1, 2, 2>(acc); C.step<2, 2, 2>(acc);
      flag_set(ws, FL_X2, tt, cell);
      C.step<3, 2, 2>(acc);
      C.step<0, 2, 2>(acc); C.step<1, 2, 2>(acc);
      flag_request(rf, 0, fv);
      __builtin_amdgcn_sched_barrier(0);
      C.step<2, 2, 2>(acc);
      wait_vm<0>();
      flag_wait(rf, 0, fv);
      xch_request(rx, rv, lane);
      __builtin_amdgcn_sched_barrier(0);
      C.load<3, 1, 2>(); C.mma<0, 2>(acc); C.interleave<8, 2>();
      C.mma<1, 2>(acc);
      xch_finish(rv);
      rsrc_t rh = mk_rsrc(ws + WS_H + ((size_t)(tt * 32 + 4 * et + 2 * wP) * 8 + m0) * 1024, 16384);
      rsrc_t rs = mk_rsrc(ws + WS_ST + ((size_t)(tt * 16 + 2 * et + wP) * 128 + 16 * m0) * 8, 256);
#pragma unroll
      for (int jj = 0; jj < 2; jj++) {
        float s = 0.f, q = 0.f;
#pragma unroll
        for (int u = 0; u < 2; u++) {
          const u32x4 o = rv[jj * 2 + u];
          const f32x4 a = acc[2 * u][jj], b = acc[2 * u + 1][jj];
          const int xrow = 32 * wQ + 16 * jj + c;
          const LAS char* xp = sm + (XOFF + wP * 8192 + xrow * 128 + (g & 1) * 8);
          const u32x2 xa = *(const LAS u32x2*)(xp + (((4 * u + (g >> 1)) ^ ((xrow >> 1) & 7)) * 16));
          const u32x2 xb = *(const LAS u32x2*)(xp + (((4 * u + 2 + (g >> 1)) ^ ((xrow >> 1) & 7)) * 16));
          u32x4 h;
          h[0] = cvtpk(a[0] + blo(o[0]) + blo(xa[0]), a[1] + bhi(o[0]) + bhi(xa[0]));
          h[1] = cvtpk(a[2] + blo(o[1]) + blo(xa[1]), a[3] + bhi(o[1]) + bhi(xa[1]));
          h[2] = cvtpk(b[0] + blo(o[2]) + blo(xb[0]), b[1] + bhi(o[2]) + bhi(xb[0]));
          h[3] = cvtpk(b[2] + blo(o[3]) + blo(xb[1]), b[3] + bhi(o[3]) + bhi(xb[1]));
          store_sc1(rh, (u * 8 + jj) * 1024 + lane * 16, h);
          hk[jj][u] = h;
#pragma unroll
          for (int e = 0; e < 4; e++) { const float lo = blo(h[e]), hi = bhi(h[e]); s += lo + hi; q += lo * lo + hi * hi; }
        }
        s = xsum32(xsum16(s)); q = xsum32(xsum16(q));
        if (g == 0) __builtin_amdgcn_raw_buffer_store_b64((u32x2){__float_as_uint(s), __float_as_uint(q)}, rs, (16 * jj + c) * 8, 0, 16);
      }
      wait_vm<0>();
      bar();
#pragma unroll
      for (int q = 0; q < 4; q++) *(LAS u32x4*)(sm + XOFF + wid * 4096 + q * 1024 + lane * 16) = hk[q >> 1][q & 1];
      bar();
    }
    rsrc_t rr = mk_rsrc(ws + WS_R + ((size_t)(tt * 112 + 7 * ft) * 8 + 4 * wQ) * 1024, 7 * 8192);
    u32x4 ro[14];
    {
      Consumer<7, M_TR448, M_TILED> C;
      C.init(sm, wP, wQ, lane);
      f32x4 acc[7][4];
      C.zero(acc);
      bar();
      C.first();
      for (int it = 0; it < 5; it++) { C.step<0, 4, 4>(acc); C.step<1, 4, 4>(acc); C.step<2, 4, 4>(acc); }
      C.last<0, 4>(acc);
      bar();
      float ra[4], rb[4];
#pragma unroll
      for (int j = 0; j < 4; j++) { ra[j] = tabA[64 * wQ + 16 * j + c]; rb[j] = tabB[64 * wQ + 16 * j + c]; }
      f32x4 cs[7], bi[7];
#pragma unroll
      for (int i = 0; i < 7; i++) {
        const int col = (i < 6 ? 128 * wP + 16 * i : 96 + 16 * wP) + 4 * g;
        cs[i] = *(const LAS f32x4*)(tabC + col); bi[i] = *(const LAS f32x4*)(tabBias + col);
      }
      auto act = [&](int i, int j) {
        f32x4 v = ra[j] * acc[i][j] + (rb[j] * cs[i] + bi[i]);
#pragma unroll
        for (int e = 0; e < 4; e++) v[e] = fmaxf(v[e], 0.f);
        v = v * v;
        return (u32x2){cvtpk(v[0], v[1]), cvtpk(v[2], v[3])};
      };
#pragma unroll
      for (int u = 0; u < 3; u++) {
        const int n = 3 * wP + u;
        LAS char* own = sm + (QOFF + (((n >> 1) + 2) & 3) * SLOT + (n & 1) * 8192 + 4 * wQ * 1024 + lane * 16);
#pragma unroll
        for (int j = 0; j < 4; j++) {
          const u32x2 a = act(2 * u, j), b = act(2 * u + 1, j);
          ro[4 * u + j] = (u32x4){a[0], a[1], b[0], b[1]};
          if (4 * u + j < EARLY) store_sc1(rr, ((4 * wP + u) * 8 + j) * 1024 + lane * 16, ro[4 * u + j]);
          *(LAS u32x4*)(own + (j ^ (2 * half)) * 1024) = ro[4 * u + j];
        }
      }
#pragma unroll
      for (int j = 0; j < 4; j++)
        *(LAS u32x2*)(sm + (QOFF + SLOT + (4 * wQ + j) * 1024 + lane * 16 + 8 * wP)) = act(6, j);
    }
    {
      Consumer<4, M_TR256, M_TILED> C;
      C.init(sm, wP, wQ, lane);
      f32x4 acc[4][4];
      wait_lgkm0(); bar();
      C.load<2, 0, 4>();
#pragma unroll
      for (int j = 0; j < 2; j++) ro[12 + j] = *(const LAS u32x4*)(sm + (QOFF + SLOT + (4 * wQ + 2 * wP + j) * 1024 + lane * 16));
      auto put = [&](int hs, int n) {
        constexpr int LEFT = 14 - EARLY;
        for (int q = 0; q < LEFT; q++)
          if (q * 88 / LEFT == hs * 16 + n) {
            const int t = EARLY + q;
            store_sc1(rr, (t < 12 ? (4 * wP + (t >> 2)) * 8 + (t & 3) : 24 + 2 * wP + (t & 1)) * 1024 + lane * 16, ro[t]);
          }
      };
      C.half_with<2, 1, true>(acc, [&](int n) { put(0, n); });
      wait_lgkm0(); bar();
      C.half_with<3, 0, false>(acc, [&](int n) { put(1, n); });
      C.half_with<3, 1, false>(acc, [&](int n) { put(2, n); });
      wait_lgkm0(); bar();
      C.half_with<0, 0, false>(acc, [&](int n) { put(3, n); });
      C.half_with<0, 1, false>(acc, [&](int n) { put(4, n); });
      wait_lgkm0();
      C.half_with<-1, 0, false>(acc, [&](int n) { put(5, n); });
      __builtin_amdgcn_sched_barrier(0);
      wait_vm<0>();
      bar();
      bar();
      bar();
      C.load<1, 0, 4>();
      C.step<1, 4, 4>(acc); C.step<2, 4, 4>(acc); C.step<3, 4, 4>(acc);
      for (int it = 0; it < 4; it++) C.step4<4, 4>(acc);
      C.step<0, 4, 4>(acc); C.step<1, 4, 2>(acc);
      xch_send(ws + WS_X4, acc, wid, lane);
      u32x2 br[4];
#pragma unroll
      for (int i = 0; i < 4; i++) br[i] = *(const u32x2*)(b2 + et * 128 + 64 * wP + 16 * i + 4 * g);
      __builtin_amdgcn_sched_barrier(0);
      rsrc_t rx = mk_rsrc(ws + WS_X4 + ((size_t)(blockIdx.x ^ 4) * 4 + wid) * 4096, 4096);
      rsrc_t rf = mk_rsrc(ws + WS_FL + ((size_t)(FL_X4 * 16 + tt) * 64 + (cell ^ 32)) * 64, 64);
      u32x4 rv[4];
      u32 fv;
      C.step<2, 2, 2>(acc); C.step<3, 2, 2>(acc); C.step<0, 2, 2>(acc);
      flag_set(ws, FL_X4, tt, cell);
      C.step<1, 2, 2>(acc); C.step<2, 2, 2>(acc); C.step<3, 2, 2>(acc);
      flag_request(rf, 0, fv);
      __builtin_amdgcn_sched_barrier(0);
      C.step<0, 2, 2>(acc);
      wait_vm<0>();
      flag_wait(rf, 0, fv);
      xch_request(rx, rv, lane);
      __builtin_amdgcn_sched_barrier(0);
      C.last<1, 2>(acc);
      xch_finish(rv);
      rsrc_t ro = mk_rsrc(out + (size_t)(tt * 128 + 16 * m0) * D_ + et * 128 + 64 * wP, 31 * D_ * 2 + 128);
#pragma unroll
      for (int jj = 0; jj < 2; jj++)
#pragma unroll
        for (int u = 0; u < 2; u++) {
          const u32x4 o = rv[jj * 2 + u], h = *(const LAS u32x4*)(sm + XOFF + wid * 4096 + (jj * 2 + u) * 1024 + lane * 16);
          const f32x4 a = acc[2 * u][jj], b = acc[2 * u + 1][jj];
          const u32x2 ba = br[2 * u], bb = br[2 * u + 1];
          const u32 a0 = cvtpk(a[0] + blo(o[0]) + blo(ba[0]) + blo(h[0]), a[1] + bhi(o[0]) + bhi(ba[0]) + bhi(h[0]));
          const u32 a1 = cvtpk(a[2] + blo(o[1]) + blo(ba[1]) + blo(h[1]), a[3] + bhi(o[1]) + bhi(ba[1]) + bhi(h[1]));
          const u32 b0 = cvtpk(b[0] + blo(o[2]) + blo(bb[0]) + blo(h[2]), b[1] + bhi(o[2]) + bhi(bb[0]) + bhi(h[2]));
          const u32 b1_ = cvtpk(b[2] + blo(o[3]) + blo(bb[1]) + blo(h[3]), b[3] + bhi(o[3]) + bhi(bb[1]) + bhi(h[3]));
          const auto s0 = __builtin_amdgcn_permlane16_swap(a0, b0, false, false);
          const auto s1 = __builtin_amdgcn_permlane16_swap(a1, b1_, false, false);
          __builtin_amdgcn_raw_buffer_store_b128((u32x4){s0[0], s1[0], s0[1], s1[1]}, ro,
                                                 (16 * jj + c) * (D_ * 2) + (32 * u + 16 * (g & 1) + 8 * (g >> 1)) * 2, 0, 16);
        }
    }
  }
}

}

extern "C" void kernel_launch(void* const* d_in, const int* in_sizes, int n_in, void* d_out, int out_size, void* d_ws, size_t ws_size,
                              hipStream_t stream) {
  const u16* const* I = (const u16* const*)d_in;
  (void)hipFuncSetAttribute((const void*)fused_kernel, hipFuncAttributeMaxDynamicSharedMemorySize, LDS_BYTES);
  fused_kernel<<<256, 512, LDS_BYTES, stream>>>(I[0], I[1], I[2], I[3], I[4], I[6], I[7], I[8], I[9], (char*)d_ws, (u16*)d_out);
}
```

```cpp
#include <hip/hip_runtime.h>
#include <hip/hip_bf16.h>
#include <type_traits>

typedef unsigned short u16;
typedef unsigned int u32;
typedef unsigned long long u64;
typedef __bf16 bf16x8 __attribute__((ext_vector_type(8)));
typedef __bf16 bf16x2 __attribute__((ext_vector_type(2)));
typedef short s16x4 __attribute__((ext_vector_type(4)));
typedef short s16x8 __attribute__((ext_vector_type(8)));
typedef float f32x2 __attribute__((ext_vector_type(2)));
typedef float f32x4 __attribute__((ext_vector_type(4)));
typedef u32 u32x2 __attribute__((ext_vector_type(2)));
typedef u32 u32x4 __attribute__((ext_vector_type(4)));
typedef u32 u32x16 __attribute__((ext_vector_type(16)));
typedef __amdgpu_buffer_rsrc_t rsrc_t;
#define GAS __attribute__((address_space(1)))
#define LAS __attribute__((address_space(3)))
#define DI __device__ __forceinline__

#define K_WN 8
#define K_SLEEP 1
#define K_SPIN (1 << 18)
#define K_GAPS(NM, NR) ((NR) < (NM) ? (NR) : (NM))
#define K_PPRIO 0
#define K_CSP 0
#define K_WB 1
#define K_REV 1
#define K_LEAD 3
#define K_NS1 6
#define K_SPR 88
#define K_RB 3
#define K_ZP 1
#define K_G4X 0
#define K_XF 3
namespace {

constexpr int S_ = 2048, D_ = 1024, F_ = 3584;
constexpr float LN_EPS = 1e-5f;

constexpr size_t WS_AB = 0;
constexpr size_t WS_H = 8u << 20;
constexpr size_t WS_R = 12u << 20;
constexpr size_t WS_X2 = 27u << 20;
constexpr size_t WS_X4 = 31u << 20;
constexpr size_t WS_ST = 35u << 20;
constexpr size_t WS_CS = 36u << 20;
constexpr size_t WS_SX = 38u << 20;
constexpr size_t WS_FL = 37u << 20;

#define DIAG_ARG
#define DIAG_BEGIN
#define STAMP(i)
#define PSTAMP(i)
#define PDIAG_END
#define DIAG_END(i)

DI u32 rfl(u32 x) { return __builtin_amdgcn_readfirstlane(x); }
DI u32 cvtpk(float lo, float hi) { f32x2 v = {lo, hi}; return __builtin_bit_cast(u32, __builtin_convertvector(v, bf16x2)); }
DI float blo(u32 v) { return __uint_as_float(v << 16); }
DI float bhi(u32 v) { return __uint_as_float(v & 0xFFFF0000u); }
DI rsrc_t mk_rsrc(const void* p, u32 bytes) { return __builtin_amdgcn_make_buffer_rsrc((void*)p, 0, bytes, 0x00020000); }
DI void bar() { asm volatile("s_barrier" ::: "memory"); }
DI void wait_lgkm0() { __builtin_amdgcn_s_waitcnt(0xC07F); }
template <int N> DI void wait_vm() { asm volatile("s_waitcnt vmcnt(%0)" ::"i"(N) : "memory"); }

template <int N> DI void dma(u32 m0, const u32* v, rsrc_t r, u32 so) {
  if constexpr (N == 4)
    asm volatile("s_mov_b32 m0, %0\n\ts_nop 3\n\t"
                 "buffer_load_dwordx4 %1, %5, %6 offen lds\n\t"
                 "buffer_load_dwordx4 %2, %5, %6 offen offset:1024 lds\n\t"
                 "buffer_load_dwordx4 %3, %5, %6 offen offset:2048 lds\n\t"
                 "buffer_load_dwordx4 %4, %5, %6 offen offset:3072 lds"
                 ::"s"(m0), "v"(v[0]), "v"(v[1]), "v"(v[2]), "v"(v[3]), "s"(r), "s"(so) : "memory");
  else if constexpr (N == 3)
    asm volatile("s_mov_b32 m0, %0\n\ts_nop 3\n\t"
                 "buffer_load_dwordx4 %1, %4, %5 offen lds\n\t"
                 "buffer_load_dwordx4 %2, %4, %5 offen offset:1024 lds\n\t"
                 "buffer_load_dwordx4 %3, %4, %5 offen offset:2048 lds"
                 ::"s"(m0), "v"(v[0]), "v"(v[1]), "v"(v[2]), "s"(r), "s"(so) : "memory");
  else
    asm volatile("s_mov_b32 m0, %0\n\ts_nop 3\n\t"
                 "buffer_load_dwordx4 %1, %3, %4 offen lds\n\t"
                 "buffer_load_dwordx4 %2, %3, %4 offen offset:1024 lds"
                 ::"s"(m0), "v"(v[0]), "v"(v[1]), "s"(r), "s"(so) : "memory");
}
template <int N> DI void dma_sc1(u32 m0, u32 v, rsrc_t r, u32 so) {
  if constexpr (N == 4)
    asm volatile("s_mov_b32 m0, %0\n\ts_nop 3\n\t"
                 "buffer_load_dwordx4 %1, %2, %3 offen sc1 lds\n\t"
                 "buffer_load_dwordx4 %1, %2, %3 offen offset:1024 sc1 lds\n\t"
                 "buffer_load_dwordx4 %1, %2, %3 offen offset:2048 sc1 lds\n\t"
                 "buffer_load_dwordx4 %1, %2, %3 offen offset:3072 sc1 lds"
                 ::"s"(m0), "v"(v), "s"(r), "s"(so) : "memory");
  else
    asm volatile("s_mov_b32 m0, %0\n\ts_nop 3\n\t"
                 "buffer_load_dwordx4 %1, %2, %3 offen sc1 lds\n\t"
                 "buffer_load_dwordx4 %1, %2, %3 offen offset:1024 sc1 lds"
                 ::"s"(m0), "v"(v), "s"(r), "s"(so) : "memory");
}

DI float xsum16(float v) { const auto r = __builtin_amdgcn_permlane16_swap(__float_as_uint(v), __float_as_uint(v), false, false); return __uint_as_float(r[0]) + __uint_as_float(r[1]); }
DI float xsum32(float v) { const auto r = __builtin_amdgcn_permlane32_swap(__float_as_uint(v), __float_as_uint(v), false, false); return __uint_as_float(r[0]) + __uint_as_float(r[1]); }

template <int A, int B, class F> DI void static_for(F f) {
  if constexpr (A < B) { f(std::integral_constant<int, A>{}); static_for<A + 1, B>(f); }
}

DI void pull_lines(u32 m0, u32 v, rsrc_t r, u32 so) {
  asm volatile("s_mov_b32 m0, %0\n\ts_nop 3\n\tbuffer_load_dword %1, %2, %3 offen lds" ::"s"(m0), "v"(v), "s"(r), "s"(so) : "memory");
}

DI int kstep(int t, int n, int phi) {
  int k = t;
  if ((t & 3) == phi) { k = t + 4; if (k >= n) k = phi; }
  return k;
}

DI int kstep2(int t, int n, int phi, int side) {
  const int k = kstep(t, n, phi), k2 = k ^ (side << 2);
  return k2 < n ? k2 : k;
}

constexpr int NST = 4;
constexpr int SLOT = 16384;
constexpr int QOFF = NST * SLOT;
constexpr int P3 = 64 * 448;
constexpr int Q3OFF = 3 * P3;
constexpr int TOFF = Q3OFF + 3 * SLOT;
constexpr int COFF = TOFF + 4096;
constexpr int XOFF = COFF + 4096;
constexpr int LDS_BYTES = XOFF + 16384;

enum { M_ROW = 0, M_TILED = 1, M_TR448 = 2, M_TR256 = 3 };

template <int PT, int PMODE, int QMODE>
struct Consumer {
  static constexpr int NS = PMODE == M_TR448 ? 3 : NST, QB = PMODE == M_TR448 ? Q3OFF : QOFF;
  const LAS char* pb[6];
  const LAS char* qb[4];
  bf16x8 fp[2][PT], fq[2][4];

  DI void init(const LAS char* sm, int wP, int wQ, int lane, int prot = 0, int qrot = 0) {
    const int r = lane & 15, g = lane >> 4;
    if constexpr (PMODE == M_ROW) {
      const int a = (64 * wP + r) * 128 + ((g ^ (r >> 1)) * 16);
      pb[0] = sm + (a + prot * 4096); pb[1] = sm + ((a ^ 64) + prot * 4096);
      pb[2] = sm + (a + 4096 - prot * 4096); pb[3] = sm + ((a ^ 64) + 4096 - prot * 4096);
    } else if constexpr (PMODE == M_TILED) {
      pb[0] = sm + (4 * wP * 1024 + lane * 16);
    } else if constexpr (PMODE == M_TR448) {
      const int b = g & 1, a = (4 * g + (r >> 2)) * 448 + (r & 3) * 8;
      pb[0] = sm + (a + (8 * wP + b) * 32);
      pb[1] = sm + (a + (8 * wP - b) * 32);
      pb[2] = sm + (a + ((6 + wP) ^ b) * 32);
#pragma unroll
      for (int i = 0; i < 3; i++) { pb[3 + i] = pb[i] + 2 * P3; asm volatile("" : "+v"(pb[3 + i])); }
    } else {
      const int k = 4 * g + (r >> 2), a = k * 256 + (r & 3) * 8;
#pragma unroll
      for (int i = 0; i < 4; i++) pb[i] = sm + (a + (((4 * wP + i) ^ (k & 7)) * 32));
    }
    if constexpr (QMODE == M_ROW) {
      const int a = QOFF + (64 * wQ + r) * 128 + ((g ^ (r >> 1)) * 16);
      qb[0] = sm + (a + qrot * 4096); qb[1] = sm + ((a ^ 64) + qrot * 4096);
      qb[2] = sm + (a + 4096 - qrot * 4096); qb[3] = sm + ((a ^ 64) + 4096 - qrot * 4096);
      asm volatile("" : "+v"(qb[0]), "+v"(qb[1]), "+v"(qb[2]), "+v"(qb[3]));
    } else {
      qb[0] = sm + (QB + 4 * wQ * 1024 + lane * 16);
      asm volatile("" : "+v"(qb[0]));
    }
  }
  static DI bf16x8 tr2(const LAS char* p, int second) {
    s16x4 a = __builtin_amdgcn_ds_read_tr16_b64_v4i16((LAS s16x4*)p);
    s16x4 b = __builtin_amdgcn_ds_read_tr16_b64_v4i16((LAS s16x4*)(p + second));
    return __builtin_bit_cast(bf16x8, __builtin_shufflevector(a, b, 0, 1, 2, 3, 4, 5, 6, 7));
  }
  template <int S, int KS> DI bf16x8 ldp(int i) {
    if constexpr (PMODE == M_ROW) return *(const LAS bf16x8*)(pb[KS + (i & 2)] + (S * SLOT + (i & 1) * 2048));
    else if constexpr (PMODE == M_TILED) return *(const LAS bf16x8*)(pb[0] + (S * SLOT + KS * 8192 + i * 1024));
    else if constexpr (PMODE == M_TR448)
      return tr2(pb[(S == 2 ? 3 : 0) + (i == 6 ? 2 : i & 1)] + ((S & 1) * P3 + KS * 32 * 448 + (i == 6 ? 0 : i * 32)), 16 * 448);
    else return tr2(pb[i] + (S * SLOT + KS * 32 * 256), 16 * 256);
  }
  template <int S, int KS> DI bf16x8 ldq(int j) {
    if constexpr (QMODE == M_ROW) return *(const LAS bf16x8*)(qb[KS + (j & 2)] + (S * SLOT + (j & 1) * 2048));
    else return *(const LAS bf16x8*)(qb[0] + (S * SLOT + KS * 8192 + j * 1024));
  }
  template <int S, int KS, int QN> DI void load() {
    fq[KS][0] = ldq<S, KS>(0);
#pragma unroll
    for (int i = 0; i < PT; i++) fp[KS][i] = ldp<S, KS>(i);
#pragma unroll
    for (int j = 1; j < QN; j++) fq[KS][j] = ldq<S, KS>(j);
  }
  template <int KS, int QN, bool ZERO = false> DI void mma(f32x4 (&acc)[PT][4]) {
#pragma unroll
    for (int n = 0; n < PT * QN; n++) {
      const int j = n / PT, i = (j & 1) ? PT - 1 - n % PT : n % PT;
      acc[i][j] = __builtin_amdgcn_mfma_f32_16x16x32_bf16(fp[KS][i], fq[KS][j], ZERO ? (f32x4){0.f, 0.f, 0.f, 0.f} : acc[i][j], 0, 0, 0);
    }
  }
  template <int KS> DI void qsums(f32x4 (&sd)[2], bf16x8 ones) {
#pragma unroll
    for (int i = 0; i < 2; i++) sd[i] = __builtin_amdgcn_mfma_f32_16x16x32_bf16(ones, fq[KS][i], sd[i], 0, 0, 0);
  }
  template <int NM, int QN> static DI void interleave() {
    constexpr int NR = PT * (PMODE >= M_TR448 ? 2 : 1) + QN, G = K_GAPS(NM, NR);
    static_for<0, G>([](auto ic) {
      __builtin_amdgcn_sched_group_barrier(0x8, 1, 0);
      __builtin_amdgcn_sched_group_barrier(0x100, NR / G + (ic.value < NR % G ? 1 : 0), 0);
    });
    __builtin_amdgcn_sched_group_barrier(0x8, NM - G, 0);
    __builtin_amdgcn_sched_barrier(0);
  }
  template <int S, int QC, int QN> DI void step(f32x4 (&acc)[PT][4]) {
    load<S, 1, QC>(); mma<0, QC>(acc); interleave<PT * QC, QC>();
    wait_lgkm0(); bar();
    __builtin_amdgcn_sched_barrier(0);
    load<(S + 1) % NS, 0, QN>(); mma<1, QC>(acc); interleave<PT * QC, QN>();
  }
  template <int S, int LS, bool ZERO, class W> DI void half_with(f32x4 (&acc)[PT][4], W w) {
    static_for<0, PT * 4>([&](auto nc) {
      constexpr int n = nc.value, j = n / PT, i = (j & 1) ? PT - 1 - n % PT : n % PT;
      acc[i][j] = __builtin_amdgcn_mfma_f32_16x16x32_bf16(fp[1 - LS][i], fq[1 - LS][j], ZERO ? (f32x4){0.f, 0.f, 0.f, 0.f} : acc[i][j], 0, 0, 0);
      if constexpr (S >= 0) {
        if constexpr (n == 0) fq[LS][0] = ldq<S, LS>(0);
        else if constexpr (n <= PT) fp[LS][n - 1] = ldp<S, LS>(n - 1);
        else if constexpr (n < PT + 4) fq[LS][n - PT] = ldq<S, LS>(n - PT);
      }
      w(nc);
      __builtin_amdgcn_sched_barrier(0);
    });
  }
  template <int S, int QC> DI void last(f32x4 (&acc)[PT][4]) {
    load<S, 1, QC>(); mma<0, QC>(acc); interleave<PT * QC, QC>();
    wait_lgkm0();
    mma<1, QC>(acc);
  }
  template <int QC, int QN> DI void step4(f32x4 (&acc)[PT][4]) {
    step<0, QC, QC>(acc); step<1, QC, QC>(acc); step<2, QC, QC>(acc); step<3, QC, QN>(acc);
  }
  static DI void zero(f32x4 (&acc)[PT][4]) {
#pragma unroll
    for (int i = 0; i < PT; i++)
#pragma unroll
      for (int j = 0; j < 4; j++) { acc[i][j] = (f32x4){0.f, 0.f, 0.f, 0.f}; asm volatile("" : "+v"(acc[i][j])); }
  }
  DI void first() { load<0, 0, 4>(); }
};

DI u32 row_src(int imgrow, int srcrow, int lane, int ldbytes) {
  return srcrow * ldbytes + (((lane & 7) ^ ((imgrow >> 1) & 7)) * 16);
}
DI void store_sc1(rsrc_t r, u32 off, u32x4 v) { __builtin_amdgcn_raw_buffer_store_b128(v, r, off, 0, 16); }

DI void xch_send(char* x, const f32x4 (&acc)[4][4], int wid, int lane) {
  rsrc_t r = mk_rsrc(x + ((size_t)blockIdx.x * 4 + wid) * 4096, 4096);
#pragma unroll
  for (int jj = 0; jj < 2; jj++)
#pragma unroll
    for (int u = 0; u < 2; u++) {
      const f32x4 a = acc[2 * u][2 + jj], b = acc[2 * u + 1][2 + jj];
      u32x4 v = {cvtpk(a[0], a[1]), cvtpk(a[2], a[3]), cvtpk(b[0], b[1]), cvtpk(b[2], b[3])};
      store_sc1(r, lane * 16 + (jj * 2 + u) * 1024, v);
    }
}
DI void xch_request(rsrc_t r, u32x4 (&v)[4], int lane) {
  asm volatile("s_nop 4\n\tbuffer_load_dwordx4 %0, %4, %5, 0 offen sc1\n\t"
               "buffer_load_dwordx4 %1, %4, %5, 0 offen offset:1024 sc1\n\t"
               "buffer_load_dwordx4 %2, %4, %5, 0 offen offset:2048 sc1\n\t"
               "buffer_load_dwordx4 %3, %4, %5, 0 offen offset:3072 sc1"
               : "=&v"(v[0]), "=&v"(v[1]), "=&v"(v[2]), "=&v"(v[3]) : "v"(lane * 16), "s"(r) : "memory");
}
DI void xch_finish(u32x4 (&v)[4]) { asm volatile("s_waitcnt vmcnt(0)" : "+v"(v[0]), "+v"(v[1]), "+v"(v[2]), "+v"(v[3])::"memory"); }

enum { FL_X2 = 0, FL_X4 = 1, FL_ACK = 2, FL_TICK = 3, FL_CNT = 4 };
enum { CN_H = 0, CN_AB = 128, CN_SX = 256, CN_RA = 384, CN_RB = 512 };
constexpr u32 NOWHERE = 0xFFFFFF00u;
DI void flag_set(char* ws, int which, int tt, int cell) {
  wait_vm<0>();
  if ((threadIdx.x & 63) == 0)
    __hip_atomic_store((u32*)(ws + WS_FL + ((size_t)(which * 16 + tt) * 64 + cell) * 64), 1u, __ATOMIC_RELAXED, __HIP_MEMORY_SCOPE_AGENT);
}
DI void flag_request(rsrc_t r, u32 off, u32& v) {
  asm volatile("s_nop 4\n\tbuffer_load_dword %0, %1, %2, 0 offen sc1" : "=&v"(v) : "v"(off), "s"(r) : "memory");
}
DI void flag_wait(rsrc_t r, u32 off, u32& v) {
  asm volatile("" : "+v"(v)::"memory");
#pragma unroll 1
  for (int spin = 0; spin < K_SPIN; spin++) {
    if (__builtin_amdgcn_ballot_w64(v != 1u) == 0) break;
    __builtin_amdgcn_s_sleep(K_SLEEP);
    asm volatile("s_nop 4\n\tbuffer_load_dword %0, %1, %2, 0 offen sc1\n\ts_waitcnt vmcnt(0)" : "=&v"(v) : "v"(off), "s"(r) : "memory");
  }
}

DI void word_store(rsrc_t r, u32 off, u32 v) { __builtin_amdgcn_raw_buffer_store_b32(v, r, off, 0, 16); }
DI void count_up(char* ws, u32 off) {
  if ((threadIdx.x & 63) == 0) __hip_atomic_fetch_add((u32*)(ws + WS_FL + off), 1u, __ATOMIC_RELAXED, __HIP_MEMORY_SCOPE_AGENT);
}
template <class Done> DI void watch(rsrc_t r, u32 off, u32& v, Done done, u32 tickoff, u32& tick) {
#pragma unroll 1
  for (int spin = 0; spin < K_SPIN; spin++) {
    if (__builtin_amdgcn_ballot_w64(!done(v)) == 0) break;
    if ((spin & 63) == 63) { tick++; word_store(r, tickoff, tick); }
    __builtin_amdgcn_s_sleep(K_SLEEP);
    asm volatile("s_nop 4\n\tbuffer_load_dword %0, %1, %2, 0 offen sc1\n\ts_waitcnt vmcnt(0)" : "=&v"(v) : "v"(off), "s"(r) : "memory");
  }
}
__global__ __launch_bounds__(512, 2) void fused_kernel(const u16*, const u16*, const u16*, const u16*, const u16*, const u16*, const u16*,
                                                       const u16*, const u16*, char*, u16* DIAG_ARG) {
  u32x16 ka; u32x4 kb4; u32x2 kc;
  asm volatile("s_load_dwordx16 %0, %3, 0x0\n\ts_load_dwordx4 %1, %3, 0x40\n\ts_load_dwordx2 %2, %3, 0x50"
               : "=&s"(ka), "=&s"(kb4), "=&s"(kc) : "s"(__builtin_amdgcn_kernarg_segment_ptr()));
  __builtin_amdgcn_sched_barrier(0);
  extern __shared__ __attribute__((aligned(16))) char smem[];
  DIAG_BEGIN
  const int lane0 = threadIdx.x & 63, wid = rfl(threadIdx.x >> 6);
  const int xcd = blockIdx.x & 7, loc = blockIdx.x >> 3, li = loc >> 2, lj = loc & 3;
  const int half = xcd >> 2, et = ((xcd >> 1) & 1) * 4 + lj, tt = (xcd & 1) * 8 + li, ft = (xcd >> 1) * 4 + lj;
  LAS char* sm = (LAS char*)smem;
  LAS float* tabA = (LAS float*)(sm + TOFF);
  LAS float* tabB = tabA + 128;
  LAS float* tabC = tabA + 256;
  LAS float* tabBias = tabA + 512;
  u32 vp1[4], vq1[4];
#pragma unroll
  for (int i = 0; i < 4; i++) {
    const int row = 32 * (wid & 3) + 8 * i + (lane0 >> 3), u = (row >> 4) & 1, ii = row & 15;
    vp1[i] = row_src(row, 1024 * (wid & 1) + 16 * (wid & 2) + 8 * (ii >> 2) + 4 * u + (ii & 3), lane0, D_ * 2) + 4096 - i * 1024;
    vq1[i] = row_src(row, row, lane0, D_ * 2) - i * 1024;
  }
  asm volatile("" : "+v"(vp1[0]), "+v"(vp1[1]), "+v"(vp1[2]), "+v"(vp1[3]), "+v"(vq1[0]), "+v"(vq1[1]), "+v"(vq1[2]), "+v"(vq1[3]));
  __builtin_amdgcn_sched_barrier(0);
  asm volatile("s_waitcnt lgkmcnt(0)" : "+s"(ka), "+s"(kb4), "+s"(kc));
  auto ptr = [](u32 lo, u32 hi) { return (u16*)(GAS u16*)(((u64)hi << 32) | lo); };
  const u16 *x = ptr(ka[0], ka[1]), *cre = ptr(ka[2], ka[3]), *cim = ptr(ka[4], ka[5]), *dre = ptr(ka[6], ka[7]), *dim = ptr(ka[8], ka[9]);
  const u16 *w1 = ptr(ka[10], ka[11]), *b1 = ptr(ka[12], ka[13]), *w2 = ptr(ka[14], ka[15]), *b2 = ptr(kb4[0], kb4[1]);
  char* ws = (char*)ptr(kb4[2], kb4[3]);
  u16* out = ptr(kc[0], kc[1]);
  if (__builtin_expect(wid >= 4, 1)) {
    if (K_PPRIO) __builtin_amdgcn_s_setprio(K_PPRIO);
    const int p = wid - 4, phi = (li + lj) & 3;
    const u32 lds0 = (u32)(size_t)smem, ldsb = lds0 + p * 4096;
    rsrc_t rall = mk_rsrc(ws + WS_FL, 5 * 65536);
    rsrc_t rp1 = mk_rsrc((const char*)x + (size_t)tt * 64 * D_ * 2 - 4096, (1024 + 64) * D_ * 2 + 4096);
    rsrc_t rq1_ = mk_rsrc((half ? dim : dre) + (size_t)et * 128 * D_, 128 * D_ * 2);
    const int side1 = (xcd >> 1) ^ (xcd & 1);
    dma<4>(ldsb, vp1, rp1, kstep2(0, 16, phi, side1) * 128);
    dma<4>(ldsb + QOFF, vq1, rq1_, kstep2(0, 16, phi, side1) * 128);
    int lane = lane0;
    asm volatile("" : "+v"(lane)::"memory");
    __builtin_amdgcn_sched_barrier(0);
    const u32 vlin = p * 4096 + lane * 16;
    const u32 grpcell = ((lane & 16) ? tt * 64 + (lane & 15) * 4 : (lane & 15) * 64 + ft * 4) * 64;
    const u32 tickoff = lane == 0 ? FL_TICK * 65536 + (tt * 64 + ft * 4) * 64 : NOWHERE;
    const u32 ackoff = lane == 0 ? FL_ACK * 65536 + (tt * 64 + ft * 4) * 64 : NOWHERE;
    const u32 rowcnt = FL_CNT * 65536 + tt * 4096, colcnt = FL_CNT * 65536 + ft * 4096;
    const int pt = threadIdx.x - 256;
    u32 base, fv, tick;
    const int side2 = (xcd >> 1) & 1;
    auto skip = [&](int r) { return r + (r >= tt ? 1 : 0); };
    auto g2k = [&](int q) {
      const int t = kstep(q - 2, 26, phi), hi = t >= 15, r = t - 15 * hi, r2 = r ^ (side2 << 2);
      return q < 2 ? 16 * q + tt : 16 * hi + skip(r2 < (hi ? 11 : 15) ? r2 : r);
    };
    rsrc_t rco = mk_rsrc((const char*)(half ? cim : cre) + (size_t)tt * 128 * S_ * 2 - 4096, 128 * S_ * 2 + 4096);
    u32 vc[4];
#pragma unroll
    for (int i = 0; i < 4; i++) {
      const int row = 32 * p + 8 * i + (lane >> 3);
      vc[i] = row_src(row, row ^ (half * 32), lane, S_ * 2) + 4096 - i * 1024;
    }
    auto g2q = [&](int q) { dma<4>(ldsb + QOFF + (q & 3) * SLOT, vc, rco, g2k(q) * 128); };
    {
      u32 vx[2];
#pragma unroll
      for (int i = 0; i < 2; i++) {
        const int row = 16 * p + 8 * i + (lane >> 3);
        vx[i] = row_src(row, 64 * (row >> 5) + 32 * half + (row & 31), lane, D_ * 2) + 4096 - i * 1024;
      }
      auto issue = [&](int g) {
        const int k = kstep2(g, 16, phi, side1);
        const u32 m = ldsb + (g & 3) * SLOT;
        dma<4>(m, vp1, rp1, k * 128);
        dma<4>(m + QOFF, vq1, rq1_, k * 128);
      };
      auto run = [&](int g0, int g1) {
#pragma unroll 1
        for (int g = g0; g < g1; g++) { issue(g); wait_vm<16>(); bar(); }
      };
      flag_request(rall, p == 0 ? FL_TICK * 65536 + grpcell : NOWHERE, base);
      issue(1); issue(2);
      {
        u32 z = lane < 8 ? (lane >> 2) * 65536 + (tt * 64 + ft * 4 + (lane & 3)) * 64 : FL_ACK * 65536 + (tt * 64 + ft * 4) * 64;
        if (lane == 9) z = tt == 0 ? colcnt + CN_AB : NOWHERE;
        if (lane >= 10) z = ft == 0 ? rowcnt + (lane == 10 ? CN_H : (lane - 9) * 128) : NOWHERE;
        word_store(rall, p == 0 && lane < 14 ? z : NOWHERE, 0u);
      }
      wait_vm<18>();
      PSTAMP(1)
      bar();
      const int js = (phi + 1) & 3;
      auto sums = [&]() {
        const int c = lane & 15, g = lane >> 4, e = c & 3;
        const u32x4 ones4 = {0x3F803F80u, 0x3F803F80u, 0x3F803F80u, 0x3F803F80u};
        const bf16x8 ones = __builtin_bit_cast(bf16x8, ones4);
        const int a = js * SLOT + (32 * p + c) * 128 + ((g ^ (c >> 1)) * 16);
        rsrc_t rx = mk_rsrc(ws + WS_SX + (size_t)(tt * 16 + (js ^ (side1 << 2))) * 1024 + p * 256, 256);
#pragma unroll
        for (int i = 0; i < 2; i++) {
          const bf16x8 x0 = *(const LAS bf16x8*)(sm + (a + i * 2048)), x1 = *(const LAS bf16x8*)(sm + ((a ^ 64) + i * 2048));
          const f32x4 z = {0.f, 0.f, 0.f, 0.f};
          const f32x4 sx = __builtin_amdgcn_mfma_f32_16x16x32_bf16(x1, ones, __builtin_amdgcn_mfma_f32_16x16x32_bf16(x0, ones, z, 0, 0, 0), 0, 0, 0);
          const f32x4 sq = __builtin_amdgcn_mfma_f32_16x16x32_bf16(x1, x1, __builtin_amdgcn_mfma_f32_16x16x32_bf16(x0, x0, z, 0, 0, 0), 0, 0, 0);
          const float q = e == 0 ? sq[0] : e == 1 ? sq[1] : e == 2 ? sq[2] : sq[3];
          const float t = e == 0 ? sx[0] : e == 1 ? sx[1] : e == 2 ? sx[2] : sx[3];
          __builtin_amdgcn_raw_buffer_store_b64((u32x2){__float_as_uint(t), __float_as_uint(q)}, rx, (c >> 2) == g ? (16 * i + c) * 8 : NOWHERE, 0, 16);
        }
      };
      issue(3);
      if (js == 0) sums();
      asm volatile("s_waitcnt vmcnt(16)" : "+v"(base)::"memory");
      tick = __builtin_amdgcn_readlane(base, tt) + 1;
      bar();
#pragma unroll 1
      for (int g = 2; g < 6; g++) {
        issue(g + 2);
        if (g == js + 1) sums();
        wait_vm<16>();
        if (g == 3 && p == 0) word_store(rall, tickoff, tick);
        bar();
      }
      issue(8); wait_vm<16>(); bar();
      flag_request(rall, p == 0 ? FL_TICK * 65536 + grpcell : NOWHERE, fv);
      issue(9); wait_vm<16>(); bar();
      issue(10);
      asm volatile("s_waitcnt vmcnt(16)" : "+v"(fv)::"memory");
      PSTAMP(14)
      if (p == 0) {
        watch(rall, FL_TICK * 65536 + grpcell, fv, [&](u32 w) { return w != base; }, tickoff, tick);
        count_up(ws, rowcnt + CN_SX);
        word_store(rall, ackoff, 1u);
      }
      PSTAMP(15)
      bar();
      run(11, 14);
      rsrc_t rx0 = mk_rsrc((const char*)x + (size_t)tt * 128 * D_ * 2 - 4096, 128 * D_ * 2 + 4096);
      dma<2>(lds0 + XOFF + p * 2048, vx, rx0, et * 256);
      dma<2>(lds0 + XOFF + 8192 + p * 2048, vx, rx0, et * 256 + 128);
      flag_request(rall, p == 0 ? rowcnt + CN_SX : NOWHERE, fv);
      issue(14); wait_vm<21>(); bar();
      issue(15);
      asm volatile("s_waitcnt vmcnt(16)" : "+v"(fv)::"memory");
      PSTAMP(9)
      if (p == 0) watch(rall, rowcnt + CN_SX, fv, [&](u32 w) { return w >= 16u; }, tickoff, tick);
      PSTAMP(10)
      bar();
      u32x2 sv[8];
      rsrc_t rsx = mk_rsrc(ws + WS_SX + (size_t)tt * 16384, 16384);
#pragma unroll
      for (int i = 0; i < 8; i++)
        asm volatile("s_nop 4\n\tbuffer_load_dwordx2 %0, %1, %2, %3 offen sc1" : "=&v"(sv[i]) : "v"((pt & 1) * 8192 + (pt >> 1) * 8), "s"(rsx), "s"(i * 1024) : "memory");
      g2q(0); wait_vm<20>(); bar();
      g2q(1); wait_vm<16>(); bar();
      g2q(2);
      asm volatile("s_waitcnt vmcnt(12)" : "+v"(sv[0]), "+v"(sv[1]), "+v"(sv[2]), "+v"(sv[3]), "+v"(sv[4]), "+v"(sv[5]), "+v"(sv[6]), "+v"(sv[7])::"memory");
      float a = 0.f, q = 0.f;
#pragma unroll
      for (int i = 0; i < 8; i++) { a += __uint_as_float(sv[i][0]); q += __uint_as_float(sv[i][1]); }
      a += __builtin_bit_cast(float, __builtin_amdgcn_update_dpp(0, __builtin_bit_cast(int, a), 0xB1, 0xF, 0xF, true));
      q += __builtin_bit_cast(float, __builtin_amdgcn_update_dpp(0, __builtin_bit_cast(int, q), 0xB1, 0xF, 0xF, true));
      const float mean = a * (1.0f / D_), var = fmaxf(q * (1.0f / D_) - mean * mean, 0.f);
      const float rstd = (half ? -1.f : 1.f) * __builtin_amdgcn_rsqf(var + LN_EPS);
      tabA[(pt & 1) * 128 + (pt >> 1)] = (pt & 1) ? -mean * rstd : rstd;
      wait_lgkm0();
    }
    {
      rsrc_t rp = mk_rsrc(ws + WS_AB + (size_t)et * 32 * 16384, 9 * 32 * 16384);
      rsrc_t rq0 = mk_rsrc((const char*)cre + (size_t)tt * 128 * S_ * 2 - 4096, 128 * S_ * 2 + 4096);
      rsrc_t rq1 = mk_rsrc((const char*)cim + (size_t)tt * 128 * S_ * 2 - 4096, 128 * S_ * 2 + 4096);
      u32 vh[2];
#pragma unroll
      for (int i = 0; i < 2; i++) {
        const int row = 64 * (p >> 1) + 16 * (p & 1) + 8 * i + (lane >> 3);
        vh[i] = row_src(row, row ^ (half * 32), lane, S_ * 2) + 4096 - i * 1024;
      }
      const u32 ldsh = lds0 + QOFF + (p >> 1) * 8192 + (p & 1) * 2048;
      auto fp = [&](int q) { dma_sc1<4>(ldsb + (q & 3) * SLOT, vlin, rp, (half * 256 + g2k(q)) * 16384); };
      auto full = [&](int q) { fp(q); g2q(q); };
      auto halfs = [&](int g) {
        const int k = kstep2(g, 8, phi, side2), term = k >> 2, ks = 16 + skip(11 + (k & 3));
        dma_sc1<4>(ldsb + (g & 3) * SLOT, vlin, rp, (term * 256 + ks) * 16384);
        if (term) dma<2>(ldsh + (g & 3) * SLOT, vh, rq1, ks * 128);
        else dma<2>(ldsh + (g & 3) * SLOT, vh, rq0, ks * 128);
      };
      bar();
      wait_vm<8>(); bar();
      g2q(3);
      rsrc_t rw = mk_rsrc((const char*)w1 + (size_t)(tt * 64 + p * 16) * F_ * 2 + ft * 448, 15 * F_ * 2 + 448);
      u32x4 v[8];
      const int l28 = lane < 28 ? lane : lane < 56 ? lane - 28 : 27;
      const u32 vo = (lane >= 28 ? F_ * 2 : 0) + l28 * 16;
      auto csload = [&]() {
#pragma unroll
      for (int i = 0; i < 8; i++)
        asm volatile("s_nop 4\n\tbuffer_load_dwordx4 %0, %1, %2, %3 offen" : "=&v"(v[i]) : "v"(vo), "s"(rw), "s"(i * 2 * F_ * 2) : "memory");
      };
      if (K_CSP == 0) csload();
      wait_vm<K_CSP == 0 ? 16 : 8>(); bar();
      if (K_CSP == 1) csload();
      bar();
      PSTAMP(12)
      if (p == 0) {
        count_up(ws, colcnt + CN_AB);
        fv = 0;
        watch(rall, colcnt + CN_AB, fv, [&](u32 w) { return w >= 16u; }, tickoff, tick);
      }
      bar();
      PSTAMP(2)
      fp(2); fp(3); full(4);
      asm volatile("s_waitcnt vmcnt(12)" : "+v"(v[0]), "+v"(v[1]), "+v"(v[2]), "+v"(v[3]), "+v"(v[4]), "+v"(v[5]), "+v"(v[6]), "+v"(v[7])::"memory");
      bar();
      if (p == 0) count_up(ws, colcnt + CN_AB);
      full(5);
      {
        float s[8] = {0, 0, 0, 0, 0, 0, 0, 0};
#pragma unroll
        for (int i = 0; i < 8; i++)
#pragma unroll
          for (int e = 0; e < 4; e++) { s[2 * e] += blo(v[i][e]); s[2 * e + 1] += bhi(v[i][e]); }
#pragma unroll
        for (int e = 0; e < 8; e++) s[e] += __shfl(s[e], lane + 28);
        if (lane < 28) {
          LAS float* d = (LAS float*)(sm + COFF) + p * 224 + lane * 8;
          *(LAS f32x4*)d = (f32x4){s[0], s[1], s[2], s[3]};
          *(LAS f32x4*)(d + 4) = (f32x4){s[4], s[5], s[6], s[7]};
        }
      }
      wait_vm<16>(); wait_lgkm0(); bar();
      full(6);
      if (p == 0 && lane < 56) {
        const LAS f32x4* d = (const LAS f32x4*)(sm + COFF) + lane;
        const f32x4 t = (d[0] + d[56]) + (d[112] + d[168]);
        store_sc1(mk_rsrc(ws + WS_CS + (size_t)tt * F_ * 4 + ft * 896, 896), lane * 16, __builtin_bit_cast(u32x4, t));
      }
      wait_vm<16>(); bar();
#pragma unroll 1
      for (int q = 7; q < 11; q++) { full(q); wait_vm<16>(); bar(); }
      flag_request(rall, p == 0 ? colcnt + CN_AB : NOWHERE, fv);
      full(11); wait_vm<16>(); bar();
      full(12);
      asm volatile("s_waitcnt vmcnt(16)" : "+v"(fv)::"memory");
      if (p == 0) {
        watch(rall, colcnt + CN_AB, fv, [&](u32 w) { return w >= 32u; }, tickoff, tick);
        count_up(ws, colcnt + CN_AB);
        word_store(rall, ackoff, 2u);
      }
      bar();
#pragma unroll 1
      for (int q = 13; q < 26; q++) { full(q); wait_vm<16>(); bar(); }
      const u32 lastoff = lane < 32 ? FL_ACK * 65536 + grpcell : colcnt + CN_AB;
      const u32 lastmin = lane >= 32 ? 48u : lane == 16 + (ft ^ 8) ? 2u : 1u;
      flag_request(rall, p == 0 ? lastoff : NOWHERE, fv);
      full(26); wait_vm<16>(); bar();
      full(27);
      asm volatile("s_waitcnt vmcnt(16)" : "+v"(fv)::"memory");
      if (p == 0) watch(rall, lastoff, fv, [&](u32 w) { return w >= lastmin; }, tickoff, tick);
      bar();
      halfs(0); wait_vm<14>(); bar();
#pragma unroll 1
      for (int g = 1; g < 8; g++) { halfs(g); wait_vm<12>(); bar(); }
    }
    rsrc_t rp4 = mk_rsrc(w2 + et * 128, F_ * D_ * 2 - et * 256);
    u32 vp4[4];
#pragma unroll
    for (int i = 0; i < 4; i++) {
      const int k = 16 * (p & 1) + 4 * i + (lane >> 4);
      vp4[i] = k * (D_ * 2) + (((lane & 15) ^ ((k & 7) << 1)) * 16) - i * 1024;
    }
    auto g4blk = [&](int q) {
      const int r = q < 10 ? kstep2(q - 3, 7, phi, xcd & 1) : kstep2(q - 10, 14, phi, xcd & 1);
      const int i = q < 10 ? r : r >> 1, pr = q < 3 ? q : q < 10 ? 0 : 1 + (r & 1);
      return 7 * (8 * half + (q < 3 ? et : i + (i >= et ? 1 : 0))) + pr + 4 * (p >> 1);
    };
    auto g4p = [&](int q) { dma<4>(ldsb + ((q + 2) & 3) * SLOT, vp4, rp4, g4blk(q) * (32 * D_ * 2)); };
    {
      rsrc_t rp = mk_rsrc((const char*)w1 + ft * 448 - 4096, D_ * F_ * 2 - ft * 448 + 4096);
      rsrc_t rq = mk_rsrc(ws + WS_H + (size_t)tt * 32 * 8192, 32 * 8192);
      u32 vp[7];
#pragma unroll
      for (int i = 0; i < 7; i++) {
        const int o = (p * 7 + i) * 64 + lane, k = o / 28, ch = o % 28;
        vp[i] = k * (F_ * 2) + ((ch ^ (((k >> 2) & 1) << 1)) * 16) + 4096 - (i & 3) * 1024;
      }
      rsrc_t rw = mk_rsrc(w2 + (size_t)(half * 1792 + tt * 112 + 28 * p) * D_ + et * 128, 27 * D_ * 2 + 256);
      const u32 vw = (lane >> 1) * (D_ * 2) + (lane & 1) * 128;
      auto pa = [&](int g, int sl) {
        const u32 so = kstep(g, 16, phi) * (64 * F_ * 2), m = lds0 + sl * P3 + p * 7168;
        dma<4>(m, vp, rp, so);
        dma<3>(m + 4096, vp + 4, rp, so);
      };
      auto q = [&](int g, int sl) { dma_sc1<4>(ldsb + Q3OFF + sl * SLOT, vlin, rq, kstep(g, 16, phi) * 16384); };
      wait_vm<6>(); bar();
      wait_vm<0>(); bar();
      bar();
      PSTAMP(13)
      if (p == 0) count_up(ws, rowcnt + CN_H);
      pa(0, 0); pa(1, 1);
      if (K_LEAD & 1) pull_lines(lds0 + COFF + 3584, p == 1 ? (128 + 16 * li + (lane >> 2)) * (F_ * 2) + (lane & 3) * 128 + 4096 : NOWHERE, rp, 0);
      rsrc_t rs = mk_rsrc(ws + WS_ST + (size_t)tt * 16384, 16384);
      rsrc_t rc = mk_rsrc(ws + WS_CS + ft * 896, 15 * F_ * 4 + 896);
      u32x2 sv[8];
      u32 cv[16], bv;
#pragma unroll
      for (int i = 0; i < 16; i++)
        asm volatile("s_nop 4\n\tbuffer_load_dword %0, %1, %2, %3 offen sc1" : "=&v"(cv[i]) : "v"(min(pt, 223) * 4), "s"(rc), "s"(i * F_ * 4) : "memory");
      asm volatile("s_nop 4\n\tbuffer_load_ushort %0, %1, %2, 0 offen" : "=&v"(bv) : "v"(min(pt, 223) * 2), "s"(mk_rsrc(b1 + ft * 224, 448)) : "memory");
      if (p == 0) { fv = 0; watch(rall, rowcnt + CN_H, fv, [&](u32 w) { return w >= 16u; }, NOWHERE, tick); }
      bar();
      PSTAMP(3)
      q(0, 0); q(1, 1);
      wait_vm<4>();
      PSTAMP(4)
      bar();
      pa(2, 2); q(2, 2);
#pragma unroll
      for (int i = 0; i < 8; i++)
        asm volatile("s_nop 4\n\tbuffer_load_dwordx2 %0, %1, %2, %3 offen sc1" : "=&v"(sv[i]) : "v"((pt & 1) * 8192 + (pt >> 1) * 8), "s"(rs), "s"(i * 1024) : "memory");
      wait_vm<19>(); bar();
      pa(3, 0); q(3, 0); wait_vm<19>(); bar();
      pa(4, 1); q(4, 1); wait_vm<11>();
      asm volatile("" : "+v"(sv[0]), "+v"(sv[1]), "+v"(sv[2]), "+v"(sv[3]), "+v"(sv[4]), "+v"(sv[5]), "+v"(sv[6]), "+v"(sv[7]), "+v"(bv)::"memory");
      asm volatile("" : "+v"(cv[0]), "+v"(cv[1]), "+v"(cv[2]), "+v"(cv[3]), "+v"(cv[4]), "+v"(cv[5]), "+v"(cv[6]), "+v"(cv[7]), "+v"(cv[8]),
                   "+v"(cv[9]), "+v"(cv[10]), "+v"(cv[11]), "+v"(cv[12]), "+v"(cv[13]), "+v"(cv[14]), "+v"(cv[15])::"memory");
      bar();
      pa(5, 2); q(5, 2);
      {
        float s = 0.f, a = 0.f, qq = 0.f;
#pragma unroll
        for (int i = 0; i < 16; i++) s += __uint_as_float(cv[i]);
        if (pt < 224) { tabC[pt] = s; tabBias[pt] = __uint_as_float(bv << 16); }
#pragma unroll
        for (int i = 0; i < 8; i++) { a += __uint_as_float(sv[i][0]); qq += __uint_as_float(sv[i][1]); }
        a += __builtin_bit_cast(float, __builtin_amdgcn_update_dpp(0, __builtin_bit_cast(int, a), 0xB1, 0xF, 0xF, true));
        qq += __builtin_bit_cast(float, __builtin_amdgcn_update_dpp(0, __builtin_bit_cast(int, qq), 0xB1, 0xF, 0xF, true));
        const float mean = a * (1.0f / D_), var = fmaxf(qq * (1.0f / D_) - mean * mean, 0.f);
        const float rstd = __builtin_amdgcn_rsqf(var + LN_EPS);
        tabA[(pt & 1) * 128 + (pt >> 1)] = (pt & 1) ? -mean * rstd : rstd;
      }
      wait_vm<11>(); wait_lgkm0(); bar();
      int sl = 0;
#pragma unroll 1
      for (int g = 6; g < 16; g++) {
        pa(g, sl); q(g, sl);
        const int gp = K_WN > 0 ? 6 + loc % (K_WN > 0 ? K_WN : 1) : 99;
        if (g == gp) pull_lines(lds0 + COFF + 3584, vw, rw, 0);
        if ((unsigned)(g - gp) < 2u) wait_vm<12>(); else wait_vm<11>();
        bar();
        sl = sl == 2 ? 0 : sl + 1;
      }
      g4p(0); wait_vm<4>(); bar();
      g4p(1);
      bar();
    }
    {
      rsrc_t rq = mk_rsrc(ws + WS_R + (size_t)tt * 112 * 8192, 112 * 8192);
      const u32 vq0 = (4 * (p & 1) + 2 * half) * 1024 + lane * 16, vq1 = (4 * (p & 1) + 2 - 2 * half) * 1024 + lane * 16;
      auto g4q = [&](int q) {
        const u32 m = ldsb + QOFF + ((q + 2) & 3) * SLOT, so = g4blk(q) * 8192;
        dma_sc1<2>(m, vq0, rq, so);
        dma_sc1<2>(m + 2048, vq1, rq, so);
      };
      auto halfs = [&](int g) {
        const int k = kstep2(g, 8, phi, xcd & 1), blk = 7 * (2 * k + (p >> 1)) + 3;
        dma<4>(ldsb + ((g + 2) & 3) * SLOT, vp4, rp4, blk * (32 * D_ * 2));
        dma_sc1<2>(ldsb + QOFF + ((g + 2) & 3) * SLOT, vq0, rq, blk * 8192);
      };
      wait_vm<4>(); bar();
      PSTAMP(5)
      if (p == 0) count_up(ws, rowcnt + CN_RA);
      g4p(2); wait_vm<4>(); bar();
      g4p(3);
      if (K_LEAD & 2) {
        const int qq = 4 + min(lane >> 3, 2);
        const u32 b4 = g4blk(4), b5 = g4blk(5), b6 = g4blk(6), bb = qq == 4 ? b4 : qq == 5 ? b5 : b6;
        pull_lines(lds0 + COFF + 3584, (p & 1) == 0 && lane < 24 ? (bb * 32 + 4 * li + ((lane >> 1) & 3)) * (D_ * 2) + (lane & 1) * 128 : NOWHERE, rp4, 0);
      }
      wait_vm<5>();
      if (p == 0) { fv = 0; watch(rall, rowcnt + CN_RA, fv, [&](u32 w) { return w >= 16u; }, NOWHERE, tick); }
      bar();
      PSTAMP(6)
      g4q(3); g4p(4); g4q(4); g4p(5); g4q(5);
      wait_vm<16>(); bar();
      if (K_RB == 3 && p == 0) count_up(ws, rowcnt + CN_RB);
      g4p(6); g4q(6); wait_vm<16>(); bar();
      if (K_RB == 4 && p == 0) count_up(ws, rowcnt + CN_RB);
      g4p(7); g4q(7); wait_vm<16>(); bar();
      if (K_RB == 3) flag_request(rall, p == 0 ? rowcnt + CN_RB : NOWHERE, fv);
      g4p(8); g4q(8); wait_vm<16>(); bar();
      if (K_RB == 4) flag_request(rall, p == 0 ? rowcnt + CN_RB : NOWHERE, fv);
      g4p(9); g4q(9);
      asm volatile("s_waitcnt vmcnt(%1)" : "+v"(fv) : "i"(K_RB == 3 ? 16 : 8) : "memory");
      PSTAMP(7)
      if (p == 0) watch(rall, rowcnt + CN_RB, fv, [&](u32 w) { return w >= 16u; }, NOWHERE, tick);
      PSTAMP(8)
      bar();
#pragma unroll 1
      for (int q = 10; q < 24; q++) { g4p(q); g4q(q); wait_vm<16>(); bar(); }
      halfs(0); wait_vm<14>(); bar();
#pragma unroll 1
      for (int g = 1; g < 8; g++) { halfs(g); wait_vm<12>(); bar(); }
      wait_vm<6>(); bar();
      wait_vm<0>(); bar();
      PDIAG_END
    }
  } else {
    const int lane = lane0;
    const int wP = wid >> 1, wQ = wid & 1, c = lane & 15, g = lane >> 4;
    const int m0 = 4 * wQ + 2 * half, cell = ft * 4 + wid;
    {
      Consumer<4, M_ROW, M_ROW> C;
      C.init(sm, wP, wQ, lane, 0, wP);
      f32x4 acc[4][4], sd[2];
      C.zero(acc);
#pragma unroll
      for (int i = 0; i < 2; i++) sd[i] = (f32x4){0.f, 0.f, 0.f, 0.f};
      const u32x4 ones4 = {0x3F803F80u, 0x3F803F80u, 0x3F803F80u, 0x3F803F80u};
      bf16x8 ones = __builtin_bit_cast(bf16x8, ones4);
      asm volatile("" : "+v"(ones));
      bar();
      STAMP(1)
      C.first();
#define G1_HALF0(S) C.load<S, 1, 4>(); C.mma<0, 4>(acc); C.qsums<0>(sd, ones); C.interleave<18, 4>();
#define G1_STEP(S) G1_HALF0(S) wait_lgkm0(); bar(); __builtin_amdgcn_sched_barrier(0); \
      C.load<(S + 1) % NST, 0, 4>(); C.mma<1, 4>(acc); C.qsums<1>(sd, ones); C.interleave<18, 4>();
      for (int it = 0; it < 3; it++) { G1_STEP(0) G1_STEP(1) G1_STEP(2) G1_STEP(3) }
      G1_STEP(0) G1_STEP(1) G1_STEP(2) G1_HALF0(3)
      wait_lgkm0();
      C.qsums<1>(sd, ones);
      __builtin_amdgcn_sched_barrier(0);
      C.mma<1, 4>(acc);
      tabC[g < 2 ? 64 * wQ + 32 * wP + 16 * g + c : 128 + lane] = g ? sd[1][0] : sd[0][0];
      __builtin_amdgcn_sched_group_barrier(0x8, 4, 0);
      __builtin_amdgcn_sched_group_barrier(0x2 | 0x200, 8, 0);
      __builtin_amdgcn_sched_group_barrier(0x8, 12, 0);
      __builtin_amdgcn_sched_barrier(0);
      STAMP(2)
      wait_lgkm0(); bar();
      rsrc_t ro = mk_rsrc(ws + WS_AB + ((size_t)((half * 8 + et) * 32 + tt) * 16 + 8 * wP + 4 * wQ) * 1024, 16 * 16384 + 4096);
      f32x4 ra[4], rb[4];
      float rs[4];
#pragma unroll
      for (int i = 0; i < 4; i++) {
        ra[i] = *(const LAS f32x4*)(tabA + 64 * wP + 16 * i + 4 * g);
        rb[i] = *(const LAS f32x4*)(tabB + 64 * wP + 16 * i + 4 * g);
        rs[i] = tabC[64 * wQ + 16 * (i ^ (2 * wP)) + c];
      }
#pragma unroll
      for (int u = 0; u < 2; u++)
#pragma unroll
        for (int j = 0; j < 4; j++) {
          const f32x4 a = ra[2 * u] * acc[2 * u][j] + rb[2 * u] * rs[j], b = ra[2 * u + 1] * acc[2 * u + 1][j] + rb[2 * u + 1] * rs[j];
          const u32x4 o = {cvtpk(a[0], a[1]), cvtpk(a[2], a[3]), cvtpk(b[0], b[1]), cvtpk(b[2], b[3])};
          const int at = (j ^ (2 * wP)) * 1024 + lane * 16;
          store_sc1(ro, u * 16 * 16384 + at, o);
          *(LAS u32x4*)(sm + (u * SLOT + wP * 8192 + wQ * 4096 + at)) = o;
          __builtin_amdgcn_sched_barrier(0);
        }
    }
    u32x4 hk[2][2];
    {
      Consumer<4, M_TILED, M_ROW> C;
      C.init(sm, wP, wQ, lane);
      f32x4 acc[4][4];
      wait_lgkm0(); bar();
      STAMP(3)
      C.first();
      C.load<0, 1, 4>(); C.mma<0, 4, true>(acc); C.interleave<16, 4>();
      wait_lgkm0(); bar();
      __builtin_amdgcn_sched_barrier(0);
      C.load<1, 0, 4>(); C.mma<1, 4>(acc); C.interleave<16, 4>();
      C.load<1, 1, 4>(); C.mma<0, 4>(acc); C.interleave<16, 4>();
      wait_lgkm0();
      if (K_ZP == 1) { wait_vm<4>(); bar(); __builtin_amdgcn_sched_barrier(0); }
      C.mma<1, 4>(acc);
      __builtin_amdgcn_sched_barrier(0);
      if (K_ZP == 0) { wait_vm<4>(); bar(); }
      STAMP(4)
      bar();
      wait_vm<0>();
      bar();
      STAMP(5)
      C.load<2, 0, 4>();
      C.step<2, 4, 4>(acc); C.step<3, 4, 4>(acc);
      for (int it = 0; it < 5; it++) C.step4<4, 4>(acc);
      C.step4<4, 2>(acc);
      STAMP(6)
      xch_send(ws + WS_X2, acc, wid, lane);
      __builtin_amdgcn_sched_barrier(0);
      rsrc_t rx = mk_rsrc(ws + WS_X2 + ((size_t)(blockIdx.x ^ 4) * 4 + wid) * 4096, 4096);
      rsrc_t rf = mk_rsrc(ws + WS_FL + ((size_t)(FL_X2 * 16 + tt) * 64 + (cell ^ 32)) * 64, 64);
      u32x4 rv[4];
      u32 fv;
      C.step<0, 2, 2>(acc); C.step<1, 2, 2>(acc); C.step<2, 2, 2>(acc);
      if (K_XF == 3) flag_set(ws, FL_X2, tt, cell);
      C.step<3, 2, 2>(acc);
      if (K_XF == 4) flag_set(ws, FL_X2, tt, cell);
      C.step<0, 2, 2>(acc); C.step<1, 2, 2>(acc);
      flag_request(rf, 0, fv);
      __builtin_amdgcn_sched_barrier(0);
      C.step<2, 2, 2>(acc);
      wait_vm<0>();
      flag_wait(rf, 0, fv);
      xch_request(rx, rv, lane);
      __builtin_amdgcn_sched_barrier(0);
      C.load<3, 1, 2>(); C.mma<0, 2>(acc); C.interleave<8, 2>();
      C.mma<1, 2>(acc);
      STAMP(7)
      xch_finish(rv);
            rsrc_t rh = mk_rsrc(ws + WS_H + ((size_t)(tt * 32 + 4 * et + 2 * wP) * 8 + m0) * 1024, 16384);
      rsrc_t rs = mk_rsrc(ws + WS_ST + ((size_t)(tt * 16 + 2 * et + wP) * 128 + 16 * m0) * 8, 256);
#pragma unroll
      for (int jj = 0; jj < 2; jj++) {
        float s = 0.f, q = 0.f;
#pragma unroll
        for (int u = 0; u < 2; u++) {
          const u32x4 o = rv[jj * 2 + u];
          const f32x4 a = acc[2 * u][jj], b = acc[2 * u + 1][jj];
          const int xrow = 32 * wQ + 16 * jj + c;
          const LAS char* xp = sm + (XOFF + wP * 8192 + xrow * 128 + (g & 1) * 8);
          const u32x2 xa = *(const LAS u32x2*)(xp + (((4 * u + (g >> 1)) ^ ((xrow >> 1) & 7)) * 16));
          const u32x2 xb = *(const LAS u32x2*)(xp + (((4 * u + 2 + (g >> 1)) ^ ((xrow >> 1) & 7)) * 16));
          u32x4 h;
          h[0] = cvtpk(a[0] + blo(o[0]) + blo(xa[0]), a[1] + bhi(o[0]) + bhi(xa[0]));
          h[1] = cvtpk(a[2] + blo(o[1]) + blo(xa[1]), a[3] + bhi(o[1]) + bhi(xa[1]));
          h[2] = cvtpk(b[0] + blo(o[2]) + blo(xb[0]), b[1] + bhi(o[2]) + bhi(xb[0]));
          h[3] = cvtpk(b[2] + blo(o[3]) + blo(xb[1]), b[3] + bhi(o[3]) + bhi(xb[1]));
          store_sc1(rh, (u * 8 + jj) * 1024 + lane * 16, h);
          hk[jj][u] = h;
#pragma unroll
          for (int e = 0; e < 4; e++) { const float lo = blo(h[e]), hi = bhi(h[e]); s += lo + hi; q += lo * lo + hi * hi; }
        }
        s = xsum32(xsum16(s)); q = xsum32(xsum16(q));
        if (g == 0) __builtin_amdgcn_raw_buffer_store_b64((u32x2){__float_as_uint(s), __float_as_uint(q)}, rs, (16 * jj + c) * 8, 0, 16);
      }
      wait_vm<0>();
      STAMP(8)
      bar();
#pragma unroll
      for (int q = 0; q < 4; q++) *(LAS u32x4*)(sm + XOFF + wid * 4096 + q * 1024 + lane * 16) = hk[q >> 1][q & 1];
      bar();
    }
    rsrc_t rr = mk_rsrc(ws + WS_R + ((size_t)(tt * 112 + 7 * ft) * 8 + 4 * wQ) * 1024, 7 * 8192);
    u32x4 ro[14];
    {
      Consumer<7, M_TR448, M_TILED> C;
      C.init(sm, wP, wQ, lane);
      f32x4 acc[7][4];
      C.zero(acc);
      bar();
      STAMP(9)
      C.first();
      for (int it = 0; it < 5; it++) { C.step<0, 4, 4>(acc); C.step<1, 4, 4>(acc); C.step<2, 4, 4>(acc); }
      C.last<0, 4>(acc);
      bar();
      STAMP(10)
      float ra[4], rb[4];
#pragma unroll
      for (int j = 0; j < 4; j++) { ra[j] = tabA[64 * wQ + 16 * j + c]; rb[j] = tabB[64 * wQ + 16 * j + c]; }
      f32x4 cs[7], bi[7];
#pragma unroll
      for (int i = 0; i < 7; i++) {
        const int col = (i < 6 ? 128 * wP + 16 * i : 96 + 16 * wP) + 4 * g;
        cs[i] = *(const LAS f32x4*)(tabC + col); bi[i] = *(const LAS f32x4*)(tabBias + col);
      }
      auto act = [&](int i, int j) {
        f32x4 v = ra[j] * acc[i][j] + (rb[j] * cs[i] + bi[i]);
#pragma unroll
        for (int e = 0; e < 4; e++) v[e] = fmaxf(v[e], 0.f);
        v = v * v;
        return (u32x2){cvtpk(v[0], v[1]), cvtpk(v[2], v[3])};
      };
#pragma unroll
      for (int u = 0; u < 3; u++) {
        LAS char* own = sm + (QOFF + ((u + 2) & 3) * SLOT + wP * 8192 + 4 * wQ * 1024 + lane * 16);
#pragma unroll
        for (int j = 0; j < 4; j++) {
          const u32x2 a = act(2 * u, j), b = act(2 * u + 1, j);
          ro[4 * u + j] = (u32x4){a[0], a[1], b[0], b[1]};
          if (4 * u + j < K_NS1) store_sc1(rr, ((4 * wP + u) * 8 + j) * 1024 + lane * 16, ro[4 * u + j]);
          *(LAS u32x4*)(own + (j ^ (2 * half)) * 1024) = ro[4 * u + j];
        }
      }
#pragma unroll
      for (int j = 0; j < 4; j++)
        *(LAS u32x2*)(sm + (QOFF + SLOT + (4 * wQ + j) * 1024 + lane * 16 + 8 * wP)) = act(6, j);
    }
    {
      Consumer<4, M_TR256, M_TILED> C;
      C.init(sm, wP, wQ, lane);
      f32x4 acc[4][4];
      wait_vm<K_NS1 - 4>();
      wait_lgkm0(); bar();
      STAMP(11)
      C.load<2, 0, 4>();
#pragma unroll
      for (int j = 0; j < 2; j++) ro[12 + j] = *(const LAS u32x4*)(sm + (QOFF + SLOT + (4 * wQ + 2 * wP + j) * 1024 + lane * 16));
      auto put = [&](int hs, int n) {
        constexpr int LEFT = 14 - K_NS1;
        for (int q = 0; q < LEFT; q++)
          if (q * K_SPR / LEFT == hs * 16 + n) {
            const int t = K_NS1 + q;
            store_sc1(rr, (t < 12 ? (4 * wP + (t >> 2)) * 8 + (t & 3) : 24 + 2 * wP + (t & 1)) * 1024 + lane * 16, ro[t]);
          }
      };
      C.half_with<2, 1, true>(acc, [&](int n) { put(0, n); });
      wait_lgkm0(); bar();
      C.half_with<3, 0, false>(acc, [&](int n) { put(1, n); });
      C.half_with<3, 1, false>(acc, [&](int n) { put(2, n); });
      wait_lgkm0(); bar();
      C.half_with<0, 0, false>(acc, [&](int n) { put(3, n); });
      C.half_with<0, 1, false>(acc, [&](int n) { put(4, n); });
      wait_lgkm0();
      C.half_with<-1, 0, false>(acc, [&](int n) { put(5, n); });
      __builtin_amdgcn_sched_barrier(0);
      STAMP(12)
      if (K_RB == 3) wait_vm<0>();
      bar();
      STAMP(13)
      C.load<1, 0, 4>();
      C.load<1, 1, 4>(); C.mma<0, 4>(acc); C.interleave<16, 4>();
      if (K_RB == 4) wait_vm<0>();
      wait_lgkm0(); bar();
      __builtin_amdgcn_sched_barrier(0);
      C.load<2, 0, 4>(); C.mma<1, 4>(acc); C.interleave<16, 4>();
      C.step<2, 4, 4>(acc); C.step<3, 4, 4>(acc);
      for (int it = 0; it < 4; it++) C.step4<4, 4>(acc);
      C.step<0, 4, 4>(acc); C.step<1, 4, 2>(acc);
      STAMP(14)
      xch_send(ws + WS_X4, acc, wid, lane);
      u32x2 br[4];
#pragma unroll
      for (int i = 0; i < 4; i++) br[i] = *(const u32x2*)(b2 + et * 128 + 64 * wP + 16 * i + 4 * g);
      __builtin_amdgcn_sched_barrier(0);
      rsrc_t rx = mk_rsrc(ws + WS_X4 + ((size_t)(blockIdx.x ^ 4) * 4 + wid) * 4096, 4096);
      rsrc_t rf = mk_rsrc(ws + WS_FL + ((size_t)(FL_X4 * 16 + tt) * 64 + (cell ^ 32)) * 64, 64);
      u32x4 rv[4];
      u32 fv;
      C.step<2, 2, 2>(acc); C.step<3, 2, 2>(acc); C.step<0, 2, 2>(acc);
      flag_set(ws, FL_X4, tt, cell);
      C.step<1, 2, 2>(acc); C.step<2, 2, 2>(acc); C.step<3, 2, 2>(acc);
      flag_request(rf, 0, fv);
      __builtin_amdgcn_sched_barrier(0);
      C.step<0, 2, 2>(acc);
      wait_vm<0>();
      flag_wait(rf, 0, fv);
      xch_request(rx, rv, lane);
      __builtin_amdgcn_sched_barrier(0);
      C.last<1, 2>(acc);
      xch_finish(rv);

      rsrc_t ro = mk_rsrc(out + (size_t)(tt * 128 + 16 * m0) * D_ + et * 128 + 64 * wP, 31 * D_ * 2 + 128);
#pragma unroll
      for (int jj = 0; jj < 2; jj++)
#pragma unroll
        for (int u = 0; u < 2; u++) {
          const u32x4 o = rv[jj * 2 + u], h = *(const LAS u32x4*)(sm + XOFF + wid * 4096 + (jj * 2 + u) * 1024 + lane * 16);
          const f32x4 a = acc[2 * u][jj], b = acc[2 * u + 1][jj];
          const u32x2 ba = br[2 * u], bb = br[2 * u + 1];
          const u32 a0 = cvtpk(a[0] + blo(o[0]) + blo(ba[0]) + blo(h[0]), a[1] + bhi(o[0]) + bhi(ba[0]) + bhi(h[0]));
          const u32 a1 = cvtpk(a[2] + blo(o[1]) + blo(ba[1]) + blo(h[1]), a[3] + bhi(o[1]) + bhi(ba[1]) + bhi(h[1]));
          const u32 b0 = cvtpk(b[0] + blo(o[2]) + blo(bb[0]) + blo(h[2]), b[1] + bhi(o[2]) + bhi(bb[0]) + bhi(h[2]));
          const u32 b1_ = cvtpk(b[2] + blo(o[3]) + blo(bb[1]) + blo(h[3]), b[3] + bhi(o[3]) + bhi(bb[1]) + bhi(h[3]));
          const auto s0 = __builtin_amdgcn_permlane16_swap(a0, b0, false, false);
          const auto s1 = __builtin_amdgcn_permlane16_swap(a1, b1_, false, false);
          __builtin_amdgcn_raw_buffer_store_b128((u32x4){s0[0], s1[0], s0[1], s1[1]}, ro,
                                                 (16 * jj + c) * (D_ * 2) + (32 * u + 16 * (g & 1) + 8 * (g >> 1)) * 2, 0, 16);
        }
      DIAG_END(15)
    }
  }
}

}

extern "C" void kernel_launch(void* const* d_in, const int* in_sizes, int n_in, void* d_out, int out_size, void* d_ws, size_t ws_size,
                              hipStream_t stream) {
  const u16* const* I = (const u16* const*)d_in;
  (void)hipFuncSetAttribute((const void*)fused_kernel, hipFuncAttributeMaxDynamicSharedMemorySize, LDS_BYTES);
  fused_kernel<<<256, 512, LDS_BYTES, stream>>>(I[0], I[1], I[2], I[3], I[4], I[6], I[7], I[8], I[9], (char*)d_ws, (u16*)d_out);
}
```

```cpp
#include <hip/hip_runtime.h>
#include <hip/hip_bf16.h>
#include <type_traits>

typedef unsigned short u16;
typedef unsigned int u32;
typedef unsigned long long u64;
typedef __bf16 bf16x8 __attribute__((ext_vector_type(8)));
typedef __bf16 bf16x2 __attribute__((ext_vector_type(2)));
typedef short s16x4 __attribute__((ext_vector_type(4)));
typedef short s16x8 __attribute__((ext_vector_type(8)));
typedef float f32x2 __attribute__((ext_vector_type(2)));
typedef float f32x4 __attribute__((ext_vector_type(4)));
typedef u32 u32x2 __attribute__((ext_vector_type(2)));
typedef u32 u32x4 __attribute__((ext_vector_type(4)));
typedef u32 u32x16 __attribute__((ext_vector_type(16)));
typedef __amdgpu_buffer_rsrc_t rsrc_t;
#define GAS __attribute__((address_space(1)))
#define LAS __attribute__((address_space(3)))
#define DI __device__ __forceinline__

#define K_WN 8
#define K_SLEEP 1
#define K_SPIN (1 << 18)
#define K_GAPS(NM, NR) ((NR) < (NM) ? (NR) : (NM))
#define K_PPRIO 0
#define K_CSP 0
#define K_WB 1
#define K_REV 1
#define K_LEAD 3
#define K_NS1 6
#define K_SPR 88
#define K_RB 3
#define K_XF 3
namespace {

constexpr int S_ = 2048, D_ = 1024, F_ = 3584;
constexpr float LN_EPS = 1e-5f;

constexpr size_t WS_AB = 0;
constexpr size_t WS_H = 8u << 20;
constexpr size_t WS_R = 12u << 20;
constexpr size_t WS_X2 = 27u << 20;
constexpr size_t WS_X4 = 31u << 20;
constexpr size_t WS_ST = 35u << 20;
constexpr size_t WS_CS = 36u << 20;
constexpr size_t WS_SX = 38u << 20;
constexpr size_t WS_FL = 37u << 20;

#define DIAG_ARG
#define DIAG_BEGIN
#define STAMP(i)
#define PSTAMP(i)
#define PDIAG_END
#define DIAG_END(i)

DI u32 rfl(u32 x) { return __builtin_amdgcn_readfirstlane(x); }
DI u32 cvtpk(float lo, float hi) { f32x2 v = {lo, hi}; return __builtin_bit_cast(u32, __builtin_convertvector(v, bf16x2)); }
DI float blo(u32 v) { return __uint_as_float(v << 16); }
DI float bhi(u32 v) { return __uint_as_float(v & 0xFFFF0000u); }
DI rsrc_t mk_rsrc(const void* p, u32 bytes) { return __builtin_amdgcn_make_buffer_rsrc((void*)p, 0, bytes, 0x00020000); }
DI void bar() { asm volatile("s_barrier" ::: "memory"); }
DI void wait_lgkm0() { __builtin_amdgcn_s_waitcnt(0xC07F); }
template <int N> DI void wait_vm() { asm volatile("s_waitcnt vmcnt(%0)" ::"i"(N) : "memory"); }

template <int N> DI void dma(u32 m0, const u32* v, rsrc_t r, u32 so) {
  if constexpr (N == 4)
    asm volatile("s_mov_b32 m0, %0\n\ts_nop 3\n\t"
                 "buffer_load_dwordx4 %1, %5, %6 offen lds\n\t"
                 "buffer_load_dwordx4 %2, %5, %6 offen offset:1024 lds\n\t"
                 "buffer_load_dwordx4 %3, %5, %6 offen offset:2048 lds\n\t"
                 "buffer_load_dwordx4 %4, %5, %6 offen offset:3072 lds"
                 ::"s"(m0), "v"(v[0]), "v"(v[1]), "v"(v[2]), "v"(v[3]), "s"(r), "s"(so) : "memory");
  else if constexpr (N == 3)
    asm volatile("s_mov_b32 m0, %0\n\ts_nop 3\n\t"
                 "buffer_load_dwordx4 %1, %4, %5 offen lds\n\t"
                 "buffer_load_dwordx4 %2, %4, %5 offen offset:1024 lds\n\t"
                 "buffer_load_dwordx4 %3, %4, %5 offen offset:2048 lds"
                 ::"s"(m0), "v"(v[0]), "v"(v[1]), "v"(v[2]), "s"(r), "s"(so) : "memory");
  else
    asm volatile("s_mov_b32 m0, %0\n\ts_nop 3\n\t"
                 "buffer_load_dwordx4 %1, %3, %4 offen lds\n\t"
                 "buffer_load_dwordx4 %2, %3, %4 offen offset:1024 lds"
                 ::"s"(m0), "v"(v[0]), "v"(v[1]), "s"(r), "s"(so) : "memory");
}
template <int N> DI void dma_sc1(u32 m0, u32 v, rsrc_t r, u32 so) {
  if constexpr (N == 4)
    asm volatile("s_mov_b32 m0, %0\n\ts_nop 3\n\t"
                 "buffer_load_dwordx4 %1, %2, %3 offen sc1 lds\n\t"
                 "buffer_load_dwordx4 %1, %2, %3 offen offset:1024 sc1 lds\n\t"
                 "buffer_load_dwordx4 %1, %2, %3 offen offset:2048 sc1 lds\n\t"
                 "buffer_load_dwordx4 %1, %2, %3 offen offset:3072 sc1 lds"
                 ::"s"(m0), "v"(v), "s"(r), "s"(so) : "memory");
  else
    asm volatile("s_mov_b32 m0, %0\n\ts_nop 3\n\t"
                 "buffer_load_dwordx4 %1, %2, %3 offen sc1 lds\n\t"
                 "buffer_load_dwordx4 %1, %2, %3 offen offset:1024 sc1 lds"
                 ::"s"(m0), "v"(v), "s"(r), "s"(so) : "memory");
}

DI float xsum16(float v) { const auto r = __builtin_amdgcn_permlane16_swap(__float_as_uint(v), __float_as_uint(v), false, false); return __uint_as_float(r[0]) + __uint_as_float(r[1]); }
DI float xsum32(float v) { const auto r = __builtin_amdgcn_permlane32_swap(__float_as_uint(v), __float_as_uint(v), false, false); return __uint_as_float(r[0]) + __uint_as_float(r[1]); }

template <int A, int B, class F> DI void static_for(F f) {
  if constexpr (A < B) { f(std::integral_constant<int, A>{}); static_for<A + 1, B>(f); }
}

DI void pull_lines(u32 m0, u32 v, rsrc_t r, u32 so) {
  asm volatile("s_mov_b32 m0, %0\n\ts_nop 3\n\tbuffer_load_dword %1, %2, %3 offen lds" ::"s"(m0), "v"(v), "s"(r), "s"(so) : "memory");
}

DI int kstep(int t, int n, int phi) {
  int k = t;
  if ((t & 3) == phi) { k = t + 4; if (k >= n) k = phi; }
  return k;
}

DI int kstep2(int t, int n, int phi, int side) {
  const int k = kstep(t, n, phi), k2 = k ^ (side << 2);
  return k2 < n ? k2 : k;
}

constexpr int NST = 4;
constexpr int SLOT = 16384;
constexpr int QOFF = NST * SLOT;
constexpr int P3 = 64 * 448;
constexpr int Q3OFF = 3 * P3;
constexpr int TOFF = Q3OFF + 3 * SLOT;
constexpr int COFF = TOFF + 4096;
constexpr int XOFF = COFF + 4096;
constexpr int LDS_BYTES = XOFF + 16384;

enum { M_ROW = 0, M_TILED = 1, M_TR448 = 2, M_TR256 = 3 };

template <int PT, int PMODE, int QMODE>
struct Consumer {
  static constexpr int NS = PMODE == M_TR448 ? 3 : NST, QB = PMODE == M_TR448 ? Q3OFF : QOFF;
  const LAS char* pb[6];
  const LAS char* qb[4];
  bf16x8 fp[2][PT], fq[2][4];

  DI void init(const LAS char* sm, int wP, int wQ, int lane, int prot = 0, int qrot = 0) {
    const int r = lane & 15, g = lane >> 4;
    if constexpr (PMODE == M_ROW) {
      const int a = (64 * wP + r) * 128 + ((g ^ (r >> 1)) * 16);
      pb[0] = sm + (a + prot * 4096); pb[1] = sm + ((a ^ 64) + prot * 4096);
      pb[2] = sm + (a + 4096 - prot * 4096); pb[3] = sm + ((a ^ 64) + 4096 - prot * 4096);
    } else if constexpr (PMODE == M_TILED) {
      pb[0] = sm + (4 * wP * 1024 + lane * 16);
    } else if constexpr (PMODE == M_TR448) {
      const int b = g & 1, a = (4 * g + (r >> 2)) * 448 + (r & 3) * 8;
      pb[0] = sm + (a + (8 * wP + b) * 32);
      pb[1] = sm + (a + (8 * wP - b) * 32);
      pb[2] = sm + (a + ((6 + wP) ^ b) * 32);
#pragma unroll
      for (int i = 0; i < 3; i++) { pb[3 + i] = pb[i] + 2 * P3; asm volatile("" : "+v"(pb[3 + i])); }
    } else {
      const int k = 4 * g + (r >> 2), a = k * 256 + (r & 3) * 8;
#pragma unroll
      for (int i = 0; i < 4; i++) pb[i] = sm + (a + (((4 * wP + i) ^ (k & 7)) * 32));
    }
    if constexpr (QMODE == M_ROW) {
      const int a = QOFF + (64 * wQ + r) * 128 + ((g ^ (r >> 1)) * 16);
      qb[0] = sm + (a + qrot * 4096); qb[1] = sm + ((a ^ 64) + qrot * 4096);
      qb[2] = sm + (a + 4096 - qrot * 4096); qb[3] = sm + ((a ^ 64) + 4096 - qrot * 4096);
      asm volatile("" : "+v"(qb[0]), "+v"(qb[1]), "+v"(qb[2]), "+v"(qb[3]));
    } else {
      qb[0] = sm + (QB + 4 * wQ * 1024 + lane * 16);
      asm volatile("" : "+v"(qb[0]));
    }
  }
  static DI bf16x8 tr2(const LAS char* p, int second) {
    s16x4 a = __builtin_amdgcn_ds_read_tr16_b64_v4i16((LAS s16x4*)p);
    s16x4 b = __builtin_amdgcn_ds_read_tr16_b64_v4i16((LAS s16x4*)(p + second));
    return __builtin_bit_cast(bf16x8, __builtin_shufflevector(a, b, 0, 1, 2, 3, 4, 5, 6, 7));
  }
  template <int S, int KS> DI bf16x8 ldp(int i) {
    if constexpr (PMODE == M_ROW) return *(const LAS bf16x8*)(pb[KS + (i & 2)] + (S * SLOT + (i & 1) * 2048));
    else if constexpr (PMODE == M_TILED) return *(const LAS bf16x8*)(pb[0] + (S * SLOT + KS * 8192 + i * 1024));
    else if constexpr (PMODE == M_TR448)
      return tr2(pb[(S == 2 ? 3 : 0) + (i == 6 ? 2 : i & 1)] + ((S & 1) * P3 + KS * 32 * 448 + (i == 6 ? 0 : i * 32)), 16 * 448);
    else return tr2(pb[i] + (S * SLOT + KS * 32 * 256), 16 * 256);
  }
  template <int S, int KS> DI bf16x8 ldq(int j) {
    if constexpr (QMODE == M_ROW) return *(const LAS bf16x8*)(qb[KS + (j & 2)] + (S * SLOT + (j & 1) * 2048));
    else return *(const LAS bf16x8*)(qb[0] + (S * SLOT + KS * 8192 + j * 1024));
  }
  template <int S, int KS, int QN> DI void load() {
    fq[KS][0] = ldq<S, KS>(0);
#pragma unroll
    for (int i = 0; i < PT; i++) fp[KS][i] = ldp<S, KS>(i);
#pragma unroll
    for (int j = 1; j < QN; j++) fq[KS][j] = ldq<S, KS>(j);
  }
  template <int KS, int QN, bool ZERO = false> DI void mma(f32x4 (&acc)[PT][4]) {
#pragma unroll
    for (int n = 0; n < PT * QN; n++) {
      const int j = n / PT, i = (j & 1) ? PT - 1 - n % PT : n % PT;
      acc[i][j] = __builtin_amdgcn_mfma_f32_16x16x32_bf16(fp[KS][i], fq[KS][j], ZERO ? (f32x4){0.f, 0.f, 0.f, 0.f} : acc[i][j], 0, 0, 0);
    }
  }
  template <int KS> DI void qsums(f32x4 (&sd)[2], bf16x8 ones) {
#pragma unroll
    for (int i = 0; i < 2; i++) sd[i] = __builtin_amdgcn_mfma_f32_16x16x32_bf16(ones, fq[KS][i], sd[i], 0, 0, 0);
  }
  template <int NM, int QN> static DI void interleave() {
    constexpr int NR = PT * (PMODE >= M_TR448 ? 2 : 1) + QN, G = K_GAPS(NM, NR);
    static_for<0, G>([](auto ic) {
      __builtin_amdgcn_sched_group_barrier(0x8, 1, 0);
      __builtin_amdgcn_sched_group_barrier(0x100, NR / G + (ic.value < NR % G ? 1 : 0), 0);
    });
    __builtin_amdgcn_sched_group_barrier(0x8, NM - G, 0);
    __builtin_amdgcn_sched_barrier(0);
  }
  template <int S, int QC, int QN> DI void step(f32x4 (&acc)[PT][4]) {
    load<S, 1, QC>(); mma<0, QC>(acc); interleave<PT * QC, QC>();
    wait_lgkm0(); bar();
    __builtin_amdgcn_sched_barrier(0);
    load<(S + 1) % NS, 0, QN>(); mma<1, QC>(acc); interleave<PT * QC, QN>();
  }
  template <int S, int LS, bool ZERO, class W> DI void half_with(f32x4 (&acc)[PT][4], W w) {
    static_for<0, PT * 4>([&](auto nc) {
      constexpr int n = nc.value, j = n / PT, i = (j & 1) ? PT - 1 - n % PT : n % PT;
      acc[i][j] = __builtin_amdgcn_mfma_f32_16x16x32_bf16(fp[1 - LS][i], fq[1 - LS][j], ZERO ? (f32x4){0.f, 0.f, 0.f, 0.f} : acc[i][j], 0, 0, 0);
      if constexpr (S >= 0) {
        if constexpr (n == 0) fq[LS][0] = ldq<S, LS>(0);
        else if constexpr (n <= PT) fp[LS][n - 1] = ldp<S, LS>(n - 1);
        else if constexpr (n < PT + 4) fq[LS][n - PT] = ldq<S, LS>(n - PT);
      }
      w(nc);
      __builtin_amdgcn_sched_barrier(0);
    });
  }
  template <int S, int QC> DI void last(f32x4 (&acc)[PT][4]) {
    load<S, 1, QC>(); mma<0, QC>(acc); interleave<PT * QC, QC>();
    wait_lgkm0();
    mma<1, QC>(acc);
  }
  template <int QC, int QN> DI void step4(f32x4 (&acc)[PT][4]) {
    step<0, QC, QC>(acc); step<1, QC, QC>(acc); step<2, QC, QC>(acc); step<3, QC, QN>(acc);
  }
  static DI void zero(f32x4 (&acc)[PT][4]) {
#pragma unroll
    for (int i = 0; i < PT; i++)
#pragma unroll
      for (int j = 0; j < 4; j++) { acc[i][j] = (f32x4){0.f, 0.f, 0.f, 0.f}; asm volatile("" : "+v"(acc[i][j])); }
  }
  DI void first() { load<0, 0, 4>(); }
};

DI u32 row_src(int imgrow, int srcrow, int lane, int ldbytes) {
  return srcrow * ldbytes + (((lane & 7) ^ ((imgrow >> 1) & 7)) * 16);
}
DI void store_sc1(rsrc_t r, u32 off, u32x4 v) { __builtin_amdgcn_raw_buffer_store_b128(v, r, off, 0, 16); }

DI void xch_send(char* x, const f32x4 (&acc)[4][4], int wid, int lane) {
  rsrc_t r = mk_rsrc(x + ((size_t)blockIdx.x * 4 + wid) * 4096, 4096);
#pragma unroll
  for (int jj = 0; jj < 2; jj++)
#pragma unroll
    for (int u = 0; u < 2; u++) {
      const f32x4 a = acc[2 * u][2 + jj], b = acc[2 * u + 1][2 + jj];
      u32x4 v = {cvtpk(a[0], a[1]), cvtpk(a[2], a[3]), cvtpk(b[0], b[1]), cvtpk(b[2], b[3])};
      store_sc1(r, lane * 16 + (jj * 2 + u) * 1024, v);
    }
}
DI void xch_request(rsrc_t r, u32x4 (&v)[4], int lane) {
  asm volatile("s_nop 4\n\tbuffer_load_dwordx4 %0, %4, %5, 0 offen sc1\n\t"
               "buffer_load_dwordx4 %1, %4, %5, 0 offen offset:1024 sc1\n\t"
               "buffer_load_dwordx4 %2, %4, %5, 0 offen offset:2048 sc1\n\t"
               "buffer_load_dwordx4 %3, %4, %5, 0 offen offset:3072 sc1"
               : "=&v"(v[0]), "=&v"(v[1]), "=&v"(v[2]), "=&v"(v[3]) : "v"(lane * 16), "s"(r) : "memory");
}
DI void xch_finish(u32x4 (&v)[4]) { asm volatile("s_waitcnt vmcnt(0)" : "+v"(v[0]), "+v"(v[1]), "+v"(v[2]), "+v"(v[3])::"memory"); }

enum { FL_X2 = 0, FL_X4 = 1, FL_ACK = 2, FL_TICK = 3, FL_CNT = 4 };
enum { CN_H = 0, CN_AB = 128, CN_SX = 256, CN_RA = 384, CN_RB = 512 };
constexpr u32 NOWHERE = 0xFFFFFF00u;
DI void flag_set(char* ws, int which, int tt, int cell) {
  wait_vm<0>();
  if ((threadIdx.x & 63) == 0)
    __hip_atomic_store((u32*)(ws + WS_FL + ((size_t)(which * 16 + tt) * 64 + cell) * 64), 1u, __ATOMIC_RELAXED, __HIP_MEMORY_SCOPE_AGENT);
}
DI void flag_request(rsrc_t r, u32 off, u32& v) {
  asm volatile("s_nop 4\n\tbuffer_load_dword %0, %1, %2, 0 offen sc1" : "=&v"(v) : "v"(off), "s"(r) : "memory");
}
DI void flag_wait(rsrc_t r, u32 off, u32& v) {
  asm volatile("" : "+v"(v)::"memory");
#pragma unroll 1
  for (int spin = 0; spin < K_SPIN; spin++) {
    if (__builtin_amdgcn_ballot_w64(v != 1u) == 0) break;
    __builtin_amdgcn_s_sleep(K_SLEEP);
    asm volatile("s_nop 4\n\tbuffer_load_dword %0, %1, %2, 0 offen sc1\n\ts_waitcnt vmcnt(0)" : "=&v"(v) : "v"(off), "s"(r) : "memory");
  }
}

DI void word_store(rsrc_t r, u32 off, u32 v) { __builtin_amdgcn_raw_buffer_store_b32(v, r, off, 0, 16); }
DI void count_up(char* ws, u32 off) {
  if ((threadIdx.x & 63) == 0) __hip_atomic_fetch_add((u32*)(ws + WS_FL + off), 1u, __ATOMIC_RELAXED, __HIP_MEMORY_SCOPE_AGENT);
}
template <class Done> DI void watch(rsrc_t r, u32 off, u32& v, Done done, u32 tickoff, u32& tick) {
#pragma unroll 1
  for (int spin = 0; spin < K_SPIN; spin++) {
    if (__builtin_amdgcn_ballot_w64(!done(v)) == 0) break;
    if ((spin & 63) == 63) { tick++; word_store(r, tickoff, tick); }
    __builtin_amdgcn_s_sleep(K_SLEEP);
    asm volatile("s_nop 4\n\tbuffer_load_dword %0, %1, %2, 0 offen sc1\n\ts_waitcnt vmcnt(0)" : "=&v"(v) : "v"(off), "s"(r) : "memory");
  }
}
__global__ __launch_bounds__(512, 2) void fused_kernel(const u16*, const u16*, const u16*, const u16*, const u16*, const u16*, const u16*,
                                                       const u16*, const u16*, char*, u16* DIAG_ARG) {
  u32x16 ka; u32x4 kb4; u32x2 kc;
  asm volatile("s_load_dwordx16 %0, %3, 0x0\n\ts_load_dwordx4 %1, %3, 0x40\n\ts_load_dwordx2 %2, %3, 0x50"
               : "=&s"(ka), "=&s"(kb4), "=&s"(kc) : "s"(__builtin_amdgcn_kernarg_segment_ptr()));
  __builtin_amdgcn_sched_barrier(0);
  extern __shared__ __attribute__((aligned(16))) char smem[];
  DIAG_BEGIN
  const int lane0 = threadIdx.x & 63, wid = rfl(threadIdx.x >> 6);
  const int xcd = blockIdx.x & 7, loc = blockIdx.x >> 3, li = loc >> 2, lj = loc & 3;
  const int half = xcd >> 2, et = ((xcd >> 1) & 1) * 4 + lj, tt = (xcd & 1) * 8 + li, ft = (xcd >> 1) * 4 + lj;
  LAS char* sm = (LAS char*)smem;
  LAS float* tabA = (LAS float*)(sm + TOFF);
  LAS float* tabB = tabA + 128;
  LAS float* tabC = tabA + 256;
  LAS float* tabBias = tabA + 512;
  u32 vp1[4], vq1[4];
#pragma unroll
  for (int i = 0; i < 4; i++) {
    const int row = 32 * (wid & 3) + 8 * i + (lane0 >> 3), u = (row >> 4) & 1, ii = row & 15;
    vp1[i] = row_src(row, 32 * (wid & 3) + 8 * (ii >> 2) + 4 * u + (ii & 3), lane0, D_ * 2) + 4096 - i * 1024;
    vq1[i] = row_src(row, row, lane0, D_ * 2) - i * 1024;
  }
  asm volatile("" : "+v"(vp1[0]), "+v"(vp1[1]), "+v"(vp1[2]), "+v"(vp1[3]), "+v"(vq1[0]), "+v"(vq1[1]), "+v"(vq1[2]), "+v"(vq1[3]));
  __builtin_amdgcn_sched_barrier(0);
  asm volatile("s_waitcnt lgkmcnt(0)" : "+s"(ka), "+s"(kb4), "+s"(kc));
  auto ptr = [](u32 lo, u32 hi) { return (u16*)(GAS u16*)(((u64)hi << 32) | lo); };
  const u16 *x = ptr(ka[0], ka[1]), *cre = ptr(ka[2], ka[3]), *cim = ptr(ka[4], ka[5]), *dre = ptr(ka[6], ka[7]), *dim = ptr(ka[8], ka[9]);
  const u16 *w1 = ptr(ka[10], ka[11]), *b1 = ptr(ka[12], ka[13]), *w2 = ptr(ka[14], ka[15]), *b2 = ptr(kb4[0], kb4[1]);
  char* ws = (char*)ptr(kb4[2], kb4[3]);
  u16* out = ptr(kc[0], kc[1]);
  if (__builtin_expect(wid >= 4, 1)) {
    if (K_PPRIO) __builtin_amdgcn_s_setprio(K_PPRIO);
    const int p = wid - 4, phi = (li + lj) & 3;
    const u32 lds0 = (u32)(size_t)smem, ldsb = lds0 + p * 4096;
    rsrc_t rall = mk_rsrc(ws + WS_FL, 5 * 65536);
    rsrc_t rp1 = mk_rsrc((const char*)x + (size_t)tt * 128 * D_ * 2 - 4096, 128 * D_ * 2 + 4096);
    rsrc_t rq1_ = mk_rsrc((half ? dim : dre) + (size_t)et * 128 * D_, 128 * D_ * 2);
    const int side1 = (xcd >> 1) ^ (xcd & 1);
    dma<4>(ldsb, vp1, rp1, kstep2(0, 16, phi, side1) * 128);
    dma<4>(ldsb + QOFF, vq1, rq1_, kstep2(0, 16, phi, side1) * 128);
    int lane = lane0;
    asm volatile("" : "+v"(lane)::"memory");
    __builtin_amdgcn_sched_barrier(0);
    const u32 vlin = p * 4096 + lane * 16;
    const u32 grpcell = ((lane & 16) ? tt * 64 + (lane & 15) * 4 : (lane & 15) * 64 + ft * 4) * 64;
    const u32 tickoff = lane == 0 ? FL_TICK * 65536 + (tt * 64 + ft * 4) * 64 : NOWHERE;
    const u32 ackoff = lane == 0 ? FL_ACK * 65536 + (tt * 64 + ft * 4) * 64 : NOWHERE;
    const u32 rowcnt = FL_CNT * 65536 + tt * 4096, colcnt = FL_CNT * 65536 + ft * 4096;
    const int pt = threadIdx.x - 256;
    u32 base, fv, tick;
    const int side2 = (xcd >> 1) & 1;
    auto skip = [&](int r) { return r + (r >= 2 * tt ? 2 : 0); };
    auto g2k = [&](int q) { return q < 2 ? 2 * tt + q : skip(kstep2(q - 2, 26, phi, side2)); };
    rsrc_t rco = mk_rsrc((const char*)(half ? cim : cre) + (size_t)tt * 128 * S_ * 2 - 4096, 128 * S_ * 2 + 4096);
    u32 vc[4];
#pragma unroll
    for (int i = 0; i < 4; i++) {
      const int row = 32 * p + 8 * i + (lane >> 3);
      vc[i] = row_src(row, row ^ (half * 32), lane, S_ * 2) + 4096 - i * 1024;
    }
    auto g2q = [&](int q) { dma<4>(ldsb + QOFF + (q & 3) * SLOT, vc, rco, g2k(q) * 128); };
    {
      u32 vx[2];
#pragma unroll
      for (int i = 0; i < 2; i++) {
        const int row = 16 * p + 8 * i + (lane >> 3);
        vx[i] = row_src(row, 64 * (row >> 5) + 32 * half + (row & 31), lane, D_ * 2) + 4096 - i * 1024;
      }
      auto issue = [&](int g) {
        const int k = kstep2(g, 16, phi, side1);
        const u32 m = ldsb + (g & 3) * SLOT;
        dma<4>(m, vp1, rp1, k * 128);
        dma<4>(m + QOFF, vq1, rq1_, k * 128);
      };
      auto run = [&](int g0, int g1) {
#pragma unroll 1
        for (int g = g0; g < g1; g++) { issue(g); wait_vm<16>(); bar(); }
      };
      flag_request(rall, p == 0 ? FL_TICK * 65536 + grpcell : NOWHERE, base);
      issue(1); issue(2);
      {
        u32 z = lane < 8 ? (lane >> 2) * 65536 + (tt * 64 + ft * 4 + (lane & 3)) * 64 : FL_ACK * 65536 + (tt * 64 + ft * 4) * 64;
        if (lane == 9) z = tt == 0 ? colcnt + CN_AB : NOWHERE;
        if (lane >= 10) z = ft == 0 ? rowcnt + (lane == 10 ? CN_H : (lane - 9) * 128) : NOWHERE;
        word_store(rall, p == 0 && lane < 14 ? z : NOWHERE, 0u);
      }
      wait_vm<18>();
      PSTAMP(1)
      bar();
      const int js = (phi + 1) & 3;
      auto sums = [&]() {
        const int c = lane & 15, g = lane >> 4, e = c & 3;
        const u32x4 ones4 = {0x3F803F80u, 0x3F803F80u, 0x3F803F80u, 0x3F803F80u};
        const bf16x8 ones = __builtin_bit_cast(bf16x8, ones4);
        const int a = js * SLOT + (32 * p + c) * 128 + ((g ^ (c >> 1)) * 16);
        rsrc_t rx = mk_rsrc(ws + WS_SX + (size_t)(tt * 16 + (js ^ (side1 << 2))) * 1024 + p * 256, 256);
#pragma unroll
        for (int i = 0; i < 2; i++) {
          const bf16x8 x0 = *(const LAS bf16x8*)(sm + (a + i * 2048)), x1 = *(const LAS bf16x8*)(sm + ((a ^ 64) + i * 2048));
          const f32x4 z = {0.f, 0.f, 0.f, 0.f};
          const f32x4 sx = __builtin_amdgcn_mfma_f32_16x16x32_bf16(x1, ones, __builtin_amdgcn_mfma_f32_16x16x32_bf16(x0, ones, z, 0, 0, 0), 0, 0, 0);
          const f32x4 sq = __builtin_amdgcn_mfma_f32_16x16x32_bf16(x1, x1, __builtin_amdgcn_mfma_f32_16x16x32_bf16(x0, x0, z, 0, 0, 0), 0, 0, 0);
          const float q = e == 0 ? sq[0] : e == 1 ? sq[1] : e == 2 ? sq[2] : sq[3];
          const float t = e == 0 ? sx[0] : e == 1 ? sx[1] : e == 2 ? sx[2] : sx[3];
          __builtin_amdgcn_raw_buffer_store_b64((u32x2){__float_as_uint(t), __float_as_uint(q)}, rx, (c >> 2) == g ? (16 * i + c) * 8 : NOWHERE, 0, 16);
        }
      };
      issue(3);
      if (js == 0) sums();
      asm volatile("s_waitcnt vmcnt(16)" : "+v"(base)::"memory");
      tick = __builtin_amdgcn_readlane(base, tt) + 1;
      bar();
#pragma unroll 1
      for (int g = 2; g < 6; g++) {
        issue(g + 2);
        if (g == js + 1) sums();
        wait_vm<16>();
        if (g == 3 && p == 0) word_store(rall, tickoff, tick);
        bar();
      }
      issue(8); wait_vm<16>(); bar();
      flag_request(rall, p == 0 ? FL_TICK * 65536 + grpcell : NOWHERE, fv);
      issue(9); wait_vm<16>(); bar();
      issue(10);
      asm volatile("s_waitcnt vmcnt(16)" : "+v"(fv)::"memory");
      PSTAMP(14)
      if (p == 0) {
        watch(rall, FL_TICK * 65536 + grpcell, fv, [&](u32 w) { return w != base; }, tickoff, tick);
        count_up(ws, rowcnt + CN_SX);
        word_store(rall, ackoff, 1u);
      }
      PSTAMP(15)
      bar();
      run(11, 14);
      dma<2>(lds0 + XOFF + p * 2048, vx, rp1, et * 256);
      dma<2>(lds0 + XOFF + 8192 + p * 2048, vx, rp1, et * 256 + 128);
      flag_request(rall, p == 0 ? rowcnt + CN_SX : NOWHERE, fv);
      issue(14); wait_vm<21>(); bar();
      issue(15);
      asm volatile("s_waitcnt vmcnt(16)" : "+v"(fv)::"memory");
      PSTAMP(9)
      if (p == 0) watch(rall, rowcnt + CN_SX, fv, [&](u32 w) { return w >= 16u; }, tickoff, tick);
      PSTAMP(10)
      bar();
      u32x2 sv[8];
      rsrc_t rsx = mk_rsrc(ws + WS_SX + (size_t)tt * 16384, 16384);
#pragma unroll
      for (int i = 0; i < 8; i++)
        asm volatile("s_nop 4\n\tbuffer_load_dwordx2 %0, %1, %2, %3 offen sc1" : "=&v"(sv[i]) : "v"((pt & 1) * 8192 + (pt >> 1) * 8), "s"(rsx), "s"(i * 1024) : "memory");
      g2q(0); wait_vm<20>(); bar();
      g2q(1); wait_vm<16>(); bar();
      g2q(2);
      asm volatile("s_waitcnt vmcnt(12)" : "+v"(sv[0]), "+v"(sv[1]), "+v"(sv[2]), "+v"(sv[3]), "+v"(sv[4]), "+v"(sv[5]), "+v"(sv[6]), "+v"(sv[7])::"memory");
      float a = 0.f, q = 0.f;
#pragma unroll
      for (int i = 0; i < 8; i++) { a += __uint_as_float(sv[i][0]); q += __uint_as_float(sv[i][1]); }
      a += __builtin_bit_cast(float, __builtin_amdgcn_update_dpp(0, __builtin_bit_cast(int, a), 0xB1, 0xF, 0xF, true));
      q += __builtin_bit_cast(float, __builtin_amdgcn_update_dpp(0, __builtin_bit_cast(int, q), 0xB1, 0xF, 0xF, true));
      const float mean = a * (1.0f / D_), var = fmaxf(q * (1.0f / D_) - mean * mean, 0.f);
      const float rstd = (half ? -1.f : 1.f) * __builtin_amdgcn_rsqf(var + LN_EPS);
      tabA[(pt & 1) * 128 + (pt >> 1)] = (pt & 1) ? -mean * rstd : rstd;
      wait_lgkm0();
    }
    {
      rsrc_t rp = mk_rsrc(ws + WS_AB + (size_t)et * 32 * 16384, 9 * 32 * 16384);
      rsrc_t rq0 = mk_rsrc((const char*)cre + (size_t)tt * 128 * S_ * 2 - 4096, 128 * S_ * 2 + 4096);
      rsrc_t rq1 = mk_rsrc((const char*)cim + (size_t)tt * 128 * S_ * 2 - 4096, 128 * S_ * 2 + 4096);
      u32 vh[2];
#pragma unroll
      for (int i = 0; i < 2; i++) {
        const int row = 64 * (p >> 1) + 16 * (p & 1) + 8 * i + (lane >> 3);
        vh[i] = row_src(row, row ^ (half * 32), lane, S_ * 2) + 4096 - i * 1024;
      }
      const u32 ldsh = lds0 + QOFF + (p >> 1) * 8192 + (p & 1) * 2048;
      auto fp = [&](int q) { dma_sc1<4>(ldsb + (q & 3) * SLOT, vlin, rp, (half * 256 + g2k(q)) * 16384); };
      auto full = [&](int q) { fp(q); g2q(q); };
      auto halfs = [&](int g) {
        const int k = kstep2(g, 8, phi, side2), term = k >> 2, ks = skip(26 + (k & 3));
        dma_sc1<4>(ldsb + (g & 3) * SLOT, vlin, rp, (term * 256 + ks) * 16384);
        if (term) dma<2>(ldsh + (g & 3) * SLOT, vh, rq1, ks * 128);
        else dma<2>(ldsh + (g & 3) * SLOT, vh, rq0, ks * 128);
      };
      bar();
      wait_vm<8>(); bar();
      g2q(3);
      rsrc_t rw = mk_rsrc((const char*)w1 + (size_t)(tt * 64 + p * 16) * F_ * 2 + ft * 448, 15 * F_ * 2 + 448);
      u32x4 v[8];
      const int l28 = lane < 28 ? lane : lane < 56 ? lane - 28 : 27;
      const u32 vo = (lane >= 28 ? F_ * 2 : 0) + l28 * 16;
      auto csload = [&]() {
#pragma unroll
      for (int i = 0; i < 8; i++)
        asm volatile("s_nop 4\n\tbuffer_load_dwordx4 %0, %1, %2, %3 offen" : "=&v"(v[i]) : "v"(vo), "s"(rw), "s"(i * 2 * F_ * 2) : "memory");
      };
      if (K_CSP == 0) csload();
      wait_vm<K_CSP == 0 ? 16 : 8>(); bar();
      if (K_CSP == 1) csload();
      bar();
      PSTAMP(12)
      if (p == 0) {
        count_up(ws, colcnt + CN_AB);
        fv = 0;
        watch(rall, colcnt + CN_AB, fv, [&](u32 w) { return w >= 16u; }, tickoff, tick);
        word_store(rall, ackoff, 2u);
      }
      bar();
      PSTAMP(2)
      fp(2); fp(3); full(4);
      asm volatile("s_waitcnt vmcnt(12)" : "+v"(v[0]), "+v"(v[1]), "+v"(v[2]), "+v"(v[3]), "+v"(v[4]), "+v"(v[5]), "+v"(v[6]), "+v"(v[7])::"memory");
      bar();
      full(5);
      {
        float s[8] = {0, 0, 0, 0, 0, 0, 0, 0};
#pragma unroll
        for (int i = 0; i < 8; i++)
#pragma unroll
          for (int e = 0; e < 4; e++) { s[2 * e] += blo(v[i][e]); s[2 * e + 1] += bhi(v[i][e]); }
#pragma unroll
        for (int e = 0; e < 8; e++) s[e] += __shfl(s[e], lane + 28);
        if (lane < 28) {
          LAS float* d = (LAS float*)(sm + COFF) + p * 224 + lane * 8;
          *(LAS f32x4*)d = (f32x4){s[0], s[1], s[2], s[3]};
          *(LAS f32x4*)(d + 4) = (f32x4){s[4], s[5], s[6], s[7]};
        }
      }
      wait_vm<16>(); wait_lgkm0(); bar();
      full(6);
      if (p == 0 && lane < 56) {
        const LAS f32x4* d = (const LAS f32x4*)(sm + COFF) + lane;
        const f32x4 t = (d[0] + d[56]) + (d[112] + d[168]);
        store_sc1(mk_rsrc(ws + WS_CS + (size_t)tt * F_ * 4 + ft * 896, 896), lane * 16, __builtin_bit_cast(u32x4, t));
      }
      wait_vm<16>(); bar();
      full(7); wait_vm<16>(); bar();
      full(8); wait_vm<16>();
      if (p == 0) count_up(ws, colcnt + CN_AB);
      bar();
#pragma unroll 1
      for (int q = 9; q < 26; q++) { full(q); wait_vm<16>(); bar(); }
      const u32 lastoff = lane < 32 ? FL_ACK * 65536 + grpcell : colcnt + CN_AB;
      const u32 lastmin = lane >= 32 ? 32u : lane == 16 + (ft ^ 8) ? 2u : 1u;
      flag_request(rall, p == 0 ? lastoff : NOWHERE, fv);
      full(26); wait_vm<16>(); bar();
      full(27);
      asm volatile("s_waitcnt vmcnt(16)" : "+v"(fv)::"memory");
      if (p == 0) watch(rall, lastoff, fv, [&](u32 w) { return w >= lastmin; }, tickoff, tick);
      bar();
      halfs(0); wait_vm<14>(); bar();
#pragma unroll 1
      for (int g = 1; g < 8; g++) { halfs(g); wait_vm<12>(); bar(); }
    }
    rsrc_t rp4 = mk_rsrc(w2 + et * 128, F_ * D_ * 2 - et * 256);
    u32 vp4[4];
#pragma unroll
    for (int i = 0; i < 4; i++) {
      const int k = 16 * (p & 1) + 4 * i + (lane >> 4);
      vp4[i] = k * (D_ * 2) + (((lane & 15) ^ ((k & 7) << 1)) * 16) - i * 1024;
    }
    auto g4blk = [&](int q) {
      const int r = q < 10 ? kstep2(q - 3, 7, phi, xcd & 1) : kstep2(q - 10, 14, phi, xcd & 1);
      const int i = q < 10 ? r : r >> 1, pr = q < 3 ? q : q < 10 ? 0 : 1 + (r & 1);
      return 7 * (8 * half + (q < 3 ? et : i + (i >= et ? 1 : 0))) + pr + 4 * (p >> 1);
    };
    auto g4p = [&](int q) { dma<4>(ldsb + ((q + 2) & 3) * SLOT, vp4, rp4, g4blk(q) * (32 * D_ * 2)); };
    {
      rsrc_t rp = mk_rsrc((const char*)w1 + ft * 448 - 4096, D_ * F_ * 2 - ft * 448 + 4096);
      rsrc_t rq = mk_rsrc(ws + WS_H + (size_t)tt * 32 * 8192, 32 * 8192);
      u32 vp[7];
#pragma unroll
      for (int i = 0; i < 7; i++) {
        const int o = (p * 7 + i) * 64 + lane, k = o / 28, ch = o % 28;
        vp[i] = k * (F_ * 2) + ((ch ^ (((k >> 2) & 1) << 1)) * 16) + 4096 - (i & 3) * 1024;
      }
      rsrc_t rw = mk_rsrc(w2 + (size_t)(half * 1792 + tt * 112 + 28 * p) * D_ + et * 128, 27 * D_ * 2 + 256);
      const u32 vw = (lane >> 1) * (D_ * 2) + (lane & 1) * 128;
      auto pa = [&](int g, int sl) {
        const u32 so = kstep(g, 16, phi) * (64 * F_ * 2), m = lds0 + sl * P3 + p * 7168;
        dma<4>(m, vp, rp, so);
        dma<3>(m + 4096, vp + 4, rp, so);
      };
      auto q = [&](int g, int sl) { dma_sc1<4>(ldsb + Q3OFF + sl * SLOT, vlin, rq, kstep(g, 16, phi) * 16384); };
      wait_vm<6>(); bar();
      wait_vm<0>(); bar();
      bar();
      PSTAMP(13)
      if (p == 0) count_up(ws, rowcnt + CN_H);
      pa(0, 0); pa(1, 1);
      if (K_LEAD & 1) pull_lines(lds0 + COFF + 3584, p == 1 ? (128 + 16 * li + (lane >> 2)) * (F_ * 2) + (lane & 3) * 128 + 4096 : NOWHERE, rp, 0);
      rsrc_t rs = mk_rsrc(ws + WS_ST + (size_t)tt * 16384, 16384);
      rsrc_t rc = mk_rsrc(ws + WS_CS + ft * 896, 15 * F_ * 4 + 896);
      u32x2 sv[8];
      u32 cv[16], bv;
#pragma unroll
      for (int i = 0; i < 16; i++)
        asm volatile("s_nop 4\n\tbuffer_load_dword %0, %1, %2, %3 offen sc1" : "=&v"(cv[i]) : "v"(min(pt, 223) * 4), "s"(rc), "s"(i * F_ * 4) : "memory");
      asm volatile("s_nop 4\n\tbuffer_load_ushort %0, %1, %2, 0 offen" : "=&v"(bv) : "v"(min(pt, 223) * 2), "s"(mk_rsrc(b1 + ft * 224, 448)) : "memory");
      if (p == 0) { fv = 0; watch(rall, rowcnt + CN_H, fv, [&](u32 w) { return w >= 16u; }, NOWHERE, tick); }
      bar();
      PSTAMP(3)
      q(0, 0); q(1, 1);
      wait_vm<4>();
      PSTAMP(4)
      bar();
      pa(2, 2); q(2, 2);
#pragma unroll
      for (int i = 0; i < 8; i++)
        asm volatile("s_nop 4\n\tbuffer_load_dwordx2 %0, %1, %2, %3 offen sc1" : "=&v"(sv[i]) : "v"((pt & 1) * 8192 + (pt >> 1) * 8), "s"(rs), "s"(i * 1024) : "memory");
      wait_vm<19>(); bar();
      pa(3, 0); q(3, 0); wait_vm<19>(); bar();
      pa(4, 1); q(4, 1); wait_vm<11>();
      asm volatile("" : "+v"(sv[0]), "+v"(sv[1]), "+v"(sv[2]), "+v"(sv[3]), "+v"(sv[4]), "+v"(sv[5]), "+v"(sv[6]), "+v"(sv[7]), "+v"(bv)::"memory");
      asm volatile("" : "+v"(cv[0]), "+v"(cv[1]), "+v"(cv[2]), "+v"(cv[3]), "+v"(cv[4]), "+v"(cv[5]), "+v"(cv[6]), "+v"(cv[7]), "+v"(cv[8]),
                   "+v"(cv[9]), "+v"(cv[10]), "+v"(cv[11]), "+v"(cv[12]), "+v"(cv[13]), "+v"(cv[14]), "+v"(cv[15])::"memory");
      bar();
      pa(5, 2); q(5, 2);
      {
        float s = 0.f, a = 0.f, qq = 0.f;
#pragma unroll
        for (int i = 0; i < 16; i++) s += __uint_as_float(cv[i]);
        if (pt < 224) { tabC[pt] = s; tabBias[pt] = __uint_as_float(bv << 16); }
#pragma unroll
        for (int i = 0; i < 8; i++) { a += __uint_as_float(sv[i][0]); qq += __uint_as_float(sv[i][1]); }
        a += __builtin_bit_cast(float, __builtin_amdgcn_update_dpp(0, __builtin_bit_cast(int, a), 0xB1, 0xF, 0xF, true));
        qq += __builtin_bit_cast(float, __builtin_amdgcn_update_dpp(0, __builtin_bit_cast(int, qq), 0xB1, 0xF, 0xF, true));
        const float mean = a * (1.0f / D_), var = fmaxf(qq * (1.0f / D_) - mean * mean, 0.f);
        const float rstd = __builtin_amdgcn_rsqf(var + LN_EPS);
        tabA[(pt & 1) * 128 + (pt >> 1)] = (pt & 1) ? -mean * rstd : rstd;
      }
      wait_vm<11>(); wait_lgkm0(); bar();
      int sl = 0;
#pragma unroll 1
      for (int g = 6; g < 16; g++) {
        pa(g, sl); q(g, sl);
        const int gp = K_WN > 0 ? 6 + loc % (K_WN > 0 ? K_WN : 1) : 99;
        if (g == gp) pull_lines(lds0 + COFF + 3584, vw, rw, 0);
        if ((unsigned)(g - gp) < 2u) wait_vm<12>(); else wait_vm<11>();
        bar();
        sl = sl == 2 ? 0 : sl + 1;
      }
      g4p(0); wait_vm<4>(); bar();
      g4p(1);
      bar();
    }
    {
      rsrc_t rq = mk_rsrc(ws + WS_R + (size_t)tt * 112 * 8192, 112 * 8192);
      const u32 vq0 = (4 * (p & 1) + 2 * half) * 1024 + lane * 16, vq1 = (4 * (p & 1) + 2 - 2 * half) * 1024 + lane * 16;
      auto g4q = [&](int q) {
        const u32 m = ldsb + QOFF + ((q + 2) & 3) * SLOT, so = g4blk(q) * 8192;
        dma_sc1<2>(m, vq0, rq, so);
        dma_sc1<2>(m + 2048, vq1, rq, so);
      };
      auto halfs = [&](int g) {
        const int k = kstep2(g, 8, phi, xcd & 1), blk = 7 * (2 * k + (p >> 1)) + 3;
        dma<4>(ldsb + ((g + 2) & 3) * SLOT, vp4, rp4, blk * (32 * D_ * 2));
        dma_sc1<2>(ldsb + QOFF + ((g + 2) & 3) * SLOT, vq0, rq, blk * 8192);
      };
      wait_vm<4>(); bar();
      PSTAMP(5)
      if (p == 0) count_up(ws, rowcnt + CN_RA);
      g4p(2); wait_vm<4>(); bar();
      g4p(3);
      if (K_LEAD & 2) {
        const int qq = 4 + min(lane >> 3, 2);
        const u32 b4 = g4blk(4), b5 = g4blk(5), b6 = g4blk(6), bb = qq == 4 ? b4 : qq == 5 ? b5 : b6;
        pull_lines(lds0 + COFF + 3584, (p & 1) == 0 && lane < 24 ? (bb * 32 + 4 * li + ((lane >> 1) & 3)) * (D_ * 2) + (lane & 1) * 128 : NOWHERE, rp4, 0);
      }
      wait_vm<5>();
      if (p == 0) { fv = 0; watch(rall, rowcnt + CN_RA, fv, [&](u32 w) { return w >= 16u; }, NOWHERE, tick); }
      bar();
      PSTAMP(6)
      g4q(3); g4p(4); g4q(4); g4p(5); g4q(5);
      wait_vm<16>(); bar();
      if (K_RB == 3 && p == 0) count_up(ws, rowcnt + CN_RB);
      g4p(6); g4q(6); wait_vm<16>(); bar();
      if (K_RB == 4 && p == 0) count_up(ws, rowcnt + CN_RB);
      g4p(7); g4q(7); wait_vm<16>(); bar();
      if (K_RB == 3) flag_request(rall, p == 0 ? rowcnt + CN_RB : NOWHERE, fv);
      g4p(8); g4q(8); wait_vm<16>(); bar();
      if (K_RB == 4) flag_request(rall, p == 0 ? rowcnt + CN_RB : NOWHERE, fv);
      g4p(9); g4q(9);
      asm volatile("s_waitcnt vmcnt(%1)" : "+v"(fv) : "i"(K_RB == 3 ? 16 : 8) : "memory");
      PSTAMP(7)
      if (p == 0) watch(rall, rowcnt + CN_RB, fv, [&](u32 w) { return w >= 16u; }, NOWHERE, tick);
      PSTAMP(8)
      bar();
#pragma unroll 1
      for (int q = 10; q < 24; q++) { g4p(q); g4q(q); wait_vm<16>(); bar(); }
      halfs(0); wait_vm<14>(); bar();
#pragma unroll 1
      for (int g = 1; g < 8; g++) { halfs(g); wait_vm<12>(); bar(); }
      wait_vm<6>(); bar();
      wait_vm<0>(); bar();
      PDIAG_END
    }
  } else {
    const int lane = lane0;
    const int wP = wid >> 1, wQ = wid & 1, c = lane & 15, g = lane >> 4;
    const int m0 = 4 * wQ + 2 * half, cell = ft * 4 + wid;
    {
      Consumer<4, M_ROW, M_ROW> C;
      C.init(sm, wP, wQ, lane, 0, wP);
      f32x4 acc[4][4], sd[2];
      C.zero(acc);
#pragma unroll
      for (int i = 0; i < 2; i++) sd[i] = (f32x4){0.f, 0.f, 0.f, 0.f};
      const u32x4 ones4 = {0x3F803F80u, 0x3F803F80u, 0x3F803F80u, 0x3F803F80u};
      bf16x8 ones = __builtin_bit_cast(bf16x8, ones4);
      asm volatile("" : "+v"(ones));
      bar();
      STAMP(1)
      C.first();
#define G1_HALF0(S) C.load<S, 1, 4>(); C.mma<0, 4>(acc); C.qsums<0>(sd, ones); C.interleave<18, 4>();
#define G1_STEP(S) G1_HALF0(S) wait_lgkm0(); bar(); __builtin_amdgcn_sched_barrier(0); \
      C.load<(S + 1) % NST, 0, 4>(); C.mma<1, 4>(acc); C.qsums<1>(sd, ones); C.interleave<18, 4>();
      for (int it = 0; it < 3; it++) { G1_STEP(0) G1_STEP(1) G1_STEP(2) G1_STEP(3) }
      G1_STEP(0) G1_STEP(1) G1_STEP(2) G1_HALF0(3)
      wait_lgkm0();
      C.qsums<1>(sd, ones);
      __builtin_amdgcn_sched_barrier(0);
      C.mma<1, 4>(acc);
      tabC[g < 2 ? 64 * wQ + 32 * wP + 16 * g + c : 128 + lane] = g ? sd[1][0] : sd[0][0];
      __builtin_amdgcn_sched_group_barrier(0x8, 4, 0);
      __builtin_amdgcn_sched_group_barrier(0x2 | 0x200, 8, 0);
      __builtin_amdgcn_sched_group_barrier(0x8, 12, 0);
      __builtin_amdgcn_sched_barrier(0);
      STAMP(2)
      wait_lgkm0(); bar();
      rsrc_t ro = mk_rsrc(ws + WS_AB + ((size_t)((half * 8 + et) * 32 + 2 * tt + wP) * 16 + 4 * wQ) * 1024, 12288);
      f32x4 ra[4], rb[4];
      float rs[4];
#pragma unroll
      for (int i = 0; i < 4; i++) {
        ra[i] = *(const LAS f32x4*)(tabA + 64 * wP + 16 * i + 4 * g);
        rb[i] = *(const LAS f32x4*)(tabB + 64 * wP + 16 * i + 4 * g);
        rs[i] = tabC[64 * wQ + 16 * (i ^ (2 * wP)) + c];
      }
#pragma unroll
      for (int j = 0; j < 4; j++)
#pragma unroll
        for (int u = 0; u < 2; u++) {
          const f32x4 a = ra[2 * u] * acc[2 * u][j] + rb[2 * u] * rs[j], b = ra[2 * u + 1] * acc[2 * u + 1][j] + rb[2 * u + 1] * rs[j];
          const u32x4 o = {cvtpk(a[0], a[1]), cvtpk(a[2], a[3]), cvtpk(b[0], b[1]), cvtpk(b[2], b[3])};
          const int at = (u * 8 + (j ^ (2 * wP))) * 1024 + lane * 16;
          store_sc1(ro, at, o);
          *(LAS u32x4*)(sm + (wP * SLOT + wQ * 4096 + at)) = o;
        }
    }
    u32x4 hk[2][2];
    {
      Consumer<4, M_TILED, M_ROW> C;
      C.init(sm, wP, wQ, lane);
      f32x4 acc[4][4];
      wait_lgkm0(); bar();
      STAMP(3)
      C.first();
      C.load<0, 1, 4>(); C.mma<0, 4, true>(acc); C.interleave<16, 4>();
      wait_lgkm0(); bar();
      __builtin_amdgcn_sched_barrier(0);
      C.load<1, 0, 4>(); C.mma<1, 4>(acc); C.interleave<16, 4>();
      C.load<1, 1, 4>(); C.mma<0, 4>(acc); C.interleave<16, 4>();
      wait_lgkm0();
      C.mma<1, 4>(acc);
      __builtin_amdgcn_sched_barrier(0);
      wait_vm<0>();
      STAMP(4)
      bar();
      bar();
      bar();
      STAMP(5)
      C.load<2, 0, 4>();
      C.step<2, 4, 4>(acc); C.step<3, 4, 4>(acc);
      for (int it = 0; it < 5; it++) C.step4<4, 4>(acc);
      C.step4<4, 2>(acc);
      STAMP(6)
      xch_send(ws + WS_X2, acc, wid, lane);
      __builtin_amdgcn_sched_barrier(0);
      rsrc_t rx = mk_rsrc(ws + WS_X2 + ((size_t)(blockIdx.x ^ 4) * 4 + wid) * 4096, 4096);
      rsrc_t rf = mk_rsrc(ws + WS_FL + ((size_t)(FL_X2 * 16 + tt) * 64 + (cell ^ 32)) * 64, 64);
      u32x4 rv[4];
      u32 fv;
      C.step<0, 2, 2>(acc); C.step<1, 2, 2>(acc); C.step<2, 2, 2>(acc);
      if (K_XF == 3) flag_set(ws, FL_X2, tt, cell);
      C.step<3, 2, 2>(acc);
      if (K_XF == 4) flag_set(ws, FL_X2, tt, cell);
      C.step<0, 2, 2>(acc); C.step<1, 2, 2>(acc);
      flag_request(rf, 0, fv);
      __builtin_amdgcn_sched_barrier(0);
      C.step<2, 2, 2>(acc);
      wait_vm<0>();
      flag_wait(rf, 0, fv);
      xch_request(rx, rv, lane);
      __builtin_amdgcn_sched_barrier(0);
      C.load<3, 1, 2>(); C.mma<0, 2>(acc); C.interleave<8, 2>();
      C.mma<1, 2>(acc);
      STAMP(7)
      xch_finish(rv);
            rsrc_t rh = mk_rsrc(ws + WS_H + ((size_t)(tt * 32 + 4 * et + 2 * wP) * 8 + m0) * 1024, 16384);
      rsrc_t rs = mk_rsrc(ws + WS_ST + ((size_t)(tt * 16 + 2 * et + wP) * 128 + 16 * m0) * 8, 256);
#pragma unroll
      for (int jj = 0; jj < 2; jj++) {
        float s = 0.f, q = 0.f;
#pragma unroll
        for (int u = 0; u < 2; u++) {
          const u32x4 o = rv[jj * 2 + u];
          const f32x4 a = acc[2 * u][jj], b = acc[2 * u + 1][jj];
          const int xrow = 32 * wQ + 16 * jj + c;
          const LAS char* xp = sm + (XOFF + wP * 8192 + xrow * 128 + (g & 1) * 8);
          const u32x2 xa = *(const LAS u32x2*)(xp + (((4 * u + (g >> 1)) ^ ((xrow >> 1) & 7)) * 16));
          const u32x2 xb = *(const LAS u32x2*)(xp + (((4 * u + 2 + (g >> 1)) ^ ((xrow >> 1) & 7)) * 16));
          u32x4 h;
          h[0] = cvtpk(a[0] + blo(o[0]) + blo(xa[0]), a[1] + bhi(o[0]) + bhi(xa[0]));
          h[1] = cvtpk(a[2] + blo(o[1]) + blo(xa[1]), a[3] + bhi(o[1]) + bhi(xa[1]));
          h[2] = cvtpk(b[0] + blo(o[2]) + blo(xb[0]), b[1] + bhi(o[2]) + bhi(xb[0]));
          h[3] = cvtpk(b[2] + blo(o[3]) + blo(xb[1]), b[3] + bhi(o[3]) + bhi(xb[1]));
          store_sc1(rh, (u * 8 + jj) * 1024 + lane * 16, h);
          hk[jj][u] = h;
#pragma unroll
          for (int e = 0; e < 4; e++) { const float lo = blo(h[e]), hi = bhi(h[e]); s += lo + hi; q += lo * lo + hi * hi; }
        }
        s = xsum32(xsum16(s)); q = xsum32(xsum16(q));
        if (g == 0) __builtin_amdgcn_raw_buffer_store_b64((u32x2){__float_as_uint(s), __float_as_uint(q)}, rs, (16 * jj + c) * 8, 0, 16);
      }
      wait_vm<0>();
      STAMP(8)
      bar();
#pragma unroll
      for (int q = 0; q < 4; q++) *(LAS u32x4*)(sm + XOFF + wid * 4096 + q * 1024 + lane * 16) = hk[q >> 1][q & 1];
      bar();
    }
    rsrc_t rr = mk_rsrc(ws + WS_R + ((size_t)(tt * 112 + 7 * ft) * 8 + 4 * wQ) * 1024, 7 * 8192);
    u32x4 ro[14];
    {
      Consumer<7, M_TR448, M_TILED> C;
      C.init(sm, wP, wQ, lane);
      f32x4 acc[7][4];
      C.zero(acc);
      bar();
      STAMP(9)
      C.first();
      for (int it = 0; it < 5; it++) { C.step<0, 4, 4>(acc); C.step<1, 4, 4>(acc); C.step<2, 4, 4>(acc); }
      C.last<0, 4>(acc);
      bar();
      STAMP(10)
      float ra[4], rb[4];
#pragma unroll
      for (int j = 0; j < 4; j++) { ra[j] = tabA[64 * wQ + 16 * j + c]; rb[j] = tabB[64 * wQ + 16 * j + c]; }
      f32x4 cs[7], bi[7];
#pragma unroll
      for (int i = 0; i < 7; i++) {
        const int col = (i < 6 ? 128 * wP + 16 * i : 96 + 16 * wP) + 4 * g;
        cs[i] = *(const LAS f32x4*)(tabC + col); bi[i] = *(const LAS f32x4*)(tabBias + col);
      }
      auto act = [&](int i, int j) {
        f32x4 v = ra[j] * acc[i][j] + (rb[j] * cs[i] + bi[i]);
#pragma unroll
        for (int e = 0; e < 4; e++) v[e] = fmaxf(v[e], 0.f);
        v = v * v;
        return (u32x2){cvtpk(v[0], v[1]), cvtpk(v[2], v[3])};
      };
#pragma unroll
      for (int u = 0; u < 3; u++) {
        LAS char* own = sm + (QOFF + ((u + 2) & 3) * SLOT + wP * 8192 + 4 * wQ * 1024 + lane * 16);
#pragma unroll
        for (int j = 0; j < 4; j++) {
          const u32x2 a = act(2 * u, j), b = act(2 * u + 1, j);
          ro[4 * u + j] = (u32x4){a[0], a[1], b[0], b[1]};
          if (4 * u + j < K_NS1) store_sc1(rr, ((4 * wP + u) * 8 + j) * 1024 + lane * 16, ro[4 * u + j]);
          *(LAS u32x4*)(own + (j ^ (2 * half)) * 1024) = ro[4 * u + j];
        }
      }
#pragma unroll
      for (int j = 0; j < 4; j++)
        *(LAS u32x2*)(sm + (QOFF + SLOT + (4 * wQ + j) * 1024 + lane * 16 + 8 * wP)) = act(6, j);
    }
    {
      Consumer<4, M_TR256, M_TILED> C;
      C.init(sm, wP, wQ, lane);
      f32x4 acc[4][4];
      wait_vm<K_NS1 - 4>();
      wait_lgkm0(); bar();
      STAMP(11)
      C.load<2, 0, 4>();
#pragma unroll
      for (int j = 0; j < 2; j++) ro[12 + j] = *(const LAS u32x4*)(sm + (QOFF + SLOT + (4 * wQ + 2 * wP + j) * 1024 + lane * 16));
      auto put = [&](int hs, int n) {
        constexpr int LEFT = 14 - K_NS1;
        for (int q = 0; q < LEFT; q++)
          if (q * K_SPR / LEFT == hs * 16 + n) {
            const int t = K_NS1 + q;
            store_sc1(rr, (t < 12 ? (4 * wP + (t >> 2)) * 8 + (t & 3) : 24 + 2 * wP + (t & 1)) * 1024 + lane * 16, ro[t]);
          }
      };
      C.half_with<2, 1, true>(acc, [&](int n) { put(0, n); });
      wait_lgkm0(); bar();
      C.half_with<3, 0, false>(acc, [&](int n) { put(1, n); });
      C.half_with<3, 1, false>(acc, [&](int n) { put(2, n); });
      wait_lgkm0(); bar();
      C.half_with<0, 0, false>(acc, [&](int n) { put(3, n); });
      C.half_with<0, 1, false>(acc, [&](int n) { put(4, n); });
      wait_lgkm0();
      C.half_with<-1, 0, false>(acc, [&](int n) { put(5, n); });
      __builtin_amdgcn_sched_barrier(0);
      STAMP(12)
      if (K_RB == 3) wait_vm<0>();
      bar();
      STAMP(13)
      C.load<1, 0, 4>();
      C.load<1, 1, 4>(); C.mma<0, 4>(acc); C.interleave<16, 4>();
      if (K_RB == 4) wait_vm<0>();
      wait_lgkm0(); bar();
      __builtin_amdgcn_sched_barrier(0);
      C.load<2, 0, 4>(); C.mma<1, 4>(acc); C.interleave<16, 4>();
      C.step<2, 4, 4>(acc); C.step<3, 4, 4>(acc);
      for (int it = 0; it < 4; it++) C.step4<4, 4>(acc);
      C.step<0, 4, 4>(acc); C.step<1, 4, 2>(acc);
      STAMP(14)
      xch_send(ws + WS_X4, acc, wid, lane);
      u32x2 br[4];
#pragma unroll
      for (int i = 0; i < 4; i++) br[i] = *(const u32x2*)(b2 + et * 128 + 64 * wP + 16 * i + 4 * g);
      __builtin_amdgcn_sched_barrier(0);
      rsrc_t rx = mk_rsrc(ws + WS_X4 + ((size_t)(blockIdx.x ^ 4) * 4 + wid) * 4096, 4096);
      rsrc_t rf = mk_rsrc(ws + WS_FL + ((size_t)(FL_X4 * 16 + tt) * 64 + (cell ^ 32)) * 64, 64);
      u32x4 rv[4];
      u32 fv;
      C.step<2, 2, 2>(acc); C.step<3, 2, 2>(acc); C.step<0, 2, 2>(acc);
      flag_set(ws, FL_X4, tt, cell);
      C.step<1, 2, 2>(acc); C.step<2, 2, 2>(acc); C.step<3, 2, 2>(acc);
      flag_request(rf, 0, fv);
      __builtin_amdgcn_sched_barrier(0);
      C.step<0, 2, 2>(acc);
      wait_vm<0>();
      flag_wait(rf, 0, fv);
      xch_request(rx, rv, lane);
      __builtin_amdgcn_sched_barrier(0);
      C.last<1, 2>(acc);
      xch_finish(rv);

      rsrc_t ro = mk_rsrc(out + (size_t)(tt * 128 + 16 * m0) * D_ + et * 128 + 64 * wP, 31 * D_ * 2 + 128);
#pragma unroll
      for (int jj = 0; jj < 2; jj++)
#pragma unroll
        for (int u = 0; u < 2; u++) {
          const u32x4 o = rv[jj * 2 + u], h = *(const LAS u32x4*)(sm + XOFF + wid * 4096 + (jj * 2 + u) * 1024 + lane * 16);
          const f32x4 a = acc[2 * u][jj], b = acc[2 * u + 1][jj];
          const u32x2 ba = br[2 * u], bb = br[2 * u + 1];
          const u32 a0 = cvtpk(a[0] + blo(o[0]) + blo(ba[0]) + blo(h[0]), a[1] + bhi(o[0]) + bhi(ba[0]) + bhi(h[0]));
          const u32 a1 = cvtpk(a[2] + blo(o[1]) + blo(ba[1]) + blo(h[1]), a[3] + bhi(o[1]) + bhi(ba[1]) + bhi(h[1]));
          const u32 b0 = cvtpk(b[0] + blo(o[2]) + blo(bb[0]) + blo(h[2]), b[1] + bhi(o[2]) + bhi(bb[0]) + bhi(h[2]));
          const u32 b1_ = cvtpk(b[2] + blo(o[3]) + blo(bb[1]) + blo(h[3]), b[3] + bhi(o[3]) + bhi(bb[1]) + bhi(h[3]));
          const auto s0 = __builtin_amdgcn_permlane16_swap(a0, b0, false, false);
          const auto s1 = __builtin_amdgcn_permlane16_swap(a1, b1_, false, false);
          __builtin_amdgcn_raw_buffer_store_b128((u32x4){s0[0], s1[0], s0[1], s1[1]}, ro,
                                                 (16 * jj + c) * (D_ * 2) + (32 * u + 16 * (g & 1) + 8 * (g >> 1)) * 2, 0, 16);
        }
      DIAG_END(15)
    }
  }
}

}

extern "C" void kernel_launch(void* const* d_in, const int* in_sizes, int n_in, void* d_out, int out_size, void* d_ws, size_t ws_size,
                              hipStream_t stream) {
  const u16* const* I = (const u16* const*)d_in;
  (void)hipFuncSetAttribute((const void*)fused_kernel, hipFuncAttributeMaxDynamicSharedMemorySize, LDS_BYTES);
  fused_kernel<<<256, 512, LDS_BYTES, stream>>>(I[0], I[1], I[2], I[3], I[4], I[6], I[7], I[8], I[9], (char*)d_ws, (u16*)d_out);
}
```

```cpp
#include <hip/hip_runtime.h>
#include <hip/hip_bf16.h>
#include <type_traits>

typedef unsigned short u16;
typedef unsigned int u32;
typedef unsigned long long u64;
typedef __bf16 bf16x8 __attribute__((ext_vector_type(8)));
typedef __bf16 bf16x2 __attribute__((ext_vector_type(2)));
typedef short s16x4 __attribute__((ext_vector_type(4)));
typedef float f32x2 __attribute__((ext_vector_type(2)));
typedef float f32x4 __attribute__((ext_vector_type(4)));
typedef u32 u32x2 __attribute__((ext_vector_type(2)));
typedef u32 u32x4 __attribute__((ext_vector_type(4)));
typedef u32 u32x16 __attribute__((ext_vector_type(16)));
typedef __amdgpu_buffer_rsrc_t rsrc_t;
#define GAS __attribute__((address_space(1)))
#define LAS __attribute__((address_space(3)))
#define DI __device__ __forceinline__

namespace {

constexpr int S_ = 2048, D_ = 1024, F_ = 3584;
constexpr float LN_EPS = 1e-5f;
constexpr int SPIN_LIMIT = 1 << 18;
constexpr int EARLY = 6;

constexpr size_t WS_AB = 0;
constexpr size_t WS_H = 8u << 20;
constexpr size_t WS_R = 12u << 20;
constexpr size_t WS_X2 = 27u << 20;
constexpr size_t WS_X4 = 31u << 20;
constexpr size_t WS_ST = 35u << 20;
constexpr size_t WS_CS = 36u << 20;
constexpr size_t WS_FL = 37u << 20;
constexpr size_t WS_SX = 38u << 20;

DI u32 rfl(u32 x) { return __builtin_amdgcn_readfirstlane(x); }
DI u32 cvtpk(float lo, float hi) { f32x2 v = {lo, hi}; return __builtin_bit_cast(u32, __builtin_convertvector(v, bf16x2)); }
DI float blo(u32 v) { return __uint_as_float(v << 16); }
DI float bhi(u32 v) { return __uint_as_float(v & 0xFFFF0000u); }
DI rsrc_t mk_rsrc(const void* p, u32 bytes) { return __builtin_amdgcn_make_buffer_rsrc((void*)p, 0, bytes, 0x00020000); }
DI void bar() { asm volatile("s_barrier" ::: "memory"); }
DI void wait_lgkm0() { __builtin_amdgcn_s_waitcnt(0xC07F); }
template <int N> DI void wait_vm() { asm volatile("s_waitcnt vmcnt(%0)" ::"i"(N) : "memory"); }

template <int N> DI void dma(u32 m0, const u32* v, rsrc_t r, u32 so) {
  if constexpr (N == 4)
    asm volatile("s_mov_b32 m0, %0\n\ts_nop 3\n\t"
                 "buffer_load_dwordx4 %1, %5, %6 offen lds\n\t"
                 "buffer_load_dwordx4 %2, %5, %6 offen offset:1024 lds\n\t"
                 "buffer_load_dwordx4 %3, %5, %6 offen offset:2048 lds\n\t"
                 "buffer_load_dwordx4 %4, %5, %6 offen offset:3072 lds"
                 ::"s"(m0), "v"(v[0]), "v"(v[1]), "v"(v[2]), "v"(v[3]), "s"(r), "s"(so) : "memory");
  else if constexpr (N == 3)
    asm volatile("s_mov_b32 m0, %0\n\ts_nop 3\n\t"
                 "buffer_load_dwordx4 %1, %4, %5 offen lds\n\t"
                 "buffer_load_dwordx4 %2, %4, %5 offen offset:1024 lds\n\t"
                 "buffer_load_dwordx4 %3, %4, %5 offen offset:2048 lds"
                 ::"s"(m0), "v"(v[0]), "v"(v[1]), "v"(v[2]), "s"(r), "s"(so) : "memory");
  else
    asm volatile("s_mov_b32 m0, %0\n\ts_nop 3\n\t"
                 "buffer_load_dwordx4 %1, %3, %4 offen lds\n\t"
                 "buffer_load_dwordx4 %2, %3, %4 offen offset:1024 lds"
                 ::"s"(m0), "v"(v[0]), "v"(v[1]), "s"(r), "s"(so) : "memory");
}
template <int N> DI void dma_sc1(u32 m0, u32 v, rsrc_t r, u32 so) {
  if constexpr (N == 4)
    asm volatile("s_mov_b32 m0, %0\n\ts_nop 3\n\t"
                 "buffer_load_dwordx4 %1, %2, %3 offen sc1 lds\n\t"
                 "buffer_load_dwordx4 %1, %2, %3 offen offset:1024 sc1 lds\n\t"
                 "buffer_load_dwordx4 %1, %2, %3 offen offset:2048 sc1 lds\n\t"
                 "buffer_load_dwordx4 %1, %2, %3 offen offset:3072 sc1 lds"
                 ::"s"(m0), "v"(v), "s"(r), "s"(so) : "memory");
  else
    asm volatile("s_mov_b32 m0, %0\n\ts_nop 3\n\t"
                 "buffer_load_dwordx4 %1, %2, %3 offen sc1 lds\n\t"
                 "buffer_load_dwordx4 %1, %2, %3 offen offset:1024 sc1 lds"
                 ::"s"(m0), "v"(v), "s"(r), "s"(so) : "memory");
}

DI float xsum16(float v) { const auto r = __builtin_amdgcn_permlane16_swap(__float_as_uint(v), __float_as_uint(v), false, false); return __uint_as_float(r[0]) + __uint_as_float(r[1]); }
DI float xsum32(float v) { const auto r = __builtin_amdgcn_permlane32_swap(__float_as_uint(v), __float_as_uint(v), false, false); return __uint_as_float(r[0]) + __uint_as_float(r[1]); }

template <int A, int B, class F> DI void static_for(F f) {
  if constexpr (A < B) { f(std::integral_constant<int, A>{}); static_for<A + 1, B>(f); }
}

DI void pull_lines(u32 m0, u32 v, rsrc_t r, u32 so) {
  asm volatile("s_mov_b32 m0, %0\n\ts_nop 3\n\tbuffer_load_dword %1, %2, %3 offen lds" ::"s"(m0), "v"(v), "s"(r), "s"(so) : "memory");
}

DI int kstep(int t, int n, int phi) {
  int k = t;
  if ((t & 3) == phi) { k = t + 4; if (k >= n) k = phi; }
  return k;
}

DI int kstep2(int t, int n, int phi, int side) {
  const int k = kstep(t, n, phi), k2 = k ^ (side << 2);
  return k2 < n ? k2 : k;
}

constexpr int NST = 4;
constexpr int SLOT = 16384;
constexpr int QOFF = NST * SLOT;
constexpr int P3 = 64 * 448;
constexpr int Q3OFF = 3 * P3;
constexpr int TOFF = Q3OFF + 3 * SLOT;
constexpr int COFF = TOFF + 4096;
constexpr int XOFF = COFF + 4096;
constexpr int LDS_BYTES = XOFF + 16384;

enum { M_ROW = 0, M_TILED = 1, M_TR448 = 2, M_TR256 = 3 };

template <int PT, int PMODE, int QMODE>
struct Consumer {
  static constexpr int NS = PMODE == M_TR448 ? 3 : NST, QB = PMODE == M_TR448 ? Q3OFF : QOFF;
  const LAS char* pb[6];
  const LAS char* qb[4];
  bf16x8 fp[2][PT], fq[2][4];

  DI void init(const LAS char* sm, int wP, int wQ, int lane, int qrot = 0) {
    const int r = lane & 15, g = lane >> 4;
    if constexpr (PMODE == M_ROW) {
      const int a = (64 * wP + r) * 128 + ((g ^ (r >> 1)) * 16);
      pb[0] = sm + a; pb[1] = sm + (a ^ 64);
    } else if constexpr (PMODE == M_TILED) {
      pb[0] = sm + (4 * wP * 1024 + lane * 16);
    } else if constexpr (PMODE == M_TR448) {
      const int b = g & 1, a = (4 * g + (r >> 2)) * 448 + (r & 3) * 8;
      pb[0] = sm + (a + (8 * wP + b) * 32);
      pb[1] = sm + (a + (8 * wP - b) * 32);
      pb[2] = sm + (a + ((6 + wP) ^ b) * 32);
#pragma unroll
      for (int i = 0; i < 3; i++) { pb[3 + i] = pb[i] + 2 * P3; asm volatile("" : "+v"(pb[3 + i])); }
    } else {
      const int k = 4 * g + (r >> 2), a = k * 256 + (r & 3) * 8;
#pragma unroll
      for (int i = 0; i < 4; i++) pb[i] = sm + (a + (((4 * wP + i) ^ (k & 7)) * 32));
    }
    if constexpr (QMODE == M_ROW) {
      const int a = QOFF + (64 * wQ + r) * 128 + ((g ^ (r >> 1)) * 16);
      qb[0] = sm + (a + qrot * 4096); qb[1] = sm + ((a ^ 64) + qrot * 4096);
      qb[2] = sm + (a + 4096 - qrot * 4096); qb[3] = sm + ((a ^ 64) + 4096 - qrot * 4096);
      asm volatile("" : "+v"(qb[0]), "+v"(qb[1]), "+v"(qb[2]), "+v"(qb[3]));
    } else {
      qb[0] = sm + (QB + 4 * wQ * 1024 + lane * 16);
      asm volatile("" : "+v"(qb[0]));
    }
  }
  static DI bf16x8 tr2(const LAS char* p, int second) {
    s16x4 a = __builtin_amdgcn_ds_read_tr16_b64_v4i16((LAS s16x4*)p);
    s16x4 b = __builtin_amdgcn_ds_read_tr16_b64_v4i16((LAS s16x4*)(p + second));
    return __builtin_bit_cast(bf16x8, __builtin_shufflevector(a, b, 0, 1, 2, 3, 4, 5, 6, 7));
  }
  template <int S, int KS> DI bf16x8 ldp(int i) {
    if constexpr (PMODE == M_ROW) return *(const LAS bf16x8*)(pb[KS] + (S * SLOT + i * 2048));
    else if constexpr (PMODE == M_TILED) return *(const LAS bf16x8*)(pb[0] + (S * SLOT + KS * 8192 + i * 1024));
    else if constexpr (PMODE == M_TR448)
      return tr2(pb[(S == 2 ? 3 : 0) + (i == 6 ? 2 : i & 1)] + ((S & 1) * P3 + KS * 32 * 448 + (i == 6 ? 0 : i * 32)), 16 * 448);
    else return tr2(pb[i] + (S * SLOT + KS * 32 * 256), 16 * 256);
  }
  template <int S, int KS> DI bf16x8 ldq(int j) {
    if constexpr (QMODE == M_ROW) return *(const LAS bf16x8*)(qb[KS + (j & 2)] + (S * SLOT + (j & 1) * 2048));
    else return *(const LAS bf16x8*)(qb[0] + (S * SLOT + KS * 8192 + j * 1024));
  }
  template <int S, int KS, int QN> DI void load() {
    fq[KS][0] = ldq<S, KS>(0);
#pragma unroll
    for (int i = 0; i < PT; i++) fp[KS][i] = ldp<S, KS>(i);
#pragma unroll
    for (int j = 1; j < QN; j++) fq[KS][j] = ldq<S, KS>(j);
  }
  template <int KS, int QN, bool ZERO = false> DI void mma(f32x4 (&acc)[PT][4]) {
#pragma unroll
    for (int n = 0; n < PT * QN; n++) {
      const int j = n / PT, i = (j & 1) ? PT - 1 - n % PT : n % PT;
      acc[i][j] = __builtin_amdgcn_mfma_f32_16x16x32_bf16(fp[KS][i], fq[KS][j], ZERO ? (f32x4){0.f, 0.f, 0.f, 0.f} : acc[i][j], 0, 0, 0);
    }
  }
  template <int KS> DI void qsums(f32x4 (&sd)[2], bf16x8 ones) {
#pragma unroll
    for (int i = 0; i < 2; i++) sd[i] = __builtin_amdgcn_mfma_f32_16x16x32_bf16(ones, fq[KS][i], sd[i], 0, 0, 0);
  }
  template <int NM, int QN> static DI void interleave() {
    constexpr int NR = PT * (PMODE >= M_TR448 ? 2 : 1) + QN, G = NR < NM ? NR : NM;
    static_for<0, G>([](auto ic) {
      __builtin_amdgcn_sched_group_barrier(0x8, 1, 0);
      __builtin_amdgcn_sched_group_barrier(0x100, NR / G + (ic.value < NR % G ? 1 : 0), 0);
    });
    __builtin_amdgcn_sched_group_barrier(0x8, NM - G, 0);
    __builtin_amdgcn_sched_barrier(0);
  }
  template <int S, int QC, int QN> DI void step(f32x4 (&acc)[PT][4]) {
    load<S, 1, QC>(); mma<0, QC>(acc); interleave<PT * QC, QC>();
    wait_lgkm0(); bar();
    __builtin_amdgcn_sched_barrier(0);
    load<(S + 1) % NS, 0, QN>(); mma<1, QC>(acc); interleave<PT * QC, QN>();
  }
  template <int S, int LS, bool ZERO, class W> DI void half_with(f32x4 (&acc)[PT][4], W w) {
    static_for<0, PT * 4>([&](auto nc) {
      constexpr int n = nc.value, j = n / PT, i = (j & 1) ? PT - 1 - n % PT : n % PT;
      acc[i][j] = __builtin_amdgcn_mfma_f32_16x16x32_bf16(fp[1 - LS][i], fq[1 - LS][j], ZERO ? (f32x4){0.f, 0.f, 0.f, 0.f} : acc[i][j], 0, 0, 0);
      if constexpr (S >= 0) {
        if constexpr (n == 0) fq[LS][0] = ldq<S, LS>(0);
        else if constexpr (n <= PT) fp[LS][n - 1] = ldp<S, LS>(n - 1);
        else if constexpr (n < PT + 4) fq[LS][n - PT] = ldq<S, LS>(n - PT);
      }
      w(nc);
      __builtin_amdgcn_sched_barrier(0);
    });
  }
  template <int S, int QC> DI void last(f32x4 (&acc)[PT][4]) {
    load<S, 1, QC>(); mma<0, QC>(acc); interleave<PT * QC, QC>();
    wait_lgkm0();
    mma<1, QC>(acc);
  }
  template <int QC, int QN> DI void step4(f32x4 (&acc)[PT][4]) {
    step<0, QC, QC>(acc); step<1, QC, QC>(acc); step<2, QC, QC>(acc); step<3, QC, QN>(acc);
  }
  static DI void zero(f32x4 (&acc)[PT][4]) {
#pragma unroll
    for (int i = 0; i < PT; i++)
#pragma unroll
      for (int j = 0; j < 4; j++) { acc[i][j] = (f32x4){0.f, 0.f, 0.f, 0.f}; asm volatile("" : "+v"(acc[i][j])); }
  }
  DI void first() { load<0, 0, 4>(); }
};

DI u32 row_src(int imgrow, int srcrow, int lane, int ldbytes) {
  return srcrow * ldbytes + (((lane & 7) ^ ((imgrow >> 1) & 7)) * 16);
}
DI void store_sc1(rsrc_t r, u32 off, u32x4 v) { __builtin_amdgcn_raw_buffer_store_b128(v, r, off, 0, 16); }

DI void xch_send(char* x, const f32x4 (&acc)[4][4], int wid, int lane) {
  rsrc_t r = mk_rsrc(x + ((size_t)blockIdx.x * 4 + wid) * 4096, 4096);
#pragma unroll
  for (int jj = 0; jj < 2; jj++)
#pragma unroll
    for (int u = 0; u < 2; u++) {
      const f32x4 a = acc[2 * u][2 + jj], b = acc[2 * u + 1][2 + jj];
      u32x4 v = {cvtpk(a[0], a[1]), cvtpk(a[2], a[3]), cvtpk(b[0], b[1]), cvtpk(b[2], b[3])};
      store_sc1(r, lane * 16 + (jj * 2 + u) * 1024, v);
    }
}
DI void xch_request(rsrc_t r, u32x4 (&v)[4], int lane) {
  asm volatile("s_nop 4\n\tbuffer_load_dwordx4 %0, %4, %5, 0 offen sc1\n\t"
               "buffer_load_dwordx4 %1, %4, %5, 0 offen offset:1024 sc1\n\t"
               "buffer_load_dwordx4 %2, %4, %5, 0 offen offset:2048 sc1\n\t"
               "buffer_load_dwordx4 %3, %4, %5, 0 offen offset:3072 sc1"
               : "=&v"(v[0]), "=&v"(v[1]), "=&v"(v[2]), "=&v"(v[3]) : "v"(lane * 16), "s"(r) : "memory");
}
DI void xch_finish(u32x4 (&v)[4]) { asm volatile("s_waitcnt vmcnt(0)" : "+v"(v[0]), "+v"(v[1]), "+v"(v[2]), "+v"(v[3])::"memory"); }

enum { FL_X2 = 0, FL_X4 = 1, FL_ACK = 2, FL_TICK = 3, FL_CNT = 4 };
enum { CN_H = 0, CN_AB = 128, CN_SX = 256, CN_RA = 384, CN_RB = 512 };
constexpr u32 NOWHERE = 0xFFFFFF00u;
DI void flag_set(char* ws, int which, int tt, int cell) {
  wait_vm<0>();
  if ((threadIdx.x & 63) == 0)
    __hip_atomic_store((u32*)(ws + WS_FL + ((size_t)(which * 16 + tt) * 64 + cell) * 64), 1u, __ATOMIC_RELAXED, __HIP_MEMORY_SCOPE_AGENT);
}
DI void flag_request(rsrc_t r, u32 off, u32& v) {
  asm volatile("s_nop 4\n\tbuffer_load_dword %0, %1, %2, 0 offen sc1" : "=&v"(v) : "v"(off), "s"(r) : "memory");
}
DI void flag_wait(rsrc_t r, u32 off, u32& v) {
  asm volatile("" : "+v"(v)::"memory");
#pragma unroll 1
  for (int spin = 0; spin < SPIN_LIMIT; spin++) {
    if (__builtin_amdgcn_ballot_w64(v != 1u) == 0) break;
    __builtin_amdgcn_s_sleep(1);
    asm volatile("s_nop 4\n\tbuffer_load_dword %0, %1, %2, 0 offen sc1\n\ts_waitcnt vmcnt(0)" : "=&v"(v) : "v"(off), "s"(r) : "memory");
  }
}

DI void word_store(rsrc_t r, u32 off, u32 v) { __builtin_amdgcn_raw_buffer_store_b32(v, r, off, 0, 16); }
DI void count_up(char* ws, u32 off) {
  if ((threadIdx.x & 63) == 0) __hip_atomic_fetch_add((u32*)(ws + WS_FL + off), 1u, __ATOMIC_RELAXED, __HIP_MEMORY_SCOPE_AGENT);
}
template <class Done> DI void watch(rsrc_t r, u32 off, u32& v, Done done, u32 tickoff, u32& tick) {
#pragma unroll 1
  for (int spin = 0; spin < SPIN_LIMIT; spin++) {
    if (__builtin_amdgcn_ballot_w64(!done(v)) == 0) break;
    if ((spin & 63) == 63) { tick++; word_store(r, tickoff, tick); }
    __builtin_amdgcn_s_sleep(1);
    asm volatile("s_nop 4\n\tbuffer_load_dword %0, %1, %2, 0 offen sc1\n\ts_waitcnt vmcnt(0)" : "=&v"(v) : "v"(off), "s"(r) : "memory");
  }
}

__global__ __launch_bounds__(512, 2) void fused_kernel(const u16*, const u16*, const u16*, const u16*, const u16*, const u16*, const u16*,
                                                       const u16*, const u16*, char*, u16*) {
  u32x16 ka; u32x4 kb4; u32x2 kc;
  asm volatile("s_load_dwordx16 %0, %3, 0x0\n\ts_load_dwordx4 %1, %3, 0x40\n\ts_load_dwordx2 %2, %3, 0x50"
               : "=&s"(ka), "=&s"(kb4), "=&s"(kc) : "s"(__builtin_amdgcn_kernarg_segment_ptr()));
  __builtin_amdgcn_sched_barrier(0);
  extern __shared__ __attribute__((aligned(16))) char smem[];
  const int lane0 = threadIdx.x & 63, wid = rfl(threadIdx.x >> 6);
  const int xcd = blockIdx.x & 7, loc = blockIdx.x >> 3, li = loc >> 2, lj = loc & 3;
  const int half = xcd >> 2, et = ((xcd >> 1) & 1) * 4 + lj, tt = (xcd & 1) * 8 + li, ft = (xcd >> 1) * 4 + lj;
  LAS char* sm = (LAS char*)smem;
  LAS float* tabA = (LAS float*)(sm + TOFF);
  LAS float* tabB = tabA + 128;
  LAS float* tabC = tabA + 256;
  LAS float* tabBias = tabA + 512;
  u32 vp1[4], vq1[4];
#pragma unroll
  for (int i = 0; i < 4; i++) {
    const int row = 32 * (wid & 3) + 8 * i + (lane0 >> 3), u = (row >> 4) & 1, ii = row & 15;
    vp1[i] = row_src(row, 32 * (wid & 3) + 8 * (ii >> 2) + 4 * u + (ii & 3), lane0, D_ * 2) + 4096 - i * 1024;
    vq1[i] = row_src(row, row, lane0, D_ * 2) - i * 1024;
  }
  asm volatile("" : "+v"(vp1[0]), "+v"(vp1[1]), "+v"(vp1[2]), "+v"(vp1[3]), "+v"(vq1[0]), "+v"(vq1[1]), "+v"(vq1[2]), "+v"(vq1[3]));
  __builtin_amdgcn_sched_barrier(0);
  asm volatile("s_waitcnt lgkmcnt(0)" : "+s"(ka), "+s"(kb4), "+s"(kc));
  auto ptr = [](u32 lo, u32 hi) { return (u16*)(GAS u16*)(((u64)hi << 32) | lo); };
  const u16 *x = ptr(ka[0], ka[1]), *cre = ptr(ka[2], ka[3]), *cim = ptr(ka[4], ka[5]), *dre = ptr(ka[6], ka[7]), *dim = ptr(ka[8], ka[9]);
  const u16 *w1 = ptr(ka[10], ka[11]), *b1 = ptr(ka[12], ka[13]), *w2 = ptr(ka[14], ka[15]), *b2 = ptr(kb4[0], kb4[1]);
  char* ws = (char*)ptr(kb4[2], kb4[3]);
  u16* out = ptr(kc[0], kc[1]);
  if (__builtin_expect(wid >= 4, 1)) {
    const int p = wid - 4, phi = (li + lj) & 3;
    const u32 lds0 = (u32)(size_t)smem, ldsb = lds0 + p * 4096;
    rsrc_t rall = mk_rsrc(ws + WS_FL, 5 * 65536);
    rsrc_t rp1 = mk_rsrc((const char*)x + (size_t)tt * 128 * D_ * 2 - 4096, 128 * D_ * 2 + 4096);
    rsrc_t rq1d = mk_rsrc((half ? dim : dre) + (size_t)et * 128 * D_, 128 * D_ * 2);
    const int side1 = (xcd >> 1) ^ (xcd & 1);
    dma<4>(ldsb, vp1, rp1, kstep2(0, 16, phi, side1) * 128);
    dma<4>(ldsb + QOFF, vq1, rq1d, kstep2(0, 16, phi, side1) * 128);
    int lane = lane0;
    asm volatile("" : "+v"(lane)::"memory");
    __builtin_amdgcn_sched_barrier(0);
    const u32 vlin = p * 4096 + lane * 16;
    const u32 grpcell = ((lane & 16) ? tt * 64 + (lane & 15) * 4 : (lane & 15) * 64 + ft * 4) * 64;
    const u32 tickoff = lane == 0 ? FL_TICK * 65536 + (tt * 64 + ft * 4) * 64 : NOWHERE;
    const u32 ackoff = lane == 0 ? FL_ACK * 65536 + (tt * 64 + ft * 4) * 64 : NOWHERE;
    const u32 rowcnt = FL_CNT * 65536 + tt * 4096, colcnt = FL_CNT * 65536 + ft * 4096;
    const int pt = threadIdx.x - 256;
    u32 base, fv, tick;
    const int side2 = (xcd >> 1) & 1;
    auto skip = [&](int r) { return r + (r >= 2 * tt ? 2 : 0); };
    auto g2k = [&](int q) { return q < 2 ? 2 * tt + q : skip(kstep2(q - 2, 26, phi, side2)); };
    rsrc_t rco = mk_rsrc((const char*)(half ? cim : cre) + (size_t)tt * 128 * S_ * 2 - 4096, 128 * S_ * 2 + 4096);
    u32 vc[4];
#pragma unroll
    for (int i = 0; i < 4; i++) {
      const int row = 32 * p + 8 * i + (lane >> 3);
      vc[i] = row_src(row, row ^ (half * 32), lane, S_ * 2) + 4096 - i * 1024;
    }
    auto g2q = [&](int q) { dma<4>(ldsb + QOFF + (q & 3) * SLOT, vc, rco, g2k(q) * 128); };
    {
      u32 vx[2];
#pragma unroll
      for (int i = 0; i < 2; i++) {
        const int row = 16 * p + 8 * i + (lane >> 3);
        vx[i] = row_src(row, 64 * (row >> 5) + 32 * half + (row & 31), lane, D_ * 2) + 4096 - i * 1024;
      }
      auto issue = [&](int g) {
        const int k = kstep2(g, 16, phi, side1);
        const u32 m = ldsb + (g & 3) * SLOT;
        dma<4>(m, vp1, rp1, k * 128);
        dma<4>(m + QOFF, vq1, rq1d, k * 128);
      };
      auto run = [&](int g0, int g1) {
#pragma unroll 1
        for (int g = g0; g < g1; g++) { issue(g); wait_vm<16>(); bar(); }
      };
      flag_request(rall, p == 0 ? FL_TICK * 65536 + grpcell : NOWHERE, base);
      issue(1); issue(2);
      {
        u32 z = lane < 8 ? (lane >> 2) * 65536 + (tt * 64 + ft * 4 + (lane & 3)) * 64 : FL_ACK * 65536 + (tt * 64 + ft * 4) * 64;
        if (lane == 9) z = tt == 0 ? colcnt + CN_AB : NOWHERE;
        if (lane >= 10) z = ft == 0 ? rowcnt + (lane == 10 ? CN_H : (lane - 9) * 128) : NOWHERE;
        word_store(rall, p == 0 && lane < 14 ? z : NOWHERE, 0u);
      }
      wait_vm<18>();
      bar();
      const int js = (phi + 1) & 3;
      auto sums = [&]() {
        const int c = lane & 15, g = lane >> 4, e = c & 3;
        const u32x4 ones4 = {0x3F803F80u, 0x3F803F80u, 0x3F803F80u, 0x3F803F80u};
        const bf16x8 ones = __builtin_bit_cast(bf16x8, ones4);
        const int a = js * SLOT + (32 * p + c) * 128 + ((g ^ (c >> 1)) * 16);
        rsrc_t rx = mk_rsrc(ws + WS_SX + (size_t)(tt * 16 + (js ^ (side1 << 2))) * 1024 + p * 256, 256);
#pragma unroll
        for (int i = 0; i < 2; i++) {
          const bf16x8 x0 = *(const LAS bf16x8*)(sm + (a + i * 2048)), x1 = *(const LAS bf16x8*)(sm + ((a ^ 64) + i * 2048));
          const f32x4 z = {0.f, 0.f, 0.f, 0.f};
          const f32x4 sx = __builtin_amdgcn_mfma_f32_16x16x32_bf16(x1, ones, __builtin_amdgcn_mfma_f32_16x16x32_bf16(x0, ones, z, 0, 0, 0), 0, 0, 0);
          const f32x4 sq = __builtin_amdgcn_mfma_f32_16x16x32_bf16(x1, x1, __builtin_amdgcn_mfma_f32_16x16x32_bf16(x0, x0, z, 0, 0, 0), 0, 0, 0);
          const float q = e == 0 ? sq[0] : e == 1 ? sq[1] : e == 2 ? sq[2] : sq[3];
          const float t = e == 0 ? sx[0] : e == 1 ? sx[1] : e == 2 ? sx[2] : sx[3];
          __builtin_amdgcn_raw_buffer_store_b64((u32x2){__float_as_uint(t), __float_as_uint(q)}, rx, (c >> 2) == g ? (16 * i + c) * 8 : NOWHERE, 0, 16);
        }
      };
      issue(3);
      if (js == 0) sums();
      asm volatile("s_waitcnt vmcnt(16)" : "+v"(base)::"memory");
      tick = __builtin_amdgcn_readlane(base, tt) + 1;
      bar();
#pragma unroll 1
      for (int g = 2; g < 6; g++) {
        issue(g + 2);
        if (g == js + 1) sums();
        wait_vm<16>();
        if (g == 3 && p == 0) word_store(rall, tickoff, tick);
        bar();
      }
      issue(8); wait_vm<16>(); bar();
      flag_request(rall, p == 0 ? FL_TICK * 65536 + grpcell : NOWHERE, fv);
      issue(9); wait_vm<16>(); bar();
      issue(10);
      asm volatile("s_waitcnt vmcnt(16)" : "+v"(fv)::"memory");
      if (p == 0) {
        watch(rall, FL_TICK * 65536 + grpcell, fv, [&](u32 w) { return w != base; }, tickoff, tick);
        count_up(ws, rowcnt + CN_SX);
        word_store(rall, ackoff, 1u);
      }
      bar();
      run(11, 14);
      dma<2>(lds0 + XOFF + p * 2048, vx, rp1, et * 256);
      dma<2>(lds0 + XOFF + 8192 + p * 2048, vx, rp1, et * 256 + 128);
      flag_request(rall, p == 0 ? rowcnt + CN_SX : NOWHERE, fv);
      issue(14); wait_vm<21>(); bar();
      issue(15);
      asm volatile("s_waitcnt vmcnt(16)" : "+v"(fv)::"memory");
      if (p == 0) watch(rall, rowcnt + CN_SX, fv, [&](u32 w) { return w >= 16u; }, tickoff, tick);
      bar();
      u32x2 sv[8];
      rsrc_t rsx = mk_rsrc(ws + WS_SX + (size_t)tt * 16384, 16384);
#pragma unroll
      for (int i = 0; i < 8; i++)
        asm volatile("s_nop 4\n\tbuffer_load_dwordx2 %0, %1, %2, %3 offen sc1" : "=&v"(sv[i]) : "v"((pt & 1) * 8192 + (pt >> 1) * 8), "s"(rsx), "s"(i * 1024) : "memory");
      g2q(0); wait_vm<20>(); bar();
      g2q(1); wait_vm<16>(); bar();
      g2q(2);
      asm volatile("s_waitcnt vmcnt(12)" : "+v"(sv[0]), "+v"(sv[1]), "+v"(sv[2]), "+v"(sv[3]), "+v"(sv[4]), "+v"(sv[5]), "+v"(sv[6]), "+v"(sv[7])::"memory");
      float a = 0.f, q = 0.f;
#pragma unroll
      for (int i = 0; i < 8; i++) { a += __uint_as_float(sv[i][0]); q += __uint_as_float(sv[i][1]); }
      a += __builtin_bit_cast(float, __builtin_amdgcn_update_dpp(0, __builtin_bit_cast(int, a), 0xB1, 0xF, 0xF, true));
      q += __builtin_bit_cast(float, __builtin_amdgcn_update_dpp(0, __builtin_bit_cast(int, q), 0xB1, 0xF, 0xF, true));
      const float mean = a * (1.0f / D_), var = fmaxf(q * (1.0f / D_) - mean * mean, 0.f);
      const float rstd = (half ? -1.f : 1.f) * __builtin_amdgcn_rsqf(var + LN_EPS);
      tabA[(pt & 1) * 128 + (pt >> 1)] = (pt & 1) ? -mean * rstd : rstd;
      wait_lgkm0();
    }
    {
      rsrc_t rp = mk_rsrc(ws + WS_AB + (size_t)et * 32 * 16384, 9 * 32 * 16384);
      rsrc_t rq0 = mk_rsrc((const char*)cre + (size_t)tt * 128 * S_ * 2 - 4096, 128 * S_ * 2 + 4096);
      rsrc_t rq1 = mk_rsrc((const char*)cim + (size_t)tt * 128 * S_ * 2 - 4096, 128 * S_ * 2 + 4096);
      u32 vh[2];
#pragma unroll
      for (int i = 0; i < 2; i++) {
        const int row = 64 * (p >> 1) + 16 * (p & 1) + 8 * i + (lane >> 3);
        vh[i] = row_src(row, row ^ (half * 32), lane, S_ * 2) + 4096 - i * 1024;
      }
      const u32 ldsh = lds0 + QOFF + (p >> 1) * 8192 + (p & 1) * 2048;
      auto fp = [&](int q) { dma_sc1<4>(ldsb + (q & 3) * SLOT, vlin, rp, (half * 256 + g2k(q)) * 16384); };
      auto full = [&](int q) { fp(q); g2q(q); };
      auto halfs = [&](int g) {
        const int k = kstep2(g, 8, phi, side2), term = k >> 2, ks = skip(26 + (k & 3));
        dma_sc1<4>(ldsb + (g & 3) * SLOT, vlin, rp, (term * 256 + ks) * 16384);
        if (term) dma<2>(ldsh + (g & 3) * SLOT, vh, rq1, ks * 128);
        else dma<2>(ldsh + (g & 3) * SLOT, vh, rq0, ks * 128);
      };
      bar();
      wait_vm<8>(); bar();
      g2q(3);
      rsrc_t rw = mk_rsrc((const char*)w1 + (size_t)(tt * 64 + p * 16) * F_ * 2 + ft * 448, 15 * F_ * 2 + 448);
      u32x4 v[8];
      const int l28 = lane < 28 ? lane : lane < 56 ? lane - 28 : 27;
      const u32 vo = (lane >= 28 ? F_ * 2 : 0) + l28 * 16;
#pragma unroll
      for (int i = 0; i < 8; i++)
        asm volatile("s_nop 4\n\tbuffer_load_dwordx4 %0, %1, %2, %3 offen" : "=&v"(v[i]) : "v"(vo), "s"(rw), "s"(i * 2 * F_ * 2) : "memory");
      wait_vm<16>(); bar();
      bar();
      if (p == 0) {
        count_up(ws, colcnt + CN_AB);
        fv = 0;
        watch(rall, colcnt + CN_AB, fv, [&](u32 w) { return w >= 16u; }, tickoff, tick);
        word_store(rall, ackoff, 2u);
      }
      bar();
      fp(2); fp(3); full(4);
      asm volatile("s_waitcnt vmcnt(12)" : "+v"(v[0]), "+v"(v[1]), "+v"(v[2]), "+v"(v[3]), "+v"(v[4]), "+v"(v[5]), "+v"(v[6]), "+v"(v[7])::"memory");
      bar();
      full(5);
      {
        float s[8] = {0, 0, 0, 0, 0, 0, 0, 0};
#pragma unroll
        for (int i = 0; i < 8; i++)
#pragma unroll
          for (int e = 0; e < 4; e++) { s[2 * e] += blo(v[i][e]); s[2 * e + 1] += bhi(v[i][e]); }
#pragma unroll
        for (int e = 0; e < 8; e++) s[e] += __shfl(s[e], lane + 28);
        if (lane < 28) {
          LAS float* d = (LAS float*)(sm + COFF) + p * 224 + lane * 8;
          *(LAS f32x4*)d = (f32x4){s[0], s[1], s[2], s[3]};
          *(LAS f32x4*)(d + 4) = (f32x4){s[4], s[5], s[6], s[7]};
        }
      }
      wait_vm<16>(); wait_lgkm0(); bar();
      full(6);
      if (p == 0 && lane < 56) {
        const LAS f32x4* d = (const LAS f32x4*)(sm + COFF) + lane;
        const f32x4 t = (d[0] + d[56]) + (d[112] + d[168]);
        store_sc1(mk_rsrc(ws + WS_CS + (size_t)tt * F_ * 4 + ft * 896, 896), lane * 16, __builtin_bit_cast(u32x4, t));
      }
      wait_vm<16>(); bar();
      full(7); wait_vm<16>(); bar();
      full(8); wait_vm<16>();
      if (p == 0) count_up(ws, colcnt + CN_AB);
      bar();
#pragma unroll 1
      for (int q = 9; q < 26; q++) { full(q); wait_vm<16>(); bar(); }
      const u32 lastoff = lane < 32 ? FL_ACK * 65536 + grpcell : colcnt + CN_AB;
      const u32 lastmin = lane >= 32 ? 32u : lane == 16 + (ft ^ 8) ? 2u : 1u;
      flag_request(rall, p == 0 ? lastoff : NOWHERE, fv);
      full(26); wait_vm<16>(); bar();
      full(27);
      asm volatile("s_waitcnt vmcnt(16)" : "+v"(fv)::"memory");
      if (p == 0) watch(rall, lastoff, fv, [&](u32 w) { return w >= lastmin; }, tickoff, tick);
      bar();
      halfs(0); wait_vm<14>(); bar();
#pragma unroll 1
      for (int g = 1; g < 8; g++) { halfs(g); wait_vm<12>(); bar(); }
    }
    rsrc_t rp4 = mk_rsrc(w2 + et * 128, F_ * D_ * 2 - et * 256);
    u32 vp4[4];
#pragma unroll
    for (int i = 0; i < 4; i++) {
      const int k = 16 * (p & 1) + 4 * i + (lane >> 4);
      vp4[i] = k * (D_ * 2) + (((lane & 15) ^ ((k & 7) << 1)) * 16) - i * 1024;
    }
    auto g4blk = [&](int q) {
      const int r = q < 10 ? kstep2(q - 3, 7, phi, xcd & 1) : kstep2(q - 10, 14, phi, xcd & 1);
      const int i = q < 10 ? r : r >> 1, pr = q < 3 ? q : q < 10 ? 0 : 1 + (r & 1);
      return 7 * (8 * half + (q < 3 ? et : i + (i >= et ? 1 : 0))) + pr + 4 * (p >> 1);
    };
    auto g4p = [&](int q) { dma<4>(ldsb + ((q + 2) & 3) * SLOT, vp4, rp4, g4blk(q) * (32 * D_ * 2)); };
    {
      rsrc_t rp = mk_rsrc((const char*)w1 + ft * 448 - 4096, D_ * F_ * 2 - ft * 448 + 4096);
      rsrc_t rq = mk_rsrc(ws + WS_H + (size_t)tt * 32 * 8192, 32 * 8192);
      u32 vp[7];
#pragma unroll
      for (int i = 0; i < 7; i++) {
        const int o = (p * 7 + i) * 64 + lane, k = o / 28, ch = o % 28;
        vp[i] = k * (F_ * 2) + ((ch ^ (((k >> 2) & 1) << 1)) * 16) + 4096 - (i & 3) * 1024;
      }
      rsrc_t rw = mk_rsrc(w2 + (size_t)(half * 1792 + tt * 112 + 28 * p) * D_ + et * 128, 27 * D_ * 2 + 256);
      const u32 vw = (lane >> 1) * (D_ * 2) + (lane & 1) * 128;
      auto pa = [&](int g, int sl) {
        const u32 so = kstep(g, 16, phi) * (64 * F_ * 2), m = lds0 + sl * P3 + p * 7168;
        dma<4>(m, vp, rp, so);
        dma<3>(m + 4096, vp + 4, rp, so);
      };
      auto q = [&](int g, int sl) { dma_sc1<4>(ldsb + Q3OFF + sl * SLOT, vlin, rq, kstep(g, 16, phi) * 16384); };
      wait_vm<6>(); bar();
      wait_vm<0>(); bar();
      bar();
      if (p == 0) count_up(ws, rowcnt + CN_H);
      pa(0, 0); pa(1, 1);
      pull_lines(lds0 + COFF + 3584, p == 1 ? (128 + 16 * li + (lane >> 2)) * (F_ * 2) + (lane & 3) * 128 + 4096 : NOWHERE, rp, 0);
      rsrc_t rs = mk_rsrc(ws + WS_ST + (size_t)tt * 16384, 16384);
      rsrc_t rc = mk_rsrc(ws + WS_CS + ft * 896, 15 * F_ * 4 + 896);
      u32x2 sv[8];
      u32 cv[16], bv;
#pragma unroll
      for (int i = 0; i < 16; i++)
        asm volatile("s_nop 4\n\tbuffer_load_dword %0, %1, %2, %3 offen sc1" : "=&v"(cv[i]) : "v"(min(pt, 223) * 4), "s"(rc), "s"(i * F_ * 4) : "memory");
      asm volatile("s_nop 4\n\tbuffer_load_ushort %0, %1, %2, 0 offen" : "=&v"(bv) : "v"(min(pt, 223) * 2), "s"(mk_rsrc(b1 + ft * 224, 448)) : "memory");
      if (p == 0) { fv = 0; watch(rall, rowcnt + CN_H, fv, [&](u32 w) { return w >= 16u; }, NOWHERE, tick); }
      bar();
      q(0, 0); q(1, 1);
      wait_vm<4>();
      bar();
      pa(2, 2); q(2, 2);
#pragma unroll
      for (int i = 0; i < 8; i++)
        asm volatile("s_nop 4\n\tbuffer_load_dwordx2 %0, %1, %2, %3 offen sc1" : "=&v"(sv[i]) : "v"((pt & 1) * 8192 + (pt >> 1) * 8), "s"(rs), "s"(i * 1024) : "memory");
      wait_vm<19>(); bar();
      pa(3, 0); q(3, 0); wait_vm<19>(); bar();
      pa(4, 1); q(4, 1); wait_vm<11>();
      asm volatile("" : "+v"(sv[0]), "+v"(sv[1]), "+v"(sv[2]), "+v"(sv[3]), "+v"(sv[4]), "+v"(sv[5]), "+v"(sv[6]), "+v"(sv[7]), "+v"(bv)::"memory");
      asm volatile("" : "+v"(cv[0]), "+v"(cv[1]), "+v"(cv[2]), "+v"(cv[3]), "+v"(cv[4]), "+v"(cv[5]), "+v"(cv[6]), "+v"(cv[7]), "+v"(cv[8]),
                   "+v"(cv[9]), "+v"(cv[10]), "+v"(cv[11]), "+v"(cv[12]), "+v"(cv[13]), "+v"(cv[14]), "+v"(cv[15])::"memory");
      bar();
      pa(5, 2); q(5, 2);
      {
        float s = 0.f, a = 0.f, qq = 0.f;
#pragma unroll
        for (int i = 0; i < 16; i++) s += __uint_as_float(cv[i]);
        if (pt < 224) { tabC[pt] = s; tabBias[pt] = __uint_as_float(bv << 16); }
#pragma unroll
        for (int i = 0; i < 8; i++) { a += __uint_as_float(sv[i][0]); qq += __uint_as_float(sv[i][1]); }
        a += __builtin_bit_cast(float, __builtin_amdgcn_update_dpp(0, __builtin_bit_cast(int, a), 0xB1, 0xF, 0xF, true));
        qq += __builtin_bit_cast(float, __builtin_amdgcn_update_dpp(0, __builtin_bit_cast(int, qq), 0xB1, 0xF, 0xF, true));
        const float mean = a * (1.0f / D_), var = fmaxf(qq * (1.0f / D_) - mean * mean, 0.f);
        const float rstd = __builtin_amdgcn_rsqf(var + LN_EPS);
        tabA[(pt & 1) * 128 + (pt >> 1)] = (pt & 1) ? -mean * rstd : rstd;
      }
      wait_vm<11>(); wait_lgkm0(); bar();
      int sl = 0;
#pragma unroll 1
      for (int g = 6; g < 16; g++) {
        pa(g, sl); q(g, sl);
        const int gp = 6 + (loc & 7);
        if (g == gp) pull_lines(lds0 + COFF + 3584, vw, rw, 0);
        if ((unsigned)(g - gp) < 2u) wait_vm<12>(); else wait_vm<11>();
        bar();
        sl = sl == 2 ? 0 : sl + 1;
      }
      g4p(0); wait_vm<4>(); bar();
      g4p(1);
      bar();
    }
    {
      rsrc_t rq = mk_rsrc(ws + WS_R + (size_t)tt * 112 * 8192, 112 * 8192);
      const u32 vq0 = (4 * (p & 1) + 2 * half) * 1024 + lane * 16, vq1 = (4 * (p & 1) + 2 - 2 * half) * 1024 + lane * 16;
      auto g4q = [&](int q) {
        const u32 m = ldsb + QOFF + ((q + 2) & 3) * SLOT, so = g4blk(q) * 8192;
        dma_sc1<2>(m, vq0, rq, so);
        dma_sc1<2>(m + 2048, vq1, rq, so);
      };
      auto halfs = [&](int g) {
        const int k = kstep2(g, 8, phi, xcd & 1), blk = 7 * (2 * k + (p >> 1)) + 3;
        dma<4>(ldsb + ((g + 2) & 3) * SLOT, vp4, rp4, blk * (32 * D_ * 2));
        dma_sc1<2>(ldsb + QOFF + ((g + 2) & 3) * SLOT, vq0, rq, blk * 8192);
      };
      wait_vm<4>(); bar();
      if (p == 0) count_up(ws, rowcnt + CN_RA);
      g4p(2); wait_vm<4>(); bar();
      g4p(3);
      {
        const int qq = 4 + min(lane >> 3, 2);
        const u32 b4 = g4blk(4), b5 = g4blk(5), b6 = g4blk(6), bb = qq == 4 ? b4 : qq == 5 ? b5 : b6;
        pull_lines(lds0 + COFF + 3584, (p & 1) == 0 && lane < 24 ? (bb * 32 + 4 * li + ((lane >> 1) & 3)) * (D_ * 2) + (lane & 1) * 128 : NOWHERE, rp4, 0);
      }
      wait_vm<5>();
      if (p == 0) { fv = 0; watch(rall, rowcnt + CN_RA, fv, [&](u32 w) { return w >= 16u; }, NOWHERE, tick); }
      bar();
      g4q(3); g4p(4); g4q(4); g4p(5); g4q(5);
      wait_vm<16>(); bar();
      if (p == 0) count_up(ws, rowcnt + CN_RB);
      g4p(6); g4q(6); wait_vm<16>(); bar();
      g4p(7); g4q(7); wait_vm<16>(); bar();
      flag_request(rall, p == 0 ? rowcnt + CN_RB : NOWHERE, fv);
      g4p(8); g4q(8); wait_vm<16>(); bar();
      g4p(9); g4q(9);
      asm volatile("s_waitcnt vmcnt(16)" : "+v"(fv)::"memory");
      if (p == 0) watch(rall, rowcnt + CN_RB, fv, [&](u32 w) { return w >= 16u; }, NOWHERE, tick);
      bar();
#pragma unroll 1
      for (int q = 10; q < 24; q++) { g4p(q); g4q(q); wait_vm<16>(); bar(); }
      halfs(0); wait_vm<14>(); bar();
#pragma unroll 1
      for (int g = 1; g < 8; g++) { halfs(g); wait_vm<12>(); bar(); }
      wait_vm<6>(); bar();
      wait_vm<0>(); bar();
    }
  } else {
    const int lane = lane0;
    const int wP = wid >> 1, wQ = wid & 1, c = lane & 15, g = lane >> 4;
    const int m0 = 4 * wQ + 2 * half, cell = ft * 4 + wid;
    {
      Consumer<4, M_ROW, M_ROW> C;
      C.init(sm, wP, wQ, lane, wP);
      f32x4 acc[4][4], sd[2];
      C.zero(acc);
#pragma unroll
      for (int i = 0; i < 2; i++) sd[i] = (f32x4){0.f, 0.f, 0.f, 0.f};
      const u32x4 ones4 = {0x3F803F80u, 0x3F803F80u, 0x3F803F80u, 0x3F803F80u};
      bf16x8 ones = __builtin_bit_cast(bf16x8, ones4);
      asm volatile("" : "+v"(ones));
      bar();
      C.first();
#define G1_HALF0(S) C.load<S, 1, 4>(); C.mma<0, 4>(acc); C.qsums<0>(sd, ones); C.interleave<18, 4>();
#define G1_STEP(S) G1_HALF0(S) wait_lgkm0(); bar(); __builtin_amdgcn_sched_barrier(0); \
      C.load<(S + 1) % NST, 0, 4>(); C.mma<1, 4>(acc); C.qsums<1>(sd, ones); C.interleave<18, 4>();
      for (int it = 0; it < 3; it++) { G1_STEP(0) G1_STEP(1) G1_STEP(2) G1_STEP(3) }
      G1_STEP(0) G1_STEP(1) G1_STEP(2) G1_HALF0(3)
      wait_lgkm0();
      C.qsums<1>(sd, ones);
      __builtin_amdgcn_sched_barrier(0);
      C.mma<1, 4>(acc);
      tabC[g < 2 ? 64 * wQ + 32 * wP + 16 * g + c : 128 + lane] = g ? sd[1][0] : sd[0][0];
      __builtin_amdgcn_sched_group_barrier(0x8, 4, 0);
      __builtin_amdgcn_sched_group_barrier(0x2 | 0x200, 8, 0);
      __builtin_amdgcn_sched_group_barrier(0x8, 12, 0);
      __builtin_amdgcn_sched_barrier(0);
      wait_lgkm0(); bar();
      rsrc_t ro = mk_rsrc(ws + WS_AB + ((size_t)((half * 8 + et) * 32 + 2 * tt + wP) * 16 + 4 * wQ) * 1024, 12288);
      f32x4 ra[4], rb[4];
      float rs[4];
#pragma unroll
      for (int i = 0; i < 4; i++) {
        ra[i] = *(const LAS f32x4*)(tabA + 64 * wP + 16 * i + 4 * g);
        rb[i] = *(const LAS f32x4*)(tabB + 64 * wP + 16 * i + 4 * g);
        rs[i] = tabC[64 * wQ + 16 * (i ^ (2 * wP)) + c];
      }
#pragma unroll
      for (int j = 0; j < 4; j++)
#pragma unroll
        for (int u = 0; u < 2; u++) {
          const f32x4 a = ra[2 * u] * acc[2 * u][j] + rb[2 * u] * rs[j], b = ra[2 * u + 1] * acc[2 * u + 1][j] + rb[2 * u + 1] * rs[j];
          const u32x4 o = {cvtpk(a[0], a[1]), cvtpk(a[2], a[3]), cvtpk(b[0], b[1]), cvtpk(b[2], b[3])};
          const int at = (u * 8 + (j ^ (2 * wP))) * 1024 + lane * 16;
          store_sc1(ro, at, o);
          *(LAS u32x4*)(sm + (wP * SLOT + wQ * 4096 + at)) = o;
        }
    }
    u32x4 hk[2][2];
    {
      Consumer<4, M_TILED, M_ROW> C;
      C.init(sm, wP, wQ, lane);
      f32x4 acc[4][4];
      wait_lgkm0(); bar();
      C.first();
      C.load<0, 1, 4>(); C.mma<0, 4, true>(acc); C.interleave<16, 4>();
      wait_lgkm0(); bar();
      __builtin_amdgcn_sched_barrier(0);
      C.load<1, 0, 4>(); C.mma<1, 4>(acc); C.interleave<16, 4>();
      C.load<1, 1, 4>(); C.mma<0, 4>(acc); C.interleave<16, 4>();
      wait_lgkm0();
      C.mma<1, 4>(acc);
      __builtin_amdgcn_sched_barrier(0);
      wait_vm<0>();
      bar();
      bar();
      bar();
      C.load<2, 0, 4>();
      C.step<2, 4, 4>(acc); C.step<3, 4, 4>(acc);
      for (int it = 0; it < 5; it++) C.step4<4, 4>(acc);
      C.step4<4, 2>(acc);
      xch_send(ws + WS_X2, acc, wid, lane);
      __builtin_amdgcn_sched_barrier(0);
      rsrc_t rx = mk_rsrc(ws + WS_X2 + ((size_t)(blockIdx.x ^ 4) * 4 + wid) * 4096, 4096);
      rsrc_t rf = mk_rsrc(ws + WS_FL + ((size_t)(FL_X2 * 16 + tt) * 64 + (cell ^ 32)) * 64, 64);
      u32x4 rv[4];
      u32 fv;
      C.step<0, 2, 2>(acc); C.step<1, 2, 2>(acc); C.step<2, 2, 2>(acc);
      flag_set(ws, FL_X2, tt, cell);
      C.step<3, 2, 2>(acc);
      C.step<0, 2, 2>(acc); C.step<1, 2, 2>(acc);
      flag_request(rf, 0, fv);
      __builtin_amdgcn_sched_barrier(0);
      C.step<2, 2, 2>(acc);
      wait_vm<0>();
      flag_wait(rf, 0, fv);
      xch_request(rx, rv, lane);
      __builtin_amdgcn_sched_barrier(0);
      C.load<3, 1, 2>(); C.mma<0, 2>(acc); C.interleave<8, 2>();
      C.mma<1, 2>(acc);
      xch_finish(rv);
      rsrc_t rh = mk_rsrc(ws + WS_H + ((size_t)(tt * 32 + 4 * et + 2 * wP) * 8 + m0) * 1024, 16384);
      rsrc_t rs = mk_rsrc(ws + WS_ST + ((size_t)(tt * 16 + 2 * et + wP) * 128 + 16 * m0) * 8, 256);
#pragma unroll
      for (int jj = 0; jj < 2; jj++) {
        float s = 0.f, q = 0.f;
#pragma unroll
        for (int u = 0; u < 2; u++) {
          const u32x4 o = rv[jj * 2 + u];
          const f32x4 a = acc[2 * u][jj], b = acc[2 * u + 1][jj];
          const int xrow = 32 * wQ + 16 * jj + c;
          const LAS char* xp = sm + (XOFF + wP * 8192 + xrow * 128 + (g & 1) * 8);
          const u32x2 xa = *(const LAS u32x2*)(xp + (((4 * u + (g >> 1)) ^ ((xrow >> 1) & 7)) * 16));
          const u32x2 xb = *(const LAS u32x2*)(xp + (((4 * u + 2 + (g >> 1)) ^ ((xrow >> 1) & 7)) * 16));
          u32x4 h;
          h[0] = cvtpk(a[0] + blo(o[0]) + blo(xa[0]), a[1] + bhi(o[0]) + bhi(xa[0]));
          h[1] = cvtpk(a[2] + blo(o[1]) + blo(xa[1]), a[3] + bhi(o[1]) + bhi(xa[1]));
          h[2] = cvtpk(b[0] + blo(o[2]) + blo(xb[0]), b[1] + bhi(o[2]) + bhi(xb[0]));
          h[3] = cvtpk(b[2] + blo(o[3]) + blo(xb[1]), b[3] + bhi(o[3]) + bhi(xb[1]));
          store_sc1(rh, (u * 8 + jj) * 1024 + lane * 16, h);
          hk[jj][u] = h;
#pragma unroll
          for (int e = 0; e < 4; e++) { const float lo = blo(h[e]), hi = bhi(h[e]); s += lo + hi; q += lo * lo + hi * hi; }
        }
        s = xsum32(xsum16(s)); q = xsum32(xsum16(q));
        if (g == 0) __builtin_amdgcn_raw_buffer_store_b64((u32x2){__float_as_uint(s), __float_as_uint(q)}, rs, (16 * jj + c) * 8, 0, 16);
      }
      wait_vm<0>();
      bar();
#pragma unroll
      for (int q = 0; q < 4; q++) *(LAS u32x4*)(sm + XOFF + wid * 4096 + q * 1024 + lane * 16) = hk[q >> 1][q & 1];
      bar();
    }
    rsrc_t rr = mk_rsrc(ws + WS_R + ((size_t)(tt * 112 + 7 * ft) * 8 + 4 * wQ) * 1024, 7 * 8192);
    u32x4 ro[14];
    {
      Consumer<7, M_TR448, M_TILED> C;
      C.init(sm, wP, wQ, lane);
      f32x4 acc[7][4];
      C.zero(acc);
      bar();
      C.first();
      for (int it = 0; it < 5; it++) { C.step<0, 4, 4>(acc); C.step<1, 4, 4>(acc); C.step<2, 4, 4>(acc); }
      C.last<0, 4>(acc);
      bar();
      float ra[4], rb[4];
#pragma unroll
      for (int j = 0; j < 4; j++) { ra[j] = tabA[64 * wQ + 16 * j + c]; rb[j] = tabB[64 * wQ + 16 * j + c]; }
      f32x4 cs[7], bi[7];
#pragma unroll
      for (int i = 0; i < 7; i++) {
        const int col = (i < 6 ? 128 * wP + 16 * i : 96 + 16 * wP) + 4 * g;
        cs[i] = *(const LAS f32x4*)(tabC + col); bi[i] = *(const LAS f32x4*)(tabBias + col);
      }
      auto act = [&](int i, int j) {
        f32x4 v = ra[j] * acc[i][j] + (rb[j] * cs[i] + bi[i]);
#pragma unroll
        for (int e = 0; e < 4; e++) v[e] = fmaxf(v[e], 0.f);
        v = v * v;
        return (u32x2){cvtpk(v[0], v[1]), cvtpk(v[2], v[3])};
      };
#pragma unroll
      for (int u = 0; u < 3; u++) {
        LAS char* own = sm + (QOFF + ((u + 2) & 3) * SLOT + wP * 8192 + 4 * wQ * 1024 + lane * 16);
#pragma unroll
        for (int j = 0; j < 4; j++) {
          const u32x2 a = act(2 * u, j), b = act(2 * u + 1, j);
          ro[4 * u + j] = (u32x4){a[0], a[1], b[0], b[1]};
          if (4 * u + j < EARLY) store_sc1(rr, ((4 * wP + u) * 8 + j) * 1024 + lane * 16, ro[4 * u + j]);
          *(LAS u32x4*)(own + (j ^ (2 * half)) * 1024) = ro[4 * u + j];
        }
      }
#pragma unroll
      for (int j = 0; j < 4; j++)
        *(LAS u32x2*)(sm + (QOFF + SLOT + (4 * wQ + j) * 1024 + lane * 16 + 8 * wP)) = act(6, j);
    }
    {
      Consumer<4, M_TR256, M_TILED> C;
      C.init(sm, wP, wQ, lane);
      f32x4 acc[4][4];
      wait_vm<EARLY - 4>();
      wait_lgkm0(); bar();
      C.load<2, 0, 4>();
#pragma unroll
      for (int j = 0; j < 2; j++) ro[12 + j] = *(const LAS u32x4*)(sm + (QOFF + SLOT + (4 * wQ + 2 * wP + j) * 1024 + lane * 16));
      auto put = [&](int hs, int n) {
        constexpr int LEFT = 14 - EARLY;
        for (int q = 0; q < LEFT; q++)
          if (q * 88 / LEFT == hs * 16 + n) {
            const int t = EARLY + q;
            store_sc1(rr, (t < 12 ? (4 * wP + (t >> 2)) * 8 + (t & 3) : 24 + 2 * wP + (t & 1)) * 1024 + lane * 16, ro[t]);
          }
      };
      C.half_with<2, 1, true>(acc, [&](int n) { put(0, n); });
      wait_lgkm0(); bar();
      C.half_with<3, 0, false>(acc, [&](int n) { put(1, n); });
      C.half_with<3, 1, false>(acc, [&](int n) { put(2, n); });
      wait_lgkm0(); bar();
      C.half_with<0, 0, false>(acc, [&](int n) { put(3, n); });
      C.half_with<0, 1, false>(acc, [&](int n) { put(4, n); });
      wait_lgkm0();
      C.half_with<-1, 0, false>(acc, [&](int n) { put(5, n); });
      __builtin_amdgcn_sched_barrier(0);
      wait_vm<0>();
      bar();
      C.load<1, 0, 4>();
      C.step<1, 4, 4>(acc); C.step<2, 4, 4>(acc); C.step<3, 4, 4>(acc);
      for (int it = 0; it < 4; it++) C.step4<4, 4>(acc);
      C.step<0, 4, 4>(acc); C.step<1, 4, 2>(acc);
      xch_send(ws + WS_X4, acc, wid, lane);
      u32x2 br[4];
#pragma unroll
      for (int i = 0; i < 4; i++) br[i] = *(const u32x2*)(b2 + et * 128 + 64 * wP + 16 * i + 4 * g);
      __builtin_amdgcn_sched_barrier(0);
      rsrc_t rx = mk_rsrc(ws + WS_X4 + ((size_t)(blockIdx.x ^ 4) * 4 + wid) * 4096, 4096);
      rsrc_t rf = mk_rsrc(ws + WS_FL + ((size_t)(FL_X4 * 16 + tt) * 64 + (cell ^ 32)) * 64, 64);
      u32x4 rv[4];
      u32 fv;
      C.step<2, 2, 2>(acc); C.step<3, 2, 2>(acc); C.step<0, 2, 2>(acc);
      flag_set(ws, FL_X4, tt, cell);
      C.step<1, 2, 2>(acc); C.step<2, 2, 2>(acc); C.step<3, 2, 2>(acc);
      flag_request(rf, 0, fv);
      __builtin_amdgcn_sched_barrier(0);
      C.step<0, 2, 2>(acc);
      wait_vm<0>();
      flag_wait(rf, 0, fv);
      xch_request(rx, rv, lane);
      __builtin_amdgcn_sched_barrier(0);
      C.last<1, 2>(acc);
      xch_finish(rv);
      rsrc_t ro = mk_rsrc(out + (size_t)(tt * 128 + 16 * m0) * D_ + et * 128 + 64 * wP, 31 * D_ * 2 + 128);
#pragma unroll
      for (int jj = 0; jj < 2; jj++)
#pragma unroll
        for (int u = 0; u < 2; u++) {
          const u32x4 o = rv[jj * 2 + u], h = *(const LAS u32x4*)(sm + XOFF + wid * 4096 + (jj * 2 + u) * 1024 + lane * 16);
          const f32x4 a = acc[2 * u][jj], b = acc[2 * u + 1][jj];
          const u32x2 ba = br[2 * u], bb = br[2 * u + 1];
          const u32 a0 = cvtpk(a[0] + blo(o[0]) + blo(ba[0]) + blo(h[0]), a[1] + bhi(o[0]) + bhi(ba[0]) + bhi(h[0]));
          const u32 a1 = cvtpk(a[2] + blo(o[1]) + blo(ba[1]) + blo(h[1]), a[3] + bhi(o[1]) + bhi(ba[1]) + bhi(h[1]));
          const u32 b0 = cvtpk(b[0] + blo(o[2]) + blo(bb[0]) + blo(h[2]), b[1] + bhi(o[2]) + bhi(bb[0]) + bhi(h[2]));
          const u32 b1_ = cvtpk(b[2] + blo(o[3]) + blo(bb[1]) + blo(h[3]), b[3] + bhi(o[3]) + bhi(bb[1]) + bhi(h[3]));
          const auto s0 = __builtin_amdgcn_permlane16_swap(a0, b0, false, false);
          const auto s1 = __builtin_amdgcn_permlane16_swap(a1, b1_, false, false);
          __builtin_amdgcn_raw_buffer_store_b128((u32x4){s0[0], s1[0], s0[1], s1[1]}, ro,
                                                 (16 * jj + c) * (D_ * 2) + (32 * u + 16 * (g & 1) + 8 * (g >> 1)) * 2, 0, 16);
        }
    }
  }
}

}

extern "C" void kernel_launch(void* const* d_in, const int* in_sizes, int n_in, void* d_out, int out_size, void* d_ws, size_t ws_size,
                              hipStream_t stream) {
  const u16* const* I = (const u16* const*)d_in;
  (void)hipFuncSetAttribute((const void*)fused_kernel, hipFuncAttributeMaxDynamicSharedMemorySize, LDS_BYTES);
  fused_kernel<<<256, 512, LDS_BYTES, stream>>>(I[0], I[1], I[2], I[3], I[4], I[6], I[7], I[8], I[9], (char*)d_ws, (u16*)d_out);
}
```

```cpp
#include <hip/hip_runtime.h>
#include <hip/hip_bf16.h>
#include <type_traits>

typedef unsigned short u16;
typedef unsigned int u32;
typedef unsigned long long u64;
typedef __bf16 bf16x8 __attribute__((ext_vector_type(8)));
typedef __bf16 bf16x2 __attribute__((ext_vector_type(2)));
typedef short s16x4 __attribute__((ext_vector_type(4)));
typedef float f32x2 __attribute__((ext_vector_type(2)));
typedef float f32x4 __attribute__((ext_vector_type(4)));
typedef u32 u32x2 __attribute__((ext_vector_type(2)));
typedef u32 u32x4 __attribute__((ext_vector_type(4)));
typedef u32 u32x16 __attribute__((ext_vector_type(16)));
typedef __amdgpu_buffer_rsrc_t rsrc_t;
#define GAS __attribute__((address_space(1)))
#define LAS __attribute__((address_space(3)))
#define DI __device__ __forceinline__

namespace {

constexpr int S_ = 2048, D_ = 1024, F_ = 3584;
constexpr float LN_EPS = 1e-5f;
constexpr int SPIN_LIMIT = 1 << 18;
constexpr int EARLY = 6;

constexpr size_t WS_AB = 0;
constexpr size_t WS_H = 8u << 20;
constexpr size_t WS_R = 12u << 20;
constexpr size_t WS_X2 = 27u << 20;
constexpr size_t WS_X4 = 31u << 20;
constexpr size_t WS_ST = 35u << 20;
constexpr size_t WS_CS = 36u << 20;
constexpr size_t WS_FL = 37u << 20;
constexpr size_t WS_SX = 38u << 20;

DI u32 rfl(u32 x) { return __builtin_amdgcn_readfirstlane(x); }
DI u32 cvtpk(float lo, float hi) { f32x2 v = {lo, hi}; return __builtin_bit_cast(u32, __builtin_convertvector(v, bf16x2)); }
DI float blo(u32 v) { return __uint_as_float(v << 16); }
DI float bhi(u32 v) { return __uint_as_float(v & 0xFFFF0000u); }
DI rsrc_t mk_rsrc(const void* p, u32 bytes) { return __builtin_amdgcn_make_buffer_rsrc((void*)p, 0, bytes, 0x00020000); }
DI int fresh_lane() { int l; asm volatile("v_mbcnt_lo_u32_b32 %0, -1, 0\n\tv_mbcnt_hi_u32_b32 %0, -1, %0" : "=v"(l)); return l; }
DI void bar() { asm volatile("s_barrier" ::: "memory"); }
DI void wait_lgkm0() { __builtin_amdgcn_s_waitcnt(0xC07F); }
template <int N> DI void wait_vm() { asm volatile("s_waitcnt vmcnt(%0)" ::"i"(N) : "memory"); }

template <int N> DI void dma(u32 m0, const u32* v, rsrc_t r, u32 so) {
  if constexpr (N == 4)
    asm volatile("s_mov_b32 m0, %0\n\ts_nop 3\n\t"
                 "buffer_load_dwordx4 %1, %5, %6 offen lds\n\t"
                 "buffer_load_dwordx4 %2, %5, %6 offen offset:1024 lds\n\t"
                 "buffer_load_dwordx4 %3, %5, %6 offen offset:2048 lds\n\t"
                 "buffer_load_dwordx4 %4, %5, %6 offen offset:3072 lds"
                 ::"s"(m0), "v"(v[0]), "v"(v[1]), "v"(v[2]), "v"(v[3]), "s"(r), "s"(so) : "memory");
  else if constexpr (N == 3)
    asm volatile("s_mov_b32 m0, %0\n\ts_nop 3\n\t"
                 "buffer_load_dwordx4 %1, %4, %5 offen lds\n\t"
                 "buffer_load_dwordx4 %2, %4, %5 offen offset:1024 lds\n\t"
                 "buffer_load_dwordx4 %3, %4, %5 offen offset:2048 lds"
                 ::"s"(m0), "v"(v[0]), "v"(v[1]), "v"(v[2]), "s"(r), "s"(so) : "memory");
  else
    asm volatile("s_mov_b32 m0, %0\n\ts_nop 3\n\t"
                 "buffer_load_dwordx4 %1, %3, %4 offen lds\n\t"
                 "buffer_load_dwordx4 %2, %3, %4 offen offset:1024 lds"
                 ::"s"(m0), "v"(v[0]), "v"(v[1]), "s"(r), "s"(so) : "memory");
}
template <int N> DI void dma_sc1(u32 m0, u32 v, rsrc_t r, u32 so) {
  if constexpr (N == 4)
    asm volatile("s_mov_b32 m0, %0\n\ts_nop 3\n\t"
                 "buffer_load_dwordx4 %1, %2, %3 offen sc1 lds\n\t"
                 "buffer_load_dwordx4 %1, %2, %3 offen offset:1024 sc1 lds\n\t"
                 "buffer_load_dwordx4 %1, %2, %3 offen offset:2048 sc1 lds\n\t"
                 "buffer_load_dwordx4 %1, %2, %3 offen offset:3072 sc1 lds"
                 ::"s"(m0), "v"(v), "s"(r), "s"(so) : "memory");
  else
    asm volatile("s_mov_b32 m0, %0\n\ts_nop 3\n\t"
                 "buffer_load_dwordx4 %1, %2, %3 offen sc1 lds\n\t"
                 "buffer_load_dwordx4 %1, %2, %3 offen offset:1024 sc1 lds"
                 ::"s"(m0), "v"(v), "s"(r), "s"(so) : "memory");
}

DI float xsum16(float v) { const auto r = __builtin_amdgcn_permlane16_swap(__float_as_uint(v), __float_as_uint(v), false, false); return __uint_as_float(r[0]) + __uint_as_float(r[1]); }
DI float xsum32(float v) { const auto r = __builtin_amdgcn_permlane32_swap(__float_as_uint(v), __float_as_uint(v), false, false); return __uint_as_float(r[0]) + __uint_as_float(r[1]); }

template <int A, int B, class F> DI void static_for(F f) {
  if constexpr (A < B) { f(std::integral_constant<int, A>{}); static_for<A + 1, B>(f); }
}

DI void pull_lines(u32 m0, u32 v, rsrc_t r, u32 so) {
  asm volatile("s_mov_b32 m0, %0\n\ts_nop 3\n\tbuffer_load_dword %1, %2, %3 offen lds" ::"s"(m0), "v"(v), "s"(r), "s"(so) : "memory");
}

DI int kstep(int t, int n, int phi) {
  int k = t;
  if ((t & 3) == phi) { k = t + 4; if (k >= n) k = phi; }
  return k;
}

DI int kstep2(int t, int n, int phi, int side) {
  const int k = kstep(t, n, phi), k2 = k ^ (side << 2);
  return k2 < n ? k2 : k;
}

constexpr int NST = 4;
constexpr int SLOT = 16384;
constexpr int QOFF = NST * SLOT;
constexpr int P3 = 64 * 448;
constexpr int Q3OFF = 3 * P3;
constexpr int TOFF = Q3OFF + 3 * SLOT;
constexpr int COFF = TOFF + 4096;
constexpr int XOFF = COFF + 4096;
constexpr int LDS_BYTES = XOFF + 16384;

enum { M_ROW = 0, M_TILED = 1, M_TR448 = 2, M_TR256 = 3 };

template <int PT, int PMODE, int QMODE>
struct Consumer {
  static constexpr int NS = PMODE == M_TR448 ? 3 : NST, QB = PMODE == M_TR448 ? Q3OFF : QOFF;
  const LAS char* pb[6];
  const LAS char* qb[4];
  bf16x8 fp[2][PT], fq[2][4];

  DI void init(const LAS char* sm, int wP, int wQ, int lane, int qrot = 0) {
    const int r = lane & 15, g = lane >> 4;
    if constexpr (PMODE == M_ROW) {
      const int a = (64 * wP + r) * 128 + ((g ^ (r >> 1)) * 16);
      pb[0] = sm + a; pb[1] = sm + (a ^ 64);
    } else if constexpr (PMODE == M_TILED) {
      pb[0] = sm + (4 * wP * 1024 + lane * 16);
    } else if constexpr (PMODE == M_TR448) {
      const int b = g & 1, a = (4 * g + (r >> 2)) * 448 + (r & 3) * 8;
      pb[0] = sm + (a + (8 * wP + b) * 32);
      pb[1] = sm + (a + (8 * wP - b) * 32);
      pb[2] = sm + (a + ((6 + wP) ^ b) * 32);
#pragma unroll
      for (int i = 0; i < 3; i++) { pb[3 + i] = pb[i] + 2 * P3; asm volatile("" : "+v"(pb[3 + i])); }
    } else {
      const int k = 4 * g + (r >> 2), a = k * 256 + (r & 3) * 8;
#pragma unroll
      for (int i = 0; i < 4; i++) pb[i] = sm + (a + (((4 * wP + i) ^ (k & 7)) * 32));
    }
    if constexpr (QMODE == M_ROW) {
      const int a = QOFF + (64 * wQ + r) * 128 + ((g ^ (r >> 1)) * 16);
      qb[0] = sm + (a + qrot * 4096); qb[1] = sm + ((a ^ 64) + qrot * 4096);
      qb[2] = sm + (a + 4096 - qrot * 4096); qb[3] = sm + ((a ^ 64) + 4096 - qrot * 4096);
      asm volatile("" : "+v"(qb[0]), "+v"(qb[1]), "+v"(qb[2]), "+v"(qb[3]));
    } else {
      qb[0] = sm + (QB + 4 * wQ * 1024 + lane * 16);
      asm volatile("" : "+v"(qb[0]));
    }
  }
  static DI bf16x8 tr2(const LAS char* p, int second) {
    s16x4 a = __builtin_amdgcn_ds_read_tr16_b64_v4i16((LAS s16x4*)p);
    s16x4 b = __builtin_amdgcn_ds_read_tr16_b64_v4i16((LAS s16x4*)(p + second));
    return __builtin_bit_cast(bf16x8, __builtin_shufflevector(a, b, 0, 1, 2, 3, 4, 5, 6, 7));
  }
  template <int S, int KS> DI bf16x8 ldp(int i) {
    if constexpr (PMODE == M_ROW) return *(const LAS bf16x8*)(pb[KS] + (S * SLOT + i * 2048));
    else if constexpr (PMODE == M_TILED) return *(const LAS bf16x8*)(pb[0] + (S * SLOT + KS * 8192 + i * 1024));
    else if constexpr (PMODE == M_TR448)
      return tr2(pb[(S == 2 ? 3 : 0) + (i == 6 ? 2 : i & 1)] + ((S & 1) * P3 + KS * 32 * 448 + (i == 6 ? 0 : i * 32)), 16 * 448);
    else return tr2(pb[i] + (S * SLOT + KS * 32 * 256), 16 * 256);
  }
  template <int S, int KS> DI bf16x8 ldq(int j) {
    if constexpr (QMODE == M_ROW) return *(const LAS bf16x8*)(qb[KS + (j & 2)] + (S * SLOT + (j & 1) * 2048));
    else return *(const LAS bf16x8*)(qb[0] + (S * SLOT + KS * 8192 + j * 1024));
  }
  template <int S, int KS, int QN> DI void load() {
    fq[KS][0] = ldq<S, KS>(0);
#pragma unroll
    for (int i = 0; i < PT; i++) fp[KS][i] = ldp<S, KS>(i);
#pragma unroll
    for (int j = 1; j < QN; j++) fq[KS][j] = ldq<S, KS>(j);
  }
  template <int KS, int QN, bool ZERO = false> DI void mma(f32x4 (&acc)[PT][4]) {
#pragma unroll
    for (int n = 0; n < PT * QN; n++) {
      const int j = n / PT, i = (j & 1) ? PT - 1 - n % PT : n % PT;
      acc[i][j] = __builtin_amdgcn_mfma_f32_16x16x32_bf16(fp[KS][i], fq[KS][j], ZERO ? (f32x4){0.f, 0.f, 0.f, 0.f} : acc[i][j], 0, 0, 0);
    }
  }
  template <int KS> DI void qsums(f32x4 (&sd)[2], bf16x8 ones) {
#pragma unroll
    for (int i = 0; i < 2; i++) sd[i] = __builtin_amdgcn_mfma_f32_16x16x32_bf16(ones, fq[KS][i], sd[i], 0, 0, 0);
  }
  template <int NM, int QN> static DI void interleave() {
    constexpr int NR = PT * (PMODE >= M_TR448 ? 2 : 1) + QN, G = NR < NM ? NR : NM;
    static_for<0, G>([](auto ic) {
      __builtin_amdgcn_sched_group_barrier(0x8, 1, 0);
      __builtin_amdgcn_sched_group_barrier(0x100, NR / G + (ic.value < NR % G ? 1 : 0), 0);
    });
    __builtin_amdgcn_sched_group_barrier(0x8, NM - G, 0);
    __builtin_amdgcn_sched_barrier(0);
  }
  template <int S, int QC, int QN> DI void step(f32x4 (&acc)[PT][4]) {
    load<S, 1, QC>(); mma<0, QC>(acc); interleave<PT * QC, QC>();
    wait_lgkm0(); bar();
    __builtin_amdgcn_sched_barrier(0);
    load<(S + 1) % NS, 0, QN>(); mma<1, QC>(acc); interleave<PT * QC, QN>();
  }
  template <int S, int LS, bool ZERO, class W> DI void half_with(f32x4 (&acc)[PT][4], W w) {
    static_for<0, PT * 4>([&](auto nc) {
      constexpr int n = nc.value, j = n / PT, i = (j & 1) ? PT - 1 - n % PT : n % PT;
      acc[i][j] = __builtin_amdgcn_mfma_f32_16x16x32_bf16(fp[1 - LS][i], fq[1 - LS][j], ZERO ? (f32x4){0.f, 0.f, 0.f, 0.f} : acc[i][j], 0, 0, 0);
      if constexpr (S >= 0) {
        if constexpr (n == 0) fq[LS][0] = ldq<S, LS>(0);
        else if constexpr (n <= PT) fp[LS][n - 1] = ldp<S, LS>(n - 1);
        else if constexpr (n < PT + 4) fq[LS][n - PT] = ldq<S, LS>(n - PT);
      }
      w(nc);
      __builtin_amdgcn_sched_barrier(0);
    });
  }
  template <int S, int QC> DI void last(f32x4 (&acc)[PT][4]) {
    load<S, 1, QC>(); mma<0, QC>(acc); interleave<PT * QC, QC>();
    wait_lgkm0();
    mma<1, QC>(acc);
  }
  template <int QC, int QN> DI void step4(f32x4 (&acc)[PT][4]) {
    step<0, QC, QC>(acc); step<1, QC, QC>(acc); step<2, QC, QC>(acc); step<3, QC, QN>(acc);
  }
  static DI void zero(f32x4 (&acc)[PT][4]) {
#pragma unroll
    for (int i = 0; i < PT; i++)
#pragma unroll
      for (int j = 0; j < 4; j++) { acc[i][j] = (f32x4){0.f, 0.f, 0.f, 0.f}; asm volatile("" : "+v"(acc[i][j])); }
  }
  DI void first() { load<0, 0, 4>(); }
};

DI u32 row_src(int imgrow, int srcrow, int lane, int ldbytes) {
  return srcrow * ldbytes + (((lane & 7) ^ ((imgrow >> 1) & 7)) * 16);
}
DI void store_sc1(rsrc_t r, u32 off, u32x4 v) { __builtin_amdgcn_raw_buffer_store_b128(v, r, off, 0, 16); }

DI void xch_send(char* x, const f32x4 (&acc)[4][4], int wid, int lane) {
  rsrc_t r = mk_rsrc(x + ((size_t)blockIdx.x * 4 + wid) * 4096, 4096);
#pragma unroll
  for (int jj = 0; jj < 2; jj++)
#pragma unroll
    for (int u = 0; u < 2; u++) {
      const f32x4 a = acc[2 * u][2 + jj], b = acc[2 * u + 1][2 + jj];
      u32x4 v = {cvtpk(a[0], a[1]), cvtpk(a[2], a[3]), cvtpk(b[0], b[1]), cvtpk(b[2], b[3])};
      store_sc1(r, lane * 16 + (jj * 2 + u) * 1024, v);
    }
}
DI void xch_request(rsrc_t r, u32x4 (&v)[4], int lane) {
  asm volatile("s_nop 4\n\tbuffer_load_dwordx4 %0, %4, %5, 0 offen sc1\n\t"
               "buffer_load_dwordx4 %1, %4, %5, 0 offen offset:1024 sc1\n\t"
               "buffer_load_dwordx4 %2, %4, %5, 0 offen offset:2048 sc1\n\t"
               "buffer_load_dwordx4 %3, %4, %5, 0 offen offset:3072 sc1"
               : "=&v"(v[0]), "=&v"(v[1]), "=&v"(v[2]), "=&v"(v[3]) : "v"(lane * 16), "s"(r) : "memory");
}
DI void xch_finish(u32x4 (&v)[4]) { asm volatile("s_waitcnt vmcnt(0)" : "+v"(v[0]), "+v"(v[1]), "+v"(v[2]), "+v"(v[3])::"memory"); }

enum { FL_X2 = 0, FL_X4 = 1, FL_ACK = 2, FL_TICK = 3, FL_CNT = 4 };
enum { CN_H = 0, CN_AB = 128, CN_SX = 256, CN_RA = 384, CN_RB = 512 };
constexpr u32 NOWHERE = 0xFFFFFF00u;
DI void flag_set(char* ws, int which, int tt, int cell) {
  wait_vm<0>();
  if ((threadIdx.x & 63) == 0)
    __hip_atomic_store((u32*)(ws + WS_FL + ((size_t)(which * 16 + tt) * 64 + cell) * 64), 1u, __ATOMIC_RELAXED, __HIP_MEMORY_SCOPE_AGENT);
}
DI void flag_request(rsrc_t r, u32 off, u32& v) {
  asm volatile("s_nop 4\n\tbuffer_load_dword %0, %1, %2, 0 offen sc1" : "=&v"(v) : "v"(off), "s"(r) : "memory");
}
DI void flag_wait(rsrc_t r, u32 off, u32& v) {
  asm volatile("" : "+v"(v)::"memory");
#pragma unroll 1
  for (int spin = 0; spin < SPIN_LIMIT; spin++) {
    if (__builtin_amdgcn_ballot_w64(v != 1u) == 0) break;
    __builtin_amdgcn_s_sleep(1);
    asm volatile("s_nop 4\n\tbuffer_load_dword %0, %1, %2, 0 offen sc1\n\ts_waitcnt vmcnt(0)" : "=&v"(v) : "v"(off), "s"(r) : "memory");
  }
}

DI void word_store(rsrc_t r, u32 off, u32 v) { __builtin_amdgcn_raw_buffer_store_b32(v, r, off, 0, 16); }
DI void count_up(char* ws, u32 off) {
  if ((threadIdx.x & 63) == 0) __hip_atomic_fetch_add((u32*)(ws + WS_FL + off), 1u, __ATOMIC_RELAXED, __HIP_MEMORY_SCOPE_AGENT);
}
template <class Done> DI void watch(rsrc_t r, u32 off, u32& v, Done done, u32 tickoff, u32& tick) {
#pragma unroll 1
  for (int spin = 0; spin < SPIN_LIMIT; spin++) {
    if (__builtin_amdgcn_ballot_w64(!done(v)) == 0) break;
    if ((spin & 63) == 63) { tick++; word_store(r, tickoff, tick); }
    __builtin_amdgcn_s_sleep(1);
    asm volatile("s_nop 4\n\tbuffer_load_dword %0, %1, %2, 0 offen sc1\n\ts_waitcnt vmcnt(0)" : "=&v"(v) : "v"(off), "s"(r) : "memory");
  }
}

__global__ __launch_bounds__(512, 2) void fused_kernel(const u16*, const u16*, const u16*, const u16*, const u16*, const u16*, const u16*,
                                                       const u16*, const u16*, char*, u16*) {
  u32x16 ka; u32x4 kb4; u32x2 kc;
  asm volatile("s_load_dwordx16 %0, %3, 0x0\n\ts_load_dwordx4 %1, %3, 0x40\n\ts_load_dwordx2 %2, %3, 0x50"
               : "=&s"(ka), "=&s"(kb4), "=&s"(kc) : "s"(__builtin_amdgcn_kernarg_segment_ptr()));
  __builtin_amdgcn_sched_barrier(0);
  extern __shared__ __attribute__((aligned(16))) char smem[];
  const int lane0 = threadIdx.x & 63, wid = rfl(threadIdx.x >> 6);
  const int xcd = blockIdx.x & 7, loc = blockIdx.x >> 3, li = loc >> 2, lj = loc & 3;
  const int half = xcd >> 2, et = ((xcd >> 1) & 1) * 4 + lj, tt = (xcd & 1) * 8 + li, ft = (xcd >> 1) * 4 + lj;
  LAS char* sm = (LAS char*)smem;
  LAS float* tabA = (LAS float*)(sm + TOFF);
  LAS float* tabB = tabA + 128;
  LAS float* tabC = tabA + 256;
  LAS float* tabBias = tabA + 512;
  u32 vp1[4], vq1[4];
#pragma unroll
  for (int i = 0; i < 4; i++) {
    const int row = 32 * (wid & 3) + 8 * i + (lane0 >> 3), u = (row >> 4) & 1, ii = row & 15;
    vp1[i] = row_src(row, 32 * (wid & 3) + 8 * (ii >> 2) + 4 * u + (ii & 3), lane0, D_ * 2) + 4096 - i * 1024;
    vq1[i] = row_src(row, row, lane0, D_ * 2) - i * 1024;
  }
  asm volatile("" : "+v"(vp1[0]), "+v"(vp1[1]), "+v"(vp1[2]), "+v"(vp1[3]), "+v"(vq1[0]), "+v"(vq1[1]), "+v"(vq1[2]), "+v"(vq1[3]));
  __builtin_amdgcn_sched_barrier(0);
  asm volatile("s_waitcnt lgkmcnt(0)" : "+s"(ka), "+s"(kb4), "+s"(kc));
  auto ptr = [](u32 lo, u32 hi) { return (u16*)(GAS u16*)(((u64)hi << 32) | lo); };
  const u16 *x = ptr(ka[0], ka[1]), *cre = ptr(ka[2], ka[3]), *cim = ptr(ka[4], ka[5]), *dre = ptr(ka[6], ka[7]), *dim = ptr(ka[8], ka[9]);
  const u16 *w1 = ptr(ka[10], ka[11]), *b1 = ptr(ka[12], ka[13]), *w2 = ptr(ka[14], ka[15]), *b2 = ptr(kb4[0], kb4[1]);
  char* ws = (char*)ptr(kb4[2], kb4[3]);
  u16* out = ptr(kc[0], kc[1]);
  if (__builtin_expect(wid >= 4, 1)) {
    const int p = wid - 4, phi = (li + lj) & 3;
    const u32 lds0 = (u32)(size_t)smem, ldsb = lds0 + p * 4096;
    rsrc_t rall = mk_rsrc(ws + WS_FL, 5 * 65536);
    rsrc_t rp1 = mk_rsrc((const char*)x + (size_t)tt * 128 * D_ * 2 - 4096, 128 * D_ * 2 + 4096);
    rsrc_t rq1d = mk_rsrc((half ? dim : dre) + (size_t)et * 128 * D_, 128 * D_ * 2);
    const int side1 = (xcd >> 1) ^ (xcd & 1);
    dma<4>(ldsb, vp1, rp1, kstep2(0, 16, phi, side1) * 128);
    dma<4>(ldsb + QOFF, vq1, rq1d, kstep2(0, 16, phi, side1) * 128);
    int lane = lane0;
    asm volatile("" : "+v"(lane)::"memory");
    __builtin_amdgcn_sched_barrier(0);
    const u32 vlin = p * 4096 + lane * 16;
    const u32 grpcell = ((lane & 16) ? tt * 64 + (lane & 15) * 4 : (lane & 15) * 64 + ft * 4) * 64;
    const u32 tickoff = lane == 0 ? FL_TICK * 65536 + (tt * 64 + ft * 4) * 64 : NOWHERE;
    const u32 ackoff = lane == 0 ? FL_ACK * 65536 + (tt * 64 + ft * 4) * 64 : NOWHERE;
    const u32 rowcnt = FL_CNT * 65536 + tt * 4096, colcnt = FL_CNT * 65536 + ft * 4096;
    const int pt = threadIdx.x - 256;
    u32 base, fv, tick;
    const int side2 = (xcd >> 1) & 1;
    auto skip = [&](int r) { return r + (r >= 2 * tt ? 2 : 0); };
    auto g2k = [&](int q) { return q < 2 ? 2 * tt + q : skip(kstep2(q - 2, 26, phi, side2)); };
    rsrc_t rco = mk_rsrc((const char*)(half ? cim : cre) + (size_t)tt * 128 * S_ * 2 - 4096, 128 * S_ * 2 + 4096);
    u32 vc[4];
#pragma unroll
    for (int i = 0; i < 4; i++) {
      const int row = 32 * p + 8 * i + (lane >> 3);
      vc[i] = row_src(row, row ^ (half * 32), lane, S_ * 2) + 4096 - i * 1024;
    }
    auto g2q = [&](int q) { dma<4>(ldsb + QOFF + (q & 3) * SLOT, vc, rco, g2k(q) * 128); };
    {
      u32 vx[2];
#pragma unroll
      for (int i = 0; i < 2; i++) {
        const int row = 16 * p + 8 * i + (lane >> 3);
        vx[i] = row_src(row, 64 * (row >> 5) + 32 * half + (row & 31), lane, D_ * 2) + 4096 - i * 1024;
      }
      auto issue = [&](int g) {
        const int k = kstep2(g, 16, phi, side1);
        const u32 m = ldsb + (g & 3) * SLOT;
        dma<4>(m, vp1, rp1, k * 128);
        dma<4>(m + QOFF, vq1, rq1d, k * 128);
      };
      auto run = [&](int g0, int g1) {
#pragma unroll 1
        for (int g = g0; g < g1; g++) { issue(g); wait_vm<16>(); bar(); }
      };
      flag_request(rall, p == 0 ? FL_TICK * 65536 + grpcell : NOWHERE, base);
      issue(1); issue(2);
      {
        u32 z = lane < 8 ? (lane >> 2) * 65536 + (tt * 64 + ft * 4 + (lane & 3)) * 64 : FL_ACK * 65536 + (tt * 64 + ft * 4) * 64;
        if (lane == 9) z = tt == 0 ? colcnt + CN_AB : NOWHERE;
        if (lane >= 10) z = ft == 0 ? rowcnt + (lane == 10 ? CN_H : (lane - 9) * 128) : NOWHERE;
        word_store(rall, p == 0 && lane < 14 ? z : NOWHERE, 0u);
      }
      wait_vm<18>();
      bar();
      const int js = (phi + 1) & 3;
      auto sums = [&]() {
        const int c = lane & 15, g = lane >> 4, e = c & 3;
        const u32x4 ones4 = {0x3F803F80u, 0x3F803F80u, 0x3F803F80u, 0x3F803F80u};
        const bf16x8 ones = __builtin_bit_cast(bf16x8, ones4);
        const int a = js * SLOT + (32 * p + c) * 128 + ((g ^ (c >> 1)) * 16);
        rsrc_t rx = mk_rsrc(ws + WS_SX + (size_t)(tt * 16 + (js ^ (side1 << 2))) * 1024 + p * 256, 256);
#pragma unroll
        for (int i = 0; i < 2; i++) {
          const bf16x8 x0 = *(const LAS bf16x8*)(sm + (a + i * 2048)), x1 = *(const LAS bf16x8*)(sm + ((a ^ 64) + i * 2048));
          const f32x4 z = {0.f, 0.f, 0.f, 0.f};
          const f32x4 sx = __builtin_amdgcn_mfma_f32_16x16x32_bf16(x1, ones, __builtin_amdgcn_mfma_f32_16x16x32_bf16(x0, ones, z, 0, 0, 0), 0, 0, 0);
          const f32x4 sq = __builtin_amdgcn_mfma_f32_16x16x32_bf16(x1, x1, __builtin_amdgcn_mfma_f32_16x16x32_bf16(x0, x0, z, 0, 0, 0), 0, 0, 0);
          const float q = e == 0 ? sq[0] : e == 1 ? sq[1] : e == 2 ? sq[2] : sq[3];
          const float t = e == 0 ? sx[0] : e == 1 ? sx[1] : e == 2 ? sx[2] : sx[3];
          __builtin_amdgcn_raw_buffer_store_b64((u32x2){__float_as_uint(t), __float_as_uint(q)}, rx, (c >> 2) == g ? (16 * i + c) * 8 : NOWHERE, 0, 16);
        }
      };
      issue(3);
      if (js == 0) sums();
      asm volatile("s_waitcnt vmcnt(16)" : "+v"(base)::"memory");
      tick = __builtin_amdgcn_readlane(base, tt) + 1;
      bar();
#pragma unroll 1
      for (int g = 2; g < 6; g++) {
        issue(g + 2);
        if (g == js + 1) sums();
        wait_vm<16>();
        if (g == 3 && p == 0) word_store(rall, tickoff, tick);
        bar();
      }
      issue(8); wait_vm<16>(); bar();
      flag_request(rall, p == 0 ? FL_TICK * 65536 + grpcell : NOWHERE, fv);
      issue(9); wait_vm<16>(); bar();
      issue(10);
      asm volatile("s_waitcnt vmcnt(16)" : "+v"(fv)::"memory");
      if (p == 0) {
        watch(rall, FL_TICK * 65536 + grpcell, fv, [&](u32 w) { return w != base; }, tickoff, tick);
        count_up(ws, rowcnt + CN_SX);
        word_store(rall, ackoff, 1u);
      }
      bar();
      run(11, 14);
      dma<2>(lds0 + XOFF + p * 2048, vx, rp1, et * 256);
      dma<2>(lds0 + XOFF + 8192 + p * 2048, vx, rp1, et * 256 + 128);
      flag_request(rall, p == 0 ? rowcnt + CN_SX : NOWHERE, fv);
      issue(14); wait_vm<21>(); bar();
      issue(15);
      asm volatile("s_waitcnt vmcnt(16)" : "+v"(fv)::"memory");
      if (p == 0) watch(rall, rowcnt + CN_SX, fv, [&](u32 w) { return w >= 16u; }, tickoff, tick);
      bar();
      u32x2 sv[8];
      rsrc_t rsx = mk_rsrc(ws + WS_SX + (size_t)tt * 16384, 16384);
#pragma unroll
      for (int i = 0; i < 8; i++)
        asm volatile("s_nop 4\n\tbuffer_load_dwordx2 %0, %1, %2, %3 offen sc1" : "=&v"(sv[i]) : "v"((pt & 1) * 8192 + (pt >> 1) * 8), "s"(rsx), "s"(i * 1024) : "memory");
      g2q(0); wait_vm<20>(); bar();
      g2q(1); wait_vm<16>(); bar();
      g2q(2);
      asm volatile("s_waitcnt vmcnt(12)" : "+v"(sv[0]), "+v"(sv[1]), "+v"(sv[2]), "+v"(sv[3]), "+v"(sv[4]), "+v"(sv[5]), "+v"(sv[6]), "+v"(sv[7])::"memory");
      float a = 0.f, q = 0.f;
#pragma unroll
      for (int i = 0; i < 8; i++) { a += __uint_as_float(sv[i][0]); q += __uint_as_float(sv[i][1]); }
      a += __builtin_bit_cast(float, __builtin_amdgcn_update_dpp(0, __builtin_bit_cast(int, a), 0xB1, 0xF, 0xF, true));
      q += __builtin_bit_cast(float, __builtin_amdgcn_update_dpp(0, __builtin_bit_cast(int, q), 0xB1, 0xF, 0xF, true));
      const float mean = a * (1.0f / D_), var = fmaxf(q * (1.0f / D_) - mean * mean, 0.f);
      const float rstd = (half ? -1.f : 1.f) * __builtin_amdgcn_rsqf(var + LN_EPS);
      tabA[(pt & 1) * 128 + (pt >> 1)] = (pt & 1) ? -mean * rstd : rstd;
      wait_lgkm0();
    }
    {
      rsrc_t rp = mk_rsrc(ws + WS_AB + (size_t)et * 32 * 16384, 9 * 32 * 16384);
      rsrc_t rq0 = mk_rsrc((const char*)cre + (size_t)tt * 128 * S_ * 2 - 4096, 128 * S_ * 2 + 4096);
      rsrc_t rq1 = mk_rsrc((const char*)cim + (size_t)tt * 128 * S_ * 2 - 4096, 128 * S_ * 2 + 4096);
      u32 vh[2];
#pragma unroll
      for (int i = 0; i < 2; i++) {
        const int row = 64 * (p >> 1) + 16 * (p & 1) + 8 * i + (lane >> 3);
        vh[i] = row_src(row, row ^ (half * 32), lane, S_ * 2) + 4096 - i * 1024;
      }
      const u32 ldsh = lds0 + QOFF + (p >> 1) * 8192 + (p & 1) * 2048;
      auto fp = [&](int q) { dma_sc1<4>(ldsb + (q & 3) * SLOT, vlin, rp, (half * 256 + g2k(q)) * 16384); };
      auto full = [&](int q) { fp(q); g2q(q); };
      auto halfs = [&](int g) {
        const int k = kstep2(g, 8, phi, side2), term = k >> 2, ks = skip(26 + (k & 3));
        dma_sc1<4>(ldsb + (g & 3) * SLOT, vlin, rp, (term * 256 + ks) * 16384);
        if (term) dma<2>(ldsh + (g & 3) * SLOT, vh, rq1, ks * 128);
        else dma<2>(ldsh + (g & 3) * SLOT, vh, rq0, ks * 128);
      };
      bar();
      wait_vm<8>(); bar();
      g2q(3);
      rsrc_t rw = mk_rsrc((const char*)w1 + (size_t)(tt * 64 + p * 16) * F_ * 2 + ft * 448, 15 * F_ * 2 + 448);
      u32x4 v[8];
      const int l28 = lane < 28 ? lane : lane < 56 ? lane - 28 : 27;
      const u32 vo = (lane >= 28 ? F_ * 2 : 0) + l28 * 16;
#pragma unroll
      for (int i = 0; i < 8; i++)
        asm volatile("s_nop 4\n\tbuffer_load_dwordx4 %0, %1, %2, %3 offen" : "=&v"(v[i]) : "v"(vo), "s"(rw), "s"(i * 2 * F_ * 2) : "memory");
      wait_vm<16>(); bar();
      bar();
      if (p == 0) {
        count_up(ws, colcnt + CN_AB);
        fv = 0;
        watch(rall, colcnt + CN_AB, fv, [&](u32 w) { return w >= 16u; }, tickoff, tick);
        word_store(rall, ackoff, 2u);
      }
      bar();
      fp(2); fp(3); full(4);
      asm volatile("s_waitcnt vmcnt(12)" : "+v"(v[0]), "+v"(v[1]), "+v"(v[2]), "+v"(v[3]), "+v"(v[4]), "+v"(v[5]), "+v"(v[6]), "+v"(v[7])::"memory");
      bar();
      full(5);
      {
        float s[8] = {0, 0, 0, 0, 0, 0, 0, 0};
#pragma unroll
        for (int i = 0; i < 8; i++)
#pragma unroll
          for (int e = 0; e < 4; e++) { s[2 * e] += blo(v[i][e]); s[2 * e + 1] += bhi(v[i][e]); }
#pragma unroll
        for (int e = 0; e < 8; e++) s[e] += __shfl(s[e], lane + 28);
        if (lane < 28) {
          LAS float* d = (LAS float*)(sm + COFF) + p * 224 + lane * 8;
          *(LAS f32x4*)d = (f32x4){s[0], s[1], s[2], s[3]};
          *(LAS f32x4*)(d + 4) = (f32x4){s[4], s[5], s[6], s[7]};
        }
      }
      wait_vm<16>(); wait_lgkm0(); bar();
      full(6);
      if (p == 0 && lane < 56) {
        const LAS f32x4* d = (const LAS f32x4*)(sm + COFF) + lane;
        const f32x4 t = (d[0] + d[56]) + (d[112] + d[168]);
        store_sc1(mk_rsrc(ws + WS_CS + (size_t)tt * F_ * 4 + ft * 896, 896), lane * 16, __builtin_bit_cast(u32x4, t));
      }
      wait_vm<16>(); bar();
      full(7); wait_vm<16>(); bar();
      full(8); wait_vm<16>();
      if (p == 0) count_up(ws, colcnt + CN_AB);
      bar();
#pragma unroll 1
      for (int q = 9; q < 26; q++) { full(q); wait_vm<16>(); bar(); }
      const u32 lastoff = lane < 32 ? FL_ACK * 65536 + grpcell : colcnt + CN_AB;
      const u32 lastmin = lane >= 32 ? 32u : lane == 16 + (ft ^ 8) ? 2u : 1u;
      flag_request(rall, p == 0 ? lastoff : NOWHERE, fv);
      full(26); wait_vm<16>(); bar();
      full(27);
      asm volatile("s_waitcnt vmcnt(16)" : "+v"(fv)::"memory");
      if (p == 0) watch(rall, lastoff, fv, [&](u32 w) { return w >= lastmin; }, tickoff, tick);
      bar();
      halfs(0); wait_vm<14>(); bar();
#pragma unroll 1
      for (int g = 1; g < 8; g++) { halfs(g); wait_vm<12>(); bar(); }
    }
    rsrc_t rp4 = mk_rsrc(w2 + et * 128, F_ * D_ * 2 - et * 256);
    u32 vp4[4];
#pragma unroll
    for (int i = 0; i < 4; i++) {
      const int k = 16 * (p & 1) + 4 * i + (lane >> 4);
      vp4[i] = k * (D_ * 2) + (((lane & 15) ^ ((k & 7) << 1)) * 16) - i * 1024;
    }
    auto g4blk = [&](int q) {
      const int r = q < 10 ? kstep2(q - 3, 7, phi, xcd & 1) : kstep2(q - 10, 14, phi, xcd & 1);
      const int i = q < 10 ? r : r >> 1, pr = q < 3 ? q : q < 10 ? 0 : 1 + (r & 1);
      return 7 * (8 * half + (q < 3 ? et : i + (i >= et ? 1 : 0))) + pr + 4 * (p >> 1);
    };
    auto g4p = [&](int q) { dma<4>(ldsb + ((q + 2) & 3) * SLOT, vp4, rp4, g4blk(q) * (32 * D_ * 2)); };
    {
      rsrc_t rp = mk_rsrc((const char*)w1 + ft * 448 - 4096, D_ * F_ * 2 - ft * 448 + 4096);
      rsrc_t rq = mk_rsrc(ws + WS_H + (size_t)tt * 32 * 8192, 32 * 8192);
      u32 vp[7];
#pragma unroll
      for (int i = 0; i < 7; i++) {
        const int o = (p * 7 + i) * 64 + lane, k = o / 28, ch = o % 28;
        vp[i] = k * (F_ * 2) + ((ch ^ (((k >> 2) & 1) << 1)) * 16) + 4096 - (i & 3) * 1024;
      }
      rsrc_t rw = mk_rsrc(w2 + (size_t)(half * 1792 + tt * 112 + 28 * p) * D_ + et * 128, 27 * D_ * 2 + 256);
      const u32 vw = (lane >> 1) * (D_ * 2) + (lane & 1) * 128;
      auto pa = [&](int g, int sl) {
        const u32 so = kstep(g, 16, phi) * (64 * F_ * 2), m = lds0 + sl * P3 + p * 7168;
        dma<4>(m, vp, rp, so);
        dma<3>(m + 4096, vp + 4, rp, so);
      };
      auto q = [&](int g, int sl) { dma_sc1<4>(ldsb + Q3OFF + sl * SLOT, vlin, rq, kstep(g, 16, phi) * 16384); };
      wait_vm<6>(); bar();
      wait_vm<0>(); bar();
      bar();
      if (p == 0) count_up(ws, rowcnt + CN_H);
      pa(0, 0); pa(1, 1);
      pull_lines(lds0 + COFF + 3584, p == 1 ? (128 + 16 * li + (lane >> 2)) * (F_ * 2) + (lane & 3) * 128 + 4096 : NOWHERE, rp, 0);
      rsrc_t rs = mk_rsrc(ws + WS_ST + (size_t)tt * 16384, 16384);
      rsrc_t rc = mk_rsrc(ws + WS_CS + ft * 896, 15 * F_ * 4 + 896);
      u32x2 sv[8];
      u32 cv[16], bv;
#pragma unroll
      for (int i = 0; i < 16; i++)
        asm volatile("s_nop 4\n\tbuffer_load_dword %0, %1, %2, %3 offen sc1" : "=&v"(cv[i]) : "v"(min(pt, 223) * 4), "s"(rc), "s"(i * F_ * 4) : "memory");
      asm volatile("s_nop 4\n\tbuffer_load_ushort %0, %1, %2, 0 offen" : "=&v"(bv) : "v"(min(pt, 223) * 2), "s"(mk_rsrc(b1 + ft * 224, 448)) : "memory");
      if (p == 0) { fv = 0; watch(rall, rowcnt + CN_H, fv, [&](u32 w) { return w >= 16u; }, NOWHERE, tick); }
      bar();
      q(0, 0); q(1, 1);
      wait_vm<4>();
      bar();
      pa(2, 2); q(2, 2);
#pragma unroll
      for (int i = 0; i < 8; i++)
        asm volatile("s_nop 4\n\tbuffer_load_dwordx2 %0, %1, %2, %3 offen sc1" : "=&v"(sv[i]) : "v"((pt & 1) * 8192 + (pt >> 1) * 8), "s"(rs), "s"(i * 1024) : "memory");
      wait_vm<19>(); bar();
      pa(3, 0); q(3, 0); wait_vm<19>(); bar();
      pa(4, 1); q(4, 1); wait_vm<11>();
      asm volatile("" : "+v"(sv[0]), "+v"(sv[1]), "+v"(sv[2]), "+v"(sv[3]), "+v"(sv[4]), "+v"(sv[5]), "+v"(sv[6]), "+v"(sv[7]), "+v"(bv)::"memory");
      asm volatile("" : "+v"(cv[0]), "+v"(cv[1]), "+v"(cv[2]), "+v"(cv[3]), "+v"(cv[4]), "+v"(cv[5]), "+v"(cv[6]), "+v"(cv[7]), "+v"(cv[8]),
                   "+v"(cv[9]), "+v"(cv[10]), "+v"(cv[11]), "+v"(cv[12]), "+v"(cv[13]), "+v"(cv[14]), "+v"(cv[15])::"memory");
      bar();
      pa(5, 2); q(5, 2);
      {
        float s = 0.f, a = 0.f, qq = 0.f;
#pragma unroll
        for (int i = 0; i < 16; i++) s += __uint_as_float(cv[i]);
        if (pt < 224) { tabC[pt] = s; tabBias[pt] = __uint_as_float(bv << 16); }
#pragma unroll
        for (int i = 0; i < 8; i++) { a += __uint_as_float(sv[i][0]); qq += __uint_as_float(sv[i][1]); }
        a += __builtin_bit_cast(float, __builtin_amdgcn_update_dpp(0, __builtin_bit_cast(int, a), 0xB1, 0xF, 0xF, true));
        qq += __builtin_bit_cast(float, __builtin_amdgcn_update_dpp(0, __builtin_bit_cast(int, qq), 0xB1, 0xF, 0xF, true));
        const float mean = a * (1.0f / D_), var = fmaxf(qq * (1.0f / D_) - mean * mean, 0.f);
        const float rstd = __builtin_amdgcn_rsqf(var + LN_EPS);
        tabA[(pt & 1) * 128 + (pt >> 1)] = (pt & 1) ? -mean * rstd : rstd;
      }
      wait_vm<11>(); wait_lgkm0(); bar();
      int sl = 0;
#pragma unroll 1
      for (int g = 6; g < 16; g++) {
        pa(g, sl); q(g, sl);
        const int gp = 6 + (loc & 7);
        if (g == gp) pull_lines(lds0 + COFF + 3584, vw, rw, 0);
        if ((unsigned)(g - gp) < 2u) wait_vm<12>(); else wait_vm<11>();
        bar();
        sl = sl == 2 ? 0 : sl + 1;
      }
      g4p(0); wait_vm<4>(); bar();
      g4p(1);
      bar();
    }
    {
      rsrc_t rq = mk_rsrc(ws + WS_R + (size_t)tt * 112 * 8192, 112 * 8192);
      const u32 vq0 = (4 * (p & 1) + 2 * half) * 1024 + lane * 16, vq1 = (4 * (p & 1) + 2 - 2 * half) * 1024 + lane * 16;
      auto g4q = [&](int q) {
        const u32 m = ldsb + QOFF + ((q + 2) & 3) * SLOT, so = g4blk(q) * 8192;
        dma_sc1<2>(m, vq0, rq, so);
        dma_sc1<2>(m + 2048, vq1, rq, so);
      };
      auto halfs = [&](int g) {
        const int k = kstep2(g, 8, phi, xcd & 1), blk = 7 * (2 * k + (p >> 1)) + 3;
        dma<4>(ldsb + ((g + 2) & 3) * SLOT, vp4, rp4, blk * (32 * D_ * 2));
        dma_sc1<2>(ldsb + QOFF + ((g + 2) & 3) * SLOT, vq0, rq, blk * 8192);
      };
      wait_vm<4>(); bar();
      if (p == 0) count_up(ws, rowcnt + CN_RA);
      g4p(2); wait_vm<4>(); bar();
      g4p(3);
      {
        const int qq = 4 + min(lane >> 3, 2);
        const u32 b4 = g4blk(4), b5 = g4blk(5), b6 = g4blk(6), bb = qq == 4 ? b4 : qq == 5 ? b5 : b6;
        pull_lines(lds0 + COFF + 3584, (p & 1) == 0 && lane < 24 ? (bb * 32 + 4 * li + ((lane >> 1) & 3)) * (D_ * 2) + (lane & 1) * 128 : NOWHERE, rp4, 0);
      }
      wait_vm<5>();
      if (p == 0) { fv = 0; watch(rall, rowcnt + CN_RA, fv, [&](u32 w) { return w >= 16u; }, NOWHERE, tick); }
      bar();
      g4q(3); g4p(4); g4q(4); g4p(5); g4q(5);
      wait_vm<16>(); bar();
      if (p == 0) count_up(ws, rowcnt + CN_RB);
      g4p(6); g4q(6); wait_vm<16>(); bar();
      g4p(7); g4q(7); wait_vm<16>(); bar();
      flag_request(rall, p == 0 ? rowcnt + CN_RB : NOWHERE, fv);
      g4p(8); g4q(8); wait_vm<16>(); bar();
      g4p(9); g4q(9);
      asm volatile("s_waitcnt vmcnt(16)" : "+v"(fv)::"memory");
      if (p == 0) watch(rall, rowcnt + CN_RB, fv, [&](u32 w) { return w >= 16u; }, NOWHERE, tick);
      bar();
#pragma unroll 1
      for (int q = 10; q < 24; q++) { g4p(q); g4q(q); wait_vm<16>(); bar(); }
      halfs(0); wait_vm<14>(); bar();
#pragma unroll 1
      for (int g = 1; g < 8; g++) { halfs(g); wait_vm<12>(); bar(); }
      wait_vm<6>(); bar();
      wait_vm<0>(); bar();
    }
  } else {
    const int lane = lane0;
    const int wP = wid >> 1, wQ = wid & 1, c = lane & 15, g = lane >> 4;
    const int m0 = 4 * wQ + 2 * half, cell = ft * 4 + wid;
    {
      Consumer<4, M_ROW, M_ROW> C;
      C.init(sm, wP, wQ, lane, wP);
      f32x4 acc[4][4], sd[2];
      C.zero(acc);
#pragma unroll
      for (int i = 0; i < 2; i++) sd[i] = (f32x4){0.f, 0.f, 0.f, 0.f};
      const u32x4 ones4 = {0x3F803F80u, 0x3F803F80u, 0x3F803F80u, 0x3F803F80u};
      bf16x8 ones = __builtin_bit_cast(bf16x8, ones4);
      asm volatile("" : "+v"(ones));
      bar();
      C.first();
#define G1_HALF0(S) C.load<S, 1, 4>(); C.mma<0, 4>(acc); C.qsums<0>(sd, ones); C.interleave<18, 4>();
#define G1_STEP(S) G1_HALF0(S) wait_lgkm0(); bar(); __builtin_amdgcn_sched_barrier(0); \
      C.load<(S + 1) % NST, 0, 4>(); C.mma<1, 4>(acc); C.qsums<1>(sd, ones); C.interleave<18, 4>();
      for (int it = 0; it < 3; it++) { G1_STEP(0) G1_STEP(1) G1_STEP(2) G1_STEP(3) }
      G1_STEP(0) G1_STEP(1) G1_STEP(2) G1_HALF0(3)
      wait_lgkm0();
      C.qsums<1>(sd, ones);
      __builtin_amdgcn_sched_barrier(0);
      C.mma<1, 4>(acc);
      tabC[g < 2 ? 64 * wQ + 32 * wP + 16 * g + c : 128 + lane] = g ? sd[1][0] : sd[0][0];
      __builtin_amdgcn_sched_group_barrier(0x8, 4, 0);
      __builtin_amdgcn_sched_group_barrier(0x2 | 0x200, 8, 0);
      __builtin_amdgcn_sched_group_barrier(0x8, 12, 0);
      __builtin_amdgcn_sched_barrier(0);
      wait_lgkm0(); bar();
      rsrc_t ro = mk_rsrc(ws + WS_AB + ((size_t)((half * 8 + et) * 32 + 2 * tt + wP) * 16 + 4 * wQ) * 1024, 12288);
      f32x4 ra[4], rb[4];
      float rs[4];
#pragma unroll
      for (int i = 0; i < 4; i++) {
        ra[i] = *(const LAS f32x4*)(tabA + 64 * wP + 16 * i + 4 * g);
        rb[i] = *(const LAS f32x4*)(tabB + 64 * wP + 16 * i + 4 * g);
        rs[i] = tabC[64 * wQ + 16 * (i ^ (2 * wP)) + c];
      }
#pragma unroll
      for (int j = 0; j < 4; j++)
#pragma unroll
        for (int u = 0; u < 2; u++) {
          const f32x4 a = ra[2 * u] * acc[2 * u][j] + rb[2 * u] * rs[j], b = ra[2 * u + 1] * acc[2 * u + 1][j] + rb[2 * u + 1] * rs[j];
          const u32x4 o = {cvtpk(a[0], a[1]), cvtpk(a[2], a[3]), cvtpk(b[0], b[1]), cvtpk(b[2], b[3])};
          const int at = (u * 8 + (j ^ (2 * wP))) * 1024 + lane * 16;
          store_sc1(ro, at, o);
          *(LAS u32x4*)(sm + (wP * SLOT + wQ * 4096 + at)) = o;
        }
    }
    u32x4 hk[2][2];
    {
      Consumer<4, M_TILED, M_ROW> C;
      C.init(sm, wP, wQ, lane);
      f32x4 acc[4][4];
      wait_lgkm0(); bar();
      C.first();
      C.load<0, 1, 4>(); C.mma<0, 4, true>(acc); C.interleave<16, 4>();
      wait_lgkm0(); bar();
      __builtin_amdgcn_sched_barrier(0);
      C.load<1, 0, 4>(); C.mma<1, 4>(acc); C.interleave<16, 4>();
      C.load<1, 1, 4>(); C.mma<0, 4>(acc); C.interleave<16, 4>();
      wait_lgkm0();
      C.mma<1, 4>(acc);
      __builtin_amdgcn_sched_barrier(0);
      wait_vm<0>();
      bar();
      bar();
      bar();
      C.load<2, 0, 4>();
      C.step<2, 4, 4>(acc); C.step<3, 4, 4>(acc);
      for (int it = 0; it < 5; it++) C.step4<4, 4>(acc);
      C.step4<4, 2>(acc);
      xch_send(ws + WS_X2, acc, wid, lane);
      __builtin_amdgcn_sched_barrier(0);
      rsrc_t rx = mk_rsrc(ws + WS_X2 + ((size_t)(blockIdx.x ^ 4) * 4 + wid) * 4096, 4096);
      rsrc_t rf = mk_rsrc(ws + WS_FL + ((size_t)(FL_X2 * 16 + tt) * 64 + (cell ^ 32)) * 64, 64);
      u32x4 rv[4];
      u32 fv;
      C.step<0, 2, 2>(acc); C.step<1, 2, 2>(acc); C.step<2, 2, 2>(acc);
      flag_set(ws, FL_X2, tt, cell);
      C.step<3, 2, 2>(acc);
      C.step<0, 2, 2>(acc); C.step<1, 2, 2>(acc);
      flag_request(rf, 0, fv);
      __builtin_amdgcn_sched_barrier(0);
      C.step<2, 2, 2>(acc);
      wait_vm<0>();
      flag_wait(rf, 0, fv);
      xch_request(rx, rv, lane);
      __builtin_amdgcn_sched_barrier(0);
      C.load<3, 1, 2>(); C.mma<0, 2>(acc); C.interleave<8, 2>();
      C.mma<1, 2>(acc);
      xch_finish(rv);
      rsrc_t rh = mk_rsrc(ws + WS_H + ((size_t)(tt * 32 + 4 * et + 2 * wP) * 8 + m0) * 1024, 16384);
      rsrc_t rs = mk_rsrc(ws + WS_ST + ((size_t)(tt * 16 + 2 * et + wP) * 128 + 16 * m0) * 8, 256);
#pragma unroll
      for (int jj = 0; jj < 2; jj++) {
        float s = 0.f, q = 0.f;
#pragma unroll
        for (int u = 0; u < 2; u++) {
          const u32x4 o = rv[jj * 2 + u];
          const f32x4 a = acc[2 * u][jj], b = acc[2 * u + 1][jj];
          const int xrow = 32 * wQ + 16 * jj + c;
          const LAS char* xp = sm + (XOFF + wP * 8192 + xrow * 128 + (g & 1) * 8);
          const u32x2 xa = *(const LAS u32x2*)(xp + (((4 * u + (g >> 1)) ^ ((xrow >> 1) & 7)) * 16));
          const u32x2 xb = *(const LAS u32x2*)(xp + (((4 * u + 2 + (g >> 1)) ^ ((xrow >> 1) & 7)) * 16));
          u32x4 h;
          h[0] = cvtpk(a[0] + blo(o[0]) + blo(xa[0]), a[1] + bhi(o[0]) + bhi(xa[0]));
          h[1] = cvtpk(a[2] + blo(o[1]) + blo(xa[1]), a[3] + bhi(o[1]) + bhi(xa[1]));
          h[2] = cvtpk(b[0] + blo(o[2]) + blo(xb[0]), b[1] + bhi(o[2]) + bhi(xb[0]));
          h[3] = cvtpk(b[2] + blo(o[3]) + blo(xb[1]), b[3] + bhi(o[3]) + bhi(xb[1]));
          store_sc1(rh, (u * 8 + jj) * 1024 + lane * 16, h);
          hk[jj][u] = h;
#pragma unroll
          for (int e = 0; e < 4; e++) { const float lo = blo(h[e]), hi = bhi(h[e]); s += lo + hi; q += lo * lo + hi * hi; }
        }
        s = xsum32(xsum16(s)); q = xsum32(xsum16(q));
        if (g == 0) __builtin_amdgcn_raw_buffer_store_b64((u32x2){__float_as_uint(s), __float_as_uint(q)}, rs, (16 * jj + c) * 8, 0, 16);
      }
      wait_vm<0>();
      bar();
#pragma unroll
      for (int q = 0; q < 4; q++) *(LAS u32x4*)(sm + XOFF + wid * 4096 + q * 1024 + lane * 16) = hk[q >> 1][q & 1];
      bar();
    }
    rsrc_t rr = mk_rsrc(ws + WS_R + ((size_t)(tt * 112 + 7 * ft) * 8 + 4 * wQ) * 1024, 7 * 8192);
    u32x4 ro[14];
    {
      Consumer<7, M_TR448, M_TILED> C;
      C.init(sm, wP, wQ, lane);
      f32x4 acc[7][4];
      C.zero(acc);
      bar();
      C.first();
      for (int it = 0; it < 5; it++) { C.step<0, 4, 4>(acc); C.step<1, 4, 4>(acc); C.step<2, 4, 4>(acc); }
      C.last<0, 4>(acc);
      bar();
      const int lane = fresh_lane(), c = lane & 15, g = lane >> 4;
      float ra[4], rb[4];
#pragma unroll
      for (int j = 0; j < 4; j++) { ra[j] = tabA[64 * wQ + 16 * j + c]; rb[j] = tabB[64 * wQ + 16 * j + c]; }
      f32x4 cs[7], bi[7];
      auto tables = [&](int i) {
        const int col = (i < 6 ? 128 * wP + 16 * i : 96 + 16 * wP) + 4 * g;
        cs[i] = *(const LAS f32x4*)(tabC + col); bi[i] = *(const LAS f32x4*)(tabBias + col);
      };
      tables(0); tables(1);
      auto act = [&](int i, int j) {
        f32x4 v = ra[j] * acc[i][j] + (rb[j] * cs[i] + bi[i]);
#pragma unroll
        for (int e = 0; e < 4; e++) v[e] = fmaxf(v[e], 0.f);
        v = v * v;
        return (u32x2){cvtpk(v[0], v[1]), cvtpk(v[2], v[3])};
      };
#pragma unroll
      for (int u = 0; u < 3; u++) {
        tables(2 * u + 2);
        if (u < 2) tables(2 * u + 3);
        __builtin_amdgcn_sched_barrier(0);
        LAS char* own = sm + (QOFF + ((u + 2) & 3) * SLOT + wP * 8192 + 4 * wQ * 1024 + lane * 16);
#pragma unroll
        for (int j = 0; j < 4; j++) {
          const u32x2 a = act(2 * u, j), b = act(2 * u + 1, j);
          ro[4 * u + j] = (u32x4){a[0], a[1], b[0], b[1]};
          if (4 * u + j < EARLY) store_sc1(rr, ((4 * wP + u) * 8 + j) * 1024 + lane * 16, ro[4 * u + j]);
          *(LAS u32x4*)(own + (j ^ (2 * half)) * 1024) = ro[4 * u + j];
          __builtin_amdgcn_sched_barrier(0);
        }
      }
#pragma unroll
      for (int j = 0; j < 4; j++)
        *(LAS u32x2*)(sm + (QOFF + SLOT + (4 * wQ + j) * 1024 + lane * 16 + 8 * wP)) = act(6, j);
    }
    {
      const int lane = fresh_lane(), c = lane & 15, g = lane >> 4;
      Consumer<4, M_TR256, M_TILED> C;
      C.init(sm, wP, wQ, lane);
      f32x4 acc[4][4];
      wait_vm<EARLY - 4>();
      wait_lgkm0(); bar();
      C.load<2, 0, 4>();
#pragma unroll
      for (int j = 0; j < 2; j++) ro[12 + j] = *(const LAS u32x4*)(sm + (QOFF + SLOT + (4 * wQ + 2 * wP + j) * 1024 + lane * 16));
      auto put = [&](int hs, int n) {
        constexpr int LEFT = 14 - EARLY;
        for (int q = 0; q < LEFT; q++)
          if (q * 88 / LEFT == hs * 16 + n) {
            const int t = EARLY + q;
            store_sc1(rr, (t < 12 ? (4 * wP + (t >> 2)) * 8 + (t & 3) : 24 + 2 * wP + (t & 1)) * 1024 + lane * 16, ro[t]);
          }
      };
      C.half_with<2, 1, true>(acc, [&](int n) { put(0, n); });
      wait_lgkm0(); bar();
      C.half_with<3, 0, false>(acc, [&](int n) { put(1, n); });
      C.half_with<3, 1, false>(acc, [&](int n) { put(2, n); });
      wait_lgkm0(); bar();
      C.half_with<0, 0, false>(acc, [&](int n) { put(3, n); });
      C.half_with<0, 1, false>(acc, [&](int n) { put(4, n); });
      wait_lgkm0();
      C.half_with<-1, 0, false>(acc, [&](int n) { put(5, n); });
      __builtin_amdgcn_sched_barrier(0);
      wait_vm<0>();
      bar();
      C.load<1, 0, 4>();
      C.step<1, 4, 4>(acc); C.step<2, 4, 4>(acc); C.step<3, 4, 4>(acc);
      for (int it = 0; it < 4; it++) C.step4<4, 4>(acc);
      C.step<0, 4, 4>(acc); C.step<1, 4, 2>(acc);
      xch_send(ws + WS_X4, acc, wid, lane);
      u32x2 br[4];
#pragma unroll
      for (int i = 0; i < 4; i++) br[i] = *(const u32x2*)(b2 + et * 128 + 64 * wP + 16 * i + 4 * g);
      __builtin_amdgcn_sched_barrier(0);
      rsrc_t rx = mk_rsrc(ws + WS_X4 + ((size_t)(blockIdx.x ^ 4) * 4 + wid) * 4096, 4096);
      rsrc_t rf = mk_rsrc(ws + WS_FL + ((size_t)(FL_X4 * 16 + tt) * 64 + (cell ^ 32)) * 64, 64);
      u32x4 rv[4];
      u32 fv;
      C.step<2, 2, 2>(acc); C.step<3, 2, 2>(acc); C.step<0, 2, 2>(acc);
      flag_set(ws, FL_X4, tt, cell);
      C.step<1, 2, 2>(acc); C.step<2, 2, 2>(acc); C.step<3, 2, 2>(acc);
      flag_request(rf, 0, fv);
      __builtin_amdgcn_sched_barrier(0);
      C.step<0, 2, 2>(acc);
      wait_vm<0>();
      flag_wait(rf, 0, fv);
      xch_request(rx, rv, lane);
      __builtin_amdgcn_sched_barrier(0);
      C.last<1, 2>(acc);
      xch_finish(rv);
      rsrc_t ro = mk_rsrc(out + (size_t)(tt * 128 + 16 * m0) * D_ + et * 128 + 64 * wP, 31 * D_ * 2 + 128);
#pragma unroll
      for (int jj = 0; jj < 2; jj++)
#pragma unroll
        for (int u = 0; u < 2; u++) {
          const u32x4 o = rv[jj * 2 + u], h = *(const LAS u32x4*)(sm + XOFF + wid * 4096 + (jj * 2 + u) * 1024 + lane * 16);
          const f32x4 a = acc[2 * u][jj], b = acc[2 * u + 1][jj];
          const u32x2 ba = br[2 * u], bb = br[2 * u + 1];
          const u32 a0 = cvtpk(a[0] + blo(o[0]) + blo(ba[0]) + blo(h[0]), a[1] + bhi(o[0]) + bhi(ba[0]) + bhi(h[0]));
          const u32 a1 = cvtpk(a[2] + blo(o[1]) + blo(ba[1]) + blo(h[1]), a[3] + bhi(o[1]) + bhi(ba[1]) + bhi(h[1]));
          const u32 b0 = cvtpk(b[0] + blo(o[2]) + blo(bb[0]) + blo(h[2]), b[1] + bhi(o[2]) + bhi(bb[0]) + bhi(h[2]));
          const u32 b1_ = cvtpk(b[2] + blo(o[3]) + blo(bb[1]) + blo(h[3]), b[3] + bhi(o[3]) + bhi(bb[1]) + bhi(h[3]));
          const auto s0 = __builtin_amdgcn_permlane16_swap(a0, b0, false, false);
          const auto s1 = __builtin_amdgcn_permlane16_swap(a1, b1_, false, false);
          __builtin_amdgcn_raw_buffer_store_b128((u32x4){s0[0], s1[0], s0[1], s1[1]}, ro,
                                                 (16 * jj + c) * (D_ * 2) + (32 * u + 16 * (g & 1) + 8 * (g >> 1)) * 2, 0, 16);
        }
    }
  }
}

}

extern "C" void kernel_launch(void* const* d_in, const int* in_sizes, int n_in, void* d_out, int out_size, void* d_ws, size_t ws_size,
                              hipStream_t stream) {
  const u16* const* I = (const u16* const*)d_in;
  (void)hipFuncSetAttribute((const void*)fused_kernel, hipFuncAttributeMaxDynamicSharedMemorySize, LDS_BYTES);
  fused_kernel<<<256, 512, LDS_BYTES, stream>>>(I[0], I[1], I[2], I[3], I[4], I[6], I[7], I[8], I[9], (char*)d_ws, (u16*)d_out);
}
```
